```python
import torch

def _softmax(x, axis=-1):
    return torch.softmax(x, dim=axis)
def _gelu(x):
    return torch.nn.functional.gelu(x, approximate="tanh")
def _silu(x):
    return torch.nn.functional.silu(x)
def _sigmoid(x):
    return torch.sigmoid(x)
def _layernorm(x, axis=-1, eps=1e-5):
    m = x.mean(dim=axis, keepdim=True)
    v = x.var(dim=axis, keepdim=True, unbiased=False)
    return (x - m) / torch.sqrt(v + eps)
def _scatter_add(tgt, idx, src, axis=0):
    return tgt.clone().index_add_(axis, idx, src)
def _segment_sum(src, seg, n):
    out = torch.zeros((n,) + src.shape[1:], dtype=src.dtype)
    return out.index_add_(0, seg, src)
def _pad(x, pw):
    flat = tuple(v for pair in reversed(pw) for v in pair)
    return torch.nn.functional.pad(x, flat)

kernel_name = 'recurrent_scan__compressed_kv_cla__mixer_s_mlp__prompt'

def setup_inputs():

    _w = (lambda a: (lambda t: t.to(torch.bfloat16) if t.is_floating_point() else t)(torch.as_tensor(a)))
    def _A_setup():
        B, H, Sq, Sk, D, M = 1, 32, 2048, 2048, 128, 512
        Dc = 4 * D
        return {
            "x": _w((lambda *s: torch.randn(*s, dtype=torch.bfloat16) * 0.05)(B, Sq, M)),
            "c_kv": _w((lambda *s: torch.randn(*s, dtype=torch.bfloat16) * 0.2)(B, Sk, Dc)),
            "w_q": _w((lambda *s: torch.randn(*s, dtype=torch.bfloat16) * 3.2)(M, H, D) * (M ** -0.5)),
            "w_uk": _w((lambda *s: torch.randn(*s, dtype=torch.bfloat16) * 3.2)(Dc, H, D) * (Dc ** -0.5)),
            "w_uv": _w((lambda *s: torch.randn(*s, dtype=torch.bfloat16) * 0.2)(Dc, H, D) * (Dc ** -0.5)),
            "w_o": _w((lambda *s: torch.randn(*s, dtype=torch.bfloat16) * 0.2)(H, D, M) * ((H * D) ** -0.5)),
        }
    _A_in = _A_setup()

    def _B_setup():
        B, S, D, F = 1, 2048, 512, 1376
        return {
            "x":  _w((lambda *s: torch.randn(*s, dtype=torch.bfloat16) * 0.05)(B, S, D)),
            "w1": _w((lambda *s: torch.randn(*s, dtype=torch.bfloat16) * 3.2)(D, D) * (D ** -0.5)),
            "w2": _w((lambda *s: torch.randn(*s, dtype=torch.bfloat16) * 3.2)(D, D) * (D ** -0.5)),
            "w3": _w((lambda *s: torch.randn(*s, dtype=torch.bfloat16) * 3.2)(D, F) * (D ** -0.5)),
            "w4": _w((lambda *s: torch.randn(*s, dtype=torch.bfloat16) * 1.6)(F, D) * (F ** -0.5)),
        }
    _B_in = _B_setup()

    return {
        "A_x": _A_in["x"],
        "A_c_kv": _A_in["c_kv"],
        "A_w_q": _A_in["w_q"],
        "A_w_uk": _A_in["w_uk"],
        "A_w_uv": _A_in["w_uv"],
        "A_w_o": _A_in["w_o"],
        "B_x": _B_in["x"],
        "B_w1": _B_in["w1"],
        "B_w2": _B_in["w2"],
        "B_w3": _B_in["w3"],
        "B_w4": _B_in["w4"],
    }

def reference(A_x, A_c_kv, A_w_q, A_w_uk, A_w_uv, A_w_o, B_x, B_w1, B_w2, B_w3, B_w4):
    def _A_ref(x, c_kv, w_q, w_uk, w_uv, w_o):
        B, Sq, M = x.shape[0], x.shape[1], x.shape[2]
        Sk = c_kv.shape[1]
        H, D = w_q.shape[1], w_q.shape[2]
        q = torch.einsum("bqm,mhd->bhqd", x, w_q)
        k = torch.einsum("bkc,chd->bhkd", c_kv, w_uk)
        v = torch.einsum("bkc,chd->bhkd", c_kv, w_uv)
        scores = torch.einsum("bhqd,bhkd->bhqk", q, k) * (D ** -0.5)
        off = Sk - Sq
        qi = torch.arange(Sq)[:, None]
        kj = torch.arange(Sk)[None, :]
        cmask = kj <= (qi + off)
        scores = torch.where(cmask[None, None], scores, -1e9)
        p = _softmax(scores, axis=-1)
        ctx = torch.einsum("bhqk,bhkd->bhqd", p, v)
        return torch.einsum("bhqd,hdm->bqm", ctx, w_o)

    def _B_ref(x, w1, w2, w3, w4):
        B, S, D = x.shape
        h = _gelu(_layernorm(x, axis=-1) @ w1)
        c = D // 4
        g0 = _pad(h[:, :, 0*c:1*c], ((0, 0), (1, 0), (0, 0)))[:, :S, :]
        g1 = _pad(h[:, :, 1*c:2*c], ((0, 0), (0, 1), (0, 0)))[:, 1:, :]
        g2 = _pad(h[:, :, 2*c:3*c], ((0, 0), (2, 0), (0, 0)))[:, :S, :]
        g3 = _pad(h[:, :, 3*c:   ], ((0, 0), (0, 2), (0, 0)))[:, 2:, :]
        hs = torch.concatenate([g0, g1, g2, g3], axis=-1) @ w2
        x1 = x + hs
        h2 = _gelu(_layernorm(x1, axis=-1) @ w3) @ w4
        return x1 + h2

    x = A_x
    xn = _layernorm(x, axis=-1)
    s = _A_ref(xn, A_c_kv, A_w_q, A_w_uk, A_w_uv, A_w_o)
    g = _silu(_B_ref(xn, B_w1, B_w2, B_w3, B_w4))
    return x + s * g
```

```cpp
#include <hip/hip_runtime.h>
#include <hip/hip_bf16.h>

extern "C" __global__ __launch_bounds__(256, 1) void m1_kernel(const void* x, const void* ckv, const void* wq, const void* wuk, const void* wuv, const void* w1, const void* w2, const void* w3, void* ws, const void* wo, const void* w4, void* out) {
  const unsigned wid = __builtin_amdgcn_readfirstlane(threadIdx.x >> 6);
  const unsigned long long ka = (unsigned long long)__builtin_amdgcn_kernarg_segment_ptr();
  asm volatile(
"s_mov_b64 s[8:9], %0\n"
"s_mov_b32 s28, %1\n"
"s_mov_b32 s29, %2\n"
"s_load_dwordx4 s[12:15], s[8:9], 0\n"
"s_load_dwordx4 s[96:99], s[8:9], 24\n"
"s_load_dwordx2 s[46:47], s[8:9], 56\n"
"s_load_dwordx2 s[50:51], s[8:9], 64\n"
"v_mbcnt_lo_u32_b32 v0, -1, 0\n"
"v_mbcnt_hi_u32_b32 v0, -1, v0\n"
"v_lshlrev_b32 v1, 4, v0\n"
"v_mbcnt_lo_u32_b32 v0, -1, 0\n"
"v_mbcnt_hi_u32_b32 v0, -1, v0\n"
"s_and_b32 s52, s28, 7\n"
"s_lshr_b32 s37, s28, 3\n"
"s_and_b32 s30, s37, 3\n"
"s_lshr_b32 s31, s37, 2\n"
"v_lshlrev_b32 v1, 4, v0\n"
"v_lshrrev_b32 v2, 3, v0\n"
"v_lshlrev_b32 v2, 10, v2\n"
"v_lshrrev_b32 v3, 4, v0\n"
"v_and_b32 v4, 7, v0\n"
"v_xor_b32 v3, v3, v4\n"
"v_xor_b32 v4, 4, v3\n"
"v_lshl_add_u32 v3, v3, 4, v2\n"
"v_lshl_add_u32 v4, v4, 4, v2\n"
"s_lshl_b32 s37, s30, 1\n"
"s_add_u32 s37, s37, 0\n"
"s_and_b32 s37, s37, 7\n"
"s_lshl_b32 s64, s37, 10\n"
"s_lshl_b32 s37, s37, 13\n"
"v_add_u32 v16, s37, v3\n"
"s_lshl_b32 s37, s30, 1\n"
"s_add_u32 s37, s37, 1\n"
"s_and_b32 s37, s37, 7\n"
"s_lshl_b32 s65, s37, 10\n"
"s_lshl_b32 s37, s37, 13\n"
"v_add_u32 v17, s37, v4\n"
"s_lshl_b32 s37, s30, 1\n"
"s_add_u32 s37, s37, 2\n"
"s_and_b32 s37, s37, 7\n"
"s_lshl_b32 s66, s37, 10\n"
"s_lshl_b32 s37, s37, 13\n"
"v_add_u32 v18, s37, v3\n"
"s_lshl_b32 s37, s30, 1\n"
"s_add_u32 s37, s37, 3\n"
"s_and_b32 s37, s37, 7\n"
"s_lshl_b32 s67, s37, 10\n"
"s_lshl_b32 s37, s37, 13\n"
"v_add_u32 v19, s37, v4\n"
"s_lshl_b32 s37, s30, 1\n"
"s_add_u32 s37, s37, 4\n"
"s_and_b32 s37, s37, 7\n"
"s_lshl_b32 s68, s37, 10\n"
"s_lshl_b32 s37, s37, 13\n"
"v_add_u32 v20, s37, v3\n"
"s_lshl_b32 s37, s30, 1\n"
"s_add_u32 s37, s37, 5\n"
"s_and_b32 s37, s37, 7\n"
"s_lshl_b32 s69, s37, 10\n"
"s_lshl_b32 s37, s37, 13\n"
"v_add_u32 v21, s37, v4\n"
"s_lshl_b32 s37, s30, 1\n"
"s_add_u32 s37, s37, 6\n"
"s_and_b32 s37, s37, 7\n"
"s_lshl_b32 s70, s37, 10\n"
"s_lshl_b32 s37, s37, 13\n"
"v_add_u32 v22, s37, v3\n"
"s_lshl_b32 s37, s30, 1\n"
"s_add_u32 s37, s37, 7\n"
"s_and_b32 s37, s37, 7\n"
"s_lshl_b32 s71, s37, 10\n"
"s_lshl_b32 s37, s37, 13\n"
"v_add_u32 v23, s37, v4\n"
"v_lshrrev_b32 v2, 3, v0\n"
"v_lshlrev_b32 v2, 13, v2\n"
"v_bfe_u32 v3, v0, 4, 1\n"
"v_lshlrev_b32 v3, 2, v3\n"
"v_and_b32 v4, 7, v0\n"
"v_xor_b32 v3, v3, v4\n"
"v_lshl_add_u32 v3, v3, 4, v2\n"
"s_add_u32 s37, s31, 0\n"
"s_and_b32 s37, s37, 7\n"
"s_lshl_b32 s72, s37, 10\n"
"s_lshl_b32 s37, s37, 16\n"
"v_add_u32 v24, s37, v3\n"
"s_add_u32 s37, s31, 1\n"
"s_and_b32 s37, s37, 7\n"
"s_lshl_b32 s73, s37, 10\n"
"s_lshl_b32 s37, s37, 16\n"
"v_add_u32 v25, s37, v3\n"
"s_add_u32 s37, s31, 2\n"
"s_and_b32 s37, s37, 7\n"
"s_lshl_b32 s74, s37, 10\n"
"s_lshl_b32 s37, s37, 16\n"
"v_add_u32 v26, s37, v3\n"
"s_add_u32 s37, s31, 3\n"
"s_and_b32 s37, s37, 7\n"
"s_lshl_b32 s75, s37, 10\n"
"s_lshl_b32 s37, s37, 16\n"
"v_add_u32 v27, s37, v3\n"
"s_add_u32 s37, s31, 4\n"
"s_and_b32 s37, s37, 7\n"
"s_lshl_b32 s76, s37, 10\n"
"s_lshl_b32 s37, s37, 16\n"
"v_add_u32 v28, s37, v3\n"
"s_add_u32 s37, s31, 5\n"
"s_and_b32 s37, s37, 7\n"
"s_lshl_b32 s77, s37, 10\n"
"s_lshl_b32 s37, s37, 16\n"
"v_add_u32 v29, s37, v3\n"
"s_add_u32 s37, s31, 6\n"
"s_and_b32 s37, s37, 7\n"
"s_lshl_b32 s78, s37, 10\n"
"s_lshl_b32 s37, s37, 16\n"
"v_add_u32 v30, s37, v3\n"
"s_add_u32 s37, s31, 7\n"
"s_and_b32 s37, s37, 7\n"
"s_lshl_b32 s79, s37, 10\n"
"s_lshl_b32 s37, s37, 16\n"
"v_add_u32 v31, s37, v3\n"
"s_lshl_b32 s53, s31, 18\n"
"s_lshl_b32 s37, s29, 16\n"
"s_add_u32 s53, s53, s37\n"
"s_and_b32 s37, s30, 1\n"
"s_lshl_b32 s37, s37, 9\n"
"s_lshl_b32 s38, s52, 10\n"
"s_add_u32 s59, s37, s38\n"
"s_lshl_b32 s37, s29, 7\n"
"s_add_u32 s59, s59, s37\n"
"s_lshl_b32 s60, s29, 13\n"
"s_add_u32 s43, s60, 32768\n"
"s_add_u32 s64, s64, s60\n"
"s_add_u32 s72, s72, s43\n"
"s_add_u32 s65, s65, s60\n"
"s_add_u32 s73, s73, s43\n"
"s_add_u32 s66, s66, s60\n"
"s_add_u32 s74, s74, s43\n"
"s_add_u32 s67, s67, s60\n"
"s_add_u32 s75, s75, s43\n"
"s_add_u32 s68, s68, s60\n"
"s_add_u32 s76, s76, s43\n"
"s_add_u32 s69, s69, s60\n"
"s_add_u32 s77, s77, s43\n"
"s_add_u32 s70, s70, s60\n"
"s_add_u32 s78, s78, s43\n"
"s_add_u32 s71, s71, s60\n"
"s_add_u32 s79, s79, s43\n"
"v_and_b32 v2, 31, v0\n"
"v_bfe_u32 v3, v0, 1, 3\n"
"v_lshrrev_b32 v4, 5, v0\n"
"v_xor_b32 v3, v3, v4\n"
"v_lshlrev_b32 v2, 7, v2\n"
"s_lshr_b32 s37, s29, 1\n"
"s_lshl_b32 s37, s37, 14\n"
"v_add_u32 v2, s37, v2\n"
"v_xor_b32 v4, 0, v3\n"
"v_lshl_add_u32 v8, v4, 4, v2\n"
"v_xor_b32 v4, 2, v3\n"
"v_lshl_add_u32 v9, v4, 4, v2\n"
"v_xor_b32 v4, 4, v3\n"
"v_lshl_add_u32 v10, v4, 4, v2\n"
"v_xor_b32 v4, 6, v3\n"
"v_lshl_add_u32 v11, v4, 4, v2\n"
"v_lshrrev_b32 v2, 5, v0\n"
"v_bfe_u32 v3, v0, 2, 2\n"
"v_lshl_add_u32 v2, v2, 3, v3\n"
"v_lshlrev_b32 v2, 7, v2\n"
"v_and_b32 v4, 1, v0\n"
"v_lshl_add_u32 v2, v4, 3, v2\n"
"s_and_b32 s37, s29, 1\n"
"s_lshl_b32 s37, s37, 14\n"
"s_add_u32 s37, s37, 32768\n"
"v_add_u32 v2, s37, v2\n"
"v_bfe_u32 v4, v0, 4, 1\n"
"v_bfe_u32 v5, v0, 1, 1\n"
"v_lshl_add_u32 v4, v4, 1, v5\n"
"v_lshrrev_b32 v3, 1, v3\n"
"v_lshlrev_b32 v3, 2, v3\n"
"v_xor_b32 v4, v4, v3\n"
"v_lshl_add_u32 v12, v4, 4, v2\n"
"v_xor_b32 v4, 4, v4\n"
"v_lshl_add_u32 v13, v4, 4, v2\n"
"s_waitcnt lgkmcnt(0)\n"
"s_load_dword s11, s[50:51], 0\n"
"s_mov_b32 s16, s14\n"
"s_and_b32 s17, s15, 0xffff\n"
"s_mov_b32 s18, 2097152\n"
"s_mov_b32 s19, 0x20000\n"
"s_cmp_lt_u32 s30, 2\n"
"s_cselect_b32 s37, s96, s98\n"
"s_cselect_b32 s38, s97, s99\n"
"s_mov_b32 s20, s37\n"
"s_and_b32 s21, s38, 0xffff\n"
"s_mov_b32 s22, 4194304\n"
"s_mov_b32 s23, 0x20000\n"
"s_mov_b64 s[44:45], s[12:13]\n"
"s_mov_b32 s12, s44\n"
"s_and_b32 s13, s45, 0xffff\n"
"s_mov_b32 s14, 2097152\n"
"s_mov_b32 s15, 0x20000\n"
"s_lshl_b32 s34, s28, 3\n"
"s_lshl_b32 s35, s29, 1\n"
"s_add_u32 s34, s34, s35\n"
"s_lshl_b32 s34, s34, 10\n"
"v_lshrrev_b32 v2, 5, v0\n"
"v_lshlrev_b32 v2, 10, v2\n"
"v_and_b32 v3, 31, v0\n"
"v_lshl_add_u32 v2, v3, 4, v2\n"
"buffer_load_dwordx4 v[128:131], v2, s[12:15], s34 offen\n"
"buffer_load_dwordx4 v[132:135], v2, s[12:15], s34 offen offset:512\n"
"s_add_u32 m0, s64, 0\n"
"s_nop 0\n"
"buffer_load_dwordx4 v16, s[16:19], s53 offen lds\n"
"s_add_u32 m0, s72, 0\n"
"s_nop 0\n"
"buffer_load_dwordx4 v24, s[20:23], s59 offen lds\n"
"s_add_u32 m0, s65, 0\n"
"s_nop 0\n"
"buffer_load_dwordx4 v17, s[16:19], s53 offen lds\n"
"s_add_u32 m0, s73, 0\n"
"s_nop 0\n"
"buffer_load_dwordx4 v25, s[20:23], s59 offen lds\n"
"s_add_u32 m0, s66, 0\n"
"s_nop 0\n"
"buffer_load_dwordx4 v18, s[16:19], s53 offen lds\n"
"s_add_u32 m0, s74, 0\n"
"s_nop 0\n"
"buffer_load_dwordx4 v26, s[20:23], s59 offen lds\n"
"s_add_u32 m0, s67, 0\n"
"s_nop 0\n"
"buffer_load_dwordx4 v19, s[16:19], s53 offen lds\n"
"s_add_u32 m0, s75, 0\n"
"s_nop 0\n"
"buffer_load_dwordx4 v27, s[20:23], s59 offen lds\n"
"s_add_u32 m0, s68, 0\n"
"s_nop 0\n"
"buffer_load_dwordx4 v20, s[16:19], s53 offen lds\n"
"s_add_u32 m0, s76, 0\n"
"s_nop 0\n"
"buffer_load_dwordx4 v28, s[20:23], s59 offen lds\n"
"s_add_u32 m0, s69, 0\n"
"s_nop 0\n"
"buffer_load_dwordx4 v21, s[16:19], s53 offen lds\n"
"s_add_u32 m0, s77, 0\n"
"s_nop 0\n"
"buffer_load_dwordx4 v29, s[20:23], s59 offen lds\n"
"s_add_u32 m0, s70, 0\n"
"s_nop 0\n"
"buffer_load_dwordx4 v22, s[16:19], s53 offen lds\n"
"s_add_u32 m0, s78, 0\n"
"s_nop 0\n"
"buffer_load_dwordx4 v30, s[20:23], s59 offen lds\n"
"s_add_u32 m0, s71, 0\n"
"s_nop 0\n"
"buffer_load_dwordx4 v23, s[16:19], s53 offen lds\n"
"s_add_u32 m0, s79, 0\n"
"s_nop 0\n"
"buffer_load_dwordx4 v31, s[20:23], s59 offen lds\n"
"s_add_u32 s53, s53, 128\n"
"s_add_u32 s59, s59, 524288\n"
"s_mov_b32 s80, s46\n"
"s_and_b32 s81, s47, 0xffff\n"
"s_mov_b32 s82, 1409024\n"
"s_mov_b32 s83, 0x20000\n"
"v_lshrrev_b32 v176, 2, v0\n"
"v_mul_u32_u24 v176, 2752, v176\n"
"v_and_b32 v177, 3, v0\n"
"v_lshl_add_u32 v176, v177, 4, v176\n"
"s_mul_i32 s84, s29, 352256\n"
"s_lshl_b32 s85, s28, 6\n"
"s_add_u32 s84, s84, s85\n"
"s_cmp_lt_u32 s28, 43\n"
"s_cselect_b32 s84, s84, 1073741824\n"
"buffer_load_dwordx4 v[180:183], v176, s[80:83], s84 offen\n"
"s_add_u32 s84, s84, 44032\n"
"buffer_load_dwordx4 v[184:187], v176, s[80:83], s84 offen\n"
"s_add_u32 s84, s84, 44032\n"
"buffer_load_dwordx4 v[188:191], v176, s[80:83], s84 offen\n"
"s_add_u32 s84, s84, 44032\n"
"buffer_load_dwordx4 v[192:195], v176, s[80:83], s84 offen\n"
"s_add_u32 s84, s84, 44032\n"
"buffer_load_dwordx4 v[196:199], v176, s[80:83], s84 offen\n"
"s_add_u32 s84, s84, 44032\n"
"buffer_load_dwordx4 v[200:203], v176, s[80:83], s84 offen\n"
"s_add_u32 s84, s84, 44032\n"
"buffer_load_dwordx4 v[204:207], v176, s[80:83], s84 offen\n"
"s_add_u32 s84, s84, 44032\n"
"buffer_load_dwordx4 v[208:211], v176, s[80:83], s84 offen\n"
"s_add_u32 s84, s84, 44032\n"
"s_lshr_b32 s34, s28, 3\n"
"s_lshl_b32 s34, s34, 2\n"
"s_add_u32 s34, s34, s29\n"
"s_getpc_b64 s[36:37]\n"
".Lm1_pc:\n"
"v_lshlrev_b32 v2, 7, v0\n"
"s_lshl_b32 s34, s34, 13\n"
"v_add_u32 v2, s34, v2\n"
"v_min_u32 v2, .Lm1_pfend-.Lm1_pc-4, v2\n"
"global_load_dword v215, v2, s[36:37]\n"
"s_mov_b32 s40, 17825792\n"
"s_movk_i32 s38, 1024\n"
"s_movk_i32 s37, 4096\n"
"s_cmp_lt_u32 s30, 2\n"
"s_cselect_b32 s39, 1048576, s40\n"
"s_cselect_b32 s88, 2048, s38\n"
"s_cselect_b32 s87, 1024, s37\n"
"s_lshl_b32 s89, s88, 1\n"
"s_add_u32 s90, s89, s88\n"
"s_add_u32 s37, s50, s39\n"
"s_addc_u32 s38, s51, 0\n"
"s_mov_b32 s92, s37\n"
"s_and_b32 s93, s38, 0xffff\n"
"s_mov_b32 s94, 16777216\n"
"s_mov_b32 s95, 0x20000\n"
"s_lshl_b32 s37, s52, 2\n"
"s_and_b32 s38, s30, 1\n"
"s_lshl_b32 s38, s38, 1\n"
"s_add_u32 s37, s37, s38\n"
"s_and_b32 s38, s29, 1\n"
"s_add_u32 s37, s37, s38\n"
"s_lshl_b32 s91, s37, 19\n"
"s_lshl_b32 s37, s31, 3\n"
"s_lshr_b32 s38, s29, 1\n"
"s_lshl_b32 s38, s38, 2\n"
"s_add_u32 s37, s37, s38\n"
"s_lshl_b32 s37, s37, 13\n"
"s_add_u32 s91, s91, s37\n"
"s_waitcnt lgkmcnt(0)\n"
"s_add_u32 s11, s11, 1\n"
"s_cmp_lt_u32 s30, 2\n"
"s_cselect_b32 s10, 1, 0\n"
"s_cmp_lt_u32 s29, 2\n"
"s_cselect_b32 s24, s16, s20\n"
"s_cselect_b32 s25, s17, s21\n"
"s_cselect_b32 s26, s18, s22\n"
"s_cselect_b32 s27, s19, s23\n"
"s_movk_i32 s34, 128\n"
"s_mov_b32 s35, 524288\n"
"s_cselect_b32 s60, s34, s35\n"
"s_lshl_b32 s34, s31, 18\n"
"s_add_u32 s34, s34, 384\n"
"s_and_b32 s35, s30, 1\n"
"s_lshl_b32 s35, s35, 9\n"
"s_lshl_b32 s36, s52, 10\n"
"s_add_u32 s35, s35, s36\n"
"s_add_u32 s35, s35, 1572864\n"
"s_cmp_lt_u32 s29, 2\n"
"s_cselect_b32 s48, s34, s35\n"
"s_sub_u32 s48, s48, s60\n"
"s_lshl_b32 s34, s30, 6\n"
"s_and_b32 s35, s29, 1\n"
"s_lshl_b32 s36, s35, 5\n"
"s_add_u32 s34, s34, s36\n"
"v_add_u32 v222, s34, v0\n"
"v_lshlrev_b32 v222, 10, v222\n"
"s_lshl_b32 s34, s31, 3\n"
"s_lshl_b32 s35, s35, 2\n"
"s_add_u32 s34, s34, s35\n"
"v_lshrrev_b32 v223, 2, v0\n"
"v_add_u32 v223, s34, v223\n"
"v_lshlrev_b32 v223, 13, v223\n"
"v_and_b32 v224, 3, v0\n"
"v_lshl_add_u32 v223, v224, 7, v223\n"
"s_cmp_lt_u32 s29, 2\n"
"s_cselect_b64 vcc, -1, 0\n"
"s_cselect_b32 s34, 32, 16\n"
"s_nop 1\n"
"v_cndmask_b32 v222, v223, v222, vcc\n"
"v_cmp_gt_u32 vcc, s34, v0\n"
"v_mov_b32 v223, 1073741824\n"
"s_nop 0\n"
"v_cndmask_b32 v222, v223, v222, vcc\n"
"s_mov_b32 s37, s48\n"
"s_cmp_eq_u32 s10, 1\n"
"s_cbranch_scc0 .Lkv_V\n"
"s_waitcnt vmcnt(9)\n"
"s_barrier\n"
"ds_read_b128 v[64:67], v8 offset:0\n"
"ds_read_b64_tr_b16 v[80:81], v12 offset:0\n"
"ds_read_b64_tr_b16 v[82:83], v12 offset:512\n"
"ds_read_b64_tr_b16 v[84:85], v13 offset:0\n"
"ds_read_b64_tr_b16 v[86:87], v13 offset:512\n"
"ds_read_b64_tr_b16 v[88:89], v12 offset:8192\n"
"ds_read_b64_tr_b16 v[90:91], v12 offset:8704\n"
"ds_read_b64_tr_b16 v[92:93], v13 offset:8192\n"
"ds_read_b64_tr_b16 v[94:95], v13 offset:8704\n"
"ds_read_b128 v[68:71], v8 offset:4096\n"
"ds_read_b128 v[72:75], v8 offset:8192\n"
"ds_read_b128 v[76:79], v8 offset:12288\n"
"s_waitcnt lgkmcnt(0)\n"
"s_add_u32 m0, s64, 65536\n"
"v_mfma_f32_32x32x16_bf16 a[0:15], v[80:83], v[64:67], 0\n"
"buffer_load_dwordx4 v16, s[16:19], s53 offen lds\n"
"ds_read_b128 v[96:99], v9 offset:0\n"
"s_load_dwordx2 s[44:45], s[8:9], 40\n"
"v_lshlrev_b32 v144, 16, v128\n"
"v_mfma_f32_32x32x16_bf16 a[16:31], v[84:87], v[64:67], 0\n"
"ds_read_b64_tr_b16 v[112:113], v12 offset:2048\n"
"v_and_b32 v145, 0xffff0000, v128\n"
"v_lshlrev_b32 v146, 16, v129\n"
"v_and_b32 v147, 0xffff0000, v129\n"
"s_add_u32 m0, s72, 65536\n"
"v_mfma_f32_32x32x16_bf16 a[32:47], v[88:91], v[64:67], 0\n"
"buffer_load_dwordx4 v24, s[20:23], s59 offen lds\n"
"ds_read_b64_tr_b16 v[114:115], v12 offset:2560\n"
"v_lshlrev_b32 v148, 16, v130\n"
"v_and_b32 v149, 0xffff0000, v130\n"
"v_mfma_f32_32x32x16_bf16 a[48:63], v[92:95], v[64:67], 0\n"
"ds_read_b64_tr_b16 v[116:117], v13 offset:2048\n"
"v_lshlrev_b32 v150, 16, v131\n"
"v_and_b32 v151, 0xffff0000, v131\n"
"v_lshlrev_b32 v152, 16, v132\n"
"s_add_u32 m0, s65, 65536\n"
"v_mfma_f32_32x32x16_bf16 a[64:79], v[80:83], v[68:71], 0\n"
"buffer_load_dwordx4 v17, s[16:19], s53 offen lds\n"
"ds_read_b64_tr_b16 v[118:119], v13 offset:2560\n"
"v_and_b32 v153, 0xffff0000, v132\n"
"v_lshlrev_b32 v154, 16, v133\n"
"v_mfma_f32_32x32x16_bf16 a[80:95], v[84:87], v[68:71], 0\n"
"ds_read_b64_tr_b16 v[120:121], v12 offset:10240\n"
"v_and_b32 v155, 0xffff0000, v133\n"
"v_lshlrev_b32 v156, 16, v134\n"
"v_and_b32 v157, 0xffff0000, v134\n"
"s_add_u32 m0, s73, 65536\n"
"v_mfma_f32_32x32x16_bf16 a[96:111], v[88:91], v[68:71], 0\n"
"buffer_load_dwordx4 v25, s[20:23], s59 offen lds\n"
"ds_read_b64_tr_b16 v[122:123], v12 offset:10752\n"
"v_lshlrev_b32 v158, 16, v135\n"
"v_and_b32 v159, 0xffff0000, v135\n"
"v_mfma_f32_32x32x16_bf16 a[112:127], v[92:95], v[68:71], 0\n"
"ds_read_b64_tr_b16 v[124:125], v13 offset:10240\n"
"v_and_b32 v176, 31, v0\n"
"v_mul_u32_u24 v176, 144, v176\n"
"v_lshrrev_b32 v177, 5, v0\n"
"s_add_u32 m0, s66, 65536\n"
"v_mfma_f32_32x32x16_bf16 a[128:143], v[80:83], v[72:75], 0\n"
"buffer_load_dwordx4 v18, s[16:19], s53 offen lds\n"
"ds_read_b64_tr_b16 v[126:127], v13 offset:10752\n"
"v_lshl_add_u32 v176, v177, 4, v176\n"
"s_lshl_b32 s84, s29, 5\n"
"v_mfma_f32_32x32x16_bf16 a[144:159], v[84:87], v[72:75], 0\n"
"ds_read_b128 v[100:103], v9 offset:4096\n"
"s_add_u32 s84, s84, 131072\n"
"v_add_u32 v176, s84, v176\n"
"v_add_f32 v160, v144, v145\n"
"s_add_u32 m0, s74, 65536\n"
"v_mfma_f32_32x32x16_bf16 a[160:175], v[88:91], v[72:75], 0\n"
"buffer_load_dwordx4 v26, s[20:23], s59 offen lds\n"
"ds_read_b128 v[104:107], v9 offset:8192\n"
"v_add_f32 v160, v160, v146\n"
"v_add_f32 v160, v160, v147\n"
"v_mfma_f32_32x32x16_bf16 a[176:191], v[92:95], v[72:75], 0\n"
"ds_read_b128 v[108:111], v9 offset:12288\n"
"v_add_f32 v160, v160, v148\n"
"v_add_f32 v160, v160, v149\n"
"v_add_f32 v160, v160, v150\n"
"s_add_u32 m0, s67, 65536\n"
"v_mfma_f32_32x32x16_bf16 a[192:207], v[80:83], v[76:79], 0\n"
"buffer_load_dwordx4 v19, s[16:19], s53 offen lds\n"
"v_add_f32 v160, v160, v151\n"
"v_add_f32 v160, v160, v152\n"
"v_add_f32 v160, v160, v153\n"
"v_mfma_f32_32x32x16_bf16 a[208:223], v[84:87], v[76:79], 0\n"
"v_add_f32 v160, v160, v154\n"
"v_add_f32 v160, v160, v155\n"
"v_add_f32 v160, v160, v156\n"
"v_add_f32 v160, v160, v157\n"
"s_add_u32 m0, s75, 65536\n"
"v_mfma_f32_32x32x16_bf16 a[224:239], v[88:91], v[76:79], 0\n"
"buffer_load_dwordx4 v27, s[20:23], s59 offen lds\n"
"v_add_f32 v160, v160, v158\n"
"v_add_f32 v160, v160, v159\n"
"v_mfma_f32_32x32x16_bf16 a[240:255], v[92:95], v[76:79], 0\n"
"s_nop 1\n"
"v_add_f32_dpp v160, v160, v160 quad_perm:[1,0,3,2] row_mask:0xf bank_mask:0xf\n"
"s_waitcnt lgkmcnt(0)\n"
"s_add_u32 m0, s68, 65536\n"
"v_mfma_f32_32x32x16_bf16 a[0:15], v[112:115], v[96:99], a[0:15]\n"
"buffer_load_dwordx4 v20, s[16:19], s53 offen lds\n"
"ds_read_b128 v[64:67], v10 offset:0\n"
"s_nop 1\n"
"v_mfma_f32_32x32x16_bf16 a[16:31], v[116:119], v[96:99], a[16:31]\n"
"ds_read_b64_tr_b16 v[80:81], v12 offset:4096\n"
"v_add_f32_dpp v160, v160, v160 quad_perm:[2,3,0,1] row_mask:0xf bank_mask:0xf\n"
"s_nop 1\n"
"s_add_u32 m0, s76, 65536\n"
"v_mfma_f32_32x32x16_bf16 a[32:47], v[120:123], v[96:99], a[32:47]\n"
"buffer_load_dwordx4 v28, s[20:23], s59 offen lds\n"
"ds_read_b64_tr_b16 v[82:83], v12 offset:4608\n"
"v_add_f32_dpp v160, v160, v160 row_half_mirror row_mask:0xf bank_mask:0xf\n"
"v_mfma_f32_32x32x16_bf16 a[48:63], v[124:127], v[96:99], a[48:63]\n"
"ds_read_b64_tr_b16 v[84:85], v13 offset:4096\n"
"s_nop 1\n"
"v_add_f32_dpp v160, v160, v160 row_mirror row_mask:0xf bank_mask:0xf\n"
"s_add_u32 m0, s69, 65536\n"
"v_mfma_f32_32x32x16_bf16 a[64:79], v[112:115], v[100:103], a[64:79]\n"
"buffer_load_dwordx4 v21, s[16:19], s53 offen lds\n"
"ds_read_b64_tr_b16 v[86:87], v13 offset:4608\n"
"v_mov_b32 v163, v160\n"
"v_mfma_f32_32x32x16_bf16 a[80:95], v[116:119], v[100:103], a[80:95]\n"
"ds_read_b64_tr_b16 v[88:89], v12 offset:12288\n"
"s_nop 1\n"
"v_permlane16_swap_b32 v160, v163\n"
"s_add_u32 m0, s77, 65536\n"
"v_mfma_f32_32x32x16_bf16 a[96:111], v[120:123], v[100:103], a[96:111]\n"
"buffer_load_dwordx4 v29, s[20:23], s59 offen lds\n"
"ds_read_b64_tr_b16 v[90:91], v12 offset:12800\n"
"v_add_f32 v160, v160, v163\n"
"v_mul_f32 v160, 0x3b000000, v160\n"
"v_mfma_f32_32x32x16_bf16 a[112:127], v[124:127], v[100:103], a[112:127]\n"
"ds_read_b64_tr_b16 v[92:93], v13 offset:12288\n"
"v_mov_b32 v162, v160\n"
"v_cvt_pk_bf16_f32 v163, v162, v162\n"
"v_lshlrev_b32 v162, 16, v163\n"
"s_add_u32 m0, s70, 65536\n"
"v_mfma_f32_32x32x16_bf16 a[128:143], v[112:115], v[104:107], a[128:143]\n"
"buffer_load_dwordx4 v22, s[16:19], s53 offen lds\n"
"ds_read_b64_tr_b16 v[94:95], v13 offset:12800\n"
"v_sub_f32 v164, v144, v160\n"
"v_mul_f32 v161, v164, v164\n"
"v_mfma_f32_32x32x16_bf16 a[144:159], v[116:119], v[104:107], a[144:159]\n"
"ds_read_b128 v[68:71], v10 offset:4096\n"
"v_sub_f32 v165, v145, v160\n"
"v_fmac_f32 v161, v165, v165\n"
"v_sub_f32 v164, v146, v160\n"
"s_add_u32 m0, s78, 65536\n"
"v_mfma_f32_32x32x16_bf16 a[160:175], v[120:123], v[104:107], a[160:175]\n"
"buffer_load_dwordx4 v30, s[20:23], s59 offen lds\n"
"ds_read_b128 v[72:75], v10 offset:8192\n"
"v_fmac_f32 v161, v164, v164\n"
"v_sub_f32 v165, v147, v160\n"
"v_mfma_f32_32x32x16_bf16 a[176:191], v[124:127], v[104:107], a[176:191]\n"
"ds_read_b128 v[76:79], v10 offset:12288\n"
"v_fmac_f32 v161, v165, v165\n"
"v_sub_f32 v164, v148, v160\n"
"v_fmac_f32 v161, v164, v164\n"
"s_add_u32 m0, s71, 65536\n"
"v_mfma_f32_32x32x16_bf16 a[192:207], v[112:115], v[108:111], a[192:207]\n"
"buffer_load_dwordx4 v23, s[16:19], s53 offen lds\n"
"v_sub_f32 v165, v149, v160\n"
"v_fmac_f32 v161, v165, v165\n"
"v_sub_f32 v164, v150, v160\n"
"v_mfma_f32_32x32x16_bf16 a[208:223], v[116:119], v[108:111], a[208:223]\n"
"v_fmac_f32 v161, v164, v164\n"
"v_sub_f32 v165, v151, v160\n"
"v_fmac_f32 v161, v165, v165\n"
"v_sub_f32 v164, v152, v160\n"
"s_add_u32 m0, s79, 65536\n"
"v_mfma_f32_32x32x16_bf16 a[224:239], v[120:123], v[108:111], a[224:239]\n"
"buffer_load_dwordx4 v31, s[20:23], s59 offen lds\n"
"v_fmac_f32 v161, v164, v164\n"
"v_sub_f32 v165, v153, v160\n"
"v_fmac_f32 v161, v165, v165\n"
"v_mfma_f32_32x32x16_bf16 a[240:255], v[124:127], v[108:111], a[240:255]\n"
"v_sub_f32 v164, v154, v160\n"
"v_fmac_f32 v161, v164, v164\n"
"v_sub_f32 v165, v155, v160\n"
"s_waitcnt lgkmcnt(0)\n"
"v_mfma_f32_32x32x16_bf16 a[0:15], v[80:83], v[64:67], a[0:15]\n"
"ds_read_b128 v[96:99], v11 offset:0\n"
"v_fmac_f32 v161, v165, v165\n"
"v_sub_f32 v164, v156, v160\n"
"v_fmac_f32 v161, v164, v164\n"
"v_sub_f32 v165, v157, v160\n"
"v_mfma_f32_32x32x16_bf16 a[16:31], v[84:87], v[64:67], a[16:31]\n"
"ds_read_b64_tr_b16 v[112:113], v12 offset:6144\n"
"v_fmac_f32 v161, v165, v165\n"
"v_sub_f32 v164, v158, v160\n"
"v_fmac_f32 v161, v164, v164\n"
"s_add_u32 s37, s37, s60\n"
"v_mfma_f32_32x32x16_bf16 a[32:47], v[88:91], v[64:67], a[32:47]\n"
"buffer_load_dword v225, v222, s[24:27], s37 offen\n"
"ds_read_b64_tr_b16 v[114:115], v12 offset:6656\n"
"v_sub_f32 v165, v159, v160\n"
"v_fmac_f32 v161, v165, v165\n"
"v_mfma_f32_32x32x16_bf16 a[48:63], v[92:95], v[64:67], a[48:63]\n"
"ds_read_b64_tr_b16 v[116:117], v13 offset:6144\n"
"s_nop 1\n"
"v_add_f32_dpp v161, v161, v161 quad_perm:[1,0,3,2] row_mask:0xf bank_mask:0xf\n"
"v_mfma_f32_32x32x16_bf16 a[64:79], v[80:83], v[68:71], a[64:79]\n"
"ds_read_b64_tr_b16 v[118:119], v13 offset:6656\n"
"s_nop 1\n"
"v_add_f32_dpp v161, v161, v161 quad_perm:[2,3,0,1] row_mask:0xf bank_mask:0xf\n"
"v_mfma_f32_32x32x16_bf16 a[80:95], v[84:87], v[68:71], a[80:95]\n"
"ds_read_b64_tr_b16 v[120:121], v12 offset:14336\n"
"s_nop 1\n"
"v_add_f32_dpp v161, v161, v161 row_half_mirror row_mask:0xf bank_mask:0xf\n"
"v_mfma_f32_32x32x16_bf16 a[96:111], v[88:91], v[68:71], a[96:111]\n"
"ds_read_b64_tr_b16 v[122:123], v12 offset:14848\n"
"s_nop 1\n"
"v_add_f32_dpp v161, v161, v161 row_mirror row_mask:0xf bank_mask:0xf\n"
"v_mov_b32 v163, v161\n"
"v_mfma_f32_32x32x16_bf16 a[112:127], v[92:95], v[68:71], a[112:127]\n"
"ds_read_b64_tr_b16 v[124:125], v13 offset:14336\n"
"s_nop 1\n"
"v_permlane16_swap_b32 v161, v163\n"
"v_add_f32 v161, v161, v163\n"
"v_mfma_f32_32x32x16_bf16 a[128:143], v[80:83], v[72:75], a[128:143]\n"
"ds_read_b64_tr_b16 v[126:127], v13 offset:14848\n"
"v_mul_f32 v161, 0x3b000000, v161\n"
"v_cvt_pk_bf16_f32 v163, v161, v161\n"
"v_lshlrev_b32 v161, 16, v163\n"
"v_add_f32 v161, 0x3727c5ac, v161\n"
"v_mfma_f32_32x32x16_bf16 a[144:159], v[84:87], v[72:75], a[144:159]\n"
"ds_read_b128 v[100:103], v11 offset:4096\n"
"v_cvt_pk_bf16_f32 v163, v161, v161\n"
"v_lshlrev_b32 v161, 16, v163\n"
"v_sqrt_f32 v161, v161\n"
"v_mfma_f32_32x32x16_bf16 a[160:175], v[88:91], v[72:75], a[160:175]\n"
"ds_read_b128 v[104:107], v11 offset:8192\n"
"s_nop 0\n"
"v_cvt_pk_bf16_f32 v163, v161, v161\n"
"v_lshlrev_b32 v161, 16, v163\n"
"v_mfma_f32_32x32x16_bf16 a[176:191], v[92:95], v[72:75], a[176:191]\n"
"ds_read_b128 v[108:111], v11 offset:12288\n"
"v_rcp_f32 v161, v161\n"
"v_sub_f32 v144, v144, v162\n"
"v_sub_f32 v145, v145, v162\n"
"v_mfma_f32_32x32x16_bf16 a[192:207], v[80:83], v[76:79], a[192:207]\n"
"v_xor_b32 v8, 0x10000, v8\n"
"v_xor_b32 v9, 0x10000, v9\n"
"v_sub_f32 v146, v146, v162\n"
"v_sub_f32 v147, v147, v162\n"
"v_sub_f32 v148, v148, v162\n"
"v_mfma_f32_32x32x16_bf16 a[208:223], v[84:87], v[76:79], a[208:223]\n"
"v_xor_b32 v10, 0x10000, v10\n"
"v_xor_b32 v11, 0x10000, v11\n"
"v_sub_f32 v149, v149, v162\n"
"v_sub_f32 v150, v150, v162\n"
"v_sub_f32 v151, v151, v162\n"
"v_mfma_f32_32x32x16_bf16 a[224:239], v[88:91], v[76:79], a[224:239]\n"
"v_xor_b32 v12, 0x10000, v12\n"
"v_xor_b32 v13, 0x10000, v13\n"
"v_sub_f32 v152, v152, v162\n"
"v_sub_f32 v153, v153, v162\n"
"v_sub_f32 v154, v154, v162\n"
"v_mfma_f32_32x32x16_bf16 a[240:255], v[92:95], v[76:79], a[240:255]\n"
"v_sub_f32 v155, v155, v162\n"
"v_sub_f32 v156, v156, v162\n"
"v_sub_f32 v157, v157, v162\n"
"s_add_u32 s53, s53, 128\n"
"s_add_u32 s59, s59, 524288\n"
"s_waitcnt vmcnt(1) lgkmcnt(0)\n"
"s_barrier\n"
"s_add_u32 m0, s64, 0\n"
"v_mfma_f32_32x32x16_bf16 a[0:15], v[112:115], v[96:99], a[0:15]\n"
"buffer_load_dwordx4 v16, s[16:19], s53 offen lds\n"
"ds_read_b128 v[64:67], v8 offset:0\n"
"v_sub_f32 v158, v158, v162\n"
"v_sub_f32 v159, v159, v162\n"
"v_mfma_f32_32x32x16_bf16 a[16:31], v[116:119], v[96:99], a[16:31]\n"
"ds_read_b64_tr_b16 v[80:81], v12 offset:0\n"
"v_cvt_pk_bf16_f32 v164, v144, v145\n"
"v_cvt_pk_bf16_f32 v165, v146, v147\n"
"v_cvt_pk_bf16_f32 v166, v148, v149\n"
"s_add_u32 m0, s72, 0\n"
"v_mfma_f32_32x32x16_bf16 a[32:47], v[120:123], v[96:99], a[32:47]\n"
"buffer_load_dwordx4 v24, s[20:23], s59 offen lds\n"
"ds_read_b64_tr_b16 v[82:83], v12 offset:512\n"
"v_cvt_pk_bf16_f32 v167, v150, v151\n"
"v_cvt_pk_bf16_f32 v168, v152, v153\n"
"v_mfma_f32_32x32x16_bf16 a[48:63], v[124:127], v[96:99], a[48:63]\n"
"ds_read_b64_tr_b16 v[84:85], v13 offset:0\n"
"v_cvt_pk_bf16_f32 v169, v154, v155\n"
"v_cvt_pk_bf16_f32 v170, v156, v157\n"
"v_cvt_pk_bf16_f32 v171, v158, v159\n"
"s_add_u32 m0, s65, 0\n"
"v_mfma_f32_32x32x16_bf16 a[64:79], v[112:115], v[100:103], a[64:79]\n"
"buffer_load_dwordx4 v17, s[16:19], s53 offen lds\n"
"ds_read_b64_tr_b16 v[86:87], v13 offset:512\n"
"v_lshlrev_b32 v144, 16, v164\n"
"v_and_b32 v145, 0xffff0000, v164\n"
"v_mfma_f32_32x32x16_bf16 a[80:95], v[116:119], v[100:103], a[80:95]\n"
"ds_read_b64_tr_b16 v[88:89], v12 offset:8192\n"
"v_lshlrev_b32 v146, 16, v165\n"
"v_and_b32 v147, 0xffff0000, v165\n"
"v_lshlrev_b32 v148, 16, v166\n"
"s_add_u32 m0, s73, 0\n"
"v_mfma_f32_32x32x16_bf16 a[96:111], v[120:123], v[100:103], a[96:111]\n"
"buffer_load_dwordx4 v25, s[20:23], s59 offen lds\n"
"ds_read_b64_tr_b16 v[90:91], v12 offset:8704\n"
"v_and_b32 v149, 0xffff0000, v166\n"
"v_lshlrev_b32 v150, 16, v167\n"
"v_mfma_f32_32x32x16_bf16 a[112:127], v[124:127], v[100:103], a[112:127]\n"
"ds_read_b64_tr_b16 v[92:93], v13 offset:8192\n"
"v_and_b32 v151, 0xffff0000, v167\n"
"v_lshlrev_b32 v152, 16, v168\n"
"v_and_b32 v153, 0xffff0000, v168\n"
"s_add_u32 m0, s66, 0\n"
"v_mfma_f32_32x32x16_bf16 a[128:143], v[112:115], v[104:107], a[128:143]\n"
"buffer_load_dwordx4 v18, s[16:19], s53 offen lds\n"
"ds_read_b64_tr_b16 v[94:95], v13 offset:8704\n"
"v_lshlrev_b32 v154, 16, v169\n"
"v_and_b32 v155, 0xffff0000, v169\n"
"v_mfma_f32_32x32x16_bf16 a[144:159], v[116:119], v[104:107], a[144:159]\n"
"ds_read_b128 v[68:71], v8 offset:4096\n"
"v_lshlrev_b32 v156, 16, v170\n"
"v_and_b32 v157, 0xffff0000, v170\n"
"v_lshlrev_b32 v158, 16, v171\n"
"s_add_u32 m0, s74, 0\n"
"v_mfma_f32_32x32x16_bf16 a[160:175], v[120:123], v[104:107], a[160:175]\n"
"buffer_load_dwordx4 v26, s[20:23], s59 offen lds\n"
"ds_read_b128 v[72:75], v8 offset:8192\n"
"v_and_b32 v159, 0xffff0000, v171\n"
"v_mul_f32 v144, v144, v161\n"
"v_mfma_f32_32x32x16_bf16 a[176:191], v[124:127], v[104:107], a[176:191]\n"
"ds_read_b128 v[76:79], v8 offset:12288\n"
"v_mul_f32 v145, v145, v161\n"
"v_mul_f32 v146, v146, v161\n"
"v_mul_f32 v147, v147, v161\n"
"s_add_u32 m0, s67, 0\n"
"v_mfma_f32_32x32x16_bf16 a[192:207], v[112:115], v[108:111], a[192:207]\n"
"buffer_load_dwordx4 v19, s[16:19], s53 offen lds\n"
"v_mul_f32 v148, v148, v161\n"
"v_mul_f32 v149, v149, v161\n"
"v_mul_f32 v150, v150, v161\n"
"v_mfma_f32_32x32x16_bf16 a[208:223], v[116:119], v[108:111], a[208:223]\n"
"v_mul_f32 v151, v151, v161\n"
"v_mul_f32 v152, v152, v161\n"
"v_mul_f32 v153, v153, v161\n"
"v_mul_f32 v154, v154, v161\n"
"s_add_u32 m0, s75, 0\n"
"v_mfma_f32_32x32x16_bf16 a[224:239], v[120:123], v[108:111], a[224:239]\n"
"buffer_load_dwordx4 v27, s[20:23], s59 offen lds\n"
"v_mul_f32 v155, v155, v161\n"
"v_mul_f32 v156, v156, v161\n"
"v_mul_f32 v157, v157, v161\n"
"v_mfma_f32_32x32x16_bf16 a[240:255], v[124:127], v[108:111], a[240:255]\n"
"v_mul_f32 v158, v158, v161\n"
"v_mul_f32 v159, v159, v161\n"
"v_cvt_pk_bf16_f32 v164, v144, v145\n"
"s_waitcnt lgkmcnt(0)\n"
"s_add_u32 m0, s68, 0\n"
"v_mfma_f32_32x32x16_bf16 a[0:15], v[80:83], v[64:67], a[0:15]\n"
"buffer_load_dwordx4 v20, s[16:19], s53 offen lds\n"
"ds_read_b128 v[96:99], v9 offset:0\n"
"v_cvt_pk_bf16_f32 v165, v146, v147\n"
"v_cvt_pk_bf16_f32 v166, v148, v149\n"
"v_mfma_f32_32x32x16_bf16 a[16:31], v[84:87], v[64:67], a[16:31]\n"
"ds_read_b64_tr_b16 v[112:113], v12 offset:2048\n"
"v_cvt_pk_bf16_f32 v167, v150, v151\n"
"v_cvt_pk_bf16_f32 v168, v152, v153\n"
"v_cvt_pk_bf16_f32 v169, v154, v155\n"
"s_add_u32 m0, s76, 0\n"
"v_mfma_f32_32x32x16_bf16 a[32:47], v[88:91], v[64:67], a[32:47]\n"
"buffer_load_dwordx4 v28, s[20:23], s59 offen lds\n"
"ds_read_b64_tr_b16 v[114:115], v12 offset:2560\n"
"v_cvt_pk_bf16_f32 v170, v156, v157\n"
"v_cvt_pk_bf16_f32 v171, v158, v159\n"
"v_mfma_f32_32x32x16_bf16 a[48:63], v[92:95], v[64:67], a[48:63]\n"
"ds_read_b64_tr_b16 v[116:117], v13 offset:2048\n"
"ds_write_b128 v176, v[164:167] offset:0\n"
"ds_write_b128 v176, v[168:171] offset:4608\n"
"v_lshlrev_b32 v144, 16, v164\n"
"s_add_u32 m0, s69, 0\n"
"v_mfma_f32_32x32x16_bf16 a[64:79], v[80:83], v[68:71], a[64:79]\n"
"buffer_load_dwordx4 v21, s[16:19], s53 offen lds\n"
"ds_read_b64_tr_b16 v[118:119], v13 offset:2560\n"
"v_and_b32 v145, 0xffff0000, v164\n"
"v_lshlrev_b32 v146, 16, v165\n"
"v_mfma_f32_32x32x16_bf16 a[80:95], v[84:87], v[68:71], a[80:95]\n"
"ds_read_b64_tr_b16 v[120:121], v12 offset:10240\n"
"v_and_b32 v147, 0xffff0000, v165\n"
"v_lshlrev_b32 v148, 16, v166\n"
"v_and_b32 v149, 0xffff0000, v166\n"
"s_add_u32 m0, s77, 0\n"
"v_mfma_f32_32x32x16_bf16 a[96:111], v[88:91], v[68:71], a[96:111]\n"
"buffer_load_dwordx4 v29, s[20:23], s59 offen lds\n"
"ds_read_b64_tr_b16 v[122:123], v12 offset:10752\n"
"v_lshlrev_b32 v150, 16, v167\n"
"v_and_b32 v151, 0xffff0000, v167\n"
"v_mfma_f32_32x32x16_bf16 a[112:127], v[92:95], v[68:71], a[112:127]\n"
"ds_read_b64_tr_b16 v[124:125], v13 offset:10240\n"
"v_lshlrev_b32 v152, 16, v168\n"
"v_and_b32 v153, 0xffff0000, v168\n"
"v_lshlrev_b32 v154, 16, v169\n"
"s_add_u32 m0, s70, 0\n"
"v_mfma_f32_32x32x16_bf16 a[128:143], v[80:83], v[72:75], a[128:143]\n"
"buffer_load_dwordx4 v22, s[16:19], s53 offen lds\n"
"ds_read_b64_tr_b16 v[126:127], v13 offset:10752\n"
"v_and_b32 v155, 0xffff0000, v169\n"
"v_lshlrev_b32 v156, 16, v170\n"
"v_mfma_f32_32x32x16_bf16 a[144:159], v[84:87], v[72:75], a[144:159]\n"
"ds_read_b128 v[100:103], v9 offset:4096\n"
"v_and_b32 v157, 0xffff0000, v170\n"
"v_lshlrev_b32 v158, 16, v171\n"
"v_and_b32 v159, 0xffff0000, v171\n"
"s_add_u32 m0, s78, 0\n"
"v_mfma_f32_32x32x16_bf16 a[160:175], v[88:91], v[72:75], a[160:175]\n"
"buffer_load_dwordx4 v30, s[20:23], s59 offen lds\n"
"ds_read_b128 v[104:107], v9 offset:8192\n"
"v_add_f32 v160, v144, v145\n"
"v_add_f32 v160, v160, v146\n"
"v_mfma_f32_32x32x16_bf16 a[176:191], v[92:95], v[72:75], a[176:191]\n"
"ds_read_b128 v[108:111], v9 offset:12288\n"
"v_add_f32 v160, v160, v147\n"
"v_add_f32 v160, v160, v148\n"
"v_add_f32 v160, v160, v149\n"
"s_add_u32 m0, s71, 0\n"
"v_mfma_f32_32x32x16_bf16 a[192:207], v[80:83], v[76:79], a[192:207]\n"
"buffer_load_dwordx4 v23, s[16:19], s53 offen lds\n"
"v_add_f32 v160, v160, v150\n"
"v_add_f32 v160, v160, v151\n"
"v_add_f32 v160, v160, v152\n"
"v_mfma_f32_32x32x16_bf16 a[208:223], v[84:87], v[76:79], a[208:223]\n"
"v_add_f32 v160, v160, v153\n"
"v_add_f32 v160, v160, v154\n"
"v_add_f32 v160, v160, v155\n"
"v_add_f32 v160, v160, v156\n"
"s_add_u32 m0, s79, 0\n"
"v_mfma_f32_32x32x16_bf16 a[224:239], v[88:91], v[76:79], a[224:239]\n"
"buffer_load_dwordx4 v31, s[20:23], s59 offen lds\n"
"v_add_f32 v160, v160, v157\n"
"v_add_f32 v160, v160, v158\n"
"v_add_f32 v160, v160, v159\n"
"v_mfma_f32_32x32x16_bf16 a[240:255], v[92:95], v[76:79], a[240:255]\n"
"s_nop 1\n"
"v_add_f32_dpp v160, v160, v160 quad_perm:[1,0,3,2] row_mask:0xf bank_mask:0xf\n"
"s_waitcnt lgkmcnt(0)\n"
"v_mfma_f32_32x32x16_bf16 a[0:15], v[112:115], v[96:99], a[0:15]\n"
"ds_read_b128 v[64:67], v10 offset:0\n"
"s_nop 1\n"
"v_add_f32_dpp v160, v160, v160 quad_perm:[2,3,0,1] row_mask:0xf bank_mask:0xf\n"
"v_mfma_f32_32x32x16_bf16 a[16:31], v[116:119], v[96:99], a[16:31]\n"
"ds_read_b64_tr_b16 v[80:81], v12 offset:4096\n"
"s_nop 1\n"
"v_add_f32_dpp v160, v160, v160 row_half_mirror row_mask:0xf bank_mask:0xf\n"
"v_mfma_f32_32x32x16_bf16 a[32:47], v[120:123], v[96:99], a[32:47]\n"
"ds_read_b64_tr_b16 v[82:83], v12 offset:4608\n"
"s_nop 1\n"
"v_add_f32_dpp v160, v160, v160 row_mirror row_mask:0xf bank_mask:0xf\n"
"v_mov_b32 v163, v160\n"
"v_mfma_f32_32x32x16_bf16 a[48:63], v[124:127], v[96:99], a[48:63]\n"
"ds_read_b64_tr_b16 v[84:85], v13 offset:4096\n"
"s_nop 1\n"
"v_permlane16_swap_b32 v160, v163\n"
"v_add_f32 v160, v160, v163\n"
"v_mfma_f32_32x32x16_bf16 a[64:79], v[112:115], v[100:103], a[64:79]\n"
"ds_read_b64_tr_b16 v[86:87], v13 offset:4608\n"
"v_mul_f32 v160, 0x3b000000, v160\n"
"v_mov_b32 v162, v160\n"
"v_cvt_pk_bf16_f32 v163, v162, v162\n"
"v_lshlrev_b32 v162, 16, v163\n"
"v_mfma_f32_32x32x16_bf16 a[80:95], v[116:119], v[100:103], a[80:95]\n"
"ds_read_b64_tr_b16 v[88:89], v12 offset:12288\n"
"v_sub_f32 v164, v144, v160\n"
"v_mul_f32 v161, v164, v164\n"
"v_sub_f32 v165, v145, v160\n"
"v_fmac_f32 v161, v165, v165\n"
"v_mfma_f32_32x32x16_bf16 a[96:111], v[120:123], v[100:103], a[96:111]\n"
"ds_read_b64_tr_b16 v[90:91], v12 offset:12800\n"
"v_sub_f32 v164, v146, v160\n"
"v_fmac_f32 v161, v164, v164\n"
"v_sub_f32 v165, v147, v160\n"
"v_fmac_f32 v161, v165, v165\n"
"v_mfma_f32_32x32x16_bf16 a[112:127], v[124:127], v[100:103], a[112:127]\n"
"ds_read_b64_tr_b16 v[92:93], v13 offset:12288\n"
"v_sub_f32 v164, v148, v160\n"
"v_fmac_f32 v161, v164, v164\n"
"v_sub_f32 v165, v149, v160\n"
"v_fmac_f32 v161, v165, v165\n"
"v_mfma_f32_32x32x16_bf16 a[128:143], v[112:115], v[104:107], a[128:143]\n"
"ds_read_b64_tr_b16 v[94:95], v13 offset:12800\n"
"v_sub_f32 v164, v150, v160\n"
"v_fmac_f32 v161, v164, v164\n"
"v_sub_f32 v165, v151, v160\n"
"v_fmac_f32 v161, v165, v165\n"
"v_mfma_f32_32x32x16_bf16 a[144:159], v[116:119], v[104:107], a[144:159]\n"
"ds_read_b128 v[68:71], v10 offset:4096\n"
"v_sub_f32 v164, v152, v160\n"
"v_fmac_f32 v161, v164, v164\n"
"v_sub_f32 v165, v153, v160\n"
"v_fmac_f32 v161, v165, v165\n"
"v_mfma_f32_32x32x16_bf16 a[160:175], v[120:123], v[104:107], a[160:175]\n"
"ds_read_b128 v[72:75], v10 offset:8192\n"
"v_sub_f32 v164, v154, v160\n"
"v_fmac_f32 v161, v164, v164\n"
"v_sub_f32 v165, v155, v160\n"
"v_fmac_f32 v161, v165, v165\n"
"v_mfma_f32_32x32x16_bf16 a[176:191], v[124:127], v[104:107], a[176:191]\n"
"ds_read_b128 v[76:79], v10 offset:12288\n"
"v_sub_f32 v164, v156, v160\n"
"v_fmac_f32 v161, v164, v164\n"
"v_sub_f32 v165, v157, v160\n"
"v_fmac_f32 v161, v165, v165\n"
"v_mfma_f32_32x32x16_bf16 a[192:207], v[112:115], v[108:111], a[192:207]\n"
"v_sub_f32 v164, v158, v160\n"
"v_fmac_f32 v161, v164, v164\n"
"v_sub_f32 v165, v159, v160\n"
"v_fmac_f32 v161, v165, v165\n"
"v_mfma_f32_32x32x16_bf16 a[208:223], v[116:119], v[108:111], a[208:223]\n"
"s_nop 1\n"
"v_add_f32_dpp v161, v161, v161 quad_perm:[1,0,3,2] row_mask:0xf bank_mask:0xf\n"
"s_nop 1\n"
"v_mfma_f32_32x32x16_bf16 a[224:239], v[120:123], v[108:111], a[224:239]\n"
"v_add_f32_dpp v161, v161, v161 quad_perm:[2,3,0,1] row_mask:0xf bank_mask:0xf\n"
"s_nop 1\n"
"v_add_f32_dpp v161, v161, v161 row_half_mirror row_mask:0xf bank_mask:0xf\n"
"v_mfma_f32_32x32x16_bf16 a[240:255], v[124:127], v[108:111], a[240:255]\n"
"s_nop 1\n"
"v_add_f32_dpp v161, v161, v161 row_mirror row_mask:0xf bank_mask:0xf\n"
"s_waitcnt lgkmcnt(0)\n"
"v_mfma_f32_32x32x16_bf16 a[0:15], v[80:83], v[64:67], a[0:15]\n"
"ds_read_b128 v[96:99], v11 offset:0\n"
"v_mov_b32 v163, v161\n"
"s_nop 1\n"
"v_permlane16_swap_b32 v161, v163\n"
"v_mfma_f32_32x32x16_bf16 a[16:31], v[84:87], v[64:67], a[16:31]\n"
"ds_read_b64_tr_b16 v[112:113], v12 offset:6144\n"
"v_add_f32 v161, v161, v163\n"
"v_mul_f32 v161, 0x3b000000, v161\n"
"v_cvt_pk_bf16_f32 v163, v161, v161\n"
"s_add_u32 s37, s37, s60\n"
"v_mfma_f32_32x32x16_bf16 a[32:47], v[88:91], v[64:67], a[32:47]\n"
"buffer_load_dword v225, v222, s[24:27], s37 offen\n"
"ds_read_b64_tr_b16 v[114:115], v12 offset:6656\n"
"v_lshlrev_b32 v161, 16, v163\n"
"v_add_f32 v161, 0x3727c5ac, v161\n"
"v_mfma_f32_32x32x16_bf16 a[48:63], v[92:95], v[64:67], a[48:63]\n"
"ds_read_b64_tr_b16 v[116:117], v13 offset:6144\n"
"v_cvt_pk_bf16_f32 v163, v161, v161\n"
"v_lshlrev_b32 v161, 16, v163\n"
"v_sqrt_f32 v161, v161\n"
"v_mfma_f32_32x32x16_bf16 a[64:79], v[80:83], v[68:71], a[64:79]\n"
"ds_read_b64_tr_b16 v[118:119], v13 offset:6656\n"
"s_nop 0\n"
"v_cvt_pk_bf16_f32 v163, v161, v161\n"
"v_lshlrev_b32 v161, 16, v163\n"
"v_mfma_f32_32x32x16_bf16 a[80:95], v[84:87], v[68:71], a[80:95]\n"
"ds_read_b64_tr_b16 v[120:121], v12 offset:14336\n"
"v_rcp_f32 v161, v161\n"
"v_sub_f32 v144, v144, v162\n"
"v_sub_f32 v145, v145, v162\n"
"v_mfma_f32_32x32x16_bf16 a[96:111], v[88:91], v[68:71], a[96:111]\n"
"ds_read_b64_tr_b16 v[122:123], v12 offset:14848\n"
"v_sub_f32 v146, v146, v162\n"
"v_sub_f32 v147, v147, v162\n"
"v_sub_f32 v148, v148, v162\n"
"v_sub_f32 v149, v149, v162\n"
"v_mfma_f32_32x32x16_bf16 a[112:127], v[92:95], v[68:71], a[112:127]\n"
"ds_read_b64_tr_b16 v[124:125], v13 offset:14336\n"
"v_sub_f32 v150, v150, v162\n"
"v_sub_f32 v151, v151, v162\n"
"v_sub_f32 v152, v152, v162\n"
"v_sub_f32 v153, v153, v162\n"
"v_mfma_f32_32x32x16_bf16 a[128:143], v[80:83], v[72:75], a[128:143]\n"
"ds_read_b64_tr_b16 v[126:127], v13 offset:14848\n"
"v_sub_f32 v154, v154, v162\n"
"v_sub_f32 v155, v155, v162\n"
"v_sub_f32 v156, v156, v162\n"
"v_sub_f32 v157, v157, v162\n"
"v_mfma_f32_32x32x16_bf16 a[144:159], v[84:87], v[72:75], a[144:159]\n"
"ds_read_b128 v[100:103], v11 offset:4096\n"
"v_sub_f32 v158, v158, v162\n"
"v_sub_f32 v159, v159, v162\n"
"v_cvt_pk_bf16_f32 v164, v144, v145\n"
"v_cvt_pk_bf16_f32 v165, v146, v147\n"
"v_mfma_f32_32x32x16_bf16 a[160:175], v[88:91], v[72:75], a[160:175]\n"
"ds_read_b128 v[104:107], v11 offset:8192\n"
"v_cvt_pk_bf16_f32 v166, v148, v149\n"
"v_cvt_pk_bf16_f32 v167, v150, v151\n"
"v_cvt_pk_bf16_f32 v168, v152, v153\n"
"v_cvt_pk_bf16_f32 v169, v154, v155\n"
"v_mfma_f32_32x32x16_bf16 a[176:191], v[92:95], v[72:75], a[176:191]\n"
"ds_read_b128 v[108:111], v11 offset:12288\n"
"v_cvt_pk_bf16_f32 v170, v156, v157\n"
"v_cvt_pk_bf16_f32 v171, v158, v159\n"
"v_lshlrev_b32 v144, 16, v164\n"
"v_and_b32 v145, 0xffff0000, v164\n"
"v_mfma_f32_32x32x16_bf16 a[192:207], v[80:83], v[76:79], a[192:207]\n"
"v_xor_b32 v8, 0x10000, v8\n"
"v_xor_b32 v9, 0x10000, v9\n"
"v_lshlrev_b32 v146, 16, v165\n"
"v_and_b32 v147, 0xffff0000, v165\n"
"v_lshlrev_b32 v148, 16, v166\n"
"v_mfma_f32_32x32x16_bf16 a[208:223], v[84:87], v[76:79], a[208:223]\n"
"v_xor_b32 v10, 0x10000, v10\n"
"v_xor_b32 v11, 0x10000, v11\n"
"v_and_b32 v149, 0xffff0000, v166\n"
"v_lshlrev_b32 v150, 16, v167\n"
"v_and_b32 v151, 0xffff0000, v167\n"
"v_mfma_f32_32x32x16_bf16 a[224:239], v[88:91], v[76:79], a[224:239]\n"
"v_xor_b32 v12, 0x10000, v12\n"
"v_xor_b32 v13, 0x10000, v13\n"
"v_lshlrev_b32 v152, 16, v168\n"
"v_and_b32 v153, 0xffff0000, v168\n"
"v_lshlrev_b32 v154, 16, v169\n"
"v_mfma_f32_32x32x16_bf16 a[240:255], v[92:95], v[76:79], a[240:255]\n"
"v_and_b32 v155, 0xffff0000, v169\n"
"v_lshlrev_b32 v156, 16, v170\n"
"v_and_b32 v157, 0xffff0000, v170\n"
"s_add_u32 s53, s53, 128\n"
"s_add_u32 s59, s59, 524288\n"
"s_waitcnt vmcnt(1) lgkmcnt(0)\n"
"s_barrier\n"
"s_add_u32 m0, s64, 65536\n"
"v_mfma_f32_32x32x16_bf16 a[0:15], v[112:115], v[96:99], a[0:15]\n"
"buffer_load_dwordx4 v16, s[16:19], s53 offen lds\n"
"ds_read_b128 v[64:67], v8 offset:0\n"
"v_lshlrev_b32 v158, 16, v171\n"
"v_and_b32 v159, 0xffff0000, v171\n"
"v_mfma_f32_32x32x16_bf16 a[16:31], v[116:119], v[96:99], a[16:31]\n"
"ds_read_b64_tr_b16 v[80:81], v12 offset:0\n"
"v_mul_f32 v144, v144, v161\n"
"v_mul_f32 v145, v145, v161\n"
"v_mul_f32 v146, v146, v161\n"
"s_add_u32 m0, s72, 65536\n"
"v_mfma_f32_32x32x16_bf16 a[32:47], v[120:123], v[96:99], a[32:47]\n"
"buffer_load_dwordx4 v24, s[20:23], s59 offen lds\n"
"ds_read_b64_tr_b16 v[82:83], v12 offset:512\n"
"v_mul_f32 v147, v147, v161\n"
"v_mul_f32 v148, v148, v161\n"
"v_mfma_f32_32x32x16_bf16 a[48:63], v[124:127], v[96:99], a[48:63]\n"
"ds_read_b64_tr_b16 v[84:85], v13 offset:0\n"
"v_mul_f32 v149, v149, v161\n"
"v_mul_f32 v150, v150, v161\n"
"v_mul_f32 v151, v151, v161\n"
"s_add_u32 m0, s65, 65536\n"
"v_mfma_f32_32x32x16_bf16 a[64:79], v[112:115], v[100:103], a[64:79]\n"
"buffer_load_dwordx4 v17, s[16:19], s53 offen lds\n"
"ds_read_b64_tr_b16 v[86:87], v13 offset:512\n"
"v_mul_f32 v152, v152, v161\n"
"v_mul_f32 v153, v153, v161\n"
"v_mfma_f32_32x32x16_bf16 a[80:95], v[116:119], v[100:103], a[80:95]\n"
"ds_read_b64_tr_b16 v[88:89], v12 offset:8192\n"
"v_mul_f32 v154, v154, v161\n"
"v_mul_f32 v155, v155, v161\n"
"v_mul_f32 v156, v156, v161\n"
"s_add_u32 m0, s73, 65536\n"
"v_mfma_f32_32x32x16_bf16 a[96:111], v[120:123], v[100:103], a[96:111]\n"
"buffer_load_dwordx4 v25, s[20:23], s59 offen lds\n"
"ds_read_b64_tr_b16 v[90:91], v12 offset:8704\n"
"v_mul_f32 v157, v157, v161\n"
"v_mul_f32 v158, v158, v161\n"
"v_mfma_f32_32x32x16_bf16 a[112:127], v[124:127], v[100:103], a[112:127]\n"
"ds_read_b64_tr_b16 v[92:93], v13 offset:8192\n"
"v_mul_f32 v159, v159, v161\n"
"v_cvt_pk_bf16_f32 v164, v144, v145\n"
"v_cvt_pk_bf16_f32 v165, v146, v147\n"
"s_add_u32 m0, s66, 65536\n"
"v_mfma_f32_32x32x16_bf16 a[128:143], v[112:115], v[104:107], a[128:143]\n"
"buffer_load_dwordx4 v18, s[16:19], s53 offen lds\n"
"ds_read_b64_tr_b16 v[94:95], v13 offset:8704\n"
"v_cvt_pk_bf16_f32 v166, v148, v149\n"
"v_cvt_pk_bf16_f32 v167, v150, v151\n"
"v_mfma_f32_32x32x16_bf16 a[144:159], v[116:119], v[104:107], a[144:159]\n"
"ds_read_b128 v[68:71], v8 offset:4096\n"
"v_cvt_pk_bf16_f32 v168, v152, v153\n"
"v_cvt_pk_bf16_f32 v169, v154, v155\n"
"v_cvt_pk_bf16_f32 v170, v156, v157\n"
"s_add_u32 m0, s74, 65536\n"
"v_mfma_f32_32x32x16_bf16 a[160:175], v[120:123], v[104:107], a[160:175]\n"
"buffer_load_dwordx4 v26, s[20:23], s59 offen lds\n"
"ds_read_b128 v[72:75], v8 offset:8192\n"
"v_cvt_pk_bf16_f32 v171, v158, v159\n"
"v_mfma_f32_32x32x16_bf16 a[176:191], v[124:127], v[104:107], a[176:191]\n"
"ds_read_b128 v[76:79], v8 offset:12288\n"
"ds_write_b128 v176, v[164:167] offset:9216\n"
"ds_write_b128 v176, v[168:171] offset:13824\n"
"s_lshr_b32 s84, s29, 1\n"
"s_add_u32 m0, s67, 65536\n"
"v_mfma_f32_32x32x16_bf16 a[192:207], v[112:115], v[108:111], a[192:207]\n"
"buffer_load_dwordx4 v19, s[16:19], s53 offen lds\n"
"s_and_b32 s85, s29, 1\n"
"s_mul_i32 s80, s84, 9216\n"
"s_mul_i32 s81, s85, 4608\n"
"v_mfma_f32_32x32x16_bf16 a[208:223], v[116:119], v[108:111], a[208:223]\n"
"s_add_u32 s80, s80, s81\n"
"s_add_u32 s80, s80, 131072\n"
"v_lshrrev_b32 v176, 3, v0\n"
"v_mul_u32_u24 v176, 144, v176\n"
"s_add_u32 m0, s75, 65536\n"
"v_mfma_f32_32x32x16_bf16 a[224:239], v[120:123], v[108:111], a[224:239]\n"
"buffer_load_dwordx4 v27, s[20:23], s59 offen lds\n"
"v_and_b32 v177, 7, v0\n"
"v_lshl_add_u32 v176, v177, 4, v176\n"
"v_add_u32 v176, s80, v176\n"
"v_mfma_f32_32x32x16_bf16 a[240:255], v[124:127], v[108:111], a[240:255]\n"
"s_mov_b32 s81, 53477376\n"
"s_cmp_eq_u32 s84, 0\n"
"s_cselect_b32 s80, 51380224, s81\n"
"s_waitcnt lgkmcnt(0)\n"
"s_add_u32 m0, s68, 65536\n"
"v_mfma_f32_32x32x16_bf16 a[0:15], v[80:83], v[64:67], a[0:15]\n"
"buffer_load_dwordx4 v20, s[16:19], s53 offen lds\n"
"ds_read_b128 v[96:99], v9 offset:0\n"
"s_add_u32 s80, s50, s80\n"
"s_addc_u32 s81, s51, 0\n"
"v_mfma_f32_32x32x16_bf16 a[16:31], v[84:87], v[64:67], a[16:31]\n"
"ds_read_b64_tr_b16 v[112:113], v12 offset:2048\n"
"s_and_b32 s81, s81, 0xffff\n"
"s_mov_b32 s82, 2097152\n"
"s_mov_b32 s83, 0x20000\n"
"s_add_u32 m0, s76, 65536\n"
"v_mfma_f32_32x32x16_bf16 a[32:47], v[88:91], v[64:67], a[32:47]\n"
"buffer_load_dwordx4 v28, s[20:23], s59 offen lds\n"
"ds_read_b64_tr_b16 v[114:115], v12 offset:2560\n"
"s_lshr_b32 s84, s28, 2\n"
"s_lshl_b32 s84, s84, 15\n"
"v_mfma_f32_32x32x16_bf16 a[48:63], v[92:95], v[64:67], a[48:63]\n"
"ds_read_b64_tr_b16 v[116:117], v13 offset:2048\n"
"s_lshl_b32 s85, s85, 14\n"
"s_add_u32 s84, s84, s85\n"
"s_and_b32 s85, s28, 3\n"
"s_add_u32 m0, s69, 65536\n"
"v_mfma_f32_32x32x16_bf16 a[64:79], v[80:83], v[68:71], a[64:79]\n"
"buffer_load_dwordx4 v21, s[16:19], s53 offen lds\n"
"ds_read_b64_tr_b16 v[118:119], v13 offset:2560\n"
"s_lshl_b32 s85, s85, 7\n"
"s_add_u32 s84, s84, s85\n"
"v_mfma_f32_32x32x16_bf16 a[80:95], v[84:87], v[68:71], a[80:95]\n"
"ds_read_b64_tr_b16 v[120:121], v12 offset:10240\n"
"v_lshrrev_b32 v177, 3, v0\n"
"v_lshlrev_b32 v177, 9, v177\n"
"v_and_b32 v178, 7, v0\n"
"s_add_u32 m0, s77, 65536\n"
"v_mfma_f32_32x32x16_bf16 a[96:111], v[88:91], v[68:71], a[96:111]\n"
"buffer_load_dwordx4 v29, s[20:23], s59 offen lds\n"
"ds_read_b64_tr_b16 v[122:123], v12 offset:10752\n"
"v_lshl_add_u32 v177, v178, 4, v177\n"
"v_mfma_f32_32x32x16_bf16 a[112:127], v[92:95], v[68:71], a[112:127]\n"
"ds_read_b64_tr_b16 v[124:125], v13 offset:10240\n"
"s_add_u32 m0, s70, 65536\n"
"v_mfma_f32_32x32x16_bf16 a[128:143], v[80:83], v[72:75], a[128:143]\n"
"buffer_load_dwordx4 v22, s[16:19], s53 offen lds\n"
"ds_read_b64_tr_b16 v[126:127], v13 offset:10752\n"
"v_mfma_f32_32x32x16_bf16 a[144:159], v[84:87], v[72:75], a[144:159]\n"
"ds_read_b128 v[100:103], v9 offset:4096\n"
"s_add_u32 m0, s78, 65536\n"
"v_mfma_f32_32x32x16_bf16 a[160:175], v[88:91], v[72:75], a[160:175]\n"
"buffer_load_dwordx4 v30, s[20:23], s59 offen lds\n"
"ds_read_b128 v[104:107], v9 offset:8192\n"
"v_mfma_f32_32x32x16_bf16 a[176:191], v[92:95], v[72:75], a[176:191]\n"
"ds_read_b128 v[108:111], v9 offset:12288\n"
"s_add_u32 m0, s71, 65536\n"
"v_mfma_f32_32x32x16_bf16 a[192:207], v[80:83], v[76:79], a[192:207]\n"
"buffer_load_dwordx4 v23, s[16:19], s53 offen lds\n"
"v_mfma_f32_32x32x16_bf16 a[208:223], v[84:87], v[76:79], a[208:223]\n"
"s_add_u32 m0, s79, 65536\n"
"v_mfma_f32_32x32x16_bf16 a[224:239], v[88:91], v[76:79], a[224:239]\n"
"buffer_load_dwordx4 v31, s[20:23], s59 offen lds\n"
"v_mfma_f32_32x32x16_bf16 a[240:255], v[92:95], v[76:79], a[240:255]\n"
"s_waitcnt lgkmcnt(0)\n"
"v_mfma_f32_32x32x16_bf16 a[0:15], v[112:115], v[96:99], a[0:15]\n"
"ds_read_b128 v[64:67], v10 offset:0\n"
"v_mfma_f32_32x32x16_bf16 a[16:31], v[116:119], v[96:99], a[16:31]\n"
"ds_read_b64_tr_b16 v[80:81], v12 offset:4096\n"
"v_mfma_f32_32x32x16_bf16 a[32:47], v[120:123], v[96:99], a[32:47]\n"
"ds_read_b64_tr_b16 v[82:83], v12 offset:4608\n"
"v_mfma_f32_32x32x16_bf16 a[48:63], v[124:127], v[96:99], a[48:63]\n"
"ds_read_b64_tr_b16 v[84:85], v13 offset:4096\n"
"v_mfma_f32_32x32x16_bf16 a[64:79], v[112:115], v[100:103], a[64:79]\n"
"ds_read_b64_tr_b16 v[86:87], v13 offset:4608\n"
"v_mfma_f32_32x32x16_bf16 a[80:95], v[116:119], v[100:103], a[80:95]\n"
"ds_read_b64_tr_b16 v[88:89], v12 offset:12288\n"
"v_mfma_f32_32x32x16_bf16 a[96:111], v[120:123], v[100:103], a[96:111]\n"
"ds_read_b64_tr_b16 v[90:91], v12 offset:12800\n"
"v_mfma_f32_32x32x16_bf16 a[112:127], v[124:127], v[100:103], a[112:127]\n"
"ds_read_b64_tr_b16 v[92:93], v13 offset:12288\n"
"v_mfma_f32_32x32x16_bf16 a[128:143], v[112:115], v[104:107], a[128:143]\n"
"ds_read_b64_tr_b16 v[94:95], v13 offset:12800\n"
"v_mfma_f32_32x32x16_bf16 a[144:159], v[116:119], v[104:107], a[144:159]\n"
"ds_read_b128 v[68:71], v10 offset:4096\n"
"v_mfma_f32_32x32x16_bf16 a[160:175], v[120:123], v[104:107], a[160:175]\n"
"ds_read_b128 v[72:75], v10 offset:8192\n"
"v_mfma_f32_32x32x16_bf16 a[176:191], v[124:127], v[104:107], a[176:191]\n"
"ds_read_b128 v[76:79], v10 offset:12288\n"
"v_mfma_f32_32x32x16_bf16 a[192:207], v[112:115], v[108:111], a[192:207]\n"
"v_mfma_f32_32x32x16_bf16 a[208:223], v[116:119], v[108:111], a[208:223]\n"
"v_mfma_f32_32x32x16_bf16 a[224:239], v[120:123], v[108:111], a[224:239]\n"
"v_mfma_f32_32x32x16_bf16 a[240:255], v[124:127], v[108:111], a[240:255]\n"
"s_waitcnt lgkmcnt(0)\n"
"v_mfma_f32_32x32x16_bf16 a[0:15], v[80:83], v[64:67], a[0:15]\n"
"ds_read_b128 v[96:99], v11 offset:0\n"
"v_mfma_f32_32x32x16_bf16 a[16:31], v[84:87], v[64:67], a[16:31]\n"
"ds_read_b64_tr_b16 v[112:113], v12 offset:6144\n"
"s_add_u32 s37, s37, s60\n"
"v_mfma_f32_32x32x16_bf16 a[32:47], v[88:91], v[64:67], a[32:47]\n"
"buffer_load_dword v225, v222, s[24:27], s37 offen\n"
"ds_read_b64_tr_b16 v[114:115], v12 offset:6656\n"
"v_mfma_f32_32x32x16_bf16 a[48:63], v[92:95], v[64:67], a[48:63]\n"
"ds_read_b64_tr_b16 v[116:117], v13 offset:6144\n"
"v_mfma_f32_32x32x16_bf16 a[64:79], v[80:83], v[68:71], a[64:79]\n"
"ds_read_b64_tr_b16 v[118:119], v13 offset:6656\n"
"v_mfma_f32_32x32x16_bf16 a[80:95], v[84:87], v[68:71], a[80:95]\n"
"ds_read_b64_tr_b16 v[120:121], v12 offset:14336\n"
"v_mfma_f32_32x32x16_bf16 a[96:111], v[88:91], v[68:71], a[96:111]\n"
"ds_read_b64_tr_b16 v[122:123], v12 offset:14848\n"
"v_mfma_f32_32x32x16_bf16 a[112:127], v[92:95], v[68:71], a[112:127]\n"
"ds_read_b64_tr_b16 v[124:125], v13 offset:14336\n"
"v_mfma_f32_32x32x16_bf16 a[128:143], v[80:83], v[72:75], a[128:143]\n"
"ds_read_b64_tr_b16 v[126:127], v13 offset:14848\n"
"v_mfma_f32_32x32x16_bf16 a[144:159], v[84:87], v[72:75], a[144:159]\n"
"ds_read_b128 v[100:103], v11 offset:4096\n"
"v_mfma_f32_32x32x16_bf16 a[160:175], v[88:91], v[72:75], a[160:175]\n"
"ds_read_b128 v[104:107], v11 offset:8192\n"
"v_mfma_f32_32x32x16_bf16 a[176:191], v[92:95], v[72:75], a[176:191]\n"
"ds_read_b128 v[108:111], v11 offset:12288\n"
"v_mfma_f32_32x32x16_bf16 a[192:207], v[80:83], v[76:79], a[192:207]\n"
"v_xor_b32 v8, 0x10000, v8\n"
"v_xor_b32 v9, 0x10000, v9\n"
"v_mfma_f32_32x32x16_bf16 a[208:223], v[84:87], v[76:79], a[208:223]\n"
"v_xor_b32 v10, 0x10000, v10\n"
"v_xor_b32 v11, 0x10000, v11\n"
"v_mfma_f32_32x32x16_bf16 a[224:239], v[88:91], v[76:79], a[224:239]\n"
"v_xor_b32 v12, 0x10000, v12\n"
"v_xor_b32 v13, 0x10000, v13\n"
"v_mfma_f32_32x32x16_bf16 a[240:255], v[92:95], v[76:79], a[240:255]\n"
"s_add_u32 s53, s53, 128\n"
"s_add_u32 s59, s59, 524288\n"
"s_waitcnt vmcnt(1) lgkmcnt(0)\n"
"s_barrier\n"
"s_add_u32 m0, s64, 0\n"
"v_mfma_f32_32x32x16_bf16 a[0:15], v[112:115], v[96:99], a[0:15]\n"
"buffer_load_dwordx4 v16, s[16:19], s53 offen lds\n"
"ds_read_b128 v[64:67], v8 offset:0\n"
"ds_read_b128 v[128:131], v176 offset:0\n"
"ds_read_b128 v[132:135], v176 offset:1152\n"
"v_mfma_f32_32x32x16_bf16 a[16:31], v[116:119], v[96:99], a[16:31]\n"
"ds_read_b64_tr_b16 v[80:81], v12 offset:0\n"
"ds_read_b128 v[136:139], v176 offset:2304\n"
"ds_read_b128 v[140:143], v176 offset:3456\n"
"s_add_u32 m0, s72, 0\n"
"v_mfma_f32_32x32x16_bf16 a[32:47], v[120:123], v[96:99], a[32:47]\n"
"buffer_load_dwordx4 v24, s[20:23], s59 offen lds\n"
"ds_read_b64_tr_b16 v[82:83], v12 offset:512\n"
"v_mfma_f32_32x32x16_bf16 a[48:63], v[124:127], v[96:99], a[48:63]\n"
"ds_read_b64_tr_b16 v[84:85], v13 offset:0\n"
"s_add_u32 m0, s65, 0\n"
"v_mfma_f32_32x32x16_bf16 a[64:79], v[112:115], v[100:103], a[64:79]\n"
"buffer_load_dwordx4 v17, s[16:19], s53 offen lds\n"
"ds_read_b64_tr_b16 v[86:87], v13 offset:512\n"
"v_mfma_f32_32x32x16_bf16 a[80:95], v[116:119], v[100:103], a[80:95]\n"
"ds_read_b64_tr_b16 v[88:89], v12 offset:8192\n"
"s_add_u32 m0, s73, 0\n"
"v_mfma_f32_32x32x16_bf16 a[96:111], v[120:123], v[100:103], a[96:111]\n"
"buffer_load_dwordx4 v25, s[20:23], s59 offen lds\n"
"ds_read_b64_tr_b16 v[90:91], v12 offset:8704\n"
"v_mfma_f32_32x32x16_bf16 a[112:127], v[124:127], v[100:103], a[112:127]\n"
"ds_read_b64_tr_b16 v[92:93], v13 offset:8192\n"
"s_add_u32 m0, s66, 0\n"
"v_mfma_f32_32x32x16_bf16 a[128:143], v[112:115], v[104:107], a[128:143]\n"
"buffer_load_dwordx4 v18, s[16:19], s53 offen lds\n"
"ds_read_b64_tr_b16 v[94:95], v13 offset:8704\n"
"v_mfma_f32_32x32x16_bf16 a[144:159], v[116:119], v[104:107], a[144:159]\n"
"ds_read_b128 v[68:71], v8 offset:4096\n"
"s_add_u32 m0, s74, 0\n"
"v_mfma_f32_32x32x16_bf16 a[160:175], v[120:123], v[104:107], a[160:175]\n"
"buffer_load_dwordx4 v26, s[20:23], s59 offen lds\n"
"ds_read_b128 v[72:75], v8 offset:8192\n"
"v_mfma_f32_32x32x16_bf16 a[176:191], v[124:127], v[104:107], a[176:191]\n"
"ds_read_b128 v[76:79], v8 offset:12288\n"
"s_add_u32 m0, s67, 0\n"
"v_mfma_f32_32x32x16_bf16 a[192:207], v[112:115], v[108:111], a[192:207]\n"
"buffer_load_dwordx4 v19, s[16:19], s53 offen lds\n"
"v_mfma_f32_32x32x16_bf16 a[208:223], v[116:119], v[108:111], a[208:223]\n"
"s_add_u32 m0, s75, 0\n"
"v_mfma_f32_32x32x16_bf16 a[224:239], v[120:123], v[108:111], a[224:239]\n"
"buffer_load_dwordx4 v27, s[20:23], s59 offen lds\n"
"v_mfma_f32_32x32x16_bf16 a[240:255], v[124:127], v[108:111], a[240:255]\n"
"s_waitcnt lgkmcnt(0)\n"
"s_add_u32 m0, s68, 0\n"
"v_mfma_f32_32x32x16_bf16 a[0:15], v[80:83], v[64:67], a[0:15]\n"
"buffer_load_dwordx4 v20, s[16:19], s53 offen lds\n"
"ds_read_b128 v[96:99], v9 offset:0\n"
"v_mfma_f32_32x32x16_bf16 a[16:31], v[84:87], v[64:67], a[16:31]\n"
"ds_read_b64_tr_b16 v[112:113], v12 offset:2048\n"
"s_add_u32 m0, s76, 0\n"
"v_mfma_f32_32x32x16_bf16 a[32:47], v[88:91], v[64:67], a[32:47]\n"
"buffer_load_dwordx4 v28, s[20:23], s59 offen lds\n"
"ds_read_b64_tr_b16 v[114:115], v12 offset:2560\n"
"v_mfma_f32_32x32x16_bf16 a[48:63], v[92:95], v[64:67], a[48:63]\n"
"ds_read_b64_tr_b16 v[116:117], v13 offset:2048\n"
"s_add_u32 m0, s69, 0\n"
"v_mfma_f32_32x32x16_bf16 a[64:79], v[80:83], v[68:71], a[64:79]\n"
"buffer_load_dwordx4 v21, s[16:19], s53 offen lds\n"
"ds_read_b64_tr_b16 v[118:119], v13 offset:2560\n"
"v_mfma_f32_32x32x16_bf16 a[80:95], v[84:87], v[68:71], a[80:95]\n"
"ds_read_b64_tr_b16 v[120:121], v12 offset:10240\n"
"s_add_u32 m0, s77, 0\n"
"v_mfma_f32_32x32x16_bf16 a[96:111], v[88:91], v[68:71], a[96:111]\n"
"buffer_load_dwordx4 v29, s[20:23], s59 offen lds\n"
"ds_read_b64_tr_b16 v[122:123], v12 offset:10752\n"
"v_mfma_f32_32x32x16_bf16 a[112:127], v[92:95], v[68:71], a[112:127]\n"
"ds_read_b64_tr_b16 v[124:125], v13 offset:10240\n"
"s_add_u32 m0, s70, 0\n"
"v_mfma_f32_32x32x16_bf16 a[128:143], v[80:83], v[72:75], a[128:143]\n"
"buffer_load_dwordx4 v22, s[16:19], s53 offen lds\n"
"ds_read_b64_tr_b16 v[126:127], v13 offset:10752\n"
"v_mfma_f32_32x32x16_bf16 a[144:159], v[84:87], v[72:75], a[144:159]\n"
"ds_read_b128 v[100:103], v9 offset:4096\n"
"s_add_u32 m0, s78, 0\n"
"v_mfma_f32_32x32x16_bf16 a[160:175], v[88:91], v[72:75], a[160:175]\n"
"buffer_load_dwordx4 v30, s[20:23], s59 offen lds\n"
"ds_read_b128 v[104:107], v9 offset:8192\n"
"v_mfma_f32_32x32x16_bf16 a[176:191], v[92:95], v[72:75], a[176:191]\n"
"ds_read_b128 v[108:111], v9 offset:12288\n"
"s_add_u32 m0, s71, 0\n"
"v_mfma_f32_32x32x16_bf16 a[192:207], v[80:83], v[76:79], a[192:207]\n"
"buffer_load_dwordx4 v23, s[16:19], s53 offen lds\n"
"v_mfma_f32_32x32x16_bf16 a[208:223], v[84:87], v[76:79], a[208:223]\n"
"s_add_u32 m0, s79, 0\n"
"v_mfma_f32_32x32x16_bf16 a[224:239], v[88:91], v[76:79], a[224:239]\n"
"buffer_load_dwordx4 v31, s[20:23], s59 offen lds\n"
"v_mfma_f32_32x32x16_bf16 a[240:255], v[92:95], v[76:79], a[240:255]\n"
"s_waitcnt lgkmcnt(0)\n"
"v_mfma_f32_32x32x16_bf16 a[0:15], v[112:115], v[96:99], a[0:15]\n"
"ds_read_b128 v[64:67], v10 offset:0\n"
"v_mfma_f32_32x32x16_bf16 a[16:31], v[116:119], v[96:99], a[16:31]\n"
"ds_read_b64_tr_b16 v[80:81], v12 offset:4096\n"
"v_mfma_f32_32x32x16_bf16 a[32:47], v[120:123], v[96:99], a[32:47]\n"
"ds_read_b64_tr_b16 v[82:83], v12 offset:4608\n"
"v_mfma_f32_32x32x16_bf16 a[48:63], v[124:127], v[96:99], a[48:63]\n"
"ds_read_b64_tr_b16 v[84:85], v13 offset:4096\n"
"v_mfma_f32_32x32x16_bf16 a[64:79], v[112:115], v[100:103], a[64:79]\n"
"ds_read_b64_tr_b16 v[86:87], v13 offset:4608\n"
"v_mfma_f32_32x32x16_bf16 a[80:95], v[116:119], v[100:103], a[80:95]\n"
"ds_read_b64_tr_b16 v[88:89], v12 offset:12288\n"
"v_mfma_f32_32x32x16_bf16 a[96:111], v[120:123], v[100:103], a[96:111]\n"
"ds_read_b64_tr_b16 v[90:91], v12 offset:12800\n"
"v_mfma_f32_32x32x16_bf16 a[112:127], v[124:127], v[100:103], a[112:127]\n"
"ds_read_b64_tr_b16 v[92:93], v13 offset:12288\n"
"v_mfma_f32_32x32x16_bf16 a[128:143], v[112:115], v[104:107], a[128:143]\n"
"ds_read_b64_tr_b16 v[94:95], v13 offset:12800\n"
"v_mfma_f32_32x32x16_bf16 a[144:159], v[116:119], v[104:107], a[144:159]\n"
"ds_read_b128 v[68:71], v10 offset:4096\n"
"v_mfma_f32_32x32x16_bf16 a[160:175], v[120:123], v[104:107], a[160:175]\n"
"ds_read_b128 v[72:75], v10 offset:8192\n"
"v_mfma_f32_32x32x16_bf16 a[176:191], v[124:127], v[104:107], a[176:191]\n"
"ds_read_b128 v[76:79], v10 offset:12288\n"
"v_mfma_f32_32x32x16_bf16 a[192:207], v[112:115], v[108:111], a[192:207]\n"
"v_mfma_f32_32x32x16_bf16 a[208:223], v[116:119], v[108:111], a[208:223]\n"
"v_mfma_f32_32x32x16_bf16 a[224:239], v[120:123], v[108:111], a[224:239]\n"
"v_mfma_f32_32x32x16_bf16 a[240:255], v[124:127], v[108:111], a[240:255]\n"
"s_waitcnt lgkmcnt(0)\n"
"v_mfma_f32_32x32x16_bf16 a[0:15], v[80:83], v[64:67], a[0:15]\n"
"ds_read_b128 v[96:99], v11 offset:0\n"
"buffer_store_dwordx4 v[128:131], v177, s[80:83], s84 offen sc1\n"
"s_add_u32 s84, s84, 4096\n"
"v_mfma_f32_32x32x16_bf16 a[16:31], v[84:87], v[64:67], a[16:31]\n"
"ds_read_b64_tr_b16 v[112:113], v12 offset:6144\n"
"buffer_store_dwordx4 v[132:135], v177, s[80:83], s84 offen sc1\n"
"s_add_u32 s84, s84, 4096\n"
"s_add_u32 s37, s37, s60\n"
"v_mfma_f32_32x32x16_bf16 a[32:47], v[88:91], v[64:67], a[32:47]\n"
"buffer_load_dword v225, v222, s[24:27], s37 offen\n"
"ds_read_b64_tr_b16 v[114:115], v12 offset:6656\n"
"buffer_store_dwordx4 v[136:139], v177, s[80:83], s84 offen sc1\n"
"v_mfma_f32_32x32x16_bf16 a[48:63], v[92:95], v[64:67], a[48:63]\n"
"ds_read_b64_tr_b16 v[116:117], v13 offset:6144\n"
"s_add_u32 s84, s84, 4096\n"
"buffer_store_dwordx4 v[140:143], v177, s[80:83], s84 offen sc1\n"
"s_add_u32 s84, s84, 4096\n"
"v_mfma_f32_32x32x16_bf16 a[64:79], v[80:83], v[68:71], a[64:79]\n"
"ds_read_b64_tr_b16 v[118:119], v13 offset:6656\n"
"s_load_dwordx2 s[80:81], s[8:9], 16\n"
"v_lshrrev_b32 v216, 3, v0\n"
"v_lshlrev_b32 v216, 13, v216\n"
"v_and_b32 v217, 7, v0\n"
"v_mfma_f32_32x32x16_bf16 a[80:95], v[84:87], v[68:71], a[80:95]\n"
"ds_read_b64_tr_b16 v[120:121], v12 offset:14336\n"
"v_lshl_add_u32 v216, v217, 7, v216\n"
"v_mfma_f32_32x32x16_bf16 a[96:111], v[88:91], v[68:71], a[96:111]\n"
"ds_read_b64_tr_b16 v[122:123], v12 offset:14848\n"
"v_cmp_gt_u32 vcc, 32, v0\n"
"v_mov_b32 v218, 1073741824\n"
"s_nop 0\n"
"v_cndmask_b32 v216, v218, v216, vcc\n"
"v_mfma_f32_32x32x16_bf16 a[112:127], v[92:95], v[68:71], a[112:127]\n"
"ds_read_b64_tr_b16 v[124:125], v13 offset:14336\n"
"s_lshr_b32 s84, s28, 3\n"
"s_lshl_b32 s84, s84, 17\n"
"s_lshl_b32 s85, s29, 15\n"
"s_add_u32 s84, s84, s85\n"
"v_mfma_f32_32x32x16_bf16 a[128:143], v[80:83], v[72:75], a[128:143]\n"
"ds_read_b64_tr_b16 v[126:127], v13 offset:14848\n"
"s_and_b32 s85, s28, 7\n"
"s_lshl_b32 s85, s85, 10\n"
"s_add_u32 s84, s84, s85\n"
"v_mfma_f32_32x32x16_bf16 a[144:159], v[84:87], v[72:75], a[144:159]\n"
"ds_read_b128 v[100:103], v11 offset:4096\n"
"v_mfma_f32_32x32x16_bf16 a[160:175], v[88:91], v[72:75], a[160:175]\n"
"ds_read_b128 v[104:107], v11 offset:8192\n"
"v_mfma_f32_32x32x16_bf16 a[176:191], v[92:95], v[72:75], a[176:191]\n"
"ds_read_b128 v[108:111], v11 offset:12288\n"
"v_mfma_f32_32x32x16_bf16 a[192:207], v[80:83], v[76:79], a[192:207]\n"
"v_xor_b32 v8, 0x10000, v8\n"
"v_xor_b32 v9, 0x10000, v9\n"
"v_mfma_f32_32x32x16_bf16 a[208:223], v[84:87], v[76:79], a[208:223]\n"
"v_xor_b32 v10, 0x10000, v10\n"
"v_xor_b32 v11, 0x10000, v11\n"
"v_mfma_f32_32x32x16_bf16 a[224:239], v[88:91], v[76:79], a[224:239]\n"
"v_xor_b32 v12, 0x10000, v12\n"
"v_xor_b32 v13, 0x10000, v13\n"
"v_mfma_f32_32x32x16_bf16 a[240:255], v[92:95], v[76:79], a[240:255]\n"
"s_add_u32 s53, s53, 128\n"
"s_add_u32 s59, s59, 524288\n"
"s_waitcnt vmcnt(5) lgkmcnt(0)\n"
"s_barrier\n"
"s_add_u32 m0, s64, 65536\n"
"v_mfma_f32_32x32x16_bf16 a[0:15], v[112:115], v[96:99], a[0:15]\n"
"buffer_load_dwordx4 v16, s[16:19], s53 offen lds\n"
"ds_read_b128 v[64:67], v8 offset:0\n"
"s_and_b32 s81, s81, 0xffff\n"
"s_mov_b32 s82, 4194304\n"
"v_mfma_f32_32x32x16_bf16 a[16:31], v[116:119], v[96:99], a[16:31]\n"
"ds_read_b64_tr_b16 v[80:81], v12 offset:0\n"
"s_mov_b32 s83, 0x20000\n"
"s_and_b32 s34, s28, 7\n"
"s_lshl_b32 s34, s34, 2\n"
"s_add_u32 m0, s72, 65536\n"
"v_mfma_f32_32x32x16_bf16 a[32:47], v[120:123], v[96:99], a[32:47]\n"
"buffer_load_dwordx4 v24, s[20:23], s59 offen lds\n"
"ds_read_b64_tr_b16 v[82:83], v12 offset:512\n"
"s_bfe_u32 s35, s28, 0x20003\n"
"s_add_u32 s30, s34, s35\n"
"v_mfma_f32_32x32x16_bf16 a[48:63], v[124:127], v[96:99], a[48:63]\n"
"ds_read_b64_tr_b16 v[84:85], v13 offset:0\n"
"s_lshr_b32 s31, s28, 5\n"
"s_add_u32 s12, s50, 53477376\n"
"s_addc_u32 s13, s51, 0\n"
"s_add_u32 m0, s65, 65536\n"
"v_mfma_f32_32x32x16_bf16 a[64:79], v[112:115], v[100:103], a[64:79]\n"
"buffer_load_dwordx4 v17, s[16:19], s53 offen lds\n"
"ds_read_b64_tr_b16 v[86:87], v13 offset:512\n"
"s_and_b32 s13, s13, 0xffff\n"
"s_mov_b32 s14, 2097152\n"
"v_mfma_f32_32x32x16_bf16 a[80:95], v[116:119], v[100:103], a[80:95]\n"
"ds_read_b64_tr_b16 v[88:89], v12 offset:8192\n"
"s_mov_b32 s15, 0x20000\n"
"s_mov_b32 s24, s44\n"
"s_and_b32 s25, s45, 0xffff\n"
"s_add_u32 m0, s73, 65536\n"
"v_mfma_f32_32x32x16_bf16 a[96:111], v[120:123], v[100:103], a[96:111]\n"
"buffer_load_dwordx4 v25, s[20:23], s59 offen lds\n"
"ds_read_b64_tr_b16 v[90:91], v12 offset:8704\n"
"s_mov_b32 s26, 524288\n"
"s_mov_b32 s27, 0x20000\n"
"v_mfma_f32_32x32x16_bf16 a[112:127], v[124:127], v[100:103], a[112:127]\n"
"ds_read_b64_tr_b16 v[92:93], v13 offset:8192\n"
"s_and_b32 s34, s29, 1\n"
"s_lshl_b32 s41, s30, 1\n"
"s_add_u32 s41, s41, s34\n"
"s_add_u32 m0, s66, 65536\n"
"v_mfma_f32_32x32x16_bf16 a[128:143], v[112:115], v[104:107], a[128:143]\n"
"buffer_load_dwordx4 v18, s[16:19], s53 offen lds\n"
"ds_read_b64_tr_b16 v[94:95], v13 offset:8704\n"
"s_lshl_b32 s40, s34, 15\n"
"s_lshr_b32 s35, s29, 1\n"
"v_mfma_f32_32x32x16_bf16 a[144:159], v[116:119], v[104:107], a[144:159]\n"
"ds_read_b128 v[68:71], v8 offset:4096\n"
"s_lshl_b32 s35, s35, 12\n"
"s_add_u32 s40, s40, s35\n"
"s_add_u32 s40, s40, 65536\n"
"s_add_u32 m0, s74, 65536\n"
"v_mfma_f32_32x32x16_bf16 a[160:175], v[120:123], v[104:107], a[160:175]\n"
"buffer_load_dwordx4 v26, s[20:23], s59 offen lds\n"
"ds_read_b128 v[72:75], v8 offset:8192\n"
"s_lshl_b32 s38, s41, 15\n"
"s_add_u32 s38, s38, s35\n"
"v_mfma_f32_32x32x16_bf16 a[176:191], v[124:127], v[104:107], a[176:191]\n"
"ds_read_b128 v[76:79], v8 offset:12288\n"
"v_lshrrev_b32 v44, 3, v0\n"
"v_lshlrev_b32 v44, 10, v44\n"
"v_bfe_u32 v45, v0, 4, 1\n"
"s_add_u32 m0, s67, 65536\n"
"v_mfma_f32_32x32x16_bf16 a[192:207], v[112:115], v[108:111], a[192:207]\n"
"buffer_load_dwordx4 v19, s[16:19], s53 offen lds\n"
"v_lshlrev_b32 v45, 2, v45\n"
"v_and_b32 v46, 7, v0\n"
"v_xor_b32 v45, v45, v46\n"
"v_mfma_f32_32x32x16_bf16 a[208:223], v[116:119], v[108:111], a[208:223]\n"
"v_lshl_add_u32 v36, v45, 4, v44\n"
"v_add_u32 v37, 8192, v36\n"
"v_add_u32 v38, 16384, v36\n"
"v_add_u32 v39, 24576, v36\n"
"s_add_u32 m0, s75, 65536\n"
"v_mfma_f32_32x32x16_bf16 a[224:239], v[120:123], v[108:111], a[224:239]\n"
"buffer_load_dwordx4 v27, s[20:23], s59 offen lds\n"
"s_lshl_b32 s39, s29, 15\n"
"s_lshl_b32 s34, s31, 7\n"
"s_add_u32 s39, s39, s34\n"
"v_mfma_f32_32x32x16_bf16 a[240:255], v[124:127], v[108:111], a[240:255]\n"
"s_lshl_b32 s43, s29, 12\n"
"s_lshr_b32 s34, s29, 1\n"
"s_lshl_b32 s35, s34, 15\n"
"s_waitcnt lgkmcnt(0)\n"
"s_add_u32 m0, s68, 65536\n"
"v_mfma_f32_32x32x16_bf16 a[0:15], v[80:83], v[64:67], a[0:15]\n"
"buffer_load_dwordx4 v20, s[16:19], s53 offen lds\n"
"ds_read_b128 v[96:99], v9 offset:0\n"
"s_add_u32 s35, s35, 65536\n"
"v_add_u32 v40, s35, v1\n"
"v_mfma_f32_32x32x16_bf16 a[16:31], v[84:87], v[64:67], a[16:31]\n"
"ds_read_b64_tr_b16 v[112:113], v12 offset:2048\n"
"v_lshrrev_b32 v44, 5, v0\n"
"v_bfe_u32 v45, v0, 2, 2\n"
"v_lshl_add_u32 v44, v44, 3, v45\n"
"s_add_u32 m0, s76, 65536\n"
"v_mfma_f32_32x32x16_bf16 a[32:47], v[88:91], v[64:67], a[32:47]\n"
"buffer_load_dwordx4 v28, s[20:23], s59 offen lds\n"
"ds_read_b64_tr_b16 v[114:115], v12 offset:2560\n"
"v_lshlrev_b32 v44, 7, v44\n"
"v_and_b32 v46, 1, v0\n"
"v_mfma_f32_32x32x16_bf16 a[48:63], v[92:95], v[64:67], a[48:63]\n"
"ds_read_b64_tr_b16 v[116:117], v13 offset:2048\n"
"v_lshl_add_u32 v44, v46, 3, v44\n"
"v_bfe_u32 v46, v0, 4, 1\n"
"v_bfe_u32 v47, v0, 1, 1\n"
"s_add_u32 m0, s69, 65536\n"
"v_mfma_f32_32x32x16_bf16 a[64:79], v[80:83], v[68:71], a[64:79]\n"
"buffer_load_dwordx4 v21, s[16:19], s53 offen lds\n"
"ds_read_b64_tr_b16 v[118:119], v13 offset:2560\n"
"v_lshl_add_u32 v46, v46, 1, v47\n"
"s_and_b32 s34, s29, 1\n"
"v_mfma_f32_32x32x16_bf16 a[80:95], v[84:87], v[68:71], a[80:95]\n"
"ds_read_b64_tr_b16 v[120:121], v12 offset:10240\n"
"s_lshl_b32 s34, s34, 2\n"
"v_add_u32 v46, s34, v46\n"
"v_lshrrev_b32 v45, 1, v45\n"
"s_add_u32 m0, s77, 65536\n"
"v_mfma_f32_32x32x16_bf16 a[96:111], v[88:91], v[68:71], a[96:111]\n"
"buffer_load_dwordx4 v29, s[20:23], s59 offen lds\n"
"ds_read_b64_tr_b16 v[122:123], v12 offset:10752\n"
"v_lshlrev_b32 v45, 2, v45\n"
"v_xor_b32 v46, v46, v45\n"
"v_mfma_f32_32x32x16_bf16 a[112:127], v[92:95], v[68:71], a[112:127]\n"
"ds_read_b64_tr_b16 v[124:125], v13 offset:10240\n"
"v_lshl_add_u32 v41, v46, 4, v44\n"
"s_add_u32 s96, s50, 51380224\n"
"s_addc_u32 s97, s51, 0\n"
"s_add_u32 m0, s70, 65536\n"
"v_mfma_f32_32x32x16_bf16 a[128:143], v[80:83], v[72:75], a[128:143]\n"
"buffer_load_dwordx4 v22, s[16:19], s53 offen lds\n"
"ds_read_b64_tr_b16 v[126:127], v13 offset:10752\n"
"s_and_b32 s97, s97, 0xffff\n"
"s_mov_b32 s98, 2097152\n"
"v_mfma_f32_32x32x16_bf16 a[144:159], v[84:87], v[72:75], a[144:159]\n"
"ds_read_b128 v[100:103], v9 offset:4096\n"
"s_mov_b32 s99, 0x20000\n"
"s_lshr_b32 s36, s28, 5\n"
"s_sub_u32 s34, 15, s36\n"
"s_add_u32 m0, s78, 65536\n"
"v_mfma_f32_32x32x16_bf16 a[160:175], v[88:91], v[72:75], a[160:175]\n"
"buffer_load_dwordx4 v30, s[20:23], s59 offen lds\n"
"ds_read_b128 v[104:107], v9 offset:8192\n"
"s_cmp_lt_u32 s29, 2\n"
"s_cselect_b32 s34, s36, s34\n"
"v_mfma_f32_32x32x16_bf16 a[176:191], v[92:95], v[72:75], a[176:191]\n"
"ds_read_b128 v[108:111], v9 offset:12288\n"
"s_lshl_b32 s34, s34, 2\n"
"s_and_b32 s35, s29, 1\n"
"s_lshl_b32 s35, s35, 1\n"
"s_add_u32 m0, s71, 65536\n"
"v_mfma_f32_32x32x16_bf16 a[192:207], v[80:83], v[76:79], a[192:207]\n"
"buffer_load_dwordx4 v23, s[16:19], s53 offen lds\n"
"s_add_u32 s34, s34, s35\n"
"s_lshl_b32 s56, s34, 15\n"
"s_add_u32 s57, s56, 32768\n"
"v_mfma_f32_32x32x16_bf16 a[208:223], v[84:87], v[76:79], a[208:223]\n"
"s_and_b32 s34, s28, 7\n"
"s_lshl_b32 s34, s34, 1\n"
"s_bfe_u32 s35, s28, 0x10004\n"
"s_add_u32 s34, s34, s35\n"
"s_add_u32 m0, s79, 65536\n"
"v_mfma_f32_32x32x16_bf16 a[224:239], v[88:91], v[76:79], a[224:239]\n"
"buffer_load_dwordx4 v31, s[20:23], s59 offen lds\n"
"v_mov_b32 v42, s34\n"
"s_lshr_b32 s34, s28, 5\n"
"v_mov_b32 v43, s34\n"
"v_mfma_f32_32x32x16_bf16 a[240:255], v[92:95], v[76:79], a[240:255]\n"
"s_waitcnt lgkmcnt(0)\n"
"v_mfma_f32_32x32x16_bf16 a[0:15], v[112:115], v[96:99], a[0:15]\n"
"ds_read_b128 v[64:67], v10 offset:0\n"
"v_cmp_le_u32 vcc, 16, v0\n"
"s_nop 1\n"
"v_cndmask_b32 v42, v42, v43, vcc\n"
"v_mfma_f32_32x32x16_bf16 a[16:31], v[116:119], v[96:99], a[16:31]\n"
"ds_read_b64_tr_b16 v[80:81], v12 offset:4096\n"
"s_sub_u32 s34, 15, s34\n"
"v_mov_b32 v43, s34\n"
"v_mfma_f32_32x32x16_bf16 a[32:47], v[120:123], v[96:99], a[32:47]\n"
"ds_read_b64_tr_b16 v[82:83], v12 offset:4608\n"
"v_cmp_le_u32 vcc, 32, v0\n"
"s_nop 1\n"
"v_cndmask_b32 v42, v42, v43, vcc\n"
"v_mfma_f32_32x32x16_bf16 a[48:63], v[124:127], v[96:99], a[48:63]\n"
"ds_read_b64_tr_b16 v[84:85], v13 offset:4096\n"
"s_lshr_b32 s34, s28, 4\n"
"v_mov_b32 v43, s34\n"
"v_mfma_f32_32x32x16_bf16 a[64:79], v[112:115], v[100:103], a[64:79]\n"
"ds_read_b64_tr_b16 v[86:87], v13 offset:4608\n"
"v_cmp_le_u32 vcc, 48, v0\n"
"s_nop 1\n"
"v_cndmask_b32 v42, v42, v43, vcc\n"
"v_mfma_f32_32x32x16_bf16 a[80:95], v[116:119], v[100:103], a[80:95]\n"
"ds_read_b64_tr_b16 v[88:89], v12 offset:12288\n"
"v_lshlrev_b32 v42, 8, v42\n"
"v_add_u32 v42, 16640, v42\n"
"v_and_b32 v43, 15, v0\n"
"v_lshl_add_u32 v42, v43, 2, v42\n"
"v_mfma_f32_32x32x16_bf16 a[96:111], v[120:123], v[100:103], a[96:111]\n"
"ds_read_b64_tr_b16 v[90:91], v12 offset:12800\n"
"v_mfma_f32_32x32x16_bf16 a[112:127], v[124:127], v[100:103], a[112:127]\n"
"ds_read_b64_tr_b16 v[92:93], v13 offset:12288\n"
"v_mfma_f32_32x32x16_bf16 a[128:143], v[112:115], v[104:107], a[128:143]\n"
"ds_read_b64_tr_b16 v[94:95], v13 offset:12800\n"
"v_mfma_f32_32x32x16_bf16 a[144:159], v[116:119], v[104:107], a[144:159]\n"
"ds_read_b128 v[68:71], v10 offset:4096\n"
"v_mfma_f32_32x32x16_bf16 a[160:175], v[120:123], v[104:107], a[160:175]\n"
"ds_read_b128 v[72:75], v10 offset:8192\n"
"v_mfma_f32_32x32x16_bf16 a[176:191], v[124:127], v[104:107], a[176:191]\n"
"ds_read_b128 v[76:79], v10 offset:12288\n"
"v_mfma_f32_32x32x16_bf16 a[192:207], v[112:115], v[108:111], a[192:207]\n"
"v_mfma_f32_32x32x16_bf16 a[208:223], v[116:119], v[108:111], a[208:223]\n"
"v_mfma_f32_32x32x16_bf16 a[224:239], v[120:123], v[108:111], a[224:239]\n"
"v_mfma_f32_32x32x16_bf16 a[240:255], v[124:127], v[108:111], a[240:255]\n"
"s_waitcnt lgkmcnt(0)\n"
"v_mfma_f32_32x32x16_bf16 a[0:15], v[80:83], v[64:67], a[0:15]\n"
"ds_read_b128 v[96:99], v11 offset:0\n"
"v_mfma_f32_32x32x16_bf16 a[16:31], v[84:87], v[64:67], a[16:31]\n"
"ds_read_b64_tr_b16 v[112:113], v12 offset:6144\n"
"v_mfma_f32_32x32x16_bf16 a[32:47], v[88:91], v[64:67], a[32:47]\n"
"buffer_load_dword v214, v216, s[80:83], s84 offen\n"
"ds_read_b64_tr_b16 v[114:115], v12 offset:6656\n"
"v_mfma_f32_32x32x16_bf16 a[48:63], v[92:95], v[64:67], a[48:63]\n"
"ds_read_b64_tr_b16 v[116:117], v13 offset:6144\n"
"v_mfma_f32_32x32x16_bf16 a[64:79], v[80:83], v[68:71], a[64:79]\n"
"ds_read_b64_tr_b16 v[118:119], v13 offset:6656\n"
"v_mfma_f32_32x32x16_bf16 a[80:95], v[84:87], v[68:71], a[80:95]\n"
"ds_read_b64_tr_b16 v[120:121], v12 offset:14336\n"
"v_mfma_f32_32x32x16_bf16 a[96:111], v[88:91], v[68:71], a[96:111]\n"
"ds_read_b64_tr_b16 v[122:123], v12 offset:14848\n"
"v_mfma_f32_32x32x16_bf16 a[112:127], v[92:95], v[68:71], a[112:127]\n"
"ds_read_b64_tr_b16 v[124:125], v13 offset:14336\n"
"v_mfma_f32_32x32x16_bf16 a[128:143], v[80:83], v[72:75], a[128:143]\n"
"ds_read_b64_tr_b16 v[126:127], v13 offset:14848\n"
"v_mfma_f32_32x32x16_bf16 a[144:159], v[84:87], v[72:75], a[144:159]\n"
"ds_read_b128 v[100:103], v11 offset:4096\n"
"v_mfma_f32_32x32x16_bf16 a[160:175], v[88:91], v[72:75], a[160:175]\n"
"ds_read_b128 v[104:107], v11 offset:8192\n"
"v_mfma_f32_32x32x16_bf16 a[176:191], v[92:95], v[72:75], a[176:191]\n"
"ds_read_b128 v[108:111], v11 offset:12288\n"
"v_mfma_f32_32x32x16_bf16 a[192:207], v[80:83], v[76:79], a[192:207]\n"
"v_xor_b32 v8, 0x10000, v8\n"
"v_xor_b32 v9, 0x10000, v9\n"
"v_mfma_f32_32x32x16_bf16 a[208:223], v[84:87], v[76:79], a[208:223]\n"
"v_xor_b32 v10, 0x10000, v10\n"
"v_xor_b32 v11, 0x10000, v11\n"
"v_mfma_f32_32x32x16_bf16 a[224:239], v[88:91], v[76:79], a[224:239]\n"
"v_xor_b32 v12, 0x10000, v12\n"
"v_xor_b32 v13, 0x10000, v13\n"
"v_mfma_f32_32x32x16_bf16 a[240:255], v[92:95], v[76:79], a[240:255]\n"
"s_add_u32 s53, s53, 128\n"
"s_add_u32 s59, s59, 524288\n"
"s_waitcnt vmcnt(1) lgkmcnt(0)\n"
"s_barrier\n"
"s_cmp_lg_u32 s29, 0\n"
"s_cbranch_scc1 .Lsig1\n"
"s_lshr_b32 s49, s28, 4\n"
"s_lshl_b32 s49, s49, 8\n"
"s_and_b32 s48, s28, 15\n"
"s_lshl_b32 s48, s48, 2\n"
"s_add_u32 s49, s49, s48\n"
"s_add_u32 s49, s49, 16640\n"
"v_mul_i32_i24 v212, 256, v0\n"
"v_add_u32 v212, s49, v212\n"
"v_mov_b32 v213, s11\n"
"s_mov_b64 exec, 1\n"
"global_store_dword v212, v213, s[50:51] sc1\n"
"s_mov_b64 exec, -1\n"
".Lsig1:\n"
"s_add_u32 m0, s64, 0\n"
"v_mfma_f32_32x32x16_bf16 a[0:15], v[112:115], v[96:99], a[0:15]\n"
"buffer_load_dwordx4 v16, s[16:19], s53 offen lds\n"
"ds_read_b128 v[64:67], v8 offset:0\n"
"v_mfma_f32_32x32x16_bf16 a[16:31], v[116:119], v[96:99], a[16:31]\n"
"ds_read_b64_tr_b16 v[80:81], v12 offset:0\n"
"s_add_u32 m0, s72, 0\n"
"v_mfma_f32_32x32x16_bf16 a[32:47], v[120:123], v[96:99], a[32:47]\n"
"buffer_load_dwordx4 v24, s[20:23], s59 offen lds\n"
"ds_read_b64_tr_b16 v[82:83], v12 offset:512\n"
"v_mfma_f32_32x32x16_bf16 a[48:63], v[124:127], v[96:99], a[48:63]\n"
"ds_read_b64_tr_b16 v[84:85], v13 offset:0\n"
"s_add_u32 m0, s65, 0\n"
"v_mfma_f32_32x32x16_bf16 a[64:79], v[112:115], v[100:103], a[64:79]\n"
"buffer_load_dwordx4 v17, s[16:19], s53 offen lds\n"
"ds_read_b64_tr_b16 v[86:87], v13 offset:512\n"
"v_mfma_f32_32x32x16_bf16 a[80:95], v[116:119], v[100:103], a[80:95]\n"
"ds_read_b64_tr_b16 v[88:89], v12 offset:8192\n"
"s_add_u32 m0, s73, 0\n"
"v_mfma_f32_32x32x16_bf16 a[96:111], v[120:123], v[100:103], a[96:111]\n"
"buffer_load_dwordx4 v25, s[20:23], s59 offen lds\n"
"ds_read_b64_tr_b16 v[90:91], v12 offset:8704\n"
"v_mfma_f32_32x32x16_bf16 a[112:127], v[124:127], v[100:103], a[112:127]\n"
"ds_read_b64_tr_b16 v[92:93], v13 offset:8192\n"
"s_add_u32 m0, s66, 0\n"
"v_mfma_f32_32x32x16_bf16 a[128:143], v[112:115], v[104:107], a[128:143]\n"
"buffer_load_dwordx4 v18, s[16:19], s53 offen lds\n"
"ds_read_b64_tr_b16 v[94:95], v13 offset:8704\n"
"v_mfma_f32_32x32x16_bf16 a[144:159], v[116:119], v[104:107], a[144:159]\n"
"ds_read_b128 v[68:71], v8 offset:4096\n"
"s_add_u32 m0, s74, 0\n"
"v_mfma_f32_32x32x16_bf16 a[160:175], v[120:123], v[104:107], a[160:175]\n"
"buffer_load_dwordx4 v26, s[20:23], s59 offen lds\n"
"ds_read_b128 v[72:75], v8 offset:8192\n"
"v_mfma_f32_32x32x16_bf16 a[176:191], v[124:127], v[104:107], a[176:191]\n"
"ds_read_b128 v[76:79], v8 offset:12288\n"
"s_add_u32 m0, s67, 0\n"
"v_mfma_f32_32x32x16_bf16 a[192:207], v[112:115], v[108:111], a[192:207]\n"
"buffer_load_dwordx4 v19, s[16:19], s53 offen lds\n"
"v_mfma_f32_32x32x16_bf16 a[208:223], v[116:119], v[108:111], a[208:223]\n"
"s_mov_b32 s44, s50\n"
"s_and_b32 s45, s51, 0xffff\n"
"s_mov_b32 s46, 53248\n"
"s_mov_b32 s47, 0x20000\n"
"s_add_u32 m0, s75, 0\n"
"v_mfma_f32_32x32x16_bf16 a[224:239], v[120:123], v[108:111], a[224:239]\n"
"buffer_load_dwordx4 v27, s[20:23], s59 offen lds\n"
"s_cmp_eq_u32 s29, 0\n"
"s_cselect_b32 s34, 0, 1073741824\n"
"v_mfma_f32_32x32x16_bf16 a[240:255], v[124:127], v[108:111], a[240:255]\n"
"s_waitcnt lgkmcnt(0)\n"
"s_add_u32 m0, s68, 0\n"
"v_mfma_f32_32x32x16_bf16 a[0:15], v[80:83], v[64:67], a[0:15]\n"
"buffer_load_dwordx4 v20, s[16:19], s53 offen lds\n"
"ds_read_b128 v[96:99], v9 offset:0\n"
"v_mfma_f32_32x32x16_bf16 a[16:31], v[84:87], v[64:67], a[16:31]\n"
"ds_read_b64_tr_b16 v[112:113], v12 offset:2048\n"
"s_add_u32 m0, s76, 0\n"
"v_mfma_f32_32x32x16_bf16 a[32:47], v[88:91], v[64:67], a[32:47]\n"
"buffer_load_dwordx4 v28, s[20:23], s59 offen lds\n"
"ds_read_b64_tr_b16 v[114:115], v12 offset:2560\n"
"v_mfma_f32_32x32x16_bf16 a[48:63], v[92:95], v[64:67], a[48:63]\n"
"ds_read_b64_tr_b16 v[116:117], v13 offset:2048\n"
"s_add_u32 m0, s69, 0\n"
"v_mfma_f32_32x32x16_bf16 a[64:79], v[80:83], v[68:71], a[64:79]\n"
"buffer_load_dwordx4 v21, s[16:19], s53 offen lds\n"
"ds_read_b64_tr_b16 v[118:119], v13 offset:2560\n"
"v_mfma_f32_32x32x16_bf16 a[80:95], v[84:87], v[68:71], a[80:95]\n"
"ds_read_b64_tr_b16 v[120:121], v12 offset:10240\n"
"s_add_u32 m0, s77, 0\n"
"v_mfma_f32_32x32x16_bf16 a[96:111], v[88:91], v[68:71], a[96:111]\n"
"buffer_load_dwordx4 v29, s[20:23], s59 offen lds\n"
"ds_read_b64_tr_b16 v[122:123], v12 offset:10752\n"
"v_mfma_f32_32x32x16_bf16 a[112:127], v[92:95], v[68:71], a[112:127]\n"
"ds_read_b64_tr_b16 v[124:125], v13 offset:10240\n"
"s_add_u32 m0, s70, 0\n"
"v_mfma_f32_32x32x16_bf16 a[128:143], v[80:83], v[72:75], a[128:143]\n"
"buffer_load_dwordx4 v22, s[16:19], s53 offen lds\n"
"ds_read_b64_tr_b16 v[126:127], v13 offset:10752\n"
"v_mfma_f32_32x32x16_bf16 a[144:159], v[84:87], v[72:75], a[144:159]\n"
"ds_read_b128 v[100:103], v9 offset:4096\n"
"s_add_u32 m0, s78, 0\n"
"v_mfma_f32_32x32x16_bf16 a[160:175], v[88:91], v[72:75], a[160:175]\n"
"buffer_load_dwordx4 v30, s[20:23], s59 offen lds\n"
"ds_read_b128 v[104:107], v9 offset:8192\n"
"v_mfma_f32_32x32x16_bf16 a[176:191], v[92:95], v[72:75], a[176:191]\n"
"ds_read_b128 v[108:111], v9 offset:12288\n"
"s_add_u32 m0, s71, 0\n"
"v_mfma_f32_32x32x16_bf16 a[192:207], v[80:83], v[76:79], a[192:207]\n"
"buffer_load_dwordx4 v23, s[16:19], s53 offen lds\n"
"v_mfma_f32_32x32x16_bf16 a[208:223], v[84:87], v[76:79], a[208:223]\n"
"s_add_u32 m0, s79, 0\n"
"v_mfma_f32_32x32x16_bf16 a[224:239], v[88:91], v[76:79], a[224:239]\n"
"buffer_load_dwordx4 v31, s[20:23], s59 offen lds\n"
"v_mfma_f32_32x32x16_bf16 a[240:255], v[92:95], v[76:79], a[240:255]\n"
"s_waitcnt lgkmcnt(0)\n"
"v_mfma_f32_32x32x16_bf16 a[0:15], v[112:115], v[96:99], a[0:15]\n"
"ds_read_b128 v[64:67], v10 offset:0\n"
"v_mfma_f32_32x32x16_bf16 a[16:31], v[116:119], v[96:99], a[16:31]\n"
"ds_read_b64_tr_b16 v[80:81], v12 offset:4096\n"
"v_mfma_f32_32x32x16_bf16 a[32:47], v[120:123], v[96:99], a[32:47]\n"
"ds_read_b64_tr_b16 v[82:83], v12 offset:4608\n"
"v_mfma_f32_32x32x16_bf16 a[48:63], v[124:127], v[96:99], a[48:63]\n"
"ds_read_b64_tr_b16 v[84:85], v13 offset:4096\n"
"v_mfma_f32_32x32x16_bf16 a[64:79], v[112:115], v[100:103], a[64:79]\n"
"ds_read_b64_tr_b16 v[86:87], v13 offset:4608\n"
"v_mfma_f32_32x32x16_bf16 a[80:95], v[116:119], v[100:103], a[80:95]\n"
"ds_read_b64_tr_b16 v[88:89], v12 offset:12288\n"
"v_mfma_f32_32x32x16_bf16 a[96:111], v[120:123], v[100:103], a[96:111]\n"
"ds_read_b64_tr_b16 v[90:91], v12 offset:12800\n"
"v_mfma_f32_32x32x16_bf16 a[112:127], v[124:127], v[100:103], a[112:127]\n"
"ds_read_b64_tr_b16 v[92:93], v13 offset:12288\n"
"v_mfma_f32_32x32x16_bf16 a[128:143], v[112:115], v[104:107], a[128:143]\n"
"ds_read_b64_tr_b16 v[94:95], v13 offset:12800\n"
"v_mfma_f32_32x32x16_bf16 a[144:159], v[116:119], v[104:107], a[144:159]\n"
"ds_read_b128 v[68:71], v10 offset:4096\n"
"v_mfma_f32_32x32x16_bf16 a[160:175], v[120:123], v[104:107], a[160:175]\n"
"ds_read_b128 v[72:75], v10 offset:8192\n"
"v_mfma_f32_32x32x16_bf16 a[176:191], v[124:127], v[104:107], a[176:191]\n"
"ds_read_b128 v[76:79], v10 offset:12288\n"
"v_mfma_f32_32x32x16_bf16 a[192:207], v[112:115], v[108:111], a[192:207]\n"
"v_mfma_f32_32x32x16_bf16 a[208:223], v[116:119], v[108:111], a[208:223]\n"
"v_mfma_f32_32x32x16_bf16 a[224:239], v[120:123], v[108:111], a[224:239]\n"
"v_mfma_f32_32x32x16_bf16 a[240:255], v[124:127], v[108:111], a[240:255]\n"
"s_waitcnt lgkmcnt(0)\n"
"v_mfma_f32_32x32x16_bf16 a[0:15], v[80:83], v[64:67], a[0:15]\n"
"ds_read_b128 v[96:99], v11 offset:0\n"
"buffer_load_dword v43, v42, s[44:47], s34 offen sc1\n"
"v_lshlrev_b32 v144, 16, v180\n"
"v_and_b32 v145, 0xffff0000, v180\n"
"v_mfma_f32_32x32x16_bf16 a[16:31], v[84:87], v[64:67], a[16:31]\n"
"ds_read_b64_tr_b16 v[112:113], v12 offset:6144\n"
"v_lshlrev_b32 v146, 16, v181\n"
"v_and_b32 v147, 0xffff0000, v181\n"
"v_lshlrev_b32 v148, 16, v182\n"
"v_and_b32 v149, 0xffff0000, v182\n"
"v_mfma_f32_32x32x16_bf16 a[32:47], v[88:91], v[64:67], a[32:47]\n"
"ds_read_b64_tr_b16 v[114:115], v12 offset:6656\n"
"v_lshlrev_b32 v150, 16, v183\n"
"v_and_b32 v151, 0xffff0000, v183\n"
"v_lshlrev_b32 v160, 16, v184\n"
"v_and_b32 v161, 0xffff0000, v184\n"
"v_mfma_f32_32x32x16_bf16 a[48:63], v[92:95], v[64:67], a[48:63]\n"
"ds_read_b64_tr_b16 v[116:117], v13 offset:6144\n"
"v_add_f32 v144, v144, v160\n"
"v_add_f32 v145, v145, v161\n"
"v_lshlrev_b32 v160, 16, v185\n"
"v_and_b32 v161, 0xffff0000, v185\n"
"v_mfma_f32_32x32x16_bf16 a[64:79], v[80:83], v[68:71], a[64:79]\n"
"ds_read_b64_tr_b16 v[118:119], v13 offset:6656\n"
"v_add_f32 v146, v146, v160\n"
"v_add_f32 v147, v147, v161\n"
"v_lshlrev_b32 v160, 16, v186\n"
"v_and_b32 v161, 0xffff0000, v186\n"
"v_mfma_f32_32x32x16_bf16 a[80:95], v[84:87], v[68:71], a[80:95]\n"
"ds_read_b64_tr_b16 v[120:121], v12 offset:14336\n"
"v_add_f32 v148, v148, v160\n"
"v_add_f32 v149, v149, v161\n"
"v_lshlrev_b32 v160, 16, v187\n"
"v_and_b32 v161, 0xffff0000, v187\n"
"v_mfma_f32_32x32x16_bf16 a[96:111], v[88:91], v[68:71], a[96:111]\n"
"ds_read_b64_tr_b16 v[122:123], v12 offset:14848\n"
"v_add_f32 v150, v150, v160\n"
"v_add_f32 v151, v151, v161\n"
"v_lshlrev_b32 v160, 16, v188\n"
"v_and_b32 v161, 0xffff0000, v188\n"
"v_mfma_f32_32x32x16_bf16 a[112:127], v[92:95], v[68:71], a[112:127]\n"
"ds_read_b64_tr_b16 v[124:125], v13 offset:14336\n"
"v_add_f32 v144, v144, v160\n"
"v_add_f32 v145, v145, v161\n"
"v_lshlrev_b32 v160, 16, v189\n"
"v_and_b32 v161, 0xffff0000, v189\n"
"v_mfma_f32_32x32x16_bf16 a[128:143], v[80:83], v[72:75], a[128:143]\n"
"ds_read_b64_tr_b16 v[126:127], v13 offset:14848\n"
"v_add_f32 v146, v146, v160\n"
"v_add_f32 v147, v147, v161\n"
"v_lshlrev_b32 v160, 16, v190\n"
"v_and_b32 v161, 0xffff0000, v190\n"
"v_mfma_f32_32x32x16_bf16 a[144:159], v[84:87], v[72:75], a[144:159]\n"
"ds_read_b128 v[100:103], v11 offset:4096\n"
"v_add_f32 v148, v148, v160\n"
"v_add_f32 v149, v149, v161\n"
"v_lshlrev_b32 v160, 16, v191\n"
"v_and_b32 v161, 0xffff0000, v191\n"
"v_mfma_f32_32x32x16_bf16 a[160:175], v[88:91], v[72:75], a[160:175]\n"
"ds_read_b128 v[104:107], v11 offset:8192\n"
"v_add_f32 v150, v150, v160\n"
"v_add_f32 v151, v151, v161\n"
"v_lshlrev_b32 v160, 16, v192\n"
"v_and_b32 v161, 0xffff0000, v192\n"
"v_mfma_f32_32x32x16_bf16 a[176:191], v[92:95], v[72:75], a[176:191]\n"
"ds_read_b128 v[108:111], v11 offset:12288\n"
"v_add_f32 v144, v144, v160\n"
"v_add_f32 v145, v145, v161\n"
"v_lshlrev_b32 v160, 16, v193\n"
"v_and_b32 v161, 0xffff0000, v193\n"
"v_mfma_f32_32x32x16_bf16 a[192:207], v[80:83], v[76:79], a[192:207]\n"
"v_xor_b32 v8, 0x10000, v8\n"
"v_xor_b32 v9, 0x10000, v9\n"
"v_add_f32 v146, v146, v160\n"
"v_add_f32 v147, v147, v161\n"
"v_lshlrev_b32 v160, 16, v194\n"
"v_mfma_f32_32x32x16_bf16 a[208:223], v[84:87], v[76:79], a[208:223]\n"
"v_xor_b32 v10, 0x10000, v10\n"
"v_xor_b32 v11, 0x10000, v11\n"
"v_and_b32 v161, 0xffff0000, v194\n"
"v_add_f32 v148, v148, v160\n"
"v_add_f32 v149, v149, v161\n"
"v_mfma_f32_32x32x16_bf16 a[224:239], v[88:91], v[76:79], a[224:239]\n"
"v_xor_b32 v12, 0x10000, v12\n"
"v_xor_b32 v13, 0x10000, v13\n"
"v_lshlrev_b32 v160, 16, v195\n"
"v_and_b32 v161, 0xffff0000, v195\n"
"v_add_f32 v150, v150, v160\n"
"v_mfma_f32_32x32x16_bf16 a[240:255], v[92:95], v[76:79], a[240:255]\n"
"v_add_f32 v151, v151, v161\n"
"v_lshlrev_b32 v160, 16, v196\n"
"v_and_b32 v161, 0xffff0000, v196\n"
"s_add_u32 s53, s53, 128\n"
"s_add_u32 s59, s59, 524288\n"
"s_waitcnt vmcnt(1) lgkmcnt(0)\n"
"s_barrier\n"
"s_add_u32 m0, s64, 65536\n"
"v_mfma_f32_32x32x16_bf16 a[0:15], v[112:115], v[96:99], a[0:15]\n"
"buffer_load_dwordx4 v16, s[16:19], s53 offen lds\n"
"ds_read_b128 v[64:67], v8 offset:0\n"
"v_add_f32 v144, v144, v160\n"
"v_add_f32 v145, v145, v161\n"
"v_mfma_f32_32x32x16_bf16 a[16:31], v[116:119], v[96:99], a[16:31]\n"
"ds_read_b64_tr_b16 v[80:81], v12 offset:0\n"
"v_lshlrev_b32 v160, 16, v197\n"
"v_and_b32 v161, 0xffff0000, v197\n"
"v_add_f32 v146, v146, v160\n"
"s_add_u32 m0, s72, 65536\n"
"v_mfma_f32_32x32x16_bf16 a[32:47], v[120:123], v[96:99], a[32:47]\n"
"buffer_load_dwordx4 v24, s[20:23], s59 offen lds\n"
"ds_read_b64_tr_b16 v[82:83], v12 offset:512\n"
"v_add_f32 v147, v147, v161\n"
"v_lshlrev_b32 v160, 16, v198\n"
"v_mfma_f32_32x32x16_bf16 a[48:63], v[124:127], v[96:99], a[48:63]\n"
"ds_read_b64_tr_b16 v[84:85], v13 offset:0\n"
"v_and_b32 v161, 0xffff0000, v198\n"
"v_add_f32 v148, v148, v160\n"
"v_add_f32 v149, v149, v161\n"
"s_add_u32 m0, s65, 65536\n"
"v_mfma_f32_32x32x16_bf16 a[64:79], v[112:115], v[100:103], a[64:79]\n"
"buffer_load_dwordx4 v17, s[16:19], s53 offen lds\n"
"ds_read_b64_tr_b16 v[86:87], v13 offset:512\n"
"v_lshlrev_b32 v160, 16, v199\n"
"v_and_b32 v161, 0xffff0000, v199\n"
"v_mfma_f32_32x32x16_bf16 a[80:95], v[116:119], v[100:103], a[80:95]\n"
"ds_read_b64_tr_b16 v[88:89], v12 offset:8192\n"
"v_add_f32 v150, v150, v160\n"
"v_add_f32 v151, v151, v161\n"
"v_lshlrev_b32 v160, 16, v200\n"
"s_add_u32 m0, s73, 65536\n"
"v_mfma_f32_32x32x16_bf16 a[96:111], v[120:123], v[100:103], a[96:111]\n"
"buffer_load_dwordx4 v25, s[20:23], s59 offen lds\n"
"ds_read_b64_tr_b16 v[90:91], v12 offset:8704\n"
"v_and_b32 v161, 0xffff0000, v200\n"
"v_add_f32 v144, v144, v160\n"
"v_mfma_f32_32x32x16_bf16 a[112:127], v[124:127], v[100:103], a[112:127]\n"
"ds_read_b64_tr_b16 v[92:93], v13 offset:8192\n"
"v_add_f32 v145, v145, v161\n"
"v_lshlrev_b32 v160, 16, v201\n"
"v_and_b32 v161, 0xffff0000, v201\n"
"s_add_u32 m0, s66, 65536\n"
"v_mfma_f32_32x32x16_bf16 a[128:143], v[112:115], v[104:107], a[128:143]\n"
"buffer_load_dwordx4 v18, s[16:19], s53 offen lds\n"
"ds_read_b64_tr_b16 v[94:95], v13 offset:8704\n"
"v_add_f32 v146, v146, v160\n"
"v_add_f32 v147, v147, v161\n"
"v_mfma_f32_32x32x16_bf16 a[144:159], v[116:119], v[104:107], a[144:159]\n"
"ds_read_b128 v[68:71], v8 offset:4096\n"
"v_lshlrev_b32 v160, 16, v202\n"
"v_and_b32 v161, 0xffff0000, v202\n"
"v_add_f32 v148, v148, v160\n"
"s_add_u32 m0, s74, 65536\n"
"v_mfma_f32_32x32x16_bf16 a[160:175], v[120:123], v[104:107], a[160:175]\n"
"buffer_load_dwordx4 v26, s[20:23], s59 offen lds\n"
"ds_read_b128 v[72:75], v8 offset:8192\n"
"v_add_f32 v149, v149, v161\n"
"v_lshlrev_b32 v160, 16, v203\n"
"v_mfma_f32_32x32x16_bf16 a[176:191], v[124:127], v[104:107], a[176:191]\n"
"ds_read_b128 v[76:79], v8 offset:12288\n"
"v_and_b32 v161, 0xffff0000, v203\n"
"v_add_f32 v150, v150, v160\n"
"v_add_f32 v151, v151, v161\n"
"s_add_u32 m0, s67, 65536\n"
"v_mfma_f32_32x32x16_bf16 a[192:207], v[112:115], v[108:111], a[192:207]\n"
"buffer_load_dwordx4 v19, s[16:19], s53 offen lds\n"
"v_lshlrev_b32 v160, 16, v204\n"
"v_and_b32 v161, 0xffff0000, v204\n"
"v_add_f32 v144, v144, v160\n"
"v_mfma_f32_32x32x16_bf16 a[208:223], v[116:119], v[108:111], a[208:223]\n"
"v_add_f32 v145, v145, v161\n"
"v_lshlrev_b32 v160, 16, v205\n"
"v_and_b32 v161, 0xffff0000, v205\n"
"v_add_f32 v146, v146, v160\n"
"s_add_u32 m0, s75, 65536\n"
"v_mfma_f32_32x32x16_bf16 a[224:239], v[120:123], v[108:111], a[224:239]\n"
"buffer_load_dwordx4 v27, s[20:23], s59 offen lds\n"
"v_add_f32 v147, v147, v161\n"
"v_lshlrev_b32 v160, 16, v206\n"
"v_and_b32 v161, 0xffff0000, v206\n"
"v_mfma_f32_32x32x16_bf16 a[240:255], v[124:127], v[108:111], a[240:255]\n"
"v_add_f32 v148, v148, v160\n"
"v_add_f32 v149, v149, v161\n"
"v_lshlrev_b32 v160, 16, v207\n"
"s_waitcnt lgkmcnt(0)\n"
"s_add_u32 m0, s68, 65536\n"
"v_mfma_f32_32x32x16_bf16 a[0:15], v[80:83], v[64:67], a[0:15]\n"
"buffer_load_dwordx4 v20, s[16:19], s53 offen lds\n"
"ds_read_b128 v[96:99], v9 offset:0\n"
"v_and_b32 v161, 0xffff0000, v207\n"
"v_add_f32 v150, v150, v160\n"
"v_mfma_f32_32x32x16_bf16 a[16:31], v[84:87], v[64:67], a[16:31]\n"
"ds_read_b64_tr_b16 v[112:113], v12 offset:2048\n"
"v_add_f32 v151, v151, v161\n"
"v_lshlrev_b32 v160, 16, v208\n"
"v_and_b32 v161, 0xffff0000, v208\n"
"s_add_u32 m0, s76, 65536\n"
"v_mfma_f32_32x32x16_bf16 a[32:47], v[88:91], v[64:67], a[32:47]\n"
"buffer_load_dwordx4 v28, s[20:23], s59 offen lds\n"
"ds_read_b64_tr_b16 v[114:115], v12 offset:2560\n"
"v_add_f32 v144, v144, v160\n"
"v_add_f32 v145, v145, v161\n"
"v_mfma_f32_32x32x16_bf16 a[48:63], v[92:95], v[64:67], a[48:63]\n"
"ds_read_b64_tr_b16 v[116:117], v13 offset:2048\n"
"v_lshlrev_b32 v160, 16, v209\n"
"v_and_b32 v161, 0xffff0000, v209\n"
"v_add_f32 v146, v146, v160\n"
"s_add_u32 m0, s69, 65536\n"
"v_mfma_f32_32x32x16_bf16 a[64:79], v[80:83], v[68:71], a[64:79]\n"
"buffer_load_dwordx4 v21, s[16:19], s53 offen lds\n"
"ds_read_b64_tr_b16 v[118:119], v13 offset:2560\n"
"v_add_f32 v147, v147, v161\n"
"v_lshlrev_b32 v160, 16, v210\n"
"v_mfma_f32_32x32x16_bf16 a[80:95], v[84:87], v[68:71], a[80:95]\n"
"ds_read_b64_tr_b16 v[120:121], v12 offset:10240\n"
"v_and_b32 v161, 0xffff0000, v210\n"
"v_add_f32 v148, v148, v160\n"
"v_add_f32 v149, v149, v161\n"
"s_add_u32 m0, s77, 65536\n"
"v_mfma_f32_32x32x16_bf16 a[96:111], v[88:91], v[68:71], a[96:111]\n"
"buffer_load_dwordx4 v29, s[20:23], s59 offen lds\n"
"ds_read_b64_tr_b16 v[122:123], v12 offset:10752\n"
"v_lshlrev_b32 v160, 16, v211\n"
"v_and_b32 v161, 0xffff0000, v211\n"
"v_mfma_f32_32x32x16_bf16 a[112:127], v[92:95], v[68:71], a[112:127]\n"
"ds_read_b64_tr_b16 v[124:125], v13 offset:10240\n"
"v_add_f32 v150, v150, v160\n"
"v_add_f32 v151, v151, v161\n"
"s_add_u32 m0, s70, 65536\n"
"v_mfma_f32_32x32x16_bf16 a[128:143], v[80:83], v[72:75], a[128:143]\n"
"buffer_load_dwordx4 v22, s[16:19], s53 offen lds\n"
"ds_read_b64_tr_b16 v[126:127], v13 offset:10752\n"
"s_nop 1\n"
"v_mfma_f32_32x32x16_bf16 a[144:159], v[84:87], v[72:75], a[144:159]\n"
"ds_read_b128 v[100:103], v9 offset:4096\n"
"v_add_f32_dpp v144, v144, v144 row_ror:4 row_mask:0xf bank_mask:0xf\n"
"v_add_f32_dpp v145, v145, v145 row_ror:4 row_mask:0xf bank_mask:0xf\n"
"v_add_f32_dpp v146, v146, v146 row_ror:4 row_mask:0xf bank_mask:0xf\n"
"s_add_u32 m0, s78, 65536\n"
"v_mfma_f32_32x32x16_bf16 a[160:175], v[88:91], v[72:75], a[160:175]\n"
"buffer_load_dwordx4 v30, s[20:23], s59 offen lds\n"
"ds_read_b128 v[104:107], v9 offset:8192\n"
"v_add_f32_dpp v147, v147, v147 row_ror:4 row_mask:0xf bank_mask:0xf\n"
"v_add_f32_dpp v148, v148, v148 row_ror:4 row_mask:0xf bank_mask:0xf\n"
"v_mfma_f32_32x32x16_bf16 a[176:191], v[92:95], v[72:75], a[176:191]\n"
"ds_read_b128 v[108:111], v9 offset:12288\n"
"v_add_f32_dpp v149, v149, v149 row_ror:4 row_mask:0xf bank_mask:0xf\n"
"v_add_f32_dpp v150, v150, v150 row_ror:4 row_mask:0xf bank_mask:0xf\n"
"v_add_f32_dpp v151, v151, v151 row_ror:4 row_mask:0xf bank_mask:0xf\n"
"s_add_u32 m0, s71, 65536\n"
"v_mfma_f32_32x32x16_bf16 a[192:207], v[80:83], v[76:79], a[192:207]\n"
"buffer_load_dwordx4 v23, s[16:19], s53 offen lds\n"
"s_nop 1\n"
"v_add_f32_dpp v144, v144, v144 row_ror:8 row_mask:0xf bank_mask:0xf\n"
"v_mfma_f32_32x32x16_bf16 a[208:223], v[84:87], v[76:79], a[208:223]\n"
"v_add_f32_dpp v145, v145, v145 row_ror:8 row_mask:0xf bank_mask:0xf\n"
"v_add_f32_dpp v146, v146, v146 row_ror:8 row_mask:0xf bank_mask:0xf\n"
"v_add_f32_dpp v147, v147, v147 row_ror:8 row_mask:0xf bank_mask:0xf\n"
"v_add_f32_dpp v148, v148, v148 row_ror:8 row_mask:0xf bank_mask:0xf\n"
"s_add_u32 m0, s79, 65536\n"
"v_mfma_f32_32x32x16_bf16 a[224:239], v[88:91], v[76:79], a[224:239]\n"
"buffer_load_dwordx4 v31, s[20:23], s59 offen lds\n"
"v_add_f32_dpp v149, v149, v149 row_ror:8 row_mask:0xf bank_mask:0xf\n"
"v_add_f32_dpp v150, v150, v150 row_ror:8 row_mask:0xf bank_mask:0xf\n"
"v_add_f32_dpp v151, v151, v151 row_ror:8 row_mask:0xf bank_mask:0xf\n"
"v_mfma_f32_32x32x16_bf16 a[240:255], v[92:95], v[76:79], a[240:255]\n"
"v_mov_b32 v152, v144\n"
"v_mov_b32 v153, v145\n"
"v_mov_b32 v154, v146\n"
"s_waitcnt lgkmcnt(0)\n"
"v_mfma_f32_32x32x16_bf16 a[0:15], v[112:115], v[96:99], a[0:15]\n"
"ds_read_b128 v[64:67], v10 offset:0\n"
"v_mov_b32 v155, v147\n"
"v_mov_b32 v156, v148\n"
"v_mov_b32 v157, v149\n"
"v_mov_b32 v158, v150\n"
"v_mfma_f32_32x32x16_bf16 a[16:31], v[116:119], v[96:99], a[16:31]\n"
"ds_read_b64_tr_b16 v[80:81], v12 offset:4096\n"
"v_mov_b32 v159, v151\n"
"s_nop 1\n"
"v_permlane16_swap_b32 v144, v152\n"
"v_mfma_f32_32x32x16_bf16 a[32:47], v[120:123], v[96:99], a[32:47]\n"
"ds_read_b64_tr_b16 v[82:83], v12 offset:4608\n"
"v_permlane16_swap_b32 v145, v153\n"
"v_permlane16_swap_b32 v146, v154\n"
"v_permlane16_swap_b32 v147, v155\n"
"v_permlane16_swap_b32 v148, v156\n"
"v_mfma_f32_32x32x16_bf16 a[48:63], v[124:127], v[96:99], a[48:63]\n"
"ds_read_b64_tr_b16 v[84:85], v13 offset:4096\n"
"v_permlane16_swap_b32 v149, v157\n"
"v_permlane16_swap_b32 v150, v158\n"
"v_permlane16_swap_b32 v151, v159\n"
"v_add_f32 v144, v144, v152\n"
"v_mfma_f32_32x32x16_bf16 a[64:79], v[112:115], v[100:103], a[64:79]\n"
"ds_read_b64_tr_b16 v[86:87], v13 offset:4608\n"
"v_add_f32 v145, v145, v153\n"
"v_add_f32 v146, v146, v154\n"
"v_add_f32 v147, v147, v155\n"
"v_add_f32 v148, v148, v156\n"
"v_mfma_f32_32x32x16_bf16 a[80:95], v[116:119], v[100:103], a[80:95]\n"
"ds_read_b64_tr_b16 v[88:89], v12 offset:12288\n"
"v_add_f32 v149, v149, v157\n"
"v_add_f32 v150, v150, v158\n"
"v_add_f32 v151, v151, v159\n"
"v_mov_b32 v152, v144\n"
"v_mfma_f32_32x32x16_bf16 a[96:111], v[120:123], v[100:103], a[96:111]\n"
"ds_read_b64_tr_b16 v[90:91], v12 offset:12800\n"
"v_mov_b32 v153, v145\n"
"v_mov_b32 v154, v146\n"
"v_mov_b32 v155, v147\n"
"v_mov_b32 v156, v148\n"
"v_mfma_f32_32x32x16_bf16 a[112:127], v[124:127], v[100:103], a[112:127]\n"
"ds_read_b64_tr_b16 v[92:93], v13 offset:12288\n"
"v_mov_b32 v157, v149\n"
"v_mov_b32 v158, v150\n"
"v_mov_b32 v159, v151\n"
"v_mfma_f32_32x32x16_bf16 a[128:143], v[112:115], v[104:107], a[128:143]\n"
"ds_read_b64_tr_b16 v[94:95], v13 offset:12800\n"
"s_nop 1\n"
"v_permlane32_swap_b32 v144, v152\n"
"v_permlane32_swap_b32 v145, v153\n"
"v_mfma_f32_32x32x16_bf16 a[144:159], v[116:119], v[104:107], a[144:159]\n"
"ds_read_b128 v[68:71], v10 offset:4096\n"
"v_permlane32_swap_b32 v146, v154\n"
"v_permlane32_swap_b32 v147, v155\n"
"v_permlane32_swap_b32 v148, v156\n"
"v_permlane32_swap_b32 v149, v157\n"
"v_mfma_f32_32x32x16_bf16 a[160:175], v[120:123], v[104:107], a[160:175]\n"
"ds_read_b128 v[72:75], v10 offset:8192\n"
"v_permlane32_swap_b32 v150, v158\n"
"v_permlane32_swap_b32 v151, v159\n"
"v_add_f32 v144, v144, v152\n"
"v_add_f32 v145, v145, v153\n"
"v_mfma_f32_32x32x16_bf16 a[176:191], v[124:127], v[104:107], a[176:191]\n"
"ds_read_b128 v[76:79], v10 offset:12288\n"
"v_add_f32 v146, v146, v154\n"
"v_add_f32 v147, v147, v155\n"
"v_add_f32 v148, v148, v156\n"
"v_add_f32 v149, v149, v157\n"
"v_mfma_f32_32x32x16_bf16 a[192:207], v[112:115], v[108:111], a[192:207]\n"
"v_add_f32 v150, v150, v158\n"
"v_add_f32 v151, v151, v159\n"
"v_and_b32 v176, 3, v0\n"
"v_lshlrev_b32 v176, 5, v176\n"
"s_lshl_b32 s84, s29, 7\n"
"v_mfma_f32_32x32x16_bf16 a[208:223], v[116:119], v[108:111], a[208:223]\n"
"s_add_u32 s84, s84, 151552\n"
"v_add_u32 v176, s84, v176\n"
"v_mfma_f32_32x32x16_bf16 a[224:239], v[120:123], v[108:111], a[224:239]\n"
"ds_write_b128 v176, v[144:147]\n"
"ds_write_b128 v176, v[148:151] offset:16\n"
"v_and_b32 v177, 31, v0\n"
"v_lshlrev_b32 v177, 2, v177\n"
"v_add_u32 v178, 151552, v177\n"
"v_mfma_f32_32x32x16_bf16 a[240:255], v[124:127], v[108:111], a[240:255]\n"
"s_add_u32 s80, s50, 74711040\n"
"s_addc_u32 s81, s51, 0\n"
"s_and_b32 s81, s81, 0xffff\n"
"s_waitcnt lgkmcnt(0)\n"
"v_mfma_f32_32x32x16_bf16 a[0:15], v[80:83], v[64:67], a[0:15]\n"
"ds_read_b128 v[96:99], v11 offset:0\n"
"s_mov_b32 s82, 5504\n"
"s_mov_b32 s83, 0x20000\n"
"s_lshl_b32 s84, s28, 7\n"
"s_lshl_b32 s85, s29, 24\n"
"v_mfma_f32_32x32x16_bf16 a[16:31], v[84:87], v[64:67], a[16:31]\n"
"ds_read_b64_tr_b16 v[112:113], v12 offset:6144\n"
"s_add_u32 s84, s84, s85\n"
"v_mfma_f32_32x32x16_bf16 a[32:47], v[88:91], v[64:67], a[32:47]\n"
"ds_read_b64_tr_b16 v[114:115], v12 offset:6656\n"
"v_mfma_f32_32x32x16_bf16 a[48:63], v[92:95], v[64:67], a[48:63]\n"
"ds_read_b64_tr_b16 v[116:117], v13 offset:6144\n"
"v_mfma_f32_32x32x16_bf16 a[64:79], v[80:83], v[68:71], a[64:79]\n"
"ds_read_b64_tr_b16 v[118:119], v13 offset:6656\n"
"v_mfma_f32_32x32x16_bf16 a[80:95], v[84:87], v[68:71], a[80:95]\n"
"ds_read_b64_tr_b16 v[120:121], v12 offset:14336\n"
"v_mfma_f32_32x32x16_bf16 a[96:111], v[88:91], v[68:71], a[96:111]\n"
"ds_read_b64_tr_b16 v[122:123], v12 offset:14848\n"
"v_mfma_f32_32x32x16_bf16 a[112:127], v[92:95], v[68:71], a[112:127]\n"
"ds_read_b64_tr_b16 v[124:125], v13 offset:14336\n"
"v_mfma_f32_32x32x16_bf16 a[128:143], v[80:83], v[72:75], a[128:143]\n"
"ds_read_b64_tr_b16 v[126:127], v13 offset:14848\n"
"v_mfma_f32_32x32x16_bf16 a[144:159], v[84:87], v[72:75], a[144:159]\n"
"ds_read_b128 v[100:103], v11 offset:4096\n"
"v_mfma_f32_32x32x16_bf16 a[160:175], v[88:91], v[72:75], a[160:175]\n"
"ds_read_b128 v[104:107], v11 offset:8192\n"
"v_mfma_f32_32x32x16_bf16 a[176:191], v[92:95], v[72:75], a[176:191]\n"
"ds_read_b128 v[108:111], v11 offset:12288\n"
"v_mfma_f32_32x32x16_bf16 a[192:207], v[80:83], v[76:79], a[192:207]\n"
"v_xor_b32 v8, 0x10000, v8\n"
"v_xor_b32 v9, 0x10000, v9\n"
"v_mfma_f32_32x32x16_bf16 a[208:223], v[84:87], v[76:79], a[208:223]\n"
"v_xor_b32 v10, 0x10000, v10\n"
"v_xor_b32 v11, 0x10000, v11\n"
"v_mfma_f32_32x32x16_bf16 a[224:239], v[88:91], v[76:79], a[224:239]\n"
"v_xor_b32 v12, 0x10000, v12\n"
"v_xor_b32 v13, 0x10000, v13\n"
"v_mfma_f32_32x32x16_bf16 a[240:255], v[92:95], v[76:79], a[240:255]\n"
"s_add_u32 s53, s53, 128\n"
"s_add_u32 s59, s59, 524288\n"
"s_waitcnt vmcnt(0) lgkmcnt(0)\n"
"s_cmp_lg_u32 s29, 0\n"
"s_cbranch_scc1 .Lwok2\n"
"v_cmp_ne_u32 vcc, s11, v43\n"
"s_nop 1\n"
"s_cbranch_vccz .Lwok2\n"
"s_mov_b32 s49, 0\n"
".Lwait2:\n"
"s_sleep 2\n"
"global_load_dword v43, v42, s[50:51] sc1\n"
"s_waitcnt vmcnt(0)\n"
"v_cmp_ne_u32 vcc, s11, v43\n"
"s_nop 1\n"
"s_cbranch_vccz .Lwok2\n"
"s_add_u32 s49, s49, 1\n"
"s_cmp_lt_u32 s49, 32768\n"
"s_cbranch_scc1 .Lwait2\n"
".Lwok2:\n"
"s_barrier\n"
"s_add_u32 m0, s43, 0\n"
"v_mfma_f32_32x32x16_bf16 a[0:15], v[112:115], v[96:99], a[0:15]\n"
"buffer_load_dwordx4 v36, s[24:27], s39 offen lds\n"
"ds_read_b128 v[64:67], v8 offset:0\n"
"ds_read_b32 v144, v178 offset:0\n"
"ds_read_b32 v145, v178 offset:128\n"
"v_mfma_f32_32x32x16_bf16 a[16:31], v[116:119], v[96:99], a[16:31]\n"
"ds_read_b64_tr_b16 v[80:81], v12 offset:0\n"
"ds_read_b32 v146, v178 offset:256\n"
"ds_read_b32 v147, v178 offset:384\n"
"s_add_u32 m0, s43, 1024\n"
"v_mfma_f32_32x32x16_bf16 a[32:47], v[120:123], v[96:99], a[32:47]\n"
"buffer_load_dwordx4 v37, s[24:27], s39 offen lds\n"
"ds_read_b64_tr_b16 v[82:83], v12 offset:512\n"
"v_mfma_f32_32x32x16_bf16 a[48:63], v[124:127], v[96:99], a[48:63]\n"
"ds_read_b64_tr_b16 v[84:85], v13 offset:0\n"
"s_add_u32 m0, s43, 2048\n"
"v_mfma_f32_32x32x16_bf16 a[64:79], v[112:115], v[100:103], a[64:79]\n"
"buffer_load_dwordx4 v38, s[24:27], s39 offen lds\n"
"ds_read_b64_tr_b16 v[86:87], v13 offset:512\n"
"v_mfma_f32_32x32x16_bf16 a[80:95], v[116:119], v[100:103], a[80:95]\n"
"ds_read_b64_tr_b16 v[88:89], v12 offset:8192\n"
"s_add_u32 m0, s43, 3072\n"
"v_mfma_f32_32x32x16_bf16 a[96:111], v[120:123], v[100:103], a[96:111]\n"
"buffer_load_dwordx4 v39, s[24:27], s39 offen lds\n"
"ds_read_b64_tr_b16 v[90:91], v12 offset:8704\n"
"v_mfma_f32_32x32x16_bf16 a[112:127], v[124:127], v[100:103], a[112:127]\n"
"ds_read_b64_tr_b16 v[92:93], v13 offset:8192\n"
"s_add_u32 s39, s39, 131072\n"
"s_add_u32 m0, s43, 16384\n"
"v_mfma_f32_32x32x16_bf16 a[128:143], v[112:115], v[104:107], a[128:143]\n"
"buffer_load_dwordx4 v36, s[24:27], s39 offen lds\n"
"ds_read_b64_tr_b16 v[94:95], v13 offset:8704\n"
"v_mfma_f32_32x32x16_bf16 a[144:159], v[116:119], v[104:107], a[144:159]\n"
"ds_read_b128 v[68:71], v8 offset:4096\n"
"s_add_u32 m0, s43, 17408\n"
"v_mfma_f32_32x32x16_bf16 a[160:175], v[120:123], v[104:107], a[160:175]\n"
"buffer_load_dwordx4 v37, s[24:27], s39 offen lds\n"
"ds_read_b128 v[72:75], v8 offset:8192\n"
"v_mfma_f32_32x32x16_bf16 a[176:191], v[124:127], v[104:107], a[176:191]\n"
"ds_read_b128 v[76:79], v8 offset:12288\n"
"s_add_u32 m0, s43, 18432\n"
"v_mfma_f32_32x32x16_bf16 a[192:207], v[112:115], v[108:111], a[192:207]\n"
"buffer_load_dwordx4 v38, s[24:27], s39 offen lds\n"
"v_mfma_f32_32x32x16_bf16 a[208:223], v[116:119], v[108:111], a[208:223]\n"
"s_add_u32 m0, s43, 19456\n"
"v_mfma_f32_32x32x16_bf16 a[224:239], v[120:123], v[108:111], a[224:239]\n"
"buffer_load_dwordx4 v39, s[24:27], s39 offen lds\n"
"v_mfma_f32_32x32x16_bf16 a[240:255], v[124:127], v[108:111], a[240:255]\n"
"s_waitcnt lgkmcnt(0)\n"
"s_add_u32 s39, s39, 131072\n"
"s_add_u32 m0, s43, 32768\n"
"v_mfma_f32_32x32x16_bf16 a[0:15], v[80:83], v[64:67], a[0:15]\n"
"buffer_load_dwordx4 v36, s[24:27], s39 offen lds\n"
"ds_read_b128 v[96:99], v9 offset:0\n"
"v_add_f32 v144, v144, v145\n"
"v_add_f32 v146, v146, v147\n"
"v_mfma_f32_32x32x16_bf16 a[16:31], v[84:87], v[64:67], a[16:31]\n"
"ds_read_b64_tr_b16 v[112:113], v12 offset:2048\n"
"v_add_f32 v144, v144, v146\n"
"s_nop 0\n"
"s_add_u32 m0, s43, 33792\n"
"v_mfma_f32_32x32x16_bf16 a[32:47], v[88:91], v[64:67], a[32:47]\n"
"buffer_load_dwordx4 v37, s[24:27], s39 offen lds\n"
"ds_read_b64_tr_b16 v[114:115], v12 offset:2560\n"
"v_mfma_f32_32x32x16_bf16 a[48:63], v[92:95], v[64:67], a[48:63]\n"
"ds_read_b64_tr_b16 v[116:117], v13 offset:2048\n"
"s_add_u32 m0, s43, 34816\n"
"v_mfma_f32_32x32x16_bf16 a[64:79], v[80:83], v[68:71], a[64:79]\n"
"buffer_load_dwordx4 v38, s[24:27], s39 offen lds\n"
"ds_read_b64_tr_b16 v[118:119], v13 offset:2560\n"
"v_mfma_f32_32x32x16_bf16 a[80:95], v[84:87], v[68:71], a[80:95]\n"
"ds_read_b64_tr_b16 v[120:121], v12 offset:10240\n"
"s_add_u32 m0, s43, 35840\n"
"v_mfma_f32_32x32x16_bf16 a[96:111], v[88:91], v[68:71], a[96:111]\n"
"buffer_load_dwordx4 v39, s[24:27], s39 offen lds\n"
"ds_read_b64_tr_b16 v[122:123], v12 offset:10752\n"
"v_mfma_f32_32x32x16_bf16 a[112:127], v[92:95], v[68:71], a[112:127]\n"
"ds_read_b64_tr_b16 v[124:125], v13 offset:10240\n"
"s_add_u32 s39, s39, 131072\n"
"s_add_u32 m0, s43, 49152\n"
"v_mfma_f32_32x32x16_bf16 a[128:143], v[80:83], v[72:75], a[128:143]\n"
"buffer_load_dwordx4 v36, s[24:27], s39 offen lds\n"
"ds_read_b64_tr_b16 v[126:127], v13 offset:10752\n"
"v_mfma_f32_32x32x16_bf16 a[144:159], v[84:87], v[72:75], a[144:159]\n"
"ds_read_b128 v[100:103], v9 offset:4096\n"
"s_add_u32 m0, s43, 50176\n"
"v_mfma_f32_32x32x16_bf16 a[160:175], v[88:91], v[72:75], a[160:175]\n"
"buffer_load_dwordx4 v37, s[24:27], s39 offen lds\n"
"ds_read_b128 v[104:107], v9 offset:8192\n"
"v_mfma_f32_32x32x16_bf16 a[176:191], v[92:95], v[72:75], a[176:191]\n"
"ds_read_b128 v[108:111], v9 offset:12288\n"
"s_add_u32 m0, s43, 51200\n"
"v_mfma_f32_32x32x16_bf16 a[192:207], v[80:83], v[76:79], a[192:207]\n"
"buffer_load_dwordx4 v38, s[24:27], s39 offen lds\n"
"v_mfma_f32_32x32x16_bf16 a[208:223], v[84:87], v[76:79], a[208:223]\n"
"s_add_u32 m0, s43, 52224\n"
"v_mfma_f32_32x32x16_bf16 a[224:239], v[88:91], v[76:79], a[224:239]\n"
"buffer_load_dwordx4 v39, s[24:27], s39 offen lds\n"
"v_mfma_f32_32x32x16_bf16 a[240:255], v[92:95], v[76:79], a[240:255]\n"
"s_waitcnt lgkmcnt(0)\n"
"v_mfma_f32_32x32x16_bf16 a[0:15], v[112:115], v[96:99], a[0:15]\n"
"ds_read_b128 v[64:67], v10 offset:0\n"
"v_mfma_f32_32x32x16_bf16 a[16:31], v[116:119], v[96:99], a[16:31]\n"
"ds_read_b64_tr_b16 v[80:81], v12 offset:4096\n"
"v_mfma_f32_32x32x16_bf16 a[32:47], v[120:123], v[96:99], a[32:47]\n"
"ds_read_b64_tr_b16 v[82:83], v12 offset:4608\n"
"v_mfma_f32_32x32x16_bf16 a[48:63], v[124:127], v[96:99], a[48:63]\n"
"ds_read_b64_tr_b16 v[84:85], v13 offset:4096\n"
"v_mfma_f32_32x32x16_bf16 a[64:79], v[112:115], v[100:103], a[64:79]\n"
"ds_read_b64_tr_b16 v[86:87], v13 offset:4608\n"
"v_mfma_f32_32x32x16_bf16 a[80:95], v[116:119], v[100:103], a[80:95]\n"
"ds_read_b64_tr_b16 v[88:89], v12 offset:12288\n"
"v_mfma_f32_32x32x16_bf16 a[96:111], v[120:123], v[100:103], a[96:111]\n"
"ds_read_b64_tr_b16 v[90:91], v12 offset:12800\n"
"v_mfma_f32_32x32x16_bf16 a[112:127], v[124:127], v[100:103], a[112:127]\n"
"ds_read_b64_tr_b16 v[92:93], v13 offset:12288\n"
"v_mfma_f32_32x32x16_bf16 a[128:143], v[112:115], v[104:107], a[128:143]\n"
"ds_read_b64_tr_b16 v[94:95], v13 offset:12800\n"
"v_mfma_f32_32x32x16_bf16 a[144:159], v[116:119], v[104:107], a[144:159]\n"
"ds_read_b128 v[68:71], v10 offset:4096\n"
"v_mfma_f32_32x32x16_bf16 a[160:175], v[120:123], v[104:107], a[160:175]\n"
"ds_read_b128 v[72:75], v10 offset:8192\n"
"v_mfma_f32_32x32x16_bf16 a[176:191], v[124:127], v[104:107], a[176:191]\n"
"ds_read_b128 v[76:79], v10 offset:12288\n"
"v_mfma_f32_32x32x16_bf16 a[192:207], v[112:115], v[108:111], a[192:207]\n"
"v_mfma_f32_32x32x16_bf16 a[208:223], v[116:119], v[108:111], a[208:223]\n"
"v_mfma_f32_32x32x16_bf16 a[224:239], v[120:123], v[108:111], a[224:239]\n"
"v_mfma_f32_32x32x16_bf16 a[240:255], v[124:127], v[108:111], a[240:255]\n"
"s_waitcnt lgkmcnt(0)\n"
"v_mfma_f32_32x32x16_bf16 a[0:15], v[80:83], v[64:67], a[0:15]\n"
"ds_read_b128 v[96:99], v11 offset:0\n"
"buffer_store_dword v144, v177, s[80:83], s84 offen sc1\n"
"v_mfma_f32_32x32x16_bf16 a[16:31], v[84:87], v[64:67], a[16:31]\n"
"ds_read_b64_tr_b16 v[112:113], v12 offset:6144\n"
"v_mfma_f32_32x32x16_bf16 a[32:47], v[88:91], v[64:67], a[32:47]\n"
"ds_read_b64_tr_b16 v[114:115], v12 offset:6656\n"
"v_mfma_f32_32x32x16_bf16 a[48:63], v[92:95], v[64:67], a[48:63]\n"
"ds_read_b64_tr_b16 v[116:117], v13 offset:6144\n"
"v_mfma_f32_32x32x16_bf16 a[64:79], v[80:83], v[68:71], a[64:79]\n"
"ds_read_b64_tr_b16 v[118:119], v13 offset:6656\n"
"v_mfma_f32_32x32x16_bf16 a[80:95], v[84:87], v[68:71], a[80:95]\n"
"ds_read_b64_tr_b16 v[120:121], v12 offset:14336\n"
"v_mfma_f32_32x32x16_bf16 a[96:111], v[88:91], v[68:71], a[96:111]\n"
"ds_read_b64_tr_b16 v[122:123], v12 offset:14848\n"
"v_mfma_f32_32x32x16_bf16 a[112:127], v[92:95], v[68:71], a[112:127]\n"
"ds_read_b64_tr_b16 v[124:125], v13 offset:14336\n"
"v_mfma_f32_32x32x16_bf16 a[128:143], v[80:83], v[72:75], a[128:143]\n"
"ds_read_b64_tr_b16 v[126:127], v13 offset:14848\n"
"v_mfma_f32_32x32x16_bf16 a[144:159], v[84:87], v[72:75], a[144:159]\n"
"ds_read_b128 v[100:103], v11 offset:4096\n"
"v_mfma_f32_32x32x16_bf16 a[160:175], v[88:91], v[72:75], a[160:175]\n"
"ds_read_b128 v[104:107], v11 offset:8192\n"
"v_mfma_f32_32x32x16_bf16 a[176:191], v[92:95], v[72:75], a[176:191]\n"
"ds_read_b128 v[108:111], v11 offset:12288\n"
"v_mfma_f32_32x32x16_bf16 a[192:207], v[80:83], v[76:79], a[192:207]\n"
"v_mfma_f32_32x32x16_bf16 a[208:223], v[84:87], v[76:79], a[208:223]\n"
"v_mfma_f32_32x32x16_bf16 a[224:239], v[88:91], v[76:79], a[224:239]\n"
"v_mfma_f32_32x32x16_bf16 a[240:255], v[92:95], v[76:79], a[240:255]\n"
"s_waitcnt vmcnt(1) lgkmcnt(0)\n"
"s_barrier\n"
"s_add_u32 m0, s40, 0\n"
"v_mfma_f32_32x32x16_bf16 a[0:15], v[112:115], v[96:99], a[0:15]\n"
"buffer_load_dwordx4 v1, s[12:15], s38 offen offset:0 sc1 lds\n"
"v_mfma_f32_32x32x16_bf16 a[16:31], v[116:119], v[96:99], a[16:31]\n"
"buffer_load_dwordx4 v1, s[12:15], s38 offen offset:1024 sc1 lds\n"
"v_mfma_f32_32x32x16_bf16 a[32:47], v[120:123], v[96:99], a[32:47]\n"
"buffer_load_dwordx4 v1, s[12:15], s38 offen offset:2048 sc1 lds\n"
"v_mfma_f32_32x32x16_bf16 a[48:63], v[124:127], v[96:99], a[48:63]\n"
"buffer_load_dwordx4 v1, s[12:15], s38 offen offset:3072 sc1 lds\n"
"s_add_u32 s38, s38, 8192\n"
"s_add_u32 m0, s40, 8192\n"
"v_mfma_f32_32x32x16_bf16 a[64:79], v[112:115], v[100:103], a[64:79]\n"
"buffer_load_dwordx4 v1, s[12:15], s38 offen offset:0 sc1 lds\n"
"v_mfma_f32_32x32x16_bf16 a[80:95], v[116:119], v[100:103], a[80:95]\n"
"buffer_load_dwordx4 v1, s[12:15], s38 offen offset:1024 sc1 lds\n"
"v_mfma_f32_32x32x16_bf16 a[96:111], v[120:123], v[100:103], a[96:111]\n"
"buffer_load_dwordx4 v1, s[12:15], s38 offen offset:2048 sc1 lds\n"
"v_mfma_f32_32x32x16_bf16 a[112:127], v[124:127], v[100:103], a[112:127]\n"
"buffer_load_dwordx4 v1, s[12:15], s38 offen offset:3072 sc1 lds\n"
"s_add_u32 s38, s38, 8192\n"
"s_add_u32 m0, s40, 16384\n"
"v_mfma_f32_32x32x16_bf16 a[128:143], v[112:115], v[104:107], a[128:143]\n"
"buffer_load_dwordx4 v1, s[12:15], s38 offen offset:0 sc1 lds\n"
"v_mfma_f32_32x32x16_bf16 a[144:159], v[116:119], v[104:107], a[144:159]\n"
"buffer_load_dwordx4 v1, s[12:15], s38 offen offset:1024 sc1 lds\n"
"v_mfma_f32_32x32x16_bf16 a[160:175], v[120:123], v[104:107], a[160:175]\n"
"buffer_load_dwordx4 v1, s[12:15], s38 offen offset:2048 sc1 lds\n"
"v_mfma_f32_32x32x16_bf16 a[176:191], v[124:127], v[104:107], a[176:191]\n"
"buffer_load_dwordx4 v1, s[12:15], s38 offen offset:3072 sc1 lds\n"
"s_add_u32 s38, s38, 8192\n"
"s_add_u32 m0, s40, 24576\n"
"v_mfma_f32_32x32x16_bf16 a[192:207], v[112:115], v[108:111], a[192:207]\n"
"buffer_load_dwordx4 v1, s[12:15], s38 offen offset:0 sc1 lds\n"
"v_mfma_f32_32x32x16_bf16 a[208:223], v[116:119], v[108:111], a[208:223]\n"
"buffer_load_dwordx4 v1, s[12:15], s38 offen offset:1024 sc1 lds\n"
"v_mfma_f32_32x32x16_bf16 a[224:239], v[120:123], v[108:111], a[224:239]\n"
"buffer_load_dwordx4 v1, s[12:15], s38 offen offset:2048 sc1 lds\n"
"v_mfma_f32_32x32x16_bf16 a[240:255], v[124:127], v[108:111], a[240:255]\n"
"buffer_load_dwordx4 v1, s[12:15], s38 offen offset:3072 sc1 lds\n"
"s_branch .Lkv_done\n"
".Lkv_V:\n"
"s_waitcnt vmcnt(9)\n"
"s_barrier\n"
"ds_read_b128 v[64:67], v8 offset:0\n"
"ds_read_b64_tr_b16 v[80:81], v12 offset:0\n"
"ds_read_b64_tr_b16 v[82:83], v12 offset:512\n"
"ds_read_b64_tr_b16 v[84:85], v13 offset:0\n"
"ds_read_b64_tr_b16 v[86:87], v13 offset:512\n"
"ds_read_b64_tr_b16 v[88:89], v12 offset:8192\n"
"ds_read_b64_tr_b16 v[90:91], v12 offset:8704\n"
"ds_read_b64_tr_b16 v[92:93], v13 offset:8192\n"
"ds_read_b64_tr_b16 v[94:95], v13 offset:8704\n"
"ds_read_b128 v[68:71], v8 offset:4096\n"
"ds_read_b128 v[72:75], v8 offset:8192\n"
"ds_read_b128 v[76:79], v8 offset:12288\n"
"s_waitcnt lgkmcnt(0)\n"
"s_add_u32 m0, s64, 65536\n"
"v_mfma_f32_32x32x16_bf16 a[0:15], v[64:67], v[80:83], 0\n"
"buffer_load_dwordx4 v16, s[16:19], s53 offen lds\n"
"ds_read_b128 v[96:99], v9 offset:0\n"
"s_load_dwordx2 s[44:45], s[8:9], 40\n"
"v_lshlrev_b32 v144, 16, v128\n"
"v_mfma_f32_32x32x16_bf16 a[16:31], v[64:67], v[84:87], 0\n"
"ds_read_b64_tr_b16 v[112:113], v12 offset:2048\n"
"v_and_b32 v145, 0xffff0000, v128\n"
"v_lshlrev_b32 v146, 16, v129\n"
"v_and_b32 v147, 0xffff0000, v129\n"
"s_add_u32 m0, s72, 65536\n"
"v_mfma_f32_32x32x16_bf16 a[32:47], v[64:67], v[88:91], 0\n"
"buffer_load_dwordx4 v24, s[20:23], s59 offen lds\n"
"ds_read_b64_tr_b16 v[114:115], v12 offset:2560\n"
"v_lshlrev_b32 v148, 16, v130\n"
"v_and_b32 v149, 0xffff0000, v130\n"
"v_mfma_f32_32x32x16_bf16 a[48:63], v[64:67], v[92:95], 0\n"
"ds_read_b64_tr_b16 v[116:117], v13 offset:2048\n"
"v_lshlrev_b32 v150, 16, v131\n"
"v_and_b32 v151, 0xffff0000, v131\n"
"v_lshlrev_b32 v152, 16, v132\n"
"s_add_u32 m0, s65, 65536\n"
"v_mfma_f32_32x32x16_bf16 a[64:79], v[68:71], v[80:83], 0\n"
"buffer_load_dwordx4 v17, s[16:19], s53 offen lds\n"
"ds_read_b64_tr_b16 v[118:119], v13 offset:2560\n"
"v_and_b32 v153, 0xffff0000, v132\n"
"v_lshlrev_b32 v154, 16, v133\n"
"v_mfma_f32_32x32x16_bf16 a[80:95], v[68:71], v[84:87], 0\n"
"ds_read_b64_tr_b16 v[120:121], v12 offset:10240\n"
"v_and_b32 v155, 0xffff0000, v133\n"
"v_lshlrev_b32 v156, 16, v134\n"
"v_and_b32 v157, 0xffff0000, v134\n"
"s_add_u32 m0, s73, 65536\n"
"v_mfma_f32_32x32x16_bf16 a[96:111], v[68:71], v[88:91], 0\n"
"buffer_load_dwordx4 v25, s[20:23], s59 offen lds\n"
"ds_read_b64_tr_b16 v[122:123], v12 offset:10752\n"
"v_lshlrev_b32 v158, 16, v135\n"
"v_and_b32 v159, 0xffff0000, v135\n"
"v_mfma_f32_32x32x16_bf16 a[112:127], v[68:71], v[92:95], 0\n"
"ds_read_b64_tr_b16 v[124:125], v13 offset:10240\n"
"v_and_b32 v176, 31, v0\n"
"v_mul_u32_u24 v176, 144, v176\n"
"v_lshrrev_b32 v177, 5, v0\n"
"s_add_u32 m0, s66, 65536\n"
"v_mfma_f32_32x32x16_bf16 a[128:143], v[72:75], v[80:83], 0\n"
"buffer_load_dwordx4 v18, s[16:19], s53 offen lds\n"
"ds_read_b64_tr_b16 v[126:127], v13 offset:10752\n"
"v_lshl_add_u32 v176, v177, 4, v176\n"
"s_lshl_b32 s84, s29, 5\n"
"v_mfma_f32_32x32x16_bf16 a[144:159], v[72:75], v[84:87], 0\n"
"ds_read_b128 v[100:103], v9 offset:4096\n"
"s_add_u32 s84, s84, 131072\n"
"v_add_u32 v176, s84, v176\n"
"v_add_f32 v160, v144, v145\n"
"s_add_u32 m0, s74, 65536\n"
"v_mfma_f32_32x32x16_bf16 a[160:175], v[72:75], v[88:91], 0\n"
"buffer_load_dwordx4 v26, s[20:23], s59 offen lds\n"
"ds_read_b128 v[104:107], v9 offset:8192\n"
"v_add_f32 v160, v160, v146\n"
"v_add_f32 v160, v160, v147\n"
"v_mfma_f32_32x32x16_bf16 a[176:191], v[72:75], v[92:95], 0\n"
"ds_read_b128 v[108:111], v9 offset:12288\n"
"v_add_f32 v160, v160, v148\n"
"v_add_f32 v160, v160, v149\n"
"v_add_f32 v160, v160, v150\n"
"s_add_u32 m0, s67, 65536\n"
"v_mfma_f32_32x32x16_bf16 a[192:207], v[76:79], v[80:83], 0\n"
"buffer_load_dwordx4 v19, s[16:19], s53 offen lds\n"
"v_add_f32 v160, v160, v151\n"
"v_add_f32 v160, v160, v152\n"
"v_add_f32 v160, v160, v153\n"
"v_mfma_f32_32x32x16_bf16 a[208:223], v[76:79], v[84:87], 0\n"
"v_add_f32 v160, v160, v154\n"
"v_add_f32 v160, v160, v155\n"
"v_add_f32 v160, v160, v156\n"
"v_add_f32 v160, v160, v157\n"
"s_add_u32 m0, s75, 65536\n"
"v_mfma_f32_32x32x16_bf16 a[224:239], v[76:79], v[88:91], 0\n"
"buffer_load_dwordx4 v27, s[20:23], s59 offen lds\n"
"v_add_f32 v160, v160, v158\n"
"v_add_f32 v160, v160, v159\n"
"v_mfma_f32_32x32x16_bf16 a[240:255], v[76:79], v[92:95], 0\n"
"s_nop 1\n"
"v_add_f32_dpp v160, v160, v160 quad_perm:[1,0,3,2] row_mask:0xf bank_mask:0xf\n"
"s_waitcnt lgkmcnt(0)\n"
"s_add_u32 m0, s68, 65536\n"
"v_mfma_f32_32x32x16_bf16 a[0:15], v[96:99], v[112:115], a[0:15]\n"
"buffer_load_dwordx4 v20, s[16:19], s53 offen lds\n"
"ds_read_b128 v[64:67], v10 offset:0\n"
"s_nop 1\n"
"v_mfma_f32_32x32x16_bf16 a[16:31], v[96:99], v[116:119], a[16:31]\n"
"ds_read_b64_tr_b16 v[80:81], v12 offset:4096\n"
"v_add_f32_dpp v160, v160, v160 quad_perm:[2,3,0,1] row_mask:0xf bank_mask:0xf\n"
"s_nop 1\n"
"s_add_u32 m0, s76, 65536\n"
"v_mfma_f32_32x32x16_bf16 a[32:47], v[96:99], v[120:123], a[32:47]\n"
"buffer_load_dwordx4 v28, s[20:23], s59 offen lds\n"
"ds_read_b64_tr_b16 v[82:83], v12 offset:4608\n"
"v_add_f32_dpp v160, v160, v160 row_half_mirror row_mask:0xf bank_mask:0xf\n"
"v_mfma_f32_32x32x16_bf16 a[48:63], v[96:99], v[124:127], a[48:63]\n"
"ds_read_b64_tr_b16 v[84:85], v13 offset:4096\n"
"s_nop 1\n"
"v_add_f32_dpp v160, v160, v160 row_mirror row_mask:0xf bank_mask:0xf\n"
"s_add_u32 m0, s69, 65536\n"
"v_mfma_f32_32x32x16_bf16 a[64:79], v[100:103], v[112:115], a[64:79]\n"
"buffer_load_dwordx4 v21, s[16:19], s53 offen lds\n"
"ds_read_b64_tr_b16 v[86:87], v13 offset:4608\n"
"v_mov_b32 v163, v160\n"
"v_mfma_f32_32x32x16_bf16 a[80:95], v[100:103], v[116:119], a[80:95]\n"
"ds_read_b64_tr_b16 v[88:89], v12 offset:12288\n"
"s_nop 1\n"
"v_permlane16_swap_b32 v160, v163\n"
"s_add_u32 m0, s77, 65536\n"
"v_mfma_f32_32x32x16_bf16 a[96:111], v[100:103], v[120:123], a[96:111]\n"
"buffer_load_dwordx4 v29, s[20:23], s59 offen lds\n"
"ds_read_b64_tr_b16 v[90:91], v12 offset:12800\n"
"v_add_f32 v160, v160, v163\n"
"v_mul_f32 v160, 0x3b000000, v160\n"
"v_mfma_f32_32x32x16_bf16 a[112:127], v[100:103], v[124:127], a[112:127]\n"
"ds_read_b64_tr_b16 v[92:93], v13 offset:12288\n"
"v_mov_b32 v162, v160\n"
"v_cvt_pk_bf16_f32 v163, v162, v162\n"
"v_lshlrev_b32 v162, 16, v163\n"
"s_add_u32 m0, s70, 65536\n"
"v_mfma_f32_32x32x16_bf16 a[128:143], v[104:107], v[112:115], a[128:143]\n"
"buffer_load_dwordx4 v22, s[16:19], s53 offen lds\n"
"ds_read_b64_tr_b16 v[94:95], v13 offset:12800\n"
"v_sub_f32 v164, v144, v160\n"
"v_mul_f32 v161, v164, v164\n"
"v_mfma_f32_32x32x16_bf16 a[144:159], v[104:107], v[116:119], a[144:159]\n"
"ds_read_b128 v[68:71], v10 offset:4096\n"
"v_sub_f32 v165, v145, v160\n"
"v_fmac_f32 v161, v165, v165\n"
"v_sub_f32 v164, v146, v160\n"
"s_add_u32 m0, s78, 65536\n"
"v_mfma_f32_32x32x16_bf16 a[160:175], v[104:107], v[120:123], a[160:175]\n"
"buffer_load_dwordx4 v30, s[20:23], s59 offen lds\n"
"ds_read_b128 v[72:75], v10 offset:8192\n"
"v_fmac_f32 v161, v164, v164\n"
"v_sub_f32 v165, v147, v160\n"
"v_mfma_f32_32x32x16_bf16 a[176:191], v[104:107], v[124:127], a[176:191]\n"
"ds_read_b128 v[76:79], v10 offset:12288\n"
"v_fmac_f32 v161, v165, v165\n"
"v_sub_f32 v164, v148, v160\n"
"v_fmac_f32 v161, v164, v164\n"
"s_add_u32 m0, s71, 65536\n"
"v_mfma_f32_32x32x16_bf16 a[192:207], v[108:111], v[112:115], a[192:207]\n"
"buffer_load_dwordx4 v23, s[16:19], s53 offen lds\n"
"v_sub_f32 v165, v149, v160\n"
"v_fmac_f32 v161, v165, v165\n"
"v_sub_f32 v164, v150, v160\n"
"v_mfma_f32_32x32x16_bf16 a[208:223], v[108:111], v[116:119], a[208:223]\n"
"v_fmac_f32 v161, v164, v164\n"
"v_sub_f32 v165, v151, v160\n"
"v_fmac_f32 v161, v165, v165\n"
"v_sub_f32 v164, v152, v160\n"
"s_add_u32 m0, s79, 65536\n"
"v_mfma_f32_32x32x16_bf16 a[224:239], v[108:111], v[120:123], a[224:239]\n"
"buffer_load_dwordx4 v31, s[20:23], s59 offen lds\n"
"v_fmac_f32 v161, v164, v164\n"
"v_sub_f32 v165, v153, v160\n"
"v_fmac_f32 v161, v165, v165\n"
"v_mfma_f32_32x32x16_bf16 a[240:255], v[108:111], v[124:127], a[240:255]\n"
"v_sub_f32 v164, v154, v160\n"
"v_fmac_f32 v161, v164, v164\n"
"v_sub_f32 v165, v155, v160\n"
"s_waitcnt lgkmcnt(0)\n"
"v_mfma_f32_32x32x16_bf16 a[0:15], v[64:67], v[80:83], a[0:15]\n"
"ds_read_b128 v[96:99], v11 offset:0\n"
"v_fmac_f32 v161, v165, v165\n"
"v_sub_f32 v164, v156, v160\n"
"v_fmac_f32 v161, v164, v164\n"
"v_sub_f32 v165, v157, v160\n"
"v_mfma_f32_32x32x16_bf16 a[16:31], v[64:67], v[84:87], a[16:31]\n"
"ds_read_b64_tr_b16 v[112:113], v12 offset:6144\n"
"v_fmac_f32 v161, v165, v165\n"
"v_sub_f32 v164, v158, v160\n"
"v_fmac_f32 v161, v164, v164\n"
"s_add_u32 s37, s37, s60\n"
"v_mfma_f32_32x32x16_bf16 a[32:47], v[64:67], v[88:91], a[32:47]\n"
"buffer_load_dword v225, v222, s[24:27], s37 offen\n"
"ds_read_b64_tr_b16 v[114:115], v12 offset:6656\n"
"v_sub_f32 v165, v159, v160\n"
"v_fmac_f32 v161, v165, v165\n"
"v_mfma_f32_32x32x16_bf16 a[48:63], v[64:67], v[92:95], a[48:63]\n"
"ds_read_b64_tr_b16 v[116:117], v13 offset:6144\n"
"s_nop 1\n"
"v_add_f32_dpp v161, v161, v161 quad_perm:[1,0,3,2] row_mask:0xf bank_mask:0xf\n"
"v_mfma_f32_32x32x16_bf16 a[64:79], v[68:71], v[80:83], a[64:79]\n"
"ds_read_b64_tr_b16 v[118:119], v13 offset:6656\n"
"s_nop 1\n"
"v_add_f32_dpp v161, v161, v161 quad_perm:[2,3,0,1] row_mask:0xf bank_mask:0xf\n"
"v_mfma_f32_32x32x16_bf16 a[80:95], v[68:71], v[84:87], a[80:95]\n"
"ds_read_b64_tr_b16 v[120:121], v12 offset:14336\n"
"s_nop 1\n"
"v_add_f32_dpp v161, v161, v161 row_half_mirror row_mask:0xf bank_mask:0xf\n"
"v_mfma_f32_32x32x16_bf16 a[96:111], v[68:71], v[88:91], a[96:111]\n"
"ds_read_b64_tr_b16 v[122:123], v12 offset:14848\n"
"s_nop 1\n"
"v_add_f32_dpp v161, v161, v161 row_mirror row_mask:0xf bank_mask:0xf\n"
"v_mov_b32 v163, v161\n"
"v_mfma_f32_32x32x16_bf16 a[112:127], v[68:71], v[92:95], a[112:127]\n"
"ds_read_b64_tr_b16 v[124:125], v13 offset:14336\n"
"s_nop 1\n"
"v_permlane16_swap_b32 v161, v163\n"
"v_add_f32 v161, v161, v163\n"
"v_mfma_f32_32x32x16_bf16 a[128:143], v[72:75], v[80:83], a[128:143]\n"
"ds_read_b64_tr_b16 v[126:127], v13 offset:14848\n"
"v_mul_f32 v161, 0x3b000000, v161\n"
"v_cvt_pk_bf16_f32 v163, v161, v161\n"
"v_lshlrev_b32 v161, 16, v163\n"
"v_add_f32 v161, 0x3727c5ac, v161\n"
"v_mfma_f32_32x32x16_bf16 a[144:159], v[72:75], v[84:87], a[144:159]\n"
"ds_read_b128 v[100:103], v11 offset:4096\n"
"v_cvt_pk_bf16_f32 v163, v161, v161\n"
"v_lshlrev_b32 v161, 16, v163\n"
"v_sqrt_f32 v161, v161\n"
"v_mfma_f32_32x32x16_bf16 a[160:175], v[72:75], v[88:91], a[160:175]\n"
"ds_read_b128 v[104:107], v11 offset:8192\n"
"s_nop 0\n"
"v_cvt_pk_bf16_f32 v163, v161, v161\n"
"v_lshlrev_b32 v161, 16, v163\n"
"v_mfma_f32_32x32x16_bf16 a[176:191], v[72:75], v[92:95], a[176:191]\n"
"ds_read_b128 v[108:111], v11 offset:12288\n"
"v_rcp_f32 v161, v161\n"
"v_sub_f32 v144, v144, v162\n"
"v_sub_f32 v145, v145, v162\n"
"v_mfma_f32_32x32x16_bf16 a[192:207], v[76:79], v[80:83], a[192:207]\n"
"v_xor_b32 v8, 0x10000, v8\n"
"v_xor_b32 v9, 0x10000, v9\n"
"v_sub_f32 v146, v146, v162\n"
"v_sub_f32 v147, v147, v162\n"
"v_sub_f32 v148, v148, v162\n"
"v_mfma_f32_32x32x16_bf16 a[208:223], v[76:79], v[84:87], a[208:223]\n"
"v_xor_b32 v10, 0x10000, v10\n"
"v_xor_b32 v11, 0x10000, v11\n"
"v_sub_f32 v149, v149, v162\n"
"v_sub_f32 v150, v150, v162\n"
"v_sub_f32 v151, v151, v162\n"
"v_mfma_f32_32x32x16_bf16 a[224:239], v[76:79], v[88:91], a[224:239]\n"
"v_xor_b32 v12, 0x10000, v12\n"
"v_xor_b32 v13, 0x10000, v13\n"
"v_sub_f32 v152, v152, v162\n"
"v_sub_f32 v153, v153, v162\n"
"v_sub_f32 v154, v154, v162\n"
"v_mfma_f32_32x32x16_bf16 a[240:255], v[76:79], v[92:95], a[240:255]\n"
"v_sub_f32 v155, v155, v162\n"
"v_sub_f32 v156, v156, v162\n"
"v_sub_f32 v157, v157, v162\n"
"s_add_u32 s53, s53, 128\n"
"s_add_u32 s59, s59, 524288\n"
"s_waitcnt vmcnt(1) lgkmcnt(0)\n"
"s_barrier\n"
"s_add_u32 m0, s64, 0\n"
"v_mfma_f32_32x32x16_bf16 a[0:15], v[96:99], v[112:115], a[0:15]\n"
"buffer_load_dwordx4 v16, s[16:19], s53 offen lds\n"
"ds_read_b128 v[64:67], v8 offset:0\n"
"v_sub_f32 v158, v158, v162\n"
"v_sub_f32 v159, v159, v162\n"
"v_mfma_f32_32x32x16_bf16 a[16:31], v[96:99], v[116:119], a[16:31]\n"
"ds_read_b64_tr_b16 v[80:81], v12 offset:0\n"
"v_cvt_pk_bf16_f32 v164, v144, v145\n"
"v_cvt_pk_bf16_f32 v165, v146, v147\n"
"v_cvt_pk_bf16_f32 v166, v148, v149\n"
"s_add_u32 m0, s72, 0\n"
"v_mfma_f32_32x32x16_bf16 a[32:47], v[96:99], v[120:123], a[32:47]\n"
"buffer_load_dwordx4 v24, s[20:23], s59 offen lds\n"
"ds_read_b64_tr_b16 v[82:83], v12 offset:512\n"
"v_cvt_pk_bf16_f32 v167, v150, v151\n"
"v_cvt_pk_bf16_f32 v168, v152, v153\n"
"v_mfma_f32_32x32x16_bf16 a[48:63], v[96:99], v[124:127], a[48:63]\n"
"ds_read_b64_tr_b16 v[84:85], v13 offset:0\n"
"v_cvt_pk_bf16_f32 v169, v154, v155\n"
"v_cvt_pk_bf16_f32 v170, v156, v157\n"
"v_cvt_pk_bf16_f32 v171, v158, v159\n"
"s_add_u32 m0, s65, 0\n"
"v_mfma_f32_32x32x16_bf16 a[64:79], v[100:103], v[112:115], a[64:79]\n"
"buffer_load_dwordx4 v17, s[16:19], s53 offen lds\n"
"ds_read_b64_tr_b16 v[86:87], v13 offset:512\n"
"v_lshlrev_b32 v144, 16, v164\n"
"v_and_b32 v145, 0xffff0000, v164\n"
"v_mfma_f32_32x32x16_bf16 a[80:95], v[100:103], v[116:119], a[80:95]\n"
"ds_read_b64_tr_b16 v[88:89], v12 offset:8192\n"
"v_lshlrev_b32 v146, 16, v165\n"
"v_and_b32 v147, 0xffff0000, v165\n"
"v_lshlrev_b32 v148, 16, v166\n"
"s_add_u32 m0, s73, 0\n"
"v_mfma_f32_32x32x16_bf16 a[96:111], v[100:103], v[120:123], a[96:111]\n"
"buffer_load_dwordx4 v25, s[20:23], s59 offen lds\n"
"ds_read_b64_tr_b16 v[90:91], v12 offset:8704\n"
"v_and_b32 v149, 0xffff0000, v166\n"
"v_lshlrev_b32 v150, 16, v167\n"
"v_mfma_f32_32x32x16_bf16 a[112:127], v[100:103], v[124:127], a[112:127]\n"
"ds_read_b64_tr_b16 v[92:93], v13 offset:8192\n"
"v_and_b32 v151, 0xffff0000, v167\n"
"v_lshlrev_b32 v152, 16, v168\n"
"v_and_b32 v153, 0xffff0000, v168\n"
"s_add_u32 m0, s66, 0\n"
"v_mfma_f32_32x32x16_bf16 a[128:143], v[104:107], v[112:115], a[128:143]\n"
"buffer_load_dwordx4 v18, s[16:19], s53 offen lds\n"
"ds_read_b64_tr_b16 v[94:95], v13 offset:8704\n"
"v_lshlrev_b32 v154, 16, v169\n"
"v_and_b32 v155, 0xffff0000, v169\n"
"v_mfma_f32_32x32x16_bf16 a[144:159], v[104:107], v[116:119], a[144:159]\n"
"ds_read_b128 v[68:71], v8 offset:4096\n"
"v_lshlrev_b32 v156, 16, v170\n"
"v_and_b32 v157, 0xffff0000, v170\n"
"v_lshlrev_b32 v158, 16, v171\n"
"s_add_u32 m0, s74, 0\n"
"v_mfma_f32_32x32x16_bf16 a[160:175], v[104:107], v[120:123], a[160:175]\n"
"buffer_load_dwordx4 v26, s[20:23], s59 offen lds\n"
"ds_read_b128 v[72:75], v8 offset:8192\n"
"v_and_b32 v159, 0xffff0000, v171\n"
"v_mul_f32 v144, v144, v161\n"
"v_mfma_f32_32x32x16_bf16 a[176:191], v[104:107], v[124:127], a[176:191]\n"
"ds_read_b128 v[76:79], v8 offset:12288\n"
"v_mul_f32 v145, v145, v161\n"
"v_mul_f32 v146, v146, v161\n"
"v_mul_f32 v147, v147, v161\n"
"s_add_u32 m0, s67, 0\n"
"v_mfma_f32_32x32x16_bf16 a[192:207], v[108:111], v[112:115], a[192:207]\n"
"buffer_load_dwordx4 v19, s[16:19], s53 offen lds\n"
"v_mul_f32 v148, v148, v161\n"
"v_mul_f32 v149, v149, v161\n"
"v_mul_f32 v150, v150, v161\n"
"v_mfma_f32_32x32x16_bf16 a[208:223], v[108:111], v[116:119], a[208:223]\n"
"v_mul_f32 v151, v151, v161\n"
"v_mul_f32 v152, v152, v161\n"
"v_mul_f32 v153, v153, v161\n"
"v_mul_f32 v154, v154, v161\n"
"s_add_u32 m0, s75, 0\n"
"v_mfma_f32_32x32x16_bf16 a[224:239], v[108:111], v[120:123], a[224:239]\n"
"buffer_load_dwordx4 v27, s[20:23], s59 offen lds\n"
"v_mul_f32 v155, v155, v161\n"
"v_mul_f32 v156, v156, v161\n"
"v_mul_f32 v157, v157, v161\n"
"v_mfma_f32_32x32x16_bf16 a[240:255], v[108:111], v[124:127], a[240:255]\n"
"v_mul_f32 v158, v158, v161\n"
"v_mul_f32 v159, v159, v161\n"
"v_cvt_pk_bf16_f32 v164, v144, v145\n"
"s_waitcnt lgkmcnt(0)\n"
"s_add_u32 m0, s68, 0\n"
"v_mfma_f32_32x32x16_bf16 a[0:15], v[64:67], v[80:83], a[0:15]\n"
"buffer_load_dwordx4 v20, s[16:19], s53 offen lds\n"
"ds_read_b128 v[96:99], v9 offset:0\n"
"v_cvt_pk_bf16_f32 v165, v146, v147\n"
"v_cvt_pk_bf16_f32 v166, v148, v149\n"
"v_mfma_f32_32x32x16_bf16 a[16:31], v[64:67], v[84:87], a[16:31]\n"
"ds_read_b64_tr_b16 v[112:113], v12 offset:2048\n"
"v_cvt_pk_bf16_f32 v167, v150, v151\n"
"v_cvt_pk_bf16_f32 v168, v152, v153\n"
"v_cvt_pk_bf16_f32 v169, v154, v155\n"
"s_add_u32 m0, s76, 0\n"
"v_mfma_f32_32x32x16_bf16 a[32:47], v[64:67], v[88:91], a[32:47]\n"
"buffer_load_dwordx4 v28, s[20:23], s59 offen lds\n"
"ds_read_b64_tr_b16 v[114:115], v12 offset:2560\n"
"v_cvt_pk_bf16_f32 v170, v156, v157\n"
"v_cvt_pk_bf16_f32 v171, v158, v159\n"
"v_mfma_f32_32x32x16_bf16 a[48:63], v[64:67], v[92:95], a[48:63]\n"
"ds_read_b64_tr_b16 v[116:117], v13 offset:2048\n"
"ds_write_b128 v176, v[164:167] offset:0\n"
"ds_write_b128 v176, v[168:171] offset:4608\n"
"v_lshlrev_b32 v144, 16, v164\n"
"s_add_u32 m0, s69, 0\n"
"v_mfma_f32_32x32x16_bf16 a[64:79], v[68:71], v[80:83], a[64:79]\n"
"buffer_load_dwordx4 v21, s[16:19], s53 offen lds\n"
"ds_read_b64_tr_b16 v[118:119], v13 offset:2560\n"
"v_and_b32 v145, 0xffff0000, v164\n"
"v_lshlrev_b32 v146, 16, v165\n"
"v_mfma_f32_32x32x16_bf16 a[80:95], v[68:71], v[84:87], a[80:95]\n"
"ds_read_b64_tr_b16 v[120:121], v12 offset:10240\n"
"v_and_b32 v147, 0xffff0000, v165\n"
"v_lshlrev_b32 v148, 16, v166\n"
"v_and_b32 v149, 0xffff0000, v166\n"
"s_add_u32 m0, s77, 0\n"
"v_mfma_f32_32x32x16_bf16 a[96:111], v[68:71], v[88:91], a[96:111]\n"
"buffer_load_dwordx4 v29, s[20:23], s59 offen lds\n"
"ds_read_b64_tr_b16 v[122:123], v12 offset:10752\n"
"v_lshlrev_b32 v150, 16, v167\n"
"v_and_b32 v151, 0xffff0000, v167\n"
"v_mfma_f32_32x32x16_bf16 a[112:127], v[68:71], v[92:95], a[112:127]\n"
"ds_read_b64_tr_b16 v[124:125], v13 offset:10240\n"
"v_lshlrev_b32 v152, 16, v168\n"
"v_and_b32 v153, 0xffff0000, v168\n"
"v_lshlrev_b32 v154, 16, v169\n"
"s_add_u32 m0, s70, 0\n"
"v_mfma_f32_32x32x16_bf16 a[128:143], v[72:75], v[80:83], a[128:143]\n"
"buffer_load_dwordx4 v22, s[16:19], s53 offen lds\n"
"ds_read_b64_tr_b16 v[126:127], v13 offset:10752\n"
"v_and_b32 v155, 0xffff0000, v169\n"
"v_lshlrev_b32 v156, 16, v170\n"
"v_mfma_f32_32x32x16_bf16 a[144:159], v[72:75], v[84:87], a[144:159]\n"
"ds_read_b128 v[100:103], v9 offset:4096\n"
"v_and_b32 v157, 0xffff0000, v170\n"
"v_lshlrev_b32 v158, 16, v171\n"
"v_and_b32 v159, 0xffff0000, v171\n"
"s_add_u32 m0, s78, 0\n"
"v_mfma_f32_32x32x16_bf16 a[160:175], v[72:75], v[88:91], a[160:175]\n"
"buffer_load_dwordx4 v30, s[20:23], s59 offen lds\n"
"ds_read_b128 v[104:107], v9 offset:8192\n"
"v_add_f32 v160, v144, v145\n"
"v_add_f32 v160, v160, v146\n"
"v_mfma_f32_32x32x16_bf16 a[176:191], v[72:75], v[92:95], a[176:191]\n"
"ds_read_b128 v[108:111], v9 offset:12288\n"
"v_add_f32 v160, v160, v147\n"
"v_add_f32 v160, v160, v148\n"
"v_add_f32 v160, v160, v149\n"
"s_add_u32 m0, s71, 0\n"
"v_mfma_f32_32x32x16_bf16 a[192:207], v[76:79], v[80:83], a[192:207]\n"
"buffer_load_dwordx4 v23, s[16:19], s53 offen lds\n"
"v_add_f32 v160, v160, v150\n"
"v_add_f32 v160, v160, v151\n"
"v_add_f32 v160, v160, v152\n"
"v_mfma_f32_32x32x16_bf16 a[208:223], v[76:79], v[84:87], a[208:223]\n"
"v_add_f32 v160, v160, v153\n"
"v_add_f32 v160, v160, v154\n"
"v_add_f32 v160, v160, v155\n"
"v_add_f32 v160, v160, v156\n"
"s_add_u32 m0, s79, 0\n"
"v_mfma_f32_32x32x16_bf16 a[224:239], v[76:79], v[88:91], a[224:239]\n"
"buffer_load_dwordx4 v31, s[20:23], s59 offen lds\n"
"v_add_f32 v160, v160, v157\n"
"v_add_f32 v160, v160, v158\n"
"v_add_f32 v160, v160, v159\n"
"v_mfma_f32_32x32x16_bf16 a[240:255], v[76:79], v[92:95], a[240:255]\n"
"s_nop 1\n"
"v_add_f32_dpp v160, v160, v160 quad_perm:[1,0,3,2] row_mask:0xf bank_mask:0xf\n"
"s_waitcnt lgkmcnt(0)\n"
"v_mfma_f32_32x32x16_bf16 a[0:15], v[96:99], v[112:115], a[0:15]\n"
"ds_read_b128 v[64:67], v10 offset:0\n"
"s_nop 1\n"
"v_add_f32_dpp v160, v160, v160 quad_perm:[2,3,0,1] row_mask:0xf bank_mask:0xf\n"
"v_mfma_f32_32x32x16_bf16 a[16:31], v[96:99], v[116:119], a[16:31]\n"
"ds_read_b64_tr_b16 v[80:81], v12 offset:4096\n"
"s_nop 1\n"
"v_add_f32_dpp v160, v160, v160 row_half_mirror row_mask:0xf bank_mask:0xf\n"
"v_mfma_f32_32x32x16_bf16 a[32:47], v[96:99], v[120:123], a[32:47]\n"
"ds_read_b64_tr_b16 v[82:83], v12 offset:4608\n"
"s_nop 1\n"
"v_add_f32_dpp v160, v160, v160 row_mirror row_mask:0xf bank_mask:0xf\n"
"v_mov_b32 v163, v160\n"
"v_mfma_f32_32x32x16_bf16 a[48:63], v[96:99], v[124:127], a[48:63]\n"
"ds_read_b64_tr_b16 v[84:85], v13 offset:4096\n"
"s_nop 1\n"
"v_permlane16_swap_b32 v160, v163\n"
"v_add_f32 v160, v160, v163\n"
"v_mfma_f32_32x32x16_bf16 a[64:79], v[100:103], v[112:115], a[64:79]\n"
"ds_read_b64_tr_b16 v[86:87], v13 offset:4608\n"
"v_mul_f32 v160, 0x3b000000, v160\n"
"v_mov_b32 v162, v160\n"
"v_cvt_pk_bf16_f32 v163, v162, v162\n"
"v_lshlrev_b32 v162, 16, v163\n"
"v_mfma_f32_32x32x16_bf16 a[80:95], v[100:103], v[116:119], a[80:95]\n"
"ds_read_b64_tr_b16 v[88:89], v12 offset:12288\n"
"v_sub_f32 v164, v144, v160\n"
"v_mul_f32 v161, v164, v164\n"
"v_sub_f32 v165, v145, v160\n"
"v_fmac_f32 v161, v165, v165\n"
"v_mfma_f32_32x32x16_bf16 a[96:111], v[100:103], v[120:123], a[96:111]\n"
"ds_read_b64_tr_b16 v[90:91], v12 offset:12800\n"
"v_sub_f32 v164, v146, v160\n"
"v_fmac_f32 v161, v164, v164\n"
"v_sub_f32 v165, v147, v160\n"
"v_fmac_f32 v161, v165, v165\n"
"v_mfma_f32_32x32x16_bf16 a[112:127], v[100:103], v[124:127], a[112:127]\n"
"ds_read_b64_tr_b16 v[92:93], v13 offset:12288\n"
"v_sub_f32 v164, v148, v160\n"
"v_fmac_f32 v161, v164, v164\n"
"v_sub_f32 v165, v149, v160\n"
"v_fmac_f32 v161, v165, v165\n"
"v_mfma_f32_32x32x16_bf16 a[128:143], v[104:107], v[112:115], a[128:143]\n"
"ds_read_b64_tr_b16 v[94:95], v13 offset:12800\n"
"v_sub_f32 v164, v150, v160\n"
"v_fmac_f32 v161, v164, v164\n"
"v_sub_f32 v165, v151, v160\n"
"v_fmac_f32 v161, v165, v165\n"
"v_mfma_f32_32x32x16_bf16 a[144:159], v[104:107], v[116:119], a[144:159]\n"
"ds_read_b128 v[68:71], v10 offset:4096\n"
"v_sub_f32 v164, v152, v160\n"
"v_fmac_f32 v161, v164, v164\n"
"v_sub_f32 v165, v153, v160\n"
"v_fmac_f32 v161, v165, v165\n"
"v_mfma_f32_32x32x16_bf16 a[160:175], v[104:107], v[120:123], a[160:175]\n"
"ds_read_b128 v[72:75], v10 offset:8192\n"
"v_sub_f32 v164, v154, v160\n"
"v_fmac_f32 v161, v164, v164\n"
"v_sub_f32 v165, v155, v160\n"
"v_fmac_f32 v161, v165, v165\n"
"v_mfma_f32_32x32x16_bf16 a[176:191], v[104:107], v[124:127], a[176:191]\n"
"ds_read_b128 v[76:79], v10 offset:12288\n"
"v_sub_f32 v164, v156, v160\n"
"v_fmac_f32 v161, v164, v164\n"
"v_sub_f32 v165, v157, v160\n"
"v_fmac_f32 v161, v165, v165\n"
"v_mfma_f32_32x32x16_bf16 a[192:207], v[108:111], v[112:115], a[192:207]\n"
"v_sub_f32 v164, v158, v160\n"
"v_fmac_f32 v161, v164, v164\n"
"v_sub_f32 v165, v159, v160\n"
"v_fmac_f32 v161, v165, v165\n"
"v_mfma_f32_32x32x16_bf16 a[208:223], v[108:111], v[116:119], a[208:223]\n"
"s_nop 1\n"
"v_add_f32_dpp v161, v161, v161 quad_perm:[1,0,3,2] row_mask:0xf bank_mask:0xf\n"
"s_nop 1\n"
"v_mfma_f32_32x32x16_bf16 a[224:239], v[108:111], v[120:123], a[224:239]\n"
"v_add_f32_dpp v161, v161, v161 quad_perm:[2,3,0,1] row_mask:0xf bank_mask:0xf\n"
"s_nop 1\n"
"v_add_f32_dpp v161, v161, v161 row_half_mirror row_mask:0xf bank_mask:0xf\n"
"v_mfma_f32_32x32x16_bf16 a[240:255], v[108:111], v[124:127], a[240:255]\n"
"s_nop 1\n"
"v_add_f32_dpp v161, v161, v161 row_mirror row_mask:0xf bank_mask:0xf\n"
"s_waitcnt lgkmcnt(0)\n"
"v_mfma_f32_32x32x16_bf16 a[0:15], v[64:67], v[80:83], a[0:15]\n"
"ds_read_b128 v[96:99], v11 offset:0\n"
"v_mov_b32 v163, v161\n"
"s_nop 1\n"
"v_permlane16_swap_b32 v161, v163\n"
"v_mfma_f32_32x32x16_bf16 a[16:31], v[64:67], v[84:87], a[16:31]\n"
"ds_read_b64_tr_b16 v[112:113], v12 offset:6144\n"
"v_add_f32 v161, v161, v163\n"
"v_mul_f32 v161, 0x3b000000, v161\n"
"v_cvt_pk_bf16_f32 v163, v161, v161\n"
"s_add_u32 s37, s37, s60\n"
"v_mfma_f32_32x32x16_bf16 a[32:47], v[64:67], v[88:91], a[32:47]\n"
"buffer_load_dword v225, v222, s[24:27], s37 offen\n"
"ds_read_b64_tr_b16 v[114:115], v12 offset:6656\n"
"v_lshlrev_b32 v161, 16, v163\n"
"v_add_f32 v161, 0x3727c5ac, v161\n"
"v_mfma_f32_32x32x16_bf16 a[48:63], v[64:67], v[92:95], a[48:63]\n"
"ds_read_b64_tr_b16 v[116:117], v13 offset:6144\n"
"v_cvt_pk_bf16_f32 v163, v161, v161\n"
"v_lshlrev_b32 v161, 16, v163\n"
"v_sqrt_f32 v161, v161\n"
"v_mfma_f32_32x32x16_bf16 a[64:79], v[68:71], v[80:83], a[64:79]\n"
"ds_read_b64_tr_b16 v[118:119], v13 offset:6656\n"
"s_nop 0\n"
"v_cvt_pk_bf16_f32 v163, v161, v161\n"
"v_lshlrev_b32 v161, 16, v163\n"
"v_mfma_f32_32x32x16_bf16 a[80:95], v[68:71], v[84:87], a[80:95]\n"
"ds_read_b64_tr_b16 v[120:121], v12 offset:14336\n"
"v_rcp_f32 v161, v161\n"
"v_sub_f32 v144, v144, v162\n"
"v_sub_f32 v145, v145, v162\n"
"v_mfma_f32_32x32x16_bf16 a[96:111], v[68:71], v[88:91], a[96:111]\n"
"ds_read_b64_tr_b16 v[122:123], v12 offset:14848\n"
"v_sub_f32 v146, v146, v162\n"
"v_sub_f32 v147, v147, v162\n"
"v_sub_f32 v148, v148, v162\n"
"v_sub_f32 v149, v149, v162\n"
"v_mfma_f32_32x32x16_bf16 a[112:127], v[68:71], v[92:95], a[112:127]\n"
"ds_read_b64_tr_b16 v[124:125], v13 offset:14336\n"
"v_sub_f32 v150, v150, v162\n"
"v_sub_f32 v151, v151, v162\n"
"v_sub_f32 v152, v152, v162\n"
"v_sub_f32 v153, v153, v162\n"
"v_mfma_f32_32x32x16_bf16 a[128:143], v[72:75], v[80:83], a[128:143]\n"
"ds_read_b64_tr_b16 v[126:127], v13 offset:14848\n"
"v_sub_f32 v154, v154, v162\n"
"v_sub_f32 v155, v155, v162\n"
"v_sub_f32 v156, v156, v162\n"
"v_sub_f32 v157, v157, v162\n"
"v_mfma_f32_32x32x16_bf16 a[144:159], v[72:75], v[84:87], a[144:159]\n"
"ds_read_b128 v[100:103], v11 offset:4096\n"
"v_sub_f32 v158, v158, v162\n"
"v_sub_f32 v159, v159, v162\n"
"v_cvt_pk_bf16_f32 v164, v144, v145\n"
"v_cvt_pk_bf16_f32 v165, v146, v147\n"
"v_mfma_f32_32x32x16_bf16 a[160:175], v[72:75], v[88:91], a[160:175]\n"
"ds_read_b128 v[104:107], v11 offset:8192\n"
"v_cvt_pk_bf16_f32 v166, v148, v149\n"
"v_cvt_pk_bf16_f32 v167, v150, v151\n"
"v_cvt_pk_bf16_f32 v168, v152, v153\n"
"v_cvt_pk_bf16_f32 v169, v154, v155\n"
"v_mfma_f32_32x32x16_bf16 a[176:191], v[72:75], v[92:95], a[176:191]\n"
"ds_read_b128 v[108:111], v11 offset:12288\n"
"v_cvt_pk_bf16_f32 v170, v156, v157\n"
"v_cvt_pk_bf16_f32 v171, v158, v159\n"
"v_lshlrev_b32 v144, 16, v164\n"
"v_and_b32 v145, 0xffff0000, v164\n"
"v_mfma_f32_32x32x16_bf16 a[192:207], v[76:79], v[80:83], a[192:207]\n"
"v_xor_b32 v8, 0x10000, v8\n"
"v_xor_b32 v9, 0x10000, v9\n"
"v_lshlrev_b32 v146, 16, v165\n"
"v_and_b32 v147, 0xffff0000, v165\n"
"v_lshlrev_b32 v148, 16, v166\n"
"v_mfma_f32_32x32x16_bf16 a[208:223], v[76:79], v[84:87], a[208:223]\n"
"v_xor_b32 v10, 0x10000, v10\n"
"v_xor_b32 v11, 0x10000, v11\n"
"v_and_b32 v149, 0xffff0000, v166\n"
"v_lshlrev_b32 v150, 16, v167\n"
"v_and_b32 v151, 0xffff0000, v167\n"
"v_mfma_f32_32x32x16_bf16 a[224:239], v[76:79], v[88:91], a[224:239]\n"
"v_xor_b32 v12, 0x10000, v12\n"
"v_xor_b32 v13, 0x10000, v13\n"
"v_lshlrev_b32 v152, 16, v168\n"
"v_and_b32 v153, 0xffff0000, v168\n"
"v_lshlrev_b32 v154, 16, v169\n"
"v_mfma_f32_32x32x16_bf16 a[240:255], v[76:79], v[92:95], a[240:255]\n"
"v_and_b32 v155, 0xffff0000, v169\n"
"v_lshlrev_b32 v156, 16, v170\n"
"v_and_b32 v157, 0xffff0000, v170\n"
"s_add_u32 s53, s53, 128\n"
"s_add_u32 s59, s59, 524288\n"
"s_waitcnt vmcnt(1) lgkmcnt(0)\n"
"s_barrier\n"
"s_add_u32 m0, s64, 65536\n"
"v_mfma_f32_32x32x16_bf16 a[0:15], v[96:99], v[112:115], a[0:15]\n"
"buffer_load_dwordx4 v16, s[16:19], s53 offen lds\n"
"ds_read_b128 v[64:67], v8 offset:0\n"
"v_lshlrev_b32 v158, 16, v171\n"
"v_and_b32 v159, 0xffff0000, v171\n"
"v_mfma_f32_32x32x16_bf16 a[16:31], v[96:99], v[116:119], a[16:31]\n"
"ds_read_b64_tr_b16 v[80:81], v12 offset:0\n"
"v_mul_f32 v144, v144, v161\n"
"v_mul_f32 v145, v145, v161\n"
"v_mul_f32 v146, v146, v161\n"
"s_add_u32 m0, s72, 65536\n"
"v_mfma_f32_32x32x16_bf16 a[32:47], v[96:99], v[120:123], a[32:47]\n"
"buffer_load_dwordx4 v24, s[20:23], s59 offen lds\n"
"ds_read_b64_tr_b16 v[82:83], v12 offset:512\n"
"v_mul_f32 v147, v147, v161\n"
"v_mul_f32 v148, v148, v161\n"
"v_mfma_f32_32x32x16_bf16 a[48:63], v[96:99], v[124:127], a[48:63]\n"
"ds_read_b64_tr_b16 v[84:85], v13 offset:0\n"
"v_mul_f32 v149, v149, v161\n"
"v_mul_f32 v150, v150, v161\n"
"v_mul_f32 v151, v151, v161\n"
"s_add_u32 m0, s65, 65536\n"
"v_mfma_f32_32x32x16_bf16 a[64:79], v[100:103], v[112:115], a[64:79]\n"
"buffer_load_dwordx4 v17, s[16:19], s53 offen lds\n"
"ds_read_b64_tr_b16 v[86:87], v13 offset:512\n"
"v_mul_f32 v152, v152, v161\n"
"v_mul_f32 v153, v153, v161\n"
"v_mfma_f32_32x32x16_bf16 a[80:95], v[100:103], v[116:119], a[80:95]\n"
"ds_read_b64_tr_b16 v[88:89], v12 offset:8192\n"
"v_mul_f32 v154, v154, v161\n"
"v_mul_f32 v155, v155, v161\n"
"v_mul_f32 v156, v156, v161\n"
"s_add_u32 m0, s73, 65536\n"
"v_mfma_f32_32x32x16_bf16 a[96:111], v[100:103], v[120:123], a[96:111]\n"
"buffer_load_dwordx4 v25, s[20:23], s59 offen lds\n"
"ds_read_b64_tr_b16 v[90:91], v12 offset:8704\n"
"v_mul_f32 v157, v157, v161\n"
"v_mul_f32 v158, v158, v161\n"
"v_mfma_f32_32x32x16_bf16 a[112:127], v[100:103], v[124:127], a[112:127]\n"
"ds_read_b64_tr_b16 v[92:93], v13 offset:8192\n"
"v_mul_f32 v159, v159, v161\n"
"v_cvt_pk_bf16_f32 v164, v144, v145\n"
"v_cvt_pk_bf16_f32 v165, v146, v147\n"
"s_add_u32 m0, s66, 65536\n"
"v_mfma_f32_32x32x16_bf16 a[128:143], v[104:107], v[112:115], a[128:143]\n"
"buffer_load_dwordx4 v18, s[16:19], s53 offen lds\n"
"ds_read_b64_tr_b16 v[94:95], v13 offset:8704\n"
"v_cvt_pk_bf16_f32 v166, v148, v149\n"
"v_cvt_pk_bf16_f32 v167, v150, v151\n"
"v_mfma_f32_32x32x16_bf16 a[144:159], v[104:107], v[116:119], a[144:159]\n"
"ds_read_b128 v[68:71], v8 offset:4096\n"
"v_cvt_pk_bf16_f32 v168, v152, v153\n"
"v_cvt_pk_bf16_f32 v169, v154, v155\n"
"v_cvt_pk_bf16_f32 v170, v156, v157\n"
"s_add_u32 m0, s74, 65536\n"
"v_mfma_f32_32x32x16_bf16 a[160:175], v[104:107], v[120:123], a[160:175]\n"
"buffer_load_dwordx4 v26, s[20:23], s59 offen lds\n"
"ds_read_b128 v[72:75], v8 offset:8192\n"
"v_cvt_pk_bf16_f32 v171, v158, v159\n"
"v_mfma_f32_32x32x16_bf16 a[176:191], v[104:107], v[124:127], a[176:191]\n"
"ds_read_b128 v[76:79], v8 offset:12288\n"
"ds_write_b128 v176, v[164:167] offset:9216\n"
"ds_write_b128 v176, v[168:171] offset:13824\n"
"s_lshr_b32 s84, s29, 1\n"
"s_add_u32 m0, s67, 65536\n"
"v_mfma_f32_32x32x16_bf16 a[192:207], v[108:111], v[112:115], a[192:207]\n"
"buffer_load_dwordx4 v19, s[16:19], s53 offen lds\n"
"s_and_b32 s85, s29, 1\n"
"s_mul_i32 s80, s84, 9216\n"
"s_mul_i32 s81, s85, 4608\n"
"v_mfma_f32_32x32x16_bf16 a[208:223], v[108:111], v[116:119], a[208:223]\n"
"s_add_u32 s80, s80, s81\n"
"s_add_u32 s80, s80, 131072\n"
"v_lshrrev_b32 v176, 3, v0\n"
"v_mul_u32_u24 v176, 144, v176\n"
"s_add_u32 m0, s75, 65536\n"
"v_mfma_f32_32x32x16_bf16 a[224:239], v[108:111], v[120:123], a[224:239]\n"
"buffer_load_dwordx4 v27, s[20:23], s59 offen lds\n"
"v_and_b32 v177, 7, v0\n"
"v_lshl_add_u32 v176, v177, 4, v176\n"
"v_add_u32 v176, s80, v176\n"
"v_mfma_f32_32x32x16_bf16 a[240:255], v[108:111], v[124:127], a[240:255]\n"
"s_mov_b32 s81, 53477376\n"
"s_cmp_eq_u32 s84, 0\n"
"s_cselect_b32 s80, 51380224, s81\n"
"s_waitcnt lgkmcnt(0)\n"
"s_add_u32 m0, s68, 65536\n"
"v_mfma_f32_32x32x16_bf16 a[0:15], v[64:67], v[80:83], a[0:15]\n"
"buffer_load_dwordx4 v20, s[16:19], s53 offen lds\n"
"ds_read_b128 v[96:99], v9 offset:0\n"
"s_add_u32 s80, s50, s80\n"
"s_addc_u32 s81, s51, 0\n"
"v_mfma_f32_32x32x16_bf16 a[16:31], v[64:67], v[84:87], a[16:31]\n"
"ds_read_b64_tr_b16 v[112:113], v12 offset:2048\n"
"s_and_b32 s81, s81, 0xffff\n"
"s_mov_b32 s82, 2097152\n"
"s_mov_b32 s83, 0x20000\n"
"s_add_u32 m0, s76, 65536\n"
"v_mfma_f32_32x32x16_bf16 a[32:47], v[64:67], v[88:91], a[32:47]\n"
"buffer_load_dwordx4 v28, s[20:23], s59 offen lds\n"
"ds_read_b64_tr_b16 v[114:115], v12 offset:2560\n"
"s_lshr_b32 s84, s28, 2\n"
"s_lshl_b32 s84, s84, 15\n"
"v_mfma_f32_32x32x16_bf16 a[48:63], v[64:67], v[92:95], a[48:63]\n"
"ds_read_b64_tr_b16 v[116:117], v13 offset:2048\n"
"s_lshl_b32 s85, s85, 14\n"
"s_add_u32 s84, s84, s85\n"
"s_and_b32 s85, s28, 3\n"
"s_add_u32 m0, s69, 65536\n"
"v_mfma_f32_32x32x16_bf16 a[64:79], v[68:71], v[80:83], a[64:79]\n"
"buffer_load_dwordx4 v21, s[16:19], s53 offen lds\n"
"ds_read_b64_tr_b16 v[118:119], v13 offset:2560\n"
"s_lshl_b32 s85, s85, 7\n"
"s_add_u32 s84, s84, s85\n"
"v_mfma_f32_32x32x16_bf16 a[80:95], v[68:71], v[84:87], a[80:95]\n"
"ds_read_b64_tr_b16 v[120:121], v12 offset:10240\n"
"v_lshrrev_b32 v177, 3, v0\n"
"v_lshlrev_b32 v177, 9, v177\n"
"v_and_b32 v178, 7, v0\n"
"s_add_u32 m0, s77, 65536\n"
"v_mfma_f32_32x32x16_bf16 a[96:111], v[68:71], v[88:91], a[96:111]\n"
"buffer_load_dwordx4 v29, s[20:23], s59 offen lds\n"
"ds_read_b64_tr_b16 v[122:123], v12 offset:10752\n"
"v_lshl_add_u32 v177, v178, 4, v177\n"
"v_mfma_f32_32x32x16_bf16 a[112:127], v[68:71], v[92:95], a[112:127]\n"
"ds_read_b64_tr_b16 v[124:125], v13 offset:10240\n"
"s_add_u32 m0, s70, 65536\n"
"v_mfma_f32_32x32x16_bf16 a[128:143], v[72:75], v[80:83], a[128:143]\n"
"buffer_load_dwordx4 v22, s[16:19], s53 offen lds\n"
"ds_read_b64_tr_b16 v[126:127], v13 offset:10752\n"
"v_mfma_f32_32x32x16_bf16 a[144:159], v[72:75], v[84:87], a[144:159]\n"
"ds_read_b128 v[100:103], v9 offset:4096\n"
"s_add_u32 m0, s78, 65536\n"
"v_mfma_f32_32x32x16_bf16 a[160:175], v[72:75], v[88:91], a[160:175]\n"
"buffer_load_dwordx4 v30, s[20:23], s59 offen lds\n"
"ds_read_b128 v[104:107], v9 offset:8192\n"
"v_mfma_f32_32x32x16_bf16 a[176:191], v[72:75], v[92:95], a[176:191]\n"
"ds_read_b128 v[108:111], v9 offset:12288\n"
"s_add_u32 m0, s71, 65536\n"
"v_mfma_f32_32x32x16_bf16 a[192:207], v[76:79], v[80:83], a[192:207]\n"
"buffer_load_dwordx4 v23, s[16:19], s53 offen lds\n"
"v_mfma_f32_32x32x16_bf16 a[208:223], v[76:79], v[84:87], a[208:223]\n"
"s_add_u32 m0, s79, 65536\n"
"v_mfma_f32_32x32x16_bf16 a[224:239], v[76:79], v[88:91], a[224:239]\n"
"buffer_load_dwordx4 v31, s[20:23], s59 offen lds\n"
"v_mfma_f32_32x32x16_bf16 a[240:255], v[76:79], v[92:95], a[240:255]\n"
"s_waitcnt lgkmcnt(0)\n"
"v_mfma_f32_32x32x16_bf16 a[0:15], v[96:99], v[112:115], a[0:15]\n"
"ds_read_b128 v[64:67], v10 offset:0\n"
"v_mfma_f32_32x32x16_bf16 a[16:31], v[96:99], v[116:119], a[16:31]\n"
"ds_read_b64_tr_b16 v[80:81], v12 offset:4096\n"
"v_mfma_f32_32x32x16_bf16 a[32:47], v[96:99], v[120:123], a[32:47]\n"
"ds_read_b64_tr_b16 v[82:83], v12 offset:4608\n"
"v_mfma_f32_32x32x16_bf16 a[48:63], v[96:99], v[124:127], a[48:63]\n"
"ds_read_b64_tr_b16 v[84:85], v13 offset:4096\n"
"v_mfma_f32_32x32x16_bf16 a[64:79], v[100:103], v[112:115], a[64:79]\n"
"ds_read_b64_tr_b16 v[86:87], v13 offset:4608\n"
"v_mfma_f32_32x32x16_bf16 a[80:95], v[100:103], v[116:119], a[80:95]\n"
"ds_read_b64_tr_b16 v[88:89], v12 offset:12288\n"
"v_mfma_f32_32x32x16_bf16 a[96:111], v[100:103], v[120:123], a[96:111]\n"
"ds_read_b64_tr_b16 v[90:91], v12 offset:12800\n"
"v_mfma_f32_32x32x16_bf16 a[112:127], v[100:103], v[124:127], a[112:127]\n"
"ds_read_b64_tr_b16 v[92:93], v13 offset:12288\n"
"v_mfma_f32_32x32x16_bf16 a[128:143], v[104:107], v[112:115], a[128:143]\n"
"ds_read_b64_tr_b16 v[94:95], v13 offset:12800\n"
"v_mfma_f32_32x32x16_bf16 a[144:159], v[104:107], v[116:119], a[144:159]\n"
"ds_read_b128 v[68:71], v10 offset:4096\n"
"v_mfma_f32_32x32x16_bf16 a[160:175], v[104:107], v[120:123], a[160:175]\n"
"ds_read_b128 v[72:75], v10 offset:8192\n"
"v_mfma_f32_32x32x16_bf16 a[176:191], v[104:107], v[124:127], a[176:191]\n"
"ds_read_b128 v[76:79], v10 offset:12288\n"
"v_mfma_f32_32x32x16_bf16 a[192:207], v[108:111], v[112:115], a[192:207]\n"
"v_mfma_f32_32x32x16_bf16 a[208:223], v[108:111], v[116:119], a[208:223]\n"
"v_mfma_f32_32x32x16_bf16 a[224:239], v[108:111], v[120:123], a[224:239]\n"
"v_mfma_f32_32x32x16_bf16 a[240:255], v[108:111], v[124:127], a[240:255]\n"
"s_waitcnt lgkmcnt(0)\n"
"v_mfma_f32_32x32x16_bf16 a[0:15], v[64:67], v[80:83], a[0:15]\n"
"ds_read_b128 v[96:99], v11 offset:0\n"
"v_mfma_f32_32x32x16_bf16 a[16:31], v[64:67], v[84:87], a[16:31]\n"
"ds_read_b64_tr_b16 v[112:113], v12 offset:6144\n"
"s_add_u32 s37, s37, s60\n"
"v_mfma_f32_32x32x16_bf16 a[32:47], v[64:67], v[88:91], a[32:47]\n"
"buffer_load_dword v225, v222, s[24:27], s37 offen\n"
"ds_read_b64_tr_b16 v[114:115], v12 offset:6656\n"
"v_mfma_f32_32x32x16_bf16 a[48:63], v[64:67], v[92:95], a[48:63]\n"
"ds_read_b64_tr_b16 v[116:117], v13 offset:6144\n"
"v_mfma_f32_32x32x16_bf16 a[64:79], v[68:71], v[80:83], a[64:79]\n"
"ds_read_b64_tr_b16 v[118:119], v13 offset:6656\n"
"v_mfma_f32_32x32x16_bf16 a[80:95], v[68:71], v[84:87], a[80:95]\n"
"ds_read_b64_tr_b16 v[120:121], v12 offset:14336\n"
"v_mfma_f32_32x32x16_bf16 a[96:111], v[68:71], v[88:91], a[96:111]\n"
"ds_read_b64_tr_b16 v[122:123], v12 offset:14848\n"
"v_mfma_f32_32x32x16_bf16 a[112:127], v[68:71], v[92:95], a[112:127]\n"
"ds_read_b64_tr_b16 v[124:125], v13 offset:14336\n"
"v_mfma_f32_32x32x16_bf16 a[128:143], v[72:75], v[80:83], a[128:143]\n"
"ds_read_b64_tr_b16 v[126:127], v13 offset:14848\n"
"v_mfma_f32_32x32x16_bf16 a[144:159], v[72:75], v[84:87], a[144:159]\n"
"ds_read_b128 v[100:103], v11 offset:4096\n"
"v_mfma_f32_32x32x16_bf16 a[160:175], v[72:75], v[88:91], a[160:175]\n"
"ds_read_b128 v[104:107], v11 offset:8192\n"
"v_mfma_f32_32x32x16_bf16 a[176:191], v[72:75], v[92:95], a[176:191]\n"
"ds_read_b128 v[108:111], v11 offset:12288\n"
"v_mfma_f32_32x32x16_bf16 a[192:207], v[76:79], v[80:83], a[192:207]\n"
"v_xor_b32 v8, 0x10000, v8\n"
"v_xor_b32 v9, 0x10000, v9\n"
"v_mfma_f32_32x32x16_bf16 a[208:223], v[76:79], v[84:87], a[208:223]\n"
"v_xor_b32 v10, 0x10000, v10\n"
"v_xor_b32 v11, 0x10000, v11\n"
"v_mfma_f32_32x32x16_bf16 a[224:239], v[76:79], v[88:91], a[224:239]\n"
"v_xor_b32 v12, 0x10000, v12\n"
"v_xor_b32 v13, 0x10000, v13\n"
"v_mfma_f32_32x32x16_bf16 a[240:255], v[76:79], v[92:95], a[240:255]\n"
"s_add_u32 s53, s53, 128\n"
"s_add_u32 s59, s59, 524288\n"
"s_waitcnt vmcnt(1) lgkmcnt(0)\n"
"s_barrier\n"
"s_add_u32 m0, s64, 0\n"
"v_mfma_f32_32x32x16_bf16 a[0:15], v[96:99], v[112:115], a[0:15]\n"
"buffer_load_dwordx4 v16, s[16:19], s53 offen lds\n"
"ds_read_b128 v[64:67], v8 offset:0\n"
"ds_read_b128 v[128:131], v176 offset:0\n"
"ds_read_b128 v[132:135], v176 offset:1152\n"
"v_mfma_f32_32x32x16_bf16 a[16:31], v[96:99], v[116:119], a[16:31]\n"
"ds_read_b64_tr_b16 v[80:81], v12 offset:0\n"
"ds_read_b128 v[136:139], v176 offset:2304\n"
"ds_read_b128 v[140:143], v176 offset:3456\n"
"s_add_u32 m0, s72, 0\n"
"v_mfma_f32_32x32x16_bf16 a[32:47], v[96:99], v[120:123], a[32:47]\n"
"buffer_load_dwordx4 v24, s[20:23], s59 offen lds\n"
"ds_read_b64_tr_b16 v[82:83], v12 offset:512\n"
"v_mfma_f32_32x32x16_bf16 a[48:63], v[96:99], v[124:127], a[48:63]\n"
"ds_read_b64_tr_b16 v[84:85], v13 offset:0\n"
"s_add_u32 m0, s65, 0\n"
"v_mfma_f32_32x32x16_bf16 a[64:79], v[100:103], v[112:115], a[64:79]\n"
"buffer_load_dwordx4 v17, s[16:19], s53 offen lds\n"
"ds_read_b64_tr_b16 v[86:87], v13 offset:512\n"
"v_mfma_f32_32x32x16_bf16 a[80:95], v[100:103], v[116:119], a[80:95]\n"
"ds_read_b64_tr_b16 v[88:89], v12 offset:8192\n"
"s_add_u32 m0, s73, 0\n"
"v_mfma_f32_32x32x16_bf16 a[96:111], v[100:103], v[120:123], a[96:111]\n"
"buffer_load_dwordx4 v25, s[20:23], s59 offen lds\n"
"ds_read_b64_tr_b16 v[90:91], v12 offset:8704\n"
"v_mfma_f32_32x32x16_bf16 a[112:127], v[100:103], v[124:127], a[112:127]\n"
"ds_read_b64_tr_b16 v[92:93], v13 offset:8192\n"
"s_add_u32 m0, s66, 0\n"
"v_mfma_f32_32x32x16_bf16 a[128:143], v[104:107], v[112:115], a[128:143]\n"
"buffer_load_dwordx4 v18, s[16:19], s53 offen lds\n"
"ds_read_b64_tr_b16 v[94:95], v13 offset:8704\n"
"v_mfma_f32_32x32x16_bf16 a[144:159], v[104:107], v[116:119], a[144:159]\n"
"ds_read_b128 v[68:71], v8 offset:4096\n"
"s_add_u32 m0, s74, 0\n"
"v_mfma_f32_32x32x16_bf16 a[160:175], v[104:107], v[120:123], a[160:175]\n"
"buffer_load_dwordx4 v26, s[20:23], s59 offen lds\n"
"ds_read_b128 v[72:75], v8 offset:8192\n"
"v_mfma_f32_32x32x16_bf16 a[176:191], v[104:107], v[124:127], a[176:191]\n"
"ds_read_b128 v[76:79], v8 offset:12288\n"
"s_add_u32 m0, s67, 0\n"
"v_mfma_f32_32x32x16_bf16 a[192:207], v[108:111], v[112:115], a[192:207]\n"
"buffer_load_dwordx4 v19, s[16:19], s53 offen lds\n"
"v_mfma_f32_32x32x16_bf16 a[208:223], v[108:111], v[116:119], a[208:223]\n"
"s_add_u32 m0, s75, 0\n"
"v_mfma_f32_32x32x16_bf16 a[224:239], v[108:111], v[120:123], a[224:239]\n"
"buffer_load_dwordx4 v27, s[20:23], s59 offen lds\n"
"v_mfma_f32_32x32x16_bf16 a[240:255], v[108:111], v[124:127], a[240:255]\n"
"s_waitcnt lgkmcnt(0)\n"
"s_add_u32 m0, s68, 0\n"
"v_mfma_f32_32x32x16_bf16 a[0:15], v[64:67], v[80:83], a[0:15]\n"
"buffer_load_dwordx4 v20, s[16:19], s53 offen lds\n"
"ds_read_b128 v[96:99], v9 offset:0\n"
"v_mfma_f32_32x32x16_bf16 a[16:31], v[64:67], v[84:87], a[16:31]\n"
"ds_read_b64_tr_b16 v[112:113], v12 offset:2048\n"
"s_add_u32 m0, s76, 0\n"
"v_mfma_f32_32x32x16_bf16 a[32:47], v[64:67], v[88:91], a[32:47]\n"
"buffer_load_dwordx4 v28, s[20:23], s59 offen lds\n"
"ds_read_b64_tr_b16 v[114:115], v12 offset:2560\n"
"v_mfma_f32_32x32x16_bf16 a[48:63], v[64:67], v[92:95], a[48:63]\n"
"ds_read_b64_tr_b16 v[116:117], v13 offset:2048\n"
"s_add_u32 m0, s69, 0\n"
"v_mfma_f32_32x32x16_bf16 a[64:79], v[68:71], v[80:83], a[64:79]\n"
"buffer_load_dwordx4 v21, s[16:19], s53 offen lds\n"
"ds_read_b64_tr_b16 v[118:119], v13 offset:2560\n"
"v_mfma_f32_32x32x16_bf16 a[80:95], v[68:71], v[84:87], a[80:95]\n"
"ds_read_b64_tr_b16 v[120:121], v12 offset:10240\n"
"s_add_u32 m0, s77, 0\n"
"v_mfma_f32_32x32x16_bf16 a[96:111], v[68:71], v[88:91], a[96:111]\n"
"buffer_load_dwordx4 v29, s[20:23], s59 offen lds\n"
"ds_read_b64_tr_b16 v[122:123], v12 offset:10752\n"
"v_mfma_f32_32x32x16_bf16 a[112:127], v[68:71], v[92:95], a[112:127]\n"
"ds_read_b64_tr_b16 v[124:125], v13 offset:10240\n"
"s_add_u32 m0, s70, 0\n"
"v_mfma_f32_32x32x16_bf16 a[128:143], v[72:75], v[80:83], a[128:143]\n"
"buffer_load_dwordx4 v22, s[16:19], s53 offen lds\n"
"ds_read_b64_tr_b16 v[126:127], v13 offset:10752\n"
"v_mfma_f32_32x32x16_bf16 a[144:159], v[72:75], v[84:87], a[144:159]\n"
"ds_read_b128 v[100:103], v9 offset:4096\n"
"s_add_u32 m0, s78, 0\n"
"v_mfma_f32_32x32x16_bf16 a[160:175], v[72:75], v[88:91], a[160:175]\n"
"buffer_load_dwordx4 v30, s[20:23], s59 offen lds\n"
"ds_read_b128 v[104:107], v9 offset:8192\n"
"v_mfma_f32_32x32x16_bf16 a[176:191], v[72:75], v[92:95], a[176:191]\n"
"ds_read_b128 v[108:111], v9 offset:12288\n"
"s_add_u32 m0, s71, 0\n"
"v_mfma_f32_32x32x16_bf16 a[192:207], v[76:79], v[80:83], a[192:207]\n"
"buffer_load_dwordx4 v23, s[16:19], s53 offen lds\n"
"v_mfma_f32_32x32x16_bf16 a[208:223], v[76:79], v[84:87], a[208:223]\n"
"s_add_u32 m0, s79, 0\n"
"v_mfma_f32_32x32x16_bf16 a[224:239], v[76:79], v[88:91], a[224:239]\n"
"buffer_load_dwordx4 v31, s[20:23], s59 offen lds\n"
"v_mfma_f32_32x32x16_bf16 a[240:255], v[76:79], v[92:95], a[240:255]\n"
"s_waitcnt lgkmcnt(0)\n"
"v_mfma_f32_32x32x16_bf16 a[0:15], v[96:99], v[112:115], a[0:15]\n"
"ds_read_b128 v[64:67], v10 offset:0\n"
"v_mfma_f32_32x32x16_bf16 a[16:31], v[96:99], v[116:119], a[16:31]\n"
"ds_read_b64_tr_b16 v[80:81], v12 offset:4096\n"
"v_mfma_f32_32x32x16_bf16 a[32:47], v[96:99], v[120:123], a[32:47]\n"
"ds_read_b64_tr_b16 v[82:83], v12 offset:4608\n"
"v_mfma_f32_32x32x16_bf16 a[48:63], v[96:99], v[124:127], a[48:63]\n"
"ds_read_b64_tr_b16 v[84:85], v13 offset:4096\n"
"v_mfma_f32_32x32x16_bf16 a[64:79], v[100:103], v[112:115], a[64:79]\n"
"ds_read_b64_tr_b16 v[86:87], v13 offset:4608\n"
"v_mfma_f32_32x32x16_bf16 a[80:95], v[100:103], v[116:119], a[80:95]\n"
"ds_read_b64_tr_b16 v[88:89], v12 offset:12288\n"
"v_mfma_f32_32x32x16_bf16 a[96:111], v[100:103], v[120:123], a[96:111]\n"
"ds_read_b64_tr_b16 v[90:91], v12 offset:12800\n"
"v_mfma_f32_32x32x16_bf16 a[112:127], v[100:103], v[124:127], a[112:127]\n"
"ds_read_b64_tr_b16 v[92:93], v13 offset:12288\n"
"v_mfma_f32_32x32x16_bf16 a[128:143], v[104:107], v[112:115], a[128:143]\n"
"ds_read_b64_tr_b16 v[94:95], v13 offset:12800\n"
"v_mfma_f32_32x32x16_bf16 a[144:159], v[104:107], v[116:119], a[144:159]\n"
"ds_read_b128 v[68:71], v10 offset:4096\n"
"v_mfma_f32_32x32x16_bf16 a[160:175], v[104:107], v[120:123], a[160:175]\n"
"ds_read_b128 v[72:75], v10 offset:8192\n"
"v_mfma_f32_32x32x16_bf16 a[176:191], v[104:107], v[124:127], a[176:191]\n"
"ds_read_b128 v[76:79], v10 offset:12288\n"
"v_mfma_f32_32x32x16_bf16 a[192:207], v[108:111], v[112:115], a[192:207]\n"
"v_mfma_f32_32x32x16_bf16 a[208:223], v[108:111], v[116:119], a[208:223]\n"
"v_mfma_f32_32x32x16_bf16 a[224:239], v[108:111], v[120:123], a[224:239]\n"
"v_mfma_f32_32x32x16_bf16 a[240:255], v[108:111], v[124:127], a[240:255]\n"
"s_waitcnt lgkmcnt(0)\n"
"v_mfma_f32_32x32x16_bf16 a[0:15], v[64:67], v[80:83], a[0:15]\n"
"ds_read_b128 v[96:99], v11 offset:0\n"
"buffer_store_dwordx4 v[128:131], v177, s[80:83], s84 offen sc1\n"
"s_add_u32 s84, s84, 4096\n"
"v_mfma_f32_32x32x16_bf16 a[16:31], v[64:67], v[84:87], a[16:31]\n"
"ds_read_b64_tr_b16 v[112:113], v12 offset:6144\n"
"buffer_store_dwordx4 v[132:135], v177, s[80:83], s84 offen sc1\n"
"s_add_u32 s84, s84, 4096\n"
"s_add_u32 s37, s37, s60\n"
"v_mfma_f32_32x32x16_bf16 a[32:47], v[64:67], v[88:91], a[32:47]\n"
"buffer_load_dword v225, v222, s[24:27], s37 offen\n"
"ds_read_b64_tr_b16 v[114:115], v12 offset:6656\n"
"buffer_store_dwordx4 v[136:139], v177, s[80:83], s84 offen sc1\n"
"v_mfma_f32_32x32x16_bf16 a[48:63], v[64:67], v[92:95], a[48:63]\n"
"ds_read_b64_tr_b16 v[116:117], v13 offset:6144\n"
"s_add_u32 s84, s84, 4096\n"
"buffer_store_dwordx4 v[140:143], v177, s[80:83], s84 offen sc1\n"
"s_add_u32 s84, s84, 4096\n"
"v_mfma_f32_32x32x16_bf16 a[64:79], v[68:71], v[80:83], a[64:79]\n"
"ds_read_b64_tr_b16 v[118:119], v13 offset:6656\n"
"s_load_dwordx2 s[80:81], s[8:9], 16\n"
"v_lshrrev_b32 v216, 3, v0\n"
"v_lshlrev_b32 v216, 13, v216\n"
"v_and_b32 v217, 7, v0\n"
"v_mfma_f32_32x32x16_bf16 a[80:95], v[68:71], v[84:87], a[80:95]\n"
"ds_read_b64_tr_b16 v[120:121], v12 offset:14336\n"
"v_lshl_add_u32 v216, v217, 7, v216\n"
"v_mfma_f32_32x32x16_bf16 a[96:111], v[68:71], v[88:91], a[96:111]\n"
"ds_read_b64_tr_b16 v[122:123], v12 offset:14848\n"
"v_cmp_gt_u32 vcc, 32, v0\n"
"v_mov_b32 v218, 1073741824\n"
"s_nop 0\n"
"v_cndmask_b32 v216, v218, v216, vcc\n"
"v_mfma_f32_32x32x16_bf16 a[112:127], v[68:71], v[92:95], a[112:127]\n"
"ds_read_b64_tr_b16 v[124:125], v13 offset:14336\n"
"s_lshr_b32 s84, s28, 3\n"
"s_lshl_b32 s84, s84, 17\n"
"s_lshl_b32 s85, s29, 15\n"
"s_add_u32 s84, s84, s85\n"
"v_mfma_f32_32x32x16_bf16 a[128:143], v[72:75], v[80:83], a[128:143]\n"
"ds_read_b64_tr_b16 v[126:127], v13 offset:14848\n"
"s_and_b32 s85, s28, 7\n"
"s_lshl_b32 s85, s85, 10\n"
"s_add_u32 s84, s84, s85\n"
"v_mfma_f32_32x32x16_bf16 a[144:159], v[72:75], v[84:87], a[144:159]\n"
"ds_read_b128 v[100:103], v11 offset:4096\n"
"v_mfma_f32_32x32x16_bf16 a[160:175], v[72:75], v[88:91], a[160:175]\n"
"ds_read_b128 v[104:107], v11 offset:8192\n"
"v_mfma_f32_32x32x16_bf16 a[176:191], v[72:75], v[92:95], a[176:191]\n"
"ds_read_b128 v[108:111], v11 offset:12288\n"
"v_mfma_f32_32x32x16_bf16 a[192:207], v[76:79], v[80:83], a[192:207]\n"
"v_xor_b32 v8, 0x10000, v8\n"
"v_xor_b32 v9, 0x10000, v9\n"
"v_mfma_f32_32x32x16_bf16 a[208:223], v[76:79], v[84:87], a[208:223]\n"
"v_xor_b32 v10, 0x10000, v10\n"
"v_xor_b32 v11, 0x10000, v11\n"
"v_mfma_f32_32x32x16_bf16 a[224:239], v[76:79], v[88:91], a[224:239]\n"
"v_xor_b32 v12, 0x10000, v12\n"
"v_xor_b32 v13, 0x10000, v13\n"
"v_mfma_f32_32x32x16_bf16 a[240:255], v[76:79], v[92:95], a[240:255]\n"
"s_add_u32 s53, s53, 128\n"
"s_add_u32 s59, s59, 524288\n"
"s_waitcnt vmcnt(5) lgkmcnt(0)\n"
"s_barrier\n"
"s_add_u32 m0, s64, 65536\n"
"v_mfma_f32_32x32x16_bf16 a[0:15], v[96:99], v[112:115], a[0:15]\n"
"buffer_load_dwordx4 v16, s[16:19], s53 offen lds\n"
"ds_read_b128 v[64:67], v8 offset:0\n"
"s_and_b32 s81, s81, 0xffff\n"
"s_mov_b32 s82, 4194304\n"
"v_mfma_f32_32x32x16_bf16 a[16:31], v[96:99], v[116:119], a[16:31]\n"
"ds_read_b64_tr_b16 v[80:81], v12 offset:0\n"
"s_mov_b32 s83, 0x20000\n"
"s_and_b32 s34, s28, 7\n"
"s_lshl_b32 s34, s34, 2\n"
"s_add_u32 m0, s72, 65536\n"
"v_mfma_f32_32x32x16_bf16 a[32:47], v[96:99], v[120:123], a[32:47]\n"
"buffer_load_dwordx4 v24, s[20:23], s59 offen lds\n"
"ds_read_b64_tr_b16 v[82:83], v12 offset:512\n"
"s_bfe_u32 s35, s28, 0x20003\n"
"s_add_u32 s30, s34, s35\n"
"v_mfma_f32_32x32x16_bf16 a[48:63], v[96:99], v[124:127], a[48:63]\n"
"ds_read_b64_tr_b16 v[84:85], v13 offset:0\n"
"s_lshr_b32 s31, s28, 5\n"
"s_add_u32 s12, s50, 53477376\n"
"s_addc_u32 s13, s51, 0\n"
"s_add_u32 m0, s65, 65536\n"
"v_mfma_f32_32x32x16_bf16 a[64:79], v[100:103], v[112:115], a[64:79]\n"
"buffer_load_dwordx4 v17, s[16:19], s53 offen lds\n"
"ds_read_b64_tr_b16 v[86:87], v13 offset:512\n"
"s_and_b32 s13, s13, 0xffff\n"
"s_mov_b32 s14, 2097152\n"
"v_mfma_f32_32x32x16_bf16 a[80:95], v[100:103], v[116:119], a[80:95]\n"
"ds_read_b64_tr_b16 v[88:89], v12 offset:8192\n"
"s_mov_b32 s15, 0x20000\n"
"s_mov_b32 s24, s44\n"
"s_and_b32 s25, s45, 0xffff\n"
"s_add_u32 m0, s73, 65536\n"
"v_mfma_f32_32x32x16_bf16 a[96:111], v[100:103], v[120:123], a[96:111]\n"
"buffer_load_dwordx4 v25, s[20:23], s59 offen lds\n"
"ds_read_b64_tr_b16 v[90:91], v12 offset:8704\n"
"s_mov_b32 s26, 524288\n"
"s_mov_b32 s27, 0x20000\n"
"v_mfma_f32_32x32x16_bf16 a[112:127], v[100:103], v[124:127], a[112:127]\n"
"ds_read_b64_tr_b16 v[92:93], v13 offset:8192\n"
"s_and_b32 s34, s29, 1\n"
"s_lshl_b32 s41, s30, 1\n"
"s_add_u32 s41, s41, s34\n"
"s_add_u32 m0, s66, 65536\n"
"v_mfma_f32_32x32x16_bf16 a[128:143], v[104:107], v[112:115], a[128:143]\n"
"buffer_load_dwordx4 v18, s[16:19], s53 offen lds\n"
"ds_read_b64_tr_b16 v[94:95], v13 offset:8704\n"
"s_lshl_b32 s40, s34, 15\n"
"s_lshr_b32 s35, s29, 1\n"
"v_mfma_f32_32x32x16_bf16 a[144:159], v[104:107], v[116:119], a[144:159]\n"
"ds_read_b128 v[68:71], v8 offset:4096\n"
"s_lshl_b32 s35, s35, 12\n"
"s_add_u32 s40, s40, s35\n"
"s_add_u32 s40, s40, 65536\n"
"s_add_u32 m0, s74, 65536\n"
"v_mfma_f32_32x32x16_bf16 a[160:175], v[104:107], v[120:123], a[160:175]\n"
"buffer_load_dwordx4 v26, s[20:23], s59 offen lds\n"
"ds_read_b128 v[72:75], v8 offset:8192\n"
"s_lshl_b32 s38, s41, 15\n"
"s_add_u32 s38, s38, s35\n"
"v_mfma_f32_32x32x16_bf16 a[176:191], v[104:107], v[124:127], a[176:191]\n"
"ds_read_b128 v[76:79], v8 offset:12288\n"
"v_lshrrev_b32 v44, 3, v0\n"
"v_lshlrev_b32 v44, 10, v44\n"
"v_bfe_u32 v45, v0, 4, 1\n"
"s_add_u32 m0, s67, 65536\n"
"v_mfma_f32_32x32x16_bf16 a[192:207], v[108:111], v[112:115], a[192:207]\n"
"buffer_load_dwordx4 v19, s[16:19], s53 offen lds\n"
"v_lshlrev_b32 v45, 2, v45\n"
"v_and_b32 v46, 7, v0\n"
"v_xor_b32 v45, v45, v46\n"
"v_mfma_f32_32x32x16_bf16 a[208:223], v[108:111], v[116:119], a[208:223]\n"
"v_lshl_add_u32 v36, v45, 4, v44\n"
"v_add_u32 v37, 8192, v36\n"
"v_add_u32 v38, 16384, v36\n"
"v_add_u32 v39, 24576, v36\n"
"s_add_u32 m0, s75, 65536\n"
"v_mfma_f32_32x32x16_bf16 a[224:239], v[108:111], v[120:123], a[224:239]\n"
"buffer_load_dwordx4 v27, s[20:23], s59 offen lds\n"
"s_lshl_b32 s39, s29, 15\n"
"s_lshl_b32 s34, s31, 7\n"
"s_add_u32 s39, s39, s34\n"
"v_mfma_f32_32x32x16_bf16 a[240:255], v[108:111], v[124:127], a[240:255]\n"
"s_lshl_b32 s43, s29, 12\n"
"s_lshr_b32 s34, s29, 1\n"
"s_lshl_b32 s35, s34, 15\n"
"s_waitcnt lgkmcnt(0)\n"
"s_add_u32 m0, s68, 65536\n"
"v_mfma_f32_32x32x16_bf16 a[0:15], v[64:67], v[80:83], a[0:15]\n"
"buffer_load_dwordx4 v20, s[16:19], s53 offen lds\n"
"ds_read_b128 v[96:99], v9 offset:0\n"
"s_add_u32 s35, s35, 65536\n"
"v_add_u32 v40, s35, v1\n"
"v_mfma_f32_32x32x16_bf16 a[16:31], v[64:67], v[84:87], a[16:31]\n"
"ds_read_b64_tr_b16 v[112:113], v12 offset:2048\n"
"v_lshrrev_b32 v44, 5, v0\n"
"v_bfe_u32 v45, v0, 2, 2\n"
"v_lshl_add_u32 v44, v44, 3, v45\n"
"s_add_u32 m0, s76, 65536\n"
"v_mfma_f32_32x32x16_bf16 a[32:47], v[64:67], v[88:91], a[32:47]\n"
"buffer_load_dwordx4 v28, s[20:23], s59 offen lds\n"
"ds_read_b64_tr_b16 v[114:115], v12 offset:2560\n"
"v_lshlrev_b32 v44, 7, v44\n"
"v_and_b32 v46, 1, v0\n"
"v_mfma_f32_32x32x16_bf16 a[48:63], v[64:67], v[92:95], a[48:63]\n"
"ds_read_b64_tr_b16 v[116:117], v13 offset:2048\n"
"v_lshl_add_u32 v44, v46, 3, v44\n"
"v_bfe_u32 v46, v0, 4, 1\n"
"v_bfe_u32 v47, v0, 1, 1\n"
"s_add_u32 m0, s69, 65536\n"
"v_mfma_f32_32x32x16_bf16 a[64:79], v[68:71], v[80:83], a[64:79]\n"
"buffer_load_dwordx4 v21, s[16:19], s53 offen lds\n"
"ds_read_b64_tr_b16 v[118:119], v13 offset:2560\n"
"v_lshl_add_u32 v46, v46, 1, v47\n"
"s_and_b32 s34, s29, 1\n"
"v_mfma_f32_32x32x16_bf16 a[80:95], v[68:71], v[84:87], a[80:95]\n"
"ds_read_b64_tr_b16 v[120:121], v12 offset:10240\n"
"s_lshl_b32 s34, s34, 2\n"
"v_add_u32 v46, s34, v46\n"
"v_lshrrev_b32 v45, 1, v45\n"
"s_add_u32 m0, s77, 65536\n"
"v_mfma_f32_32x32x16_bf16 a[96:111], v[68:71], v[88:91], a[96:111]\n"
"buffer_load_dwordx4 v29, s[20:23], s59 offen lds\n"
"ds_read_b64_tr_b16 v[122:123], v12 offset:10752\n"
"v_lshlrev_b32 v45, 2, v45\n"
"v_xor_b32 v46, v46, v45\n"
"v_mfma_f32_32x32x16_bf16 a[112:127], v[68:71], v[92:95], a[112:127]\n"
"ds_read_b64_tr_b16 v[124:125], v13 offset:10240\n"
"v_lshl_add_u32 v41, v46, 4, v44\n"
"s_add_u32 s96, s50, 51380224\n"
"s_addc_u32 s97, s51, 0\n"
"s_add_u32 m0, s70, 65536\n"
"v_mfma_f32_32x32x16_bf16 a[128:143], v[72:75], v[80:83], a[128:143]\n"
"buffer_load_dwordx4 v22, s[16:19], s53 offen lds\n"
"ds_read_b64_tr_b16 v[126:127], v13 offset:10752\n"
"s_and_b32 s97, s97, 0xffff\n"
"s_mov_b32 s98, 2097152\n"
"v_mfma_f32_32x32x16_bf16 a[144:159], v[72:75], v[84:87], a[144:159]\n"
"ds_read_b128 v[100:103], v9 offset:4096\n"
"s_mov_b32 s99, 0x20000\n"
"s_lshr_b32 s36, s28, 5\n"
"s_sub_u32 s34, 15, s36\n"
"s_add_u32 m0, s78, 65536\n"
"v_mfma_f32_32x32x16_bf16 a[160:175], v[72:75], v[88:91], a[160:175]\n"
"buffer_load_dwordx4 v30, s[20:23], s59 offen lds\n"
"ds_read_b128 v[104:107], v9 offset:8192\n"
"s_cmp_lt_u32 s29, 2\n"
"s_cselect_b32 s34, s36, s34\n"
"v_mfma_f32_32x32x16_bf16 a[176:191], v[72:75], v[92:95], a[176:191]\n"
"ds_read_b128 v[108:111], v9 offset:12288\n"
"s_lshl_b32 s34, s34, 2\n"
"s_and_b32 s35, s29, 1\n"
"s_lshl_b32 s35, s35, 1\n"
"s_add_u32 m0, s71, 65536\n"
"v_mfma_f32_32x32x16_bf16 a[192:207], v[76:79], v[80:83], a[192:207]\n"
"buffer_load_dwordx4 v23, s[16:19], s53 offen lds\n"
"s_add_u32 s34, s34, s35\n"
"s_lshl_b32 s56, s34, 15\n"
"s_add_u32 s57, s56, 32768\n"
"v_mfma_f32_32x32x16_bf16 a[208:223], v[76:79], v[84:87], a[208:223]\n"
"s_and_b32 s34, s28, 7\n"
"s_lshl_b32 s34, s34, 1\n"
"s_bfe_u32 s35, s28, 0x10004\n"
"s_add_u32 s34, s34, s35\n"
"s_add_u32 m0, s79, 65536\n"
"v_mfma_f32_32x32x16_bf16 a[224:239], v[76:79], v[88:91], a[224:239]\n"
"buffer_load_dwordx4 v31, s[20:23], s59 offen lds\n"
"v_mov_b32 v42, s34\n"
"s_lshr_b32 s34, s28, 5\n"
"v_mov_b32 v43, s34\n"
"v_mfma_f32_32x32x16_bf16 a[240:255], v[76:79], v[92:95], a[240:255]\n"
"s_waitcnt lgkmcnt(0)\n"
"v_mfma_f32_32x32x16_bf16 a[0:15], v[96:99], v[112:115], a[0:15]\n"
"ds_read_b128 v[64:67], v10 offset:0\n"
"v_cmp_le_u32 vcc, 16, v0\n"
"s_nop 1\n"
"v_cndmask_b32 v42, v42, v43, vcc\n"
"v_mfma_f32_32x32x16_bf16 a[16:31], v[96:99], v[116:119], a[16:31]\n"
"ds_read_b64_tr_b16 v[80:81], v12 offset:4096\n"
"s_sub_u32 s34, 15, s34\n"
"v_mov_b32 v43, s34\n"
"v_mfma_f32_32x32x16_bf16 a[32:47], v[96:99], v[120:123], a[32:47]\n"
"ds_read_b64_tr_b16 v[82:83], v12 offset:4608\n"
"v_cmp_le_u32 vcc, 32, v0\n"
"s_nop 1\n"
"v_cndmask_b32 v42, v42, v43, vcc\n"
"v_mfma_f32_32x32x16_bf16 a[48:63], v[96:99], v[124:127], a[48:63]\n"
"ds_read_b64_tr_b16 v[84:85], v13 offset:4096\n"
"s_lshr_b32 s34, s28, 4\n"
"v_mov_b32 v43, s34\n"
"v_mfma_f32_32x32x16_bf16 a[64:79], v[100:103], v[112:115], a[64:79]\n"
"ds_read_b64_tr_b16 v[86:87], v13 offset:4608\n"
"v_cmp_le_u32 vcc, 48, v0\n"
"s_nop 1\n"
"v_cndmask_b32 v42, v42, v43, vcc\n"
"v_mfma_f32_32x32x16_bf16 a[80:95], v[100:103], v[116:119], a[80:95]\n"
"ds_read_b64_tr_b16 v[88:89], v12 offset:12288\n"
"v_lshlrev_b32 v42, 8, v42\n"
"v_add_u32 v42, 16640, v42\n"
"v_and_b32 v43, 15, v0\n"
"v_lshl_add_u32 v42, v43, 2, v42\n"
"v_mfma_f32_32x32x16_bf16 a[96:111], v[100:103], v[120:123], a[96:111]\n"
"ds_read_b64_tr_b16 v[90:91], v12 offset:12800\n"
"v_mfma_f32_32x32x16_bf16 a[112:127], v[100:103], v[124:127], a[112:127]\n"
"ds_read_b64_tr_b16 v[92:93], v13 offset:12288\n"
"v_mfma_f32_32x32x16_bf16 a[128:143], v[104:107], v[112:115], a[128:143]\n"
"ds_read_b64_tr_b16 v[94:95], v13 offset:12800\n"
"v_mfma_f32_32x32x16_bf16 a[144:159], v[104:107], v[116:119], a[144:159]\n"
"ds_read_b128 v[68:71], v10 offset:4096\n"
"v_mfma_f32_32x32x16_bf16 a[160:175], v[104:107], v[120:123], a[160:175]\n"
"ds_read_b128 v[72:75], v10 offset:8192\n"
"v_mfma_f32_32x32x16_bf16 a[176:191], v[104:107], v[124:127], a[176:191]\n"
"ds_read_b128 v[76:79], v10 offset:12288\n"
"v_mfma_f32_32x32x16_bf16 a[192:207], v[108:111], v[112:115], a[192:207]\n"
"v_mfma_f32_32x32x16_bf16 a[208:223], v[108:111], v[116:119], a[208:223]\n"
"v_mfma_f32_32x32x16_bf16 a[224:239], v[108:111], v[120:123], a[224:239]\n"
"v_mfma_f32_32x32x16_bf16 a[240:255], v[108:111], v[124:127], a[240:255]\n"
"s_waitcnt lgkmcnt(0)\n"
"v_mfma_f32_32x32x16_bf16 a[0:15], v[64:67], v[80:83], a[0:15]\n"
"ds_read_b128 v[96:99], v11 offset:0\n"
"v_mfma_f32_32x32x16_bf16 a[16:31], v[64:67], v[84:87], a[16:31]\n"
"ds_read_b64_tr_b16 v[112:113], v12 offset:6144\n"
"v_mfma_f32_32x32x16_bf16 a[32:47], v[64:67], v[88:91], a[32:47]\n"
"buffer_load_dword v214, v216, s[80:83], s84 offen\n"
"ds_read_b64_tr_b16 v[114:115], v12 offset:6656\n"
"v_mfma_f32_32x32x16_bf16 a[48:63], v[64:67], v[92:95], a[48:63]\n"
"ds_read_b64_tr_b16 v[116:117], v13 offset:6144\n"
"v_mfma_f32_32x32x16_bf16 a[64:79], v[68:71], v[80:83], a[64:79]\n"
"ds_read_b64_tr_b16 v[118:119], v13 offset:6656\n"
"v_mfma_f32_32x32x16_bf16 a[80:95], v[68:71], v[84:87], a[80:95]\n"
"ds_read_b64_tr_b16 v[120:121], v12 offset:14336\n"
"v_mfma_f32_32x32x16_bf16 a[96:111], v[68:71], v[88:91], a[96:111]\n"
"ds_read_b64_tr_b16 v[122:123], v12 offset:14848\n"
"v_mfma_f32_32x32x16_bf16 a[112:127], v[68:71], v[92:95], a[112:127]\n"
"ds_read_b64_tr_b16 v[124:125], v13 offset:14336\n"
"v_mfma_f32_32x32x16_bf16 a[128:143], v[72:75], v[80:83], a[128:143]\n"
"ds_read_b64_tr_b16 v[126:127], v13 offset:14848\n"
"v_mfma_f32_32x32x16_bf16 a[144:159], v[72:75], v[84:87], a[144:159]\n"
"ds_read_b128 v[100:103], v11 offset:4096\n"
"v_mfma_f32_32x32x16_bf16 a[160:175], v[72:75], v[88:91], a[160:175]\n"
"ds_read_b128 v[104:107], v11 offset:8192\n"
"v_mfma_f32_32x32x16_bf16 a[176:191], v[72:75], v[92:95], a[176:191]\n"
"ds_read_b128 v[108:111], v11 offset:12288\n"
"v_mfma_f32_32x32x16_bf16 a[192:207], v[76:79], v[80:83], a[192:207]\n"
"v_xor_b32 v8, 0x10000, v8\n"
"v_xor_b32 v9, 0x10000, v9\n"
"v_mfma_f32_32x32x16_bf16 a[208:223], v[76:79], v[84:87], a[208:223]\n"
"v_xor_b32 v10, 0x10000, v10\n"
"v_xor_b32 v11, 0x10000, v11\n"
"v_mfma_f32_32x32x16_bf16 a[224:239], v[76:79], v[88:91], a[224:239]\n"
"v_xor_b32 v12, 0x10000, v12\n"
"v_xor_b32 v13, 0x10000, v13\n"
"v_mfma_f32_32x32x16_bf16 a[240:255], v[76:79], v[92:95], a[240:255]\n"
"s_add_u32 s53, s53, 128\n"
"s_add_u32 s59, s59, 524288\n"
"s_waitcnt vmcnt(1) lgkmcnt(0)\n"
"s_barrier\n"
"s_cmp_lg_u32 s29, 0\n"
"s_cbranch_scc1 .Lsig3\n"
"s_lshr_b32 s49, s28, 4\n"
"s_lshl_b32 s49, s49, 8\n"
"s_and_b32 s48, s28, 15\n"
"s_lshl_b32 s48, s48, 2\n"
"s_add_u32 s49, s49, s48\n"
"s_add_u32 s49, s49, 16640\n"
"v_mul_i32_i24 v212, 256, v0\n"
"v_add_u32 v212, s49, v212\n"
"v_mov_b32 v213, s11\n"
"s_mov_b64 exec, 1\n"
"global_store_dword v212, v213, s[50:51] sc1\n"
"s_mov_b64 exec, -1\n"
".Lsig3:\n"
"s_add_u32 m0, s64, 0\n"
"v_mfma_f32_32x32x16_bf16 a[0:15], v[96:99], v[112:115], a[0:15]\n"
"buffer_load_dwordx4 v16, s[16:19], s53 offen lds\n"
"ds_read_b128 v[64:67], v8 offset:0\n"
"v_mfma_f32_32x32x16_bf16 a[16:31], v[96:99], v[116:119], a[16:31]\n"
"ds_read_b64_tr_b16 v[80:81], v12 offset:0\n"
"s_add_u32 m0, s72, 0\n"
"v_mfma_f32_32x32x16_bf16 a[32:47], v[96:99], v[120:123], a[32:47]\n"
"buffer_load_dwordx4 v24, s[20:23], s59 offen lds\n"
"ds_read_b64_tr_b16 v[82:83], v12 offset:512\n"
"v_mfma_f32_32x32x16_bf16 a[48:63], v[96:99], v[124:127], a[48:63]\n"
"ds_read_b64_tr_b16 v[84:85], v13 offset:0\n"
"s_add_u32 m0, s65, 0\n"
"v_mfma_f32_32x32x16_bf16 a[64:79], v[100:103], v[112:115], a[64:79]\n"
"buffer_load_dwordx4 v17, s[16:19], s53 offen lds\n"
"ds_read_b64_tr_b16 v[86:87], v13 offset:512\n"
"v_mfma_f32_32x32x16_bf16 a[80:95], v[100:103], v[116:119], a[80:95]\n"
"ds_read_b64_tr_b16 v[88:89], v12 offset:8192\n"
"s_add_u32 m0, s73, 0\n"
"v_mfma_f32_32x32x16_bf16 a[96:111], v[100:103], v[120:123], a[96:111]\n"
"buffer_load_dwordx4 v25, s[20:23], s59 offen lds\n"
"ds_read_b64_tr_b16 v[90:91], v12 offset:8704\n"
"v_mfma_f32_32x32x16_bf16 a[112:127], v[100:103], v[124:127], a[112:127]\n"
"ds_read_b64_tr_b16 v[92:93], v13 offset:8192\n"
"s_add_u32 m0, s66, 0\n"
"v_mfma_f32_32x32x16_bf16 a[128:143], v[104:107], v[112:115], a[128:143]\n"
"buffer_load_dwordx4 v18, s[16:19], s53 offen lds\n"
"ds_read_b64_tr_b16 v[94:95], v13 offset:8704\n"
"v_mfma_f32_32x32x16_bf16 a[144:159], v[104:107], v[116:119], a[144:159]\n"
"ds_read_b128 v[68:71], v8 offset:4096\n"
"s_add_u32 m0, s74, 0\n"
"v_mfma_f32_32x32x16_bf16 a[160:175], v[104:107], v[120:123], a[160:175]\n"
"buffer_load_dwordx4 v26, s[20:23], s59 offen lds\n"
"ds_read_b128 v[72:75], v8 offset:8192\n"
"v_mfma_f32_32x32x16_bf16 a[176:191], v[104:107], v[124:127], a[176:191]\n"
"ds_read_b128 v[76:79], v8 offset:12288\n"
"s_add_u32 m0, s67, 0\n"
"v_mfma_f32_32x32x16_bf16 a[192:207], v[108:111], v[112:115], a[192:207]\n"
"buffer_load_dwordx4 v19, s[16:19], s53 offen lds\n"
"v_mfma_f32_32x32x16_bf16 a[208:223], v[108:111], v[116:119], a[208:223]\n"
"s_mov_b32 s44, s50\n"
"s_and_b32 s45, s51, 0xffff\n"
"s_mov_b32 s46, 53248\n"
"s_mov_b32 s47, 0x20000\n"
"s_add_u32 m0, s75, 0\n"
"v_mfma_f32_32x32x16_bf16 a[224:239], v[108:111], v[120:123], a[224:239]\n"
"buffer_load_dwordx4 v27, s[20:23], s59 offen lds\n"
"s_cmp_eq_u32 s29, 0\n"
"s_cselect_b32 s34, 0, 1073741824\n"
"v_mfma_f32_32x32x16_bf16 a[240:255], v[108:111], v[124:127], a[240:255]\n"
"s_waitcnt lgkmcnt(0)\n"
"s_add_u32 m0, s68, 0\n"
"v_mfma_f32_32x32x16_bf16 a[0:15], v[64:67], v[80:83], a[0:15]\n"
"buffer_load_dwordx4 v20, s[16:19], s53 offen lds\n"
"ds_read_b128 v[96:99], v9 offset:0\n"
"v_mfma_f32_32x32x16_bf16 a[16:31], v[64:67], v[84:87], a[16:31]\n"
"ds_read_b64_tr_b16 v[112:113], v12 offset:2048\n"
"s_add_u32 m0, s76, 0\n"
"v_mfma_f32_32x32x16_bf16 a[32:47], v[64:67], v[88:91], a[32:47]\n"
"buffer_load_dwordx4 v28, s[20:23], s59 offen lds\n"
"ds_read_b64_tr_b16 v[114:115], v12 offset:2560\n"
"v_mfma_f32_32x32x16_bf16 a[48:63], v[64:67], v[92:95], a[48:63]\n"
"ds_read_b64_tr_b16 v[116:117], v13 offset:2048\n"
"s_add_u32 m0, s69, 0\n"
"v_mfma_f32_32x32x16_bf16 a[64:79], v[68:71], v[80:83], a[64:79]\n"
"buffer_load_dwordx4 v21, s[16:19], s53 offen lds\n"
"ds_read_b64_tr_b16 v[118:119], v13 offset:2560\n"
"v_mfma_f32_32x32x16_bf16 a[80:95], v[68:71], v[84:87], a[80:95]\n"
"ds_read_b64_tr_b16 v[120:121], v12 offset:10240\n"
"s_add_u32 m0, s77, 0\n"
"v_mfma_f32_32x32x16_bf16 a[96:111], v[68:71], v[88:91], a[96:111]\n"
"buffer_load_dwordx4 v29, s[20:23], s59 offen lds\n"
"ds_read_b64_tr_b16 v[122:123], v12 offset:10752\n"
"v_mfma_f32_32x32x16_bf16 a[112:127], v[68:71], v[92:95], a[112:127]\n"
"ds_read_b64_tr_b16 v[124:125], v13 offset:10240\n"
"s_add_u32 m0, s70, 0\n"
"v_mfma_f32_32x32x16_bf16 a[128:143], v[72:75], v[80:83], a[128:143]\n"
"buffer_load_dwordx4 v22, s[16:19], s53 offen lds\n"
"ds_read_b64_tr_b16 v[126:127], v13 offset:10752\n"
"v_mfma_f32_32x32x16_bf16 a[144:159], v[72:75], v[84:87], a[144:159]\n"
"ds_read_b128 v[100:103], v9 offset:4096\n"
"s_add_u32 m0, s78, 0\n"
"v_mfma_f32_32x32x16_bf16 a[160:175], v[72:75], v[88:91], a[160:175]\n"
"buffer_load_dwordx4 v30, s[20:23], s59 offen lds\n"
"ds_read_b128 v[104:107], v9 offset:8192\n"
"v_mfma_f32_32x32x16_bf16 a[176:191], v[72:75], v[92:95], a[176:191]\n"
"ds_read_b128 v[108:111], v9 offset:12288\n"
"s_add_u32 m0, s71, 0\n"
"v_mfma_f32_32x32x16_bf16 a[192:207], v[76:79], v[80:83], a[192:207]\n"
"buffer_load_dwordx4 v23, s[16:19], s53 offen lds\n"
"v_mfma_f32_32x32x16_bf16 a[208:223], v[76:79], v[84:87], a[208:223]\n"
"s_add_u32 m0, s79, 0\n"
"v_mfma_f32_32x32x16_bf16 a[224:239], v[76:79], v[88:91], a[224:239]\n"
"buffer_load_dwordx4 v31, s[20:23], s59 offen lds\n"
"v_mfma_f32_32x32x16_bf16 a[240:255], v[76:79], v[92:95], a[240:255]\n"
"s_waitcnt lgkmcnt(0)\n"
"v_mfma_f32_32x32x16_bf16 a[0:15], v[96:99], v[112:115], a[0:15]\n"
"ds_read_b128 v[64:67], v10 offset:0\n"
"v_mfma_f32_32x32x16_bf16 a[16:31], v[96:99], v[116:119], a[16:31]\n"
"ds_read_b64_tr_b16 v[80:81], v12 offset:4096\n"
"v_mfma_f32_32x32x16_bf16 a[32:47], v[96:99], v[120:123], a[32:47]\n"
"ds_read_b64_tr_b16 v[82:83], v12 offset:4608\n"
"v_mfma_f32_32x32x16_bf16 a[48:63], v[96:99], v[124:127], a[48:63]\n"
"ds_read_b64_tr_b16 v[84:85], v13 offset:4096\n"
"v_mfma_f32_32x32x16_bf16 a[64:79], v[100:103], v[112:115], a[64:79]\n"
"ds_read_b64_tr_b16 v[86:87], v13 offset:4608\n"
"v_mfma_f32_32x32x16_bf16 a[80:95], v[100:103], v[116:119], a[80:95]\n"
"ds_read_b64_tr_b16 v[88:89], v12 offset:12288\n"
"v_mfma_f32_32x32x16_bf16 a[96:111], v[100:103], v[120:123], a[96:111]\n"
"ds_read_b64_tr_b16 v[90:91], v12 offset:12800\n"
"v_mfma_f32_32x32x16_bf16 a[112:127], v[100:103], v[124:127], a[112:127]\n"
"ds_read_b64_tr_b16 v[92:93], v13 offset:12288\n"
"v_mfma_f32_32x32x16_bf16 a[128:143], v[104:107], v[112:115], a[128:143]\n"
"ds_read_b64_tr_b16 v[94:95], v13 offset:12800\n"
"v_mfma_f32_32x32x16_bf16 a[144:159], v[104:107], v[116:119], a[144:159]\n"
"ds_read_b128 v[68:71], v10 offset:4096\n"
"v_mfma_f32_32x32x16_bf16 a[160:175], v[104:107], v[120:123], a[160:175]\n"
"ds_read_b128 v[72:75], v10 offset:8192\n"
"v_mfma_f32_32x32x16_bf16 a[176:191], v[104:107], v[124:127], a[176:191]\n"
"ds_read_b128 v[76:79], v10 offset:12288\n"
"v_mfma_f32_32x32x16_bf16 a[192:207], v[108:111], v[112:115], a[192:207]\n"
"v_mfma_f32_32x32x16_bf16 a[208:223], v[108:111], v[116:119], a[208:223]\n"
"v_mfma_f32_32x32x16_bf16 a[224:239], v[108:111], v[120:123], a[224:239]\n"
"v_mfma_f32_32x32x16_bf16 a[240:255], v[108:111], v[124:127], a[240:255]\n"
"s_waitcnt lgkmcnt(0)\n"
"v_mfma_f32_32x32x16_bf16 a[0:15], v[64:67], v[80:83], a[0:15]\n"
"ds_read_b128 v[96:99], v11 offset:0\n"
"buffer_load_dword v43, v42, s[44:47], s34 offen sc1\n"
"v_lshlrev_b32 v144, 16, v180\n"
"v_and_b32 v145, 0xffff0000, v180\n"
"v_mfma_f32_32x32x16_bf16 a[16:31], v[64:67], v[84:87], a[16:31]\n"
"ds_read_b64_tr_b16 v[112:113], v12 offset:6144\n"
"v_lshlrev_b32 v146, 16, v181\n"
"v_and_b32 v147, 0xffff0000, v181\n"
"v_lshlrev_b32 v148, 16, v182\n"
"v_and_b32 v149, 0xffff0000, v182\n"
"v_mfma_f32_32x32x16_bf16 a[32:47], v[64:67], v[88:91], a[32:47]\n"
"ds_read_b64_tr_b16 v[114:115], v12 offset:6656\n"
"v_lshlrev_b32 v150, 16, v183\n"
"v_and_b32 v151, 0xffff0000, v183\n"
"v_lshlrev_b32 v160, 16, v184\n"
"v_and_b32 v161, 0xffff0000, v184\n"
"v_mfma_f32_32x32x16_bf16 a[48:63], v[64:67], v[92:95], a[48:63]\n"
"ds_read_b64_tr_b16 v[116:117], v13 offset:6144\n"
"v_add_f32 v144, v144, v160\n"
"v_add_f32 v145, v145, v161\n"
"v_lshlrev_b32 v160, 16, v185\n"
"v_and_b32 v161, 0xffff0000, v185\n"
"v_mfma_f32_32x32x16_bf16 a[64:79], v[68:71], v[80:83], a[64:79]\n"
"ds_read_b64_tr_b16 v[118:119], v13 offset:6656\n"
"v_add_f32 v146, v146, v160\n"
"v_add_f32 v147, v147, v161\n"
"v_lshlrev_b32 v160, 16, v186\n"
"v_and_b32 v161, 0xffff0000, v186\n"
"v_mfma_f32_32x32x16_bf16 a[80:95], v[68:71], v[84:87], a[80:95]\n"
"ds_read_b64_tr_b16 v[120:121], v12 offset:14336\n"
"v_add_f32 v148, v148, v160\n"
"v_add_f32 v149, v149, v161\n"
"v_lshlrev_b32 v160, 16, v187\n"
"v_and_b32 v161, 0xffff0000, v187\n"
"v_mfma_f32_32x32x16_bf16 a[96:111], v[68:71], v[88:91], a[96:111]\n"
"ds_read_b64_tr_b16 v[122:123], v12 offset:14848\n"
"v_add_f32 v150, v150, v160\n"
"v_add_f32 v151, v151, v161\n"
"v_lshlrev_b32 v160, 16, v188\n"
"v_and_b32 v161, 0xffff0000, v188\n"
"v_mfma_f32_32x32x16_bf16 a[112:127], v[68:71], v[92:95], a[112:127]\n"
"ds_read_b64_tr_b16 v[124:125], v13 offset:14336\n"
"v_add_f32 v144, v144, v160\n"
"v_add_f32 v145, v145, v161\n"
"v_lshlrev_b32 v160, 16, v189\n"
"v_and_b32 v161, 0xffff0000, v189\n"
"v_mfma_f32_32x32x16_bf16 a[128:143], v[72:75], v[80:83], a[128:143]\n"
"ds_read_b64_tr_b16 v[126:127], v13 offset:14848\n"
"v_add_f32 v146, v146, v160\n"
"v_add_f32 v147, v147, v161\n"
"v_lshlrev_b32 v160, 16, v190\n"
"v_and_b32 v161, 0xffff0000, v190\n"
"v_mfma_f32_32x32x16_bf16 a[144:159], v[72:75], v[84:87], a[144:159]\n"
"ds_read_b128 v[100:103], v11 offset:4096\n"
"v_add_f32 v148, v148, v160\n"
"v_add_f32 v149, v149, v161\n"
"v_lshlrev_b32 v160, 16, v191\n"
"v_and_b32 v161, 0xffff0000, v191\n"
"v_mfma_f32_32x32x16_bf16 a[160:175], v[72:75], v[88:91], a[160:175]\n"
"ds_read_b128 v[104:107], v11 offset:8192\n"
"v_add_f32 v150, v150, v160\n"
"v_add_f32 v151, v151, v161\n"
"v_lshlrev_b32 v160, 16, v192\n"
"v_and_b32 v161, 0xffff0000, v192\n"
"v_mfma_f32_32x32x16_bf16 a[176:191], v[72:75], v[92:95], a[176:191]\n"
"ds_read_b128 v[108:111], v11 offset:12288\n"
"v_add_f32 v144, v144, v160\n"
"v_add_f32 v145, v145, v161\n"
"v_lshlrev_b32 v160, 16, v193\n"
"v_and_b32 v161, 0xffff0000, v193\n"
"v_mfma_f32_32x32x16_bf16 a[192:207], v[76:79], v[80:83], a[192:207]\n"
"v_xor_b32 v8, 0x10000, v8\n"
"v_xor_b32 v9, 0x10000, v9\n"
"v_add_f32 v146, v146, v160\n"
"v_add_f32 v147, v147, v161\n"
"v_lshlrev_b32 v160, 16, v194\n"
"v_mfma_f32_32x32x16_bf16 a[208:223], v[76:79], v[84:87], a[208:223]\n"
"v_xor_b32 v10, 0x10000, v10\n"
"v_xor_b32 v11, 0x10000, v11\n"
"v_and_b32 v161, 0xffff0000, v194\n"
"v_add_f32 v148, v148, v160\n"
"v_add_f32 v149, v149, v161\n"
"v_mfma_f32_32x32x16_bf16 a[224:239], v[76:79], v[88:91], a[224:239]\n"
"v_xor_b32 v12, 0x10000, v12\n"
"v_xor_b32 v13, 0x10000, v13\n"
"v_lshlrev_b32 v160, 16, v195\n"
"v_and_b32 v161, 0xffff0000, v195\n"
"v_add_f32 v150, v150, v160\n"
"v_mfma_f32_32x32x16_bf16 a[240:255], v[76:79], v[92:95], a[240:255]\n"
"v_add_f32 v151, v151, v161\n"
"v_lshlrev_b32 v160, 16, v196\n"
"v_and_b32 v161, 0xffff0000, v196\n"
"s_add_u32 s53, s53, 128\n"
"s_add_u32 s59, s59, 524288\n"
"s_waitcnt vmcnt(1) lgkmcnt(0)\n"
"s_barrier\n"
"s_add_u32 m0, s64, 65536\n"
"v_mfma_f32_32x32x16_bf16 a[0:15], v[96:99], v[112:115], a[0:15]\n"
"buffer_load_dwordx4 v16, s[16:19], s53 offen lds\n"
"ds_read_b128 v[64:67], v8 offset:0\n"
"v_add_f32 v144, v144, v160\n"
"v_add_f32 v145, v145, v161\n"
"v_mfma_f32_32x32x16_bf16 a[16:31], v[96:99], v[116:119], a[16:31]\n"
"ds_read_b64_tr_b16 v[80:81], v12 offset:0\n"
"v_lshlrev_b32 v160, 16, v197\n"
"v_and_b32 v161, 0xffff0000, v197\n"
"v_add_f32 v146, v146, v160\n"
"s_add_u32 m0, s72, 65536\n"
"v_mfma_f32_32x32x16_bf16 a[32:47], v[96:99], v[120:123], a[32:47]\n"
"buffer_load_dwordx4 v24, s[20:23], s59 offen lds\n"
"ds_read_b64_tr_b16 v[82:83], v12 offset:512\n"
"v_add_f32 v147, v147, v161\n"
"v_lshlrev_b32 v160, 16, v198\n"
"v_mfma_f32_32x32x16_bf16 a[48:63], v[96:99], v[124:127], a[48:63]\n"
"ds_read_b64_tr_b16 v[84:85], v13 offset:0\n"
"v_and_b32 v161, 0xffff0000, v198\n"
"v_add_f32 v148, v148, v160\n"
"v_add_f32 v149, v149, v161\n"
"s_add_u32 m0, s65, 65536\n"
"v_mfma_f32_32x32x16_bf16 a[64:79], v[100:103], v[112:115], a[64:79]\n"
"buffer_load_dwordx4 v17, s[16:19], s53 offen lds\n"
"ds_read_b64_tr_b16 v[86:87], v13 offset:512\n"
"v_lshlrev_b32 v160, 16, v199\n"
"v_and_b32 v161, 0xffff0000, v199\n"
"v_mfma_f32_32x32x16_bf16 a[80:95], v[100:103], v[116:119], a[80:95]\n"
"ds_read_b64_tr_b16 v[88:89], v12 offset:8192\n"
"v_add_f32 v150, v150, v160\n"
"v_add_f32 v151, v151, v161\n"
"v_lshlrev_b32 v160, 16, v200\n"
"s_add_u32 m0, s73, 65536\n"
"v_mfma_f32_32x32x16_bf16 a[96:111], v[100:103], v[120:123], a[96:111]\n"
"buffer_load_dwordx4 v25, s[20:23], s59 offen lds\n"
"ds_read_b64_tr_b16 v[90:91], v12 offset:8704\n"
"v_and_b32 v161, 0xffff0000, v200\n"
"v_add_f32 v144, v144, v160\n"
"v_mfma_f32_32x32x16_bf16 a[112:127], v[100:103], v[124:127], a[112:127]\n"
"ds_read_b64_tr_b16 v[92:93], v13 offset:8192\n"
"v_add_f32 v145, v145, v161\n"
"v_lshlrev_b32 v160, 16, v201\n"
"v_and_b32 v161, 0xffff0000, v201\n"
"s_add_u32 m0, s66, 65536\n"
"v_mfma_f32_32x32x16_bf16 a[128:143], v[104:107], v[112:115], a[128:143]\n"
"buffer_load_dwordx4 v18, s[16:19], s53 offen lds\n"
"ds_read_b64_tr_b16 v[94:95], v13 offset:8704\n"
"v_add_f32 v146, v146, v160\n"
"v_add_f32 v147, v147, v161\n"
"v_mfma_f32_32x32x16_bf16 a[144:159], v[104:107], v[116:119], a[144:159]\n"
"ds_read_b128 v[68:71], v8 offset:4096\n"
"v_lshlrev_b32 v160, 16, v202\n"
"v_and_b32 v161, 0xffff0000, v202\n"
"v_add_f32 v148, v148, v160\n"
"s_add_u32 m0, s74, 65536\n"
"v_mfma_f32_32x32x16_bf16 a[160:175], v[104:107], v[120:123], a[160:175]\n"
"buffer_load_dwordx4 v26, s[20:23], s59 offen lds\n"
"ds_read_b128 v[72:75], v8 offset:8192\n"
"v_add_f32 v149, v149, v161\n"
"v_lshlrev_b32 v160, 16, v203\n"
"v_mfma_f32_32x32x16_bf16 a[176:191], v[104:107], v[124:127], a[176:191]\n"
"ds_read_b128 v[76:79], v8 offset:12288\n"
"v_and_b32 v161, 0xffff0000, v203\n"
"v_add_f32 v150, v150, v160\n"
"v_add_f32 v151, v151, v161\n"
"s_add_u32 m0, s67, 65536\n"
"v_mfma_f32_32x32x16_bf16 a[192:207], v[108:111], v[112:115], a[192:207]\n"
"buffer_load_dwordx4 v19, s[16:19], s53 offen lds\n"
"v_lshlrev_b32 v160, 16, v204\n"
"v_and_b32 v161, 0xffff0000, v204\n"
"v_add_f32 v144, v144, v160\n"
"v_mfma_f32_32x32x16_bf16 a[208:223], v[108:111], v[116:119], a[208:223]\n"
"v_add_f32 v145, v145, v161\n"
"v_lshlrev_b32 v160, 16, v205\n"
"v_and_b32 v161, 0xffff0000, v205\n"
"v_add_f32 v146, v146, v160\n"
"s_add_u32 m0, s75, 65536\n"
"v_mfma_f32_32x32x16_bf16 a[224:239], v[108:111], v[120:123], a[224:239]\n"
"buffer_load_dwordx4 v27, s[20:23], s59 offen lds\n"
"v_add_f32 v147, v147, v161\n"
"v_lshlrev_b32 v160, 16, v206\n"
"v_and_b32 v161, 0xffff0000, v206\n"
"v_mfma_f32_32x32x16_bf16 a[240:255], v[108:111], v[124:127], a[240:255]\n"
"v_add_f32 v148, v148, v160\n"
"v_add_f32 v149, v149, v161\n"
"v_lshlrev_b32 v160, 16, v207\n"
"s_waitcnt lgkmcnt(0)\n"
"s_add_u32 m0, s68, 65536\n"
"v_mfma_f32_32x32x16_bf16 a[0:15], v[64:67], v[80:83], a[0:15]\n"
"buffer_load_dwordx4 v20, s[16:19], s53 offen lds\n"
"ds_read_b128 v[96:99], v9 offset:0\n"
"v_and_b32 v161, 0xffff0000, v207\n"
"v_add_f32 v150, v150, v160\n"
"v_mfma_f32_32x32x16_bf16 a[16:31], v[64:67], v[84:87], a[16:31]\n"
"ds_read_b64_tr_b16 v[112:113], v12 offset:2048\n"
"v_add_f32 v151, v151, v161\n"
"v_lshlrev_b32 v160, 16, v208\n"
"v_and_b32 v161, 0xffff0000, v208\n"
"s_add_u32 m0, s76, 65536\n"
"v_mfma_f32_32x32x16_bf16 a[32:47], v[64:67], v[88:91], a[32:47]\n"
"buffer_load_dwordx4 v28, s[20:23], s59 offen lds\n"
"ds_read_b64_tr_b16 v[114:115], v12 offset:2560\n"
"v_add_f32 v144, v144, v160\n"
"v_add_f32 v145, v145, v161\n"
"v_mfma_f32_32x32x16_bf16 a[48:63], v[64:67], v[92:95], a[48:63]\n"
"ds_read_b64_tr_b16 v[116:117], v13 offset:2048\n"
"v_lshlrev_b32 v160, 16, v209\n"
"v_and_b32 v161, 0xffff0000, v209\n"
"v_add_f32 v146, v146, v160\n"
"s_add_u32 m0, s69, 65536\n"
"v_mfma_f32_32x32x16_bf16 a[64:79], v[68:71], v[80:83], a[64:79]\n"
"buffer_load_dwordx4 v21, s[16:19], s53 offen lds\n"
"ds_read_b64_tr_b16 v[118:119], v13 offset:2560\n"
"v_add_f32 v147, v147, v161\n"
"v_lshlrev_b32 v160, 16, v210\n"
"v_mfma_f32_32x32x16_bf16 a[80:95], v[68:71], v[84:87], a[80:95]\n"
"ds_read_b64_tr_b16 v[120:121], v12 offset:10240\n"
"v_and_b32 v161, 0xffff0000, v210\n"
"v_add_f32 v148, v148, v160\n"
"v_add_f32 v149, v149, v161\n"
"s_add_u32 m0, s77, 65536\n"
"v_mfma_f32_32x32x16_bf16 a[96:111], v[68:71], v[88:91], a[96:111]\n"
"buffer_load_dwordx4 v29, s[20:23], s59 offen lds\n"
"ds_read_b64_tr_b16 v[122:123], v12 offset:10752\n"
"v_lshlrev_b32 v160, 16, v211\n"
"v_and_b32 v161, 0xffff0000, v211\n"
"v_mfma_f32_32x32x16_bf16 a[112:127], v[68:71], v[92:95], a[112:127]\n"
"ds_read_b64_tr_b16 v[124:125], v13 offset:10240\n"
"v_add_f32 v150, v150, v160\n"
"v_add_f32 v151, v151, v161\n"
"s_add_u32 m0, s70, 65536\n"
"v_mfma_f32_32x32x16_bf16 a[128:143], v[72:75], v[80:83], a[128:143]\n"
"buffer_load_dwordx4 v22, s[16:19], s53 offen lds\n"
"ds_read_b64_tr_b16 v[126:127], v13 offset:10752\n"
"s_nop 1\n"
"v_mfma_f32_32x32x16_bf16 a[144:159], v[72:75], v[84:87], a[144:159]\n"
"ds_read_b128 v[100:103], v9 offset:4096\n"
"v_add_f32_dpp v144, v144, v144 row_ror:4 row_mask:0xf bank_mask:0xf\n"
"v_add_f32_dpp v145, v145, v145 row_ror:4 row_mask:0xf bank_mask:0xf\n"
"v_add_f32_dpp v146, v146, v146 row_ror:4 row_mask:0xf bank_mask:0xf\n"
"s_add_u32 m0, s78, 65536\n"
"v_mfma_f32_32x32x16_bf16 a[160:175], v[72:75], v[88:91], a[160:175]\n"
"buffer_load_dwordx4 v30, s[20:23], s59 offen lds\n"
"ds_read_b128 v[104:107], v9 offset:8192\n"
"v_add_f32_dpp v147, v147, v147 row_ror:4 row_mask:0xf bank_mask:0xf\n"
"v_add_f32_dpp v148, v148, v148 row_ror:4 row_mask:0xf bank_mask:0xf\n"
"v_mfma_f32_32x32x16_bf16 a[176:191], v[72:75], v[92:95], a[176:191]\n"
"ds_read_b128 v[108:111], v9 offset:12288\n"
"v_add_f32_dpp v149, v149, v149 row_ror:4 row_mask:0xf bank_mask:0xf\n"
"v_add_f32_dpp v150, v150, v150 row_ror:4 row_mask:0xf bank_mask:0xf\n"
"v_add_f32_dpp v151, v151, v151 row_ror:4 row_mask:0xf bank_mask:0xf\n"
"s_add_u32 m0, s71, 65536\n"
"v_mfma_f32_32x32x16_bf16 a[192:207], v[76:79], v[80:83], a[192:207]\n"
"buffer_load_dwordx4 v23, s[16:19], s53 offen lds\n"
"s_nop 1\n"
"v_add_f32_dpp v144, v144, v144 row_ror:8 row_mask:0xf bank_mask:0xf\n"
"v_mfma_f32_32x32x16_bf16 a[208:223], v[76:79], v[84:87], a[208:223]\n"
"v_add_f32_dpp v145, v145, v145 row_ror:8 row_mask:0xf bank_mask:0xf\n"
"v_add_f32_dpp v146, v146, v146 row_ror:8 row_mask:0xf bank_mask:0xf\n"
"v_add_f32_dpp v147, v147, v147 row_ror:8 row_mask:0xf bank_mask:0xf\n"
"v_add_f32_dpp v148, v148, v148 row_ror:8 row_mask:0xf bank_mask:0xf\n"
"s_add_u32 m0, s79, 65536\n"
"v_mfma_f32_32x32x16_bf16 a[224:239], v[76:79], v[88:91], a[224:239]\n"
"buffer_load_dwordx4 v31, s[20:23], s59 offen lds\n"
"v_add_f32_dpp v149, v149, v149 row_ror:8 row_mask:0xf bank_mask:0xf\n"
"v_add_f32_dpp v150, v150, v150 row_ror:8 row_mask:0xf bank_mask:0xf\n"
"v_add_f32_dpp v151, v151, v151 row_ror:8 row_mask:0xf bank_mask:0xf\n"
"v_mfma_f32_32x32x16_bf16 a[240:255], v[76:79], v[92:95], a[240:255]\n"
"v_mov_b32 v152, v144\n"
"v_mov_b32 v153, v145\n"
"v_mov_b32 v154, v146\n"
"s_waitcnt lgkmcnt(0)\n"
"v_mfma_f32_32x32x16_bf16 a[0:15], v[96:99], v[112:115], a[0:15]\n"
"ds_read_b128 v[64:67], v10 offset:0\n"
"v_mov_b32 v155, v147\n"
"v_mov_b32 v156, v148\n"
"v_mov_b32 v157, v149\n"
"v_mov_b32 v158, v150\n"
"v_mfma_f32_32x32x16_bf16 a[16:31], v[96:99], v[116:119], a[16:31]\n"
"ds_read_b64_tr_b16 v[80:81], v12 offset:4096\n"
"v_mov_b32 v159, v151\n"
"s_nop 1\n"
"v_permlane16_swap_b32 v144, v152\n"
"v_mfma_f32_32x32x16_bf16 a[32:47], v[96:99], v[120:123], a[32:47]\n"
"ds_read_b64_tr_b16 v[82:83], v12 offset:4608\n"
"v_permlane16_swap_b32 v145, v153\n"
"v_permlane16_swap_b32 v146, v154\n"
"v_permlane16_swap_b32 v147, v155\n"
"v_permlane16_swap_b32 v148, v156\n"
"v_mfma_f32_32x32x16_bf16 a[48:63], v[96:99], v[124:127], a[48:63]\n"
"ds_read_b64_tr_b16 v[84:85], v13 offset:4096\n"
"v_permlane16_swap_b32 v149, v157\n"
"v_permlane16_swap_b32 v150, v158\n"
"v_permlane16_swap_b32 v151, v159\n"
"v_add_f32 v144, v144, v152\n"
"v_mfma_f32_32x32x16_bf16 a[64:79], v[100:103], v[112:115], a[64:79]\n"
"ds_read_b64_tr_b16 v[86:87], v13 offset:4608\n"
"v_add_f32 v145, v145, v153\n"
"v_add_f32 v146, v146, v154\n"
"v_add_f32 v147, v147, v155\n"
"v_add_f32 v148, v148, v156\n"
"v_mfma_f32_32x32x16_bf16 a[80:95], v[100:103], v[116:119], a[80:95]\n"
"ds_read_b64_tr_b16 v[88:89], v12 offset:12288\n"
"v_add_f32 v149, v149, v157\n"
"v_add_f32 v150, v150, v158\n"
"v_add_f32 v151, v151, v159\n"
"v_mov_b32 v152, v144\n"
"v_mfma_f32_32x32x16_bf16 a[96:111], v[100:103], v[120:123], a[96:111]\n"
"ds_read_b64_tr_b16 v[90:91], v12 offset:12800\n"
"v_mov_b32 v153, v145\n"
"v_mov_b32 v154, v146\n"
"v_mov_b32 v155, v147\n"
"v_mov_b32 v156, v148\n"
"v_mfma_f32_32x32x16_bf16 a[112:127], v[100:103], v[124:127], a[112:127]\n"
"ds_read_b64_tr_b16 v[92:93], v13 offset:12288\n"
"v_mov_b32 v157, v149\n"
"v_mov_b32 v158, v150\n"
"v_mov_b32 v159, v151\n"
"v_mfma_f32_32x32x16_bf16 a[128:143], v[104:107], v[112:115], a[128:143]\n"
"ds_read_b64_tr_b16 v[94:95], v13 offset:12800\n"
"s_nop 1\n"
"v_permlane32_swap_b32 v144, v152\n"
"v_permlane32_swap_b32 v145, v153\n"
"v_mfma_f32_32x32x16_bf16 a[144:159], v[104:107], v[116:119], a[144:159]\n"
"ds_read_b128 v[68:71], v10 offset:4096\n"
"v_permlane32_swap_b32 v146, v154\n"
"v_permlane32_swap_b32 v147, v155\n"
"v_permlane32_swap_b32 v148, v156\n"
"v_permlane32_swap_b32 v149, v157\n"
"v_mfma_f32_32x32x16_bf16 a[160:175], v[104:107], v[120:123], a[160:175]\n"
"ds_read_b128 v[72:75], v10 offset:8192\n"
"v_permlane32_swap_b32 v150, v158\n"
"v_permlane32_swap_b32 v151, v159\n"
"v_add_f32 v144, v144, v152\n"
"v_add_f32 v145, v145, v153\n"
"v_mfma_f32_32x32x16_bf16 a[176:191], v[104:107], v[124:127], a[176:191]\n"
"ds_read_b128 v[76:79], v10 offset:12288\n"
"v_add_f32 v146, v146, v154\n"
"v_add_f32 v147, v147, v155\n"
"v_add_f32 v148, v148, v156\n"
"v_add_f32 v149, v149, v157\n"
"v_mfma_f32_32x32x16_bf16 a[192:207], v[108:111], v[112:115], a[192:207]\n"
"v_add_f32 v150, v150, v158\n"
"v_add_f32 v151, v151, v159\n"
"v_and_b32 v176, 3, v0\n"
"v_lshlrev_b32 v176, 5, v176\n"
"s_lshl_b32 s84, s29, 7\n"
"v_mfma_f32_32x32x16_bf16 a[208:223], v[108:111], v[116:119], a[208:223]\n"
"s_add_u32 s84, s84, 151552\n"
"v_add_u32 v176, s84, v176\n"
"v_mfma_f32_32x32x16_bf16 a[224:239], v[108:111], v[120:123], a[224:239]\n"
"ds_write_b128 v176, v[144:147]\n"
"ds_write_b128 v176, v[148:151] offset:16\n"
"v_and_b32 v177, 31, v0\n"
"v_lshlrev_b32 v177, 2, v177\n"
"v_add_u32 v178, 151552, v177\n"
"v_mfma_f32_32x32x16_bf16 a[240:255], v[108:111], v[124:127], a[240:255]\n"
"s_add_u32 s80, s50, 74711040\n"
"s_addc_u32 s81, s51, 0\n"
"s_and_b32 s81, s81, 0xffff\n"
"s_waitcnt lgkmcnt(0)\n"
"v_mfma_f32_32x32x16_bf16 a[0:15], v[64:67], v[80:83], a[0:15]\n"
"ds_read_b128 v[96:99], v11 offset:0\n"
"s_mov_b32 s82, 5504\n"
"s_mov_b32 s83, 0x20000\n"
"s_lshl_b32 s84, s28, 7\n"
"s_lshl_b32 s85, s29, 24\n"
"v_mfma_f32_32x32x16_bf16 a[16:31], v[64:67], v[84:87], a[16:31]\n"
"ds_read_b64_tr_b16 v[112:113], v12 offset:6144\n"
"s_add_u32 s84, s84, s85\n"
"v_mfma_f32_32x32x16_bf16 a[32:47], v[64:67], v[88:91], a[32:47]\n"
"ds_read_b64_tr_b16 v[114:115], v12 offset:6656\n"
"v_mfma_f32_32x32x16_bf16 a[48:63], v[64:67], v[92:95], a[48:63]\n"
"ds_read_b64_tr_b16 v[116:117], v13 offset:6144\n"
"v_mfma_f32_32x32x16_bf16 a[64:79], v[68:71], v[80:83], a[64:79]\n"
"ds_read_b64_tr_b16 v[118:119], v13 offset:6656\n"
"v_mfma_f32_32x32x16_bf16 a[80:95], v[68:71], v[84:87], a[80:95]\n"
"ds_read_b64_tr_b16 v[120:121], v12 offset:14336\n"
"v_mfma_f32_32x32x16_bf16 a[96:111], v[68:71], v[88:91], a[96:111]\n"
"ds_read_b64_tr_b16 v[122:123], v12 offset:14848\n"
"v_mfma_f32_32x32x16_bf16 a[112:127], v[68:71], v[92:95], a[112:127]\n"
"ds_read_b64_tr_b16 v[124:125], v13 offset:14336\n"
"v_mfma_f32_32x32x16_bf16 a[128:143], v[72:75], v[80:83], a[128:143]\n"
"ds_read_b64_tr_b16 v[126:127], v13 offset:14848\n"
"v_mfma_f32_32x32x16_bf16 a[144:159], v[72:75], v[84:87], a[144:159]\n"
"ds_read_b128 v[100:103], v11 offset:4096\n"
"v_mfma_f32_32x32x16_bf16 a[160:175], v[72:75], v[88:91], a[160:175]\n"
"ds_read_b128 v[104:107], v11 offset:8192\n"
"v_mfma_f32_32x32x16_bf16 a[176:191], v[72:75], v[92:95], a[176:191]\n"
"ds_read_b128 v[108:111], v11 offset:12288\n"
"v_mfma_f32_32x32x16_bf16 a[192:207], v[76:79], v[80:83], a[192:207]\n"
"v_xor_b32 v8, 0x10000, v8\n"
"v_xor_b32 v9, 0x10000, v9\n"
"v_mfma_f32_32x32x16_bf16 a[208:223], v[76:79], v[84:87], a[208:223]\n"
"v_xor_b32 v10, 0x10000, v10\n"
"v_xor_b32 v11, 0x10000, v11\n"
"v_mfma_f32_32x32x16_bf16 a[224:239], v[76:79], v[88:91], a[224:239]\n"
"v_xor_b32 v12, 0x10000, v12\n"
"v_xor_b32 v13, 0x10000, v13\n"
"v_mfma_f32_32x32x16_bf16 a[240:255], v[76:79], v[92:95], a[240:255]\n"
"s_add_u32 s53, s53, 128\n"
"s_add_u32 s59, s59, 524288\n"
"s_waitcnt vmcnt(0) lgkmcnt(0)\n"
"s_cmp_lg_u32 s29, 0\n"
"s_cbranch_scc1 .Lwok4\n"
"v_cmp_ne_u32 vcc, s11, v43\n"
"s_nop 1\n"
"s_cbranch_vccz .Lwok4\n"
"s_mov_b32 s49, 0\n"
".Lwait4:\n"
"s_sleep 2\n"
"global_load_dword v43, v42, s[50:51] sc1\n"
"s_waitcnt vmcnt(0)\n"
"v_cmp_ne_u32 vcc, s11, v43\n"
"s_nop 1\n"
"s_cbranch_vccz .Lwok4\n"
"s_add_u32 s49, s49, 1\n"
"s_cmp_lt_u32 s49, 32768\n"
"s_cbranch_scc1 .Lwait4\n"
".Lwok4:\n"
"s_barrier\n"
"s_add_u32 m0, s43, 0\n"
"v_mfma_f32_32x32x16_bf16 a[0:15], v[96:99], v[112:115], a[0:15]\n"
"buffer_load_dwordx4 v36, s[24:27], s39 offen lds\n"
"ds_read_b128 v[64:67], v8 offset:0\n"
"ds_read_b32 v144, v178 offset:0\n"
"ds_read_b32 v145, v178 offset:128\n"
"v_mfma_f32_32x32x16_bf16 a[16:31], v[96:99], v[116:119], a[16:31]\n"
"ds_read_b64_tr_b16 v[80:81], v12 offset:0\n"
"ds_read_b32 v146, v178 offset:256\n"
"ds_read_b32 v147, v178 offset:384\n"
"s_add_u32 m0, s43, 1024\n"
"v_mfma_f32_32x32x16_bf16 a[32:47], v[96:99], v[120:123], a[32:47]\n"
"buffer_load_dwordx4 v37, s[24:27], s39 offen lds\n"
"ds_read_b64_tr_b16 v[82:83], v12 offset:512\n"
"v_mfma_f32_32x32x16_bf16 a[48:63], v[96:99], v[124:127], a[48:63]\n"
"ds_read_b64_tr_b16 v[84:85], v13 offset:0\n"
"s_add_u32 m0, s43, 2048\n"
"v_mfma_f32_32x32x16_bf16 a[64:79], v[100:103], v[112:115], a[64:79]\n"
"buffer_load_dwordx4 v38, s[24:27], s39 offen lds\n"
"ds_read_b64_tr_b16 v[86:87], v13 offset:512\n"
"v_mfma_f32_32x32x16_bf16 a[80:95], v[100:103], v[116:119], a[80:95]\n"
"ds_read_b64_tr_b16 v[88:89], v12 offset:8192\n"
"s_add_u32 m0, s43, 3072\n"
"v_mfma_f32_32x32x16_bf16 a[96:111], v[100:103], v[120:123], a[96:111]\n"
"buffer_load_dwordx4 v39, s[24:27], s39 offen lds\n"
"ds_read_b64_tr_b16 v[90:91], v12 offset:8704\n"
"v_mfma_f32_32x32x16_bf16 a[112:127], v[100:103], v[124:127], a[112:127]\n"
"ds_read_b64_tr_b16 v[92:93], v13 offset:8192\n"
"s_add_u32 s39, s39, 131072\n"
"s_add_u32 m0, s43, 16384\n"
"v_mfma_f32_32x32x16_bf16 a[128:143], v[104:107], v[112:115], a[128:143]\n"
"buffer_load_dwordx4 v36, s[24:27], s39 offen lds\n"
"ds_read_b64_tr_b16 v[94:95], v13 offset:8704\n"
"v_mfma_f32_32x32x16_bf16 a[144:159], v[104:107], v[116:119], a[144:159]\n"
"ds_read_b128 v[68:71], v8 offset:4096\n"
"s_add_u32 m0, s43, 17408\n"
"v_mfma_f32_32x32x16_bf16 a[160:175], v[104:107], v[120:123], a[160:175]\n"
"buffer_load_dwordx4 v37, s[24:27], s39 offen lds\n"
"ds_read_b128 v[72:75], v8 offset:8192\n"
"v_mfma_f32_32x32x16_bf16 a[176:191], v[104:107], v[124:127], a[176:191]\n"
"ds_read_b128 v[76:79], v8 offset:12288\n"
"s_add_u32 m0, s43, 18432\n"
"v_mfma_f32_32x32x16_bf16 a[192:207], v[108:111], v[112:115], a[192:207]\n"
"buffer_load_dwordx4 v38, s[24:27], s39 offen lds\n"
"v_mfma_f32_32x32x16_bf16 a[208:223], v[108:111], v[116:119], a[208:223]\n"
"s_add_u32 m0, s43, 19456\n"
"v_mfma_f32_32x32x16_bf16 a[224:239], v[108:111], v[120:123], a[224:239]\n"
"buffer_load_dwordx4 v39, s[24:27], s39 offen lds\n"
"v_mfma_f32_32x32x16_bf16 a[240:255], v[108:111], v[124:127], a[240:255]\n"
"s_waitcnt lgkmcnt(0)\n"
"s_add_u32 s39, s39, 131072\n"
"s_add_u32 m0, s43, 32768\n"
"v_mfma_f32_32x32x16_bf16 a[0:15], v[64:67], v[80:83], a[0:15]\n"
"buffer_load_dwordx4 v36, s[24:27], s39 offen lds\n"
"ds_read_b128 v[96:99], v9 offset:0\n"
"v_add_f32 v144, v144, v145\n"
"v_add_f32 v146, v146, v147\n"
"v_mfma_f32_32x32x16_bf16 a[16:31], v[64:67], v[84:87], a[16:31]\n"
"ds_read_b64_tr_b16 v[112:113], v12 offset:2048\n"
"v_add_f32 v144, v144, v146\n"
"s_nop 0\n"
"s_add_u32 m0, s43, 33792\n"
"v_mfma_f32_32x32x16_bf16 a[32:47], v[64:67], v[88:91], a[32:47]\n"
"buffer_load_dwordx4 v37, s[24:27], s39 offen lds\n"
"ds_read_b64_tr_b16 v[114:115], v12 offset:2560\n"
"v_mfma_f32_32x32x16_bf16 a[48:63], v[64:67], v[92:95], a[48:63]\n"
"ds_read_b64_tr_b16 v[116:117], v13 offset:2048\n"
"s_add_u32 m0, s43, 34816\n"
"v_mfma_f32_32x32x16_bf16 a[64:79], v[68:71], v[80:83], a[64:79]\n"
"buffer_load_dwordx4 v38, s[24:27], s39 offen lds\n"
"ds_read_b64_tr_b16 v[118:119], v13 offset:2560\n"
"v_mfma_f32_32x32x16_bf16 a[80:95], v[68:71], v[84:87], a[80:95]\n"
"ds_read_b64_tr_b16 v[120:121], v12 offset:10240\n"
"s_add_u32 m0, s43, 35840\n"
"v_mfma_f32_32x32x16_bf16 a[96:111], v[68:71], v[88:91], a[96:111]\n"
"buffer_load_dwordx4 v39, s[24:27], s39 offen lds\n"
"ds_read_b64_tr_b16 v[122:123], v12 offset:10752\n"
"v_mfma_f32_32x32x16_bf16 a[112:127], v[68:71], v[92:95], a[112:127]\n"
"ds_read_b64_tr_b16 v[124:125], v13 offset:10240\n"
"s_add_u32 s39, s39, 131072\n"
"s_add_u32 m0, s43, 49152\n"
"v_mfma_f32_32x32x16_bf16 a[128:143], v[72:75], v[80:83], a[128:143]\n"
"buffer_load_dwordx4 v36, s[24:27], s39 offen lds\n"
"ds_read_b64_tr_b16 v[126:127], v13 offset:10752\n"
"v_mfma_f32_32x32x16_bf16 a[144:159], v[72:75], v[84:87], a[144:159]\n"
"ds_read_b128 v[100:103], v9 offset:4096\n"
"s_add_u32 m0, s43, 50176\n"
"v_mfma_f32_32x32x16_bf16 a[160:175], v[72:75], v[88:91], a[160:175]\n"
"buffer_load_dwordx4 v37, s[24:27], s39 offen lds\n"
"ds_read_b128 v[104:107], v9 offset:8192\n"
"v_mfma_f32_32x32x16_bf16 a[176:191], v[72:75], v[92:95], a[176:191]\n"
"ds_read_b128 v[108:111], v9 offset:12288\n"
"s_add_u32 m0, s43, 51200\n"
"v_mfma_f32_32x32x16_bf16 a[192:207], v[76:79], v[80:83], a[192:207]\n"
"buffer_load_dwordx4 v38, s[24:27], s39 offen lds\n"
"v_mfma_f32_32x32x16_bf16 a[208:223], v[76:79], v[84:87], a[208:223]\n"
"s_add_u32 m0, s43, 52224\n"
"v_mfma_f32_32x32x16_bf16 a[224:239], v[76:79], v[88:91], a[224:239]\n"
"buffer_load_dwordx4 v39, s[24:27], s39 offen lds\n"
"v_mfma_f32_32x32x16_bf16 a[240:255], v[76:79], v[92:95], a[240:255]\n"
"s_waitcnt lgkmcnt(0)\n"
"v_mfma_f32_32x32x16_bf16 a[0:15], v[96:99], v[112:115], a[0:15]\n"
"ds_read_b128 v[64:67], v10 offset:0\n"
"v_mfma_f32_32x32x16_bf16 a[16:31], v[96:99], v[116:119], a[16:31]\n"
"ds_read_b64_tr_b16 v[80:81], v12 offset:4096\n"
"v_mfma_f32_32x32x16_bf16 a[32:47], v[96:99], v[120:123], a[32:47]\n"
"ds_read_b64_tr_b16 v[82:83], v12 offset:4608\n"
"v_mfma_f32_32x32x16_bf16 a[48:63], v[96:99], v[124:127], a[48:63]\n"
"ds_read_b64_tr_b16 v[84:85], v13 offset:4096\n"
"v_mfma_f32_32x32x16_bf16 a[64:79], v[100:103], v[112:115], a[64:79]\n"
"ds_read_b64_tr_b16 v[86:87], v13 offset:4608\n"
"v_mfma_f32_32x32x16_bf16 a[80:95], v[100:103], v[116:119], a[80:95]\n"
"ds_read_b64_tr_b16 v[88:89], v12 offset:12288\n"
"v_mfma_f32_32x32x16_bf16 a[96:111], v[100:103], v[120:123], a[96:111]\n"
"ds_read_b64_tr_b16 v[90:91], v12 offset:12800\n"
"v_mfma_f32_32x32x16_bf16 a[112:127], v[100:103], v[124:127], a[112:127]\n"
"ds_read_b64_tr_b16 v[92:93], v13 offset:12288\n"
"v_mfma_f32_32x32x16_bf16 a[128:143], v[104:107], v[112:115], a[128:143]\n"
"ds_read_b64_tr_b16 v[94:95], v13 offset:12800\n"
"v_mfma_f32_32x32x16_bf16 a[144:159], v[104:107], v[116:119], a[144:159]\n"
"ds_read_b128 v[68:71], v10 offset:4096\n"
"v_mfma_f32_32x32x16_bf16 a[160:175], v[104:107], v[120:123], a[160:175]\n"
"ds_read_b128 v[72:75], v10 offset:8192\n"
"v_mfma_f32_32x32x16_bf16 a[176:191], v[104:107], v[124:127], a[176:191]\n"
"ds_read_b128 v[76:79], v10 offset:12288\n"
"v_mfma_f32_32x32x16_bf16 a[192:207], v[108:111], v[112:115], a[192:207]\n"
"v_mfma_f32_32x32x16_bf16 a[208:223], v[108:111], v[116:119], a[208:223]\n"
"v_mfma_f32_32x32x16_bf16 a[224:239], v[108:111], v[120:123], a[224:239]\n"
"v_mfma_f32_32x32x16_bf16 a[240:255], v[108:111], v[124:127], a[240:255]\n"
"s_waitcnt lgkmcnt(0)\n"
"v_mfma_f32_32x32x16_bf16 a[0:15], v[64:67], v[80:83], a[0:15]\n"
"ds_read_b128 v[96:99], v11 offset:0\n"
"buffer_store_dword v144, v177, s[80:83], s84 offen sc1\n"
"v_mfma_f32_32x32x16_bf16 a[16:31], v[64:67], v[84:87], a[16:31]\n"
"ds_read_b64_tr_b16 v[112:113], v12 offset:6144\n"
"v_mfma_f32_32x32x16_bf16 a[32:47], v[64:67], v[88:91], a[32:47]\n"
"ds_read_b64_tr_b16 v[114:115], v12 offset:6656\n"
"v_mfma_f32_32x32x16_bf16 a[48:63], v[64:67], v[92:95], a[48:63]\n"
"ds_read_b64_tr_b16 v[116:117], v13 offset:6144\n"
"v_mfma_f32_32x32x16_bf16 a[64:79], v[68:71], v[80:83], a[64:79]\n"
"ds_read_b64_tr_b16 v[118:119], v13 offset:6656\n"
"v_mfma_f32_32x32x16_bf16 a[80:95], v[68:71], v[84:87], a[80:95]\n"
"ds_read_b64_tr_b16 v[120:121], v12 offset:14336\n"
"v_mfma_f32_32x32x16_bf16 a[96:111], v[68:71], v[88:91], a[96:111]\n"
"ds_read_b64_tr_b16 v[122:123], v12 offset:14848\n"
"v_mfma_f32_32x32x16_bf16 a[112:127], v[68:71], v[92:95], a[112:127]\n"
"ds_read_b64_tr_b16 v[124:125], v13 offset:14336\n"
"v_mfma_f32_32x32x16_bf16 a[128:143], v[72:75], v[80:83], a[128:143]\n"
"ds_read_b64_tr_b16 v[126:127], v13 offset:14848\n"
"v_mfma_f32_32x32x16_bf16 a[144:159], v[72:75], v[84:87], a[144:159]\n"
"ds_read_b128 v[100:103], v11 offset:4096\n"
"v_mfma_f32_32x32x16_bf16 a[160:175], v[72:75], v[88:91], a[160:175]\n"
"ds_read_b128 v[104:107], v11 offset:8192\n"
"v_mfma_f32_32x32x16_bf16 a[176:191], v[72:75], v[92:95], a[176:191]\n"
"ds_read_b128 v[108:111], v11 offset:12288\n"
"v_mfma_f32_32x32x16_bf16 a[192:207], v[76:79], v[80:83], a[192:207]\n"
"v_mfma_f32_32x32x16_bf16 a[208:223], v[76:79], v[84:87], a[208:223]\n"
"v_mfma_f32_32x32x16_bf16 a[224:239], v[76:79], v[88:91], a[224:239]\n"
"v_mfma_f32_32x32x16_bf16 a[240:255], v[76:79], v[92:95], a[240:255]\n"
"s_waitcnt vmcnt(1) lgkmcnt(0)\n"
"s_barrier\n"
"s_add_u32 m0, s40, 0\n"
"v_mfma_f32_32x32x16_bf16 a[0:15], v[96:99], v[112:115], a[0:15]\n"
"buffer_load_dwordx4 v1, s[12:15], s38 offen offset:0 sc1 lds\n"
"v_mfma_f32_32x32x16_bf16 a[16:31], v[96:99], v[116:119], a[16:31]\n"
"buffer_load_dwordx4 v1, s[12:15], s38 offen offset:1024 sc1 lds\n"
"v_mfma_f32_32x32x16_bf16 a[32:47], v[96:99], v[120:123], a[32:47]\n"
"buffer_load_dwordx4 v1, s[12:15], s38 offen offset:2048 sc1 lds\n"
"v_mfma_f32_32x32x16_bf16 a[48:63], v[96:99], v[124:127], a[48:63]\n"
"buffer_load_dwordx4 v1, s[12:15], s38 offen offset:3072 sc1 lds\n"
"s_add_u32 s38, s38, 8192\n"
"s_add_u32 m0, s40, 8192\n"
"v_mfma_f32_32x32x16_bf16 a[64:79], v[100:103], v[112:115], a[64:79]\n"
"buffer_load_dwordx4 v1, s[12:15], s38 offen offset:0 sc1 lds\n"
"v_mfma_f32_32x32x16_bf16 a[80:95], v[100:103], v[116:119], a[80:95]\n"
"buffer_load_dwordx4 v1, s[12:15], s38 offen offset:1024 sc1 lds\n"
"v_mfma_f32_32x32x16_bf16 a[96:111], v[100:103], v[120:123], a[96:111]\n"
"buffer_load_dwordx4 v1, s[12:15], s38 offen offset:2048 sc1 lds\n"
"v_mfma_f32_32x32x16_bf16 a[112:127], v[100:103], v[124:127], a[112:127]\n"
"buffer_load_dwordx4 v1, s[12:15], s38 offen offset:3072 sc1 lds\n"
"s_add_u32 s38, s38, 8192\n"
"s_add_u32 m0, s40, 16384\n"
"v_mfma_f32_32x32x16_bf16 a[128:143], v[104:107], v[112:115], a[128:143]\n"
"buffer_load_dwordx4 v1, s[12:15], s38 offen offset:0 sc1 lds\n"
"v_mfma_f32_32x32x16_bf16 a[144:159], v[104:107], v[116:119], a[144:159]\n"
"buffer_load_dwordx4 v1, s[12:15], s38 offen offset:1024 sc1 lds\n"
"v_mfma_f32_32x32x16_bf16 a[160:175], v[104:107], v[120:123], a[160:175]\n"
"buffer_load_dwordx4 v1, s[12:15], s38 offen offset:2048 sc1 lds\n"
"v_mfma_f32_32x32x16_bf16 a[176:191], v[104:107], v[124:127], a[176:191]\n"
"buffer_load_dwordx4 v1, s[12:15], s38 offen offset:3072 sc1 lds\n"
"s_add_u32 s38, s38, 8192\n"
"s_add_u32 m0, s40, 24576\n"
"v_mfma_f32_32x32x16_bf16 a[192:207], v[108:111], v[112:115], a[192:207]\n"
"buffer_load_dwordx4 v1, s[12:15], s38 offen offset:0 sc1 lds\n"
"v_mfma_f32_32x32x16_bf16 a[208:223], v[108:111], v[116:119], a[208:223]\n"
"buffer_load_dwordx4 v1, s[12:15], s38 offen offset:1024 sc1 lds\n"
"v_mfma_f32_32x32x16_bf16 a[224:239], v[108:111], v[120:123], a[224:239]\n"
"buffer_load_dwordx4 v1, s[12:15], s38 offen offset:2048 sc1 lds\n"
"v_mfma_f32_32x32x16_bf16 a[240:255], v[108:111], v[124:127], a[240:255]\n"
"buffer_load_dwordx4 v1, s[12:15], s38 offen offset:3072 sc1 lds\n"
".Lkv_done:\n"
"s_load_dwordx2 s[46:47], s[8:9], 16\n"
"s_lshr_b32 s34, s29, 1\n"
"s_lshl_b32 s41, s30, 1\n"
"s_add_u32 s41, s41, s34\n"
"s_and_b32 s34, s29, 1\n"
"s_lshl_b32 s42, s31, 1\n"
"s_add_u32 s42, s42, s34\n"
"s_nop 7\n"
"v_accvgpr_read_b32 v112, a0\n"
"v_accvgpr_read_b32 v113, a1\n"
"v_accvgpr_read_b32 v114, a2\n"
"v_accvgpr_read_b32 v115, a3\n"
"v_accvgpr_read_b32 v116, a4\n"
"v_accvgpr_read_b32 v117, a5\n"
"v_accvgpr_read_b32 v118, a6\n"
"v_accvgpr_read_b32 v119, a7\n"
"s_waitcnt vmcnt(8)\n"
"s_barrier\n"
"ds_read_b128 v[48:51], v40 offset:0\n"
"ds_read_b64_tr_b16 v[52:53], v41 offset:0\n"
"ds_read_b64_tr_b16 v[54:55], v41 offset:512\n"
"ds_read_b128 v[56:59], v40 offset:1024\n"
"ds_read_b64_tr_b16 v[60:61], v41 offset:2048\n"
"ds_read_b64_tr_b16 v[62:63], v41 offset:2560\n"
"ds_read_b128 v[64:67], v40 offset:2048\n"
"ds_read_b64_tr_b16 v[68:69], v41 offset:4096\n"
"ds_read_b64_tr_b16 v[70:71], v41 offset:4608\n"
"ds_read_b128 v[72:75], v40 offset:3072\n"
"ds_read_b64_tr_b16 v[76:77], v41 offset:6144\n"
"ds_read_b64_tr_b16 v[78:79], v41 offset:6656\n"
"ds_read_b128 v[80:83], v40 offset:4096\n"
"ds_read_b64_tr_b16 v[84:85], v41 offset:8192\n"
"ds_read_b64_tr_b16 v[86:87], v41 offset:8704\n"
"ds_read_b128 v[88:91], v40 offset:5120\n"
"ds_read_b64_tr_b16 v[92:93], v41 offset:10240\n"
"ds_read_b64_tr_b16 v[94:95], v41 offset:10752\n"
"ds_read_b128 v[96:99], v40 offset:6144\n"
"ds_read_b64_tr_b16 v[100:101], v41 offset:12288\n"
"ds_read_b64_tr_b16 v[102:103], v41 offset:12800\n"
"ds_read_b128 v[104:107], v40 offset:7168\n"
"ds_read_b64_tr_b16 v[108:109], v41 offset:14336\n"
"ds_read_b64_tr_b16 v[110:111], v41 offset:14848\n"
"s_waitcnt lgkmcnt(12)\n"
"v_mfma_f32_32x32x16_bf16 v[128:143], v[52:55], v[48:51], 0\n"
"buffer_load_dwordx4 a[0:3], v1, s[96:99], s56 offen offset:0 sc1\n"
"buffer_load_dwordx4 a[4:7], v1, s[96:99], s57 offen offset:0 sc1\n"
"s_add_u32 s86, s91, 0\n"
"v_cvt_pk_bf16_f32 v120, v112, v113\n"
"v_mfma_f32_32x32x16_bf16 v[128:143], v[60:63], v[56:59], v[128:143]\n"
"v_cvt_pk_bf16_f32 v121, v114, v115\n"
"v_cvt_pk_bf16_f32 v122, v116, v117\n"
"v_cvt_pk_bf16_f32 v123, v118, v119\n"
"v_mfma_f32_32x32x16_bf16 v[128:143], v[68:71], v[64:67], v[128:143]\n"
"buffer_store_dwordx4 v[120:123], v1, s[92:95], s86 offen sc1\n"
"v_accvgpr_read_b32 v112, a8\n"
"v_accvgpr_read_b32 v113, a9\n"
"v_accvgpr_read_b32 v114, a10\n"
"v_accvgpr_read_b32 v115, a11\n"
"v_mfma_f32_32x32x16_bf16 v[128:143], v[76:79], v[72:75], v[128:143]\n"
"v_accvgpr_read_b32 v116, a12\n"
"v_accvgpr_read_b32 v117, a13\n"
"v_accvgpr_read_b32 v118, a14\n"
"v_accvgpr_read_b32 v119, a15\n"
"ds_read_b128 v[48:51], v40 offset:8192\n"
"ds_read_b64_tr_b16 v[52:53], v41 offset:16384\n"
"ds_read_b64_tr_b16 v[54:55], v41 offset:16896\n"
"ds_read_b128 v[56:59], v40 offset:9216\n"
"ds_read_b64_tr_b16 v[60:61], v41 offset:18432\n"
"ds_read_b64_tr_b16 v[62:63], v41 offset:18944\n"
"ds_read_b128 v[64:67], v40 offset:10240\n"
"ds_read_b64_tr_b16 v[68:69], v41 offset:20480\n"
"ds_read_b64_tr_b16 v[70:71], v41 offset:20992\n"
"ds_read_b128 v[72:75], v40 offset:11264\n"
"ds_read_b64_tr_b16 v[76:77], v41 offset:22528\n"
"ds_read_b64_tr_b16 v[78:79], v41 offset:23040\n"
"s_waitcnt lgkmcnt(12)\n"
"v_mfma_f32_32x32x16_bf16 v[128:143], v[84:87], v[80:83], v[128:143]\n"
"buffer_load_dwordx4 a[8:11], v1, s[96:99], s56 offen offset:1024 sc1\n"
"buffer_load_dwordx4 a[12:15], v1, s[96:99], s57 offen offset:1024 sc1\n"
"s_add_u32 s86, s91, 0\n"
"s_add_u32 s86, s86, s87\n"
"v_mfma_f32_32x32x16_bf16 v[128:143], v[92:95], v[88:91], v[128:143]\n"
"v_cvt_pk_bf16_f32 v120, v112, v113\n"
"v_cvt_pk_bf16_f32 v121, v114, v115\n"
"v_cvt_pk_bf16_f32 v122, v116, v117\n"
"v_cvt_pk_bf16_f32 v123, v118, v119\n"
"v_mfma_f32_32x32x16_bf16 v[128:143], v[100:103], v[96:99], v[128:143]\n"
"buffer_store_dwordx4 v[120:123], v1, s[92:95], s86 offen sc1\n"
"v_accvgpr_read_b32 v112, a16\n"
"v_accvgpr_read_b32 v113, a17\n"
"v_accvgpr_read_b32 v114, a18\n"
"v_accvgpr_read_b32 v115, a19\n"
"v_mfma_f32_32x32x16_bf16 v[128:143], v[108:111], v[104:107], v[128:143]\n"
"v_accvgpr_read_b32 v116, a20\n"
"v_accvgpr_read_b32 v117, a21\n"
"v_accvgpr_read_b32 v118, a22\n"
"v_accvgpr_read_b32 v119, a23\n"
"ds_read_b128 v[80:83], v40 offset:12288\n"
"ds_read_b64_tr_b16 v[84:85], v41 offset:24576\n"
"ds_read_b64_tr_b16 v[86:87], v41 offset:25088\n"
"ds_read_b128 v[88:91], v40 offset:13312\n"
"ds_read_b64_tr_b16 v[92:93], v41 offset:26624\n"
"ds_read_b64_tr_b16 v[94:95], v41 offset:27136\n"
"ds_read_b128 v[96:99], v40 offset:14336\n"
"ds_read_b64_tr_b16 v[100:101], v41 offset:28672\n"
"ds_read_b64_tr_b16 v[102:103], v41 offset:29184\n"
"ds_read_b128 v[104:107], v40 offset:15360\n"
"ds_read_b64_tr_b16 v[108:109], v41 offset:30720\n"
"ds_read_b64_tr_b16 v[110:111], v41 offset:31232\n"
"s_waitcnt lgkmcnt(12)\n"
"v_mfma_f32_32x32x16_bf16 v[128:143], v[52:55], v[48:51], v[128:143]\n"
"buffer_load_dwordx4 a[16:19], v1, s[96:99], s56 offen offset:2048 sc1\n"
"buffer_load_dwordx4 a[20:23], v1, s[96:99], s57 offen offset:2048 sc1\n"
"s_add_u32 s86, s91, 0\n"
"s_add_u32 s86, s86, s88\n"
"v_mfma_f32_32x32x16_bf16 v[128:143], v[60:63], v[56:59], v[128:143]\n"
"v_cvt_pk_bf16_f32 v120, v112, v113\n"
"v_cvt_pk_bf16_f32 v121, v114, v115\n"
"v_cvt_pk_bf16_f32 v122, v116, v117\n"
"v_cvt_pk_bf16_f32 v123, v118, v119\n"
"v_mfma_f32_32x32x16_bf16 v[128:143], v[68:71], v[64:67], v[128:143]\n"
"buffer_store_dwordx4 v[120:123], v1, s[92:95], s86 offen sc1\n"
"v_accvgpr_read_b32 v112, a24\n"
"v_accvgpr_read_b32 v113, a25\n"
"v_accvgpr_read_b32 v114, a26\n"
"v_accvgpr_read_b32 v115, a27\n"
"v_mfma_f32_32x32x16_bf16 v[128:143], v[76:79], v[72:75], v[128:143]\n"
"v_accvgpr_read_b32 v116, a28\n"
"v_accvgpr_read_b32 v117, a29\n"
"v_accvgpr_read_b32 v118, a30\n"
"v_accvgpr_read_b32 v119, a31\n"
"s_waitcnt lgkmcnt(0)\n"
"v_mfma_f32_32x32x16_bf16 v[128:143], v[84:87], v[80:83], v[128:143]\n"
"buffer_load_dwordx4 a[24:27], v1, s[96:99], s56 offen offset:3072 sc1\n"
"buffer_load_dwordx4 a[28:31], v1, s[96:99], s57 offen offset:3072 sc1\n"
"s_add_u32 s56, s56, 4096\n"
"s_add_u32 s57, s57, 4096\n"
"v_mfma_f32_32x32x16_bf16 v[128:143], v[92:95], v[88:91], v[128:143]\n"
"s_add_u32 s86, s91, 0\n"
"s_add_u32 s86, s86, s88\n"
"s_add_u32 s86, s86, s87\n"
"v_cvt_pk_bf16_f32 v120, v112, v113\n"
"v_cvt_pk_bf16_f32 v121, v114, v115\n"
"v_cvt_pk_bf16_f32 v122, v116, v117\n"
"v_mfma_f32_32x32x16_bf16 v[128:143], v[100:103], v[96:99], v[128:143]\n"
"v_cvt_pk_bf16_f32 v123, v118, v119\n"
"v_mfma_f32_32x32x16_bf16 v[128:143], v[108:111], v[104:107], v[128:143]\n"
"buffer_store_dwordx4 v[120:123], v1, s[92:95], s86 offen sc1\n"
"v_accvgpr_read_b32 v112, a32\n"
"v_accvgpr_read_b32 v113, a33\n"
"v_accvgpr_read_b32 v114, a34\n"
"v_accvgpr_read_b32 v115, a35\n"
"s_waitcnt vmcnt(12)\n"
"s_barrier\n"
"ds_read_b128 v[48:51], v40 offset:16384\n"
"ds_read_b64_tr_b16 v[52:53], v41 offset:32768\n"
"ds_read_b64_tr_b16 v[54:55], v41 offset:33280\n"
"ds_read_b128 v[56:59], v40 offset:17408\n"
"ds_read_b64_tr_b16 v[60:61], v41 offset:34816\n"
"ds_read_b64_tr_b16 v[62:63], v41 offset:35328\n"
"ds_read_b128 v[64:67], v40 offset:18432\n"
"ds_read_b64_tr_b16 v[68:69], v41 offset:36864\n"
"ds_read_b64_tr_b16 v[70:71], v41 offset:37376\n"
"ds_read_b128 v[72:75], v40 offset:19456\n"
"ds_read_b64_tr_b16 v[76:77], v41 offset:38912\n"
"ds_read_b64_tr_b16 v[78:79], v41 offset:39424\n"
"ds_read_b128 v[80:83], v40 offset:20480\n"
"ds_read_b64_tr_b16 v[84:85], v41 offset:40960\n"
"ds_read_b64_tr_b16 v[86:87], v41 offset:41472\n"
"ds_read_b128 v[88:91], v40 offset:21504\n"
"ds_read_b64_tr_b16 v[92:93], v41 offset:43008\n"
"ds_read_b64_tr_b16 v[94:95], v41 offset:43520\n"
"ds_read_b128 v[96:99], v40 offset:22528\n"
"ds_read_b64_tr_b16 v[100:101], v41 offset:45056\n"
"ds_read_b64_tr_b16 v[102:103], v41 offset:45568\n"
"ds_read_b128 v[104:107], v40 offset:23552\n"
"ds_read_b64_tr_b16 v[108:109], v41 offset:47104\n"
"ds_read_b64_tr_b16 v[110:111], v41 offset:47616\n"
"s_waitcnt lgkmcnt(12)\n"
"v_mfma_f32_32x32x16_bf16 v[128:143], v[52:55], v[48:51], v[128:143]\n"
"v_accvgpr_read_b32 v116, a36\n"
"v_accvgpr_read_b32 v117, a37\n"
"v_accvgpr_read_b32 v118, a38\n"
"v_accvgpr_read_b32 v119, a39\n"
"v_mfma_f32_32x32x16_bf16 v[128:143], v[60:63], v[56:59], v[128:143]\n"
"buffer_load_dwordx4 a[32:35], v1, s[96:99], s56 offen offset:0 sc1\n"
"buffer_load_dwordx4 a[36:39], v1, s[96:99], s57 offen offset:0 sc1\n"
"s_add_u32 s86, s91, 0\n"
"s_add_u32 s86, s86, s89\n"
"v_mfma_f32_32x32x16_bf16 v[128:143], v[68:71], v[64:67], v[128:143]\n"
"v_cvt_pk_bf16_f32 v120, v112, v113\n"
"v_cvt_pk_bf16_f32 v121, v114, v115\n"
"v_cvt_pk_bf16_f32 v122, v116, v117\n"
"v_cvt_pk_bf16_f32 v123, v118, v119\n"
"v_mfma_f32_32x32x16_bf16 v[128:143], v[76:79], v[72:75], v[128:143]\n"
"buffer_store_dwordx4 v[120:123], v1, s[92:95], s86 offen sc1\n"
"v_accvgpr_read_b32 v112, a40\n"
"v_accvgpr_read_b32 v113, a41\n"
"v_accvgpr_read_b32 v114, a42\n"
"v_accvgpr_read_b32 v115, a43\n"
"ds_read_b128 v[48:51], v40 offset:24576\n"
"ds_read_b64_tr_b16 v[52:53], v41 offset:49152\n"
"ds_read_b64_tr_b16 v[54:55], v41 offset:49664\n"
"ds_read_b128 v[56:59], v40 offset:25600\n"
"ds_read_b64_tr_b16 v[60:61], v41 offset:51200\n"
"ds_read_b64_tr_b16 v[62:63], v41 offset:51712\n"
"ds_read_b128 v[64:67], v40 offset:26624\n"
"ds_read_b64_tr_b16 v[68:69], v41 offset:53248\n"
"ds_read_b64_tr_b16 v[70:71], v41 offset:53760\n"
"ds_read_b128 v[72:75], v40 offset:27648\n"
"ds_read_b64_tr_b16 v[76:77], v41 offset:55296\n"
"ds_read_b64_tr_b16 v[78:79], v41 offset:55808\n"
"s_waitcnt lgkmcnt(12)\n"
"v_mfma_f32_32x32x16_bf16 v[128:143], v[84:87], v[80:83], v[128:143]\n"
"v_accvgpr_read_b32 v116, a44\n"
"v_accvgpr_read_b32 v117, a45\n"
"v_accvgpr_read_b32 v118, a46\n"
"v_accvgpr_read_b32 v119, a47\n"
"v_mfma_f32_32x32x16_bf16 v[128:143], v[92:95], v[88:91], v[128:143]\n"
"buffer_load_dwordx4 a[40:43], v1, s[96:99], s56 offen offset:1024 sc1\n"
"buffer_load_dwordx4 a[44:47], v1, s[96:99], s57 offen offset:1024 sc1\n"
"s_add_u32 s86, s91, 0\n"
"s_add_u32 s86, s86, s89\n"
"v_mfma_f32_32x32x16_bf16 v[128:143], v[100:103], v[96:99], v[128:143]\n"
"s_add_u32 s86, s86, s87\n"
"v_cvt_pk_bf16_f32 v120, v112, v113\n"
"v_cvt_pk_bf16_f32 v121, v114, v115\n"
"v_cvt_pk_bf16_f32 v122, v116, v117\n"
"v_cvt_pk_bf16_f32 v123, v118, v119\n"
"v_mfma_f32_32x32x16_bf16 v[128:143], v[108:111], v[104:107], v[128:143]\n"
"buffer_store_dwordx4 v[120:123], v1, s[92:95], s86 offen sc1\n"
"v_accvgpr_read_b32 v112, a48\n"
"v_accvgpr_read_b32 v113, a49\n"
"v_accvgpr_read_b32 v114, a50\n"
"v_accvgpr_read_b32 v115, a51\n"
"ds_read_b128 v[80:83], v40 offset:28672\n"
"ds_read_b64_tr_b16 v[84:85], v41 offset:57344\n"
"ds_read_b64_tr_b16 v[86:87], v41 offset:57856\n"
"ds_read_b128 v[88:91], v40 offset:29696\n"
"ds_read_b64_tr_b16 v[92:93], v41 offset:59392\n"
"ds_read_b64_tr_b16 v[94:95], v41 offset:59904\n"
"ds_read_b128 v[96:99], v40 offset:30720\n"
"ds_read_b64_tr_b16 v[100:101], v41 offset:61440\n"
"ds_read_b64_tr_b16 v[102:103], v41 offset:61952\n"
"ds_read_b128 v[104:107], v40 offset:31744\n"
"ds_read_b64_tr_b16 v[108:109], v41 offset:63488\n"
"ds_read_b64_tr_b16 v[110:111], v41 offset:64000\n"
"s_waitcnt lgkmcnt(12)\n"
"v_mfma_f32_32x32x16_bf16 v[128:143], v[52:55], v[48:51], v[128:143]\n"
"v_accvgpr_read_b32 v116, a52\n"
"v_accvgpr_read_b32 v117, a53\n"
"v_accvgpr_read_b32 v118, a54\n"
"v_accvgpr_read_b32 v119, a55\n"
"v_mfma_f32_32x32x16_bf16 v[128:143], v[60:63], v[56:59], v[128:143]\n"
"buffer_load_dwordx4 a[48:51], v1, s[96:99], s56 offen offset:2048 sc1\n"
"buffer_load_dwordx4 a[52:55], v1, s[96:99], s57 offen offset:2048 sc1\n"
"s_add_u32 s86, s91, 0\n"
"s_add_u32 s86, s86, s90\n"
"v_mfma_f32_32x32x16_bf16 v[128:143], v[68:71], v[64:67], v[128:143]\n"
"v_cvt_pk_bf16_f32 v120, v112, v113\n"
"v_cvt_pk_bf16_f32 v121, v114, v115\n"
"v_cvt_pk_bf16_f32 v122, v116, v117\n"
"v_cvt_pk_bf16_f32 v123, v118, v119\n"
"v_mfma_f32_32x32x16_bf16 v[128:143], v[76:79], v[72:75], v[128:143]\n"
"buffer_store_dwordx4 v[120:123], v1, s[92:95], s86 offen sc1\n"
"v_accvgpr_read_b32 v112, a56\n"
"v_accvgpr_read_b32 v113, a57\n"
"v_accvgpr_read_b32 v114, a58\n"
"v_accvgpr_read_b32 v115, a59\n"
"s_waitcnt lgkmcnt(0)\n"
"s_and_b32 s34, s28, 7\n"
"s_lshr_b32 s35, s28, 3\n"
"s_and_b32 s36, s35, 3\n"
"s_lshl_b32 s34, s34, 2\n"
"s_add_u32 s37, s34, s36\n"
"s_lshr_b32 s36, s35, 2\n"
"s_mov_b32 s12, s46\n"
"s_and_b32 s13, s47, 0xffff\n"
"s_mov_b32 s14, 4194304\n"
"s_mov_b32 s15, 0x20000\n"
"s_lshl_b32 s55, s29, 17\n"
"s_lshl_b32 s34, s37, 8\n"
"s_add_u32 s55, s55, s34\n"
"s_add_u32 s58, s55, 65536\n"
"s_lshl_b32 s54, s29, 11\n"
"v_lshrrev_b32 v2, 3, v0\n"
"v_lshlrev_b32 v2, 13, v2\n"
"v_bfe_u32 v3, v0, 4, 1\n"
"v_lshlrev_b32 v3, 2, v3\n"
"v_and_b32 v4, 7, v0\n"
"v_xor_b32 v3, v3, v4\n"
"v_lshl_add_u32 v9, v3, 4, v2\n"
"v_lshrrev_b32 v2, 5, v0\n"
"v_bfe_u32 v3, v0, 2, 2\n"
"v_lshl_add_u32 v2, v2, 3, v3\n"
"v_lshlrev_b32 v2, 7, v2\n"
"v_and_b32 v4, 1, v0\n"
"v_lshl_add_u32 v2, v4, 3, v2\n"
"v_bfe_u32 v4, v0, 4, 1\n"
"v_bfe_u32 v5, v0, 1, 1\n"
"v_lshl_add_u32 v4, v4, 1, v5\n"
"v_lshrrev_b32 v3, 1, v3\n"
"v_lshlrev_b32 v3, 2, v3\n"
"v_xor_b32 v4, v4, v3\n"
"v_lshl_add_u32 v10, v4, 4, v2\n"
"v_xor_b32 v4, 4, v4\n"
"v_lshl_add_u32 v11, v4, 4, v2\n"
"s_barrier\n"
"v_mfma_f32_32x32x16_bf16 v[128:143], v[84:87], v[80:83], v[128:143]\n"
"v_accvgpr_read_b32 v116, a60\n"
"v_accvgpr_read_b32 v117, a61\n"
"v_accvgpr_read_b32 v118, a62\n"
"v_accvgpr_read_b32 v119, a63\n"
"v_mfma_f32_32x32x16_bf16 v[128:143], v[92:95], v[88:91], v[128:143]\n"
"buffer_load_dwordx4 a[56:59], v1, s[96:99], s56 offen offset:3072 sc1\n"
"buffer_load_dwordx4 a[60:63], v1, s[96:99], s57 offen offset:3072 sc1\n"
"s_add_u32 s56, s56, 4096\n"
"s_add_u32 s57, s57, 4096\n"
"v_mfma_f32_32x32x16_bf16 v[128:143], v[100:103], v[96:99], v[128:143]\n"
"s_add_u32 s86, s91, 0\n"
"s_add_u32 s86, s86, s90\n"
"s_add_u32 s86, s86, s87\n"
"v_cvt_pk_bf16_f32 v120, v112, v113\n"
"v_cvt_pk_bf16_f32 v121, v114, v115\n"
"v_cvt_pk_bf16_f32 v122, v116, v117\n"
"v_mfma_f32_32x32x16_bf16 v[128:143], v[108:111], v[104:107], v[128:143]\n"
"v_cvt_pk_bf16_f32 v123, v118, v119\n"
"s_add_u32 m0, s54, 0\n"
"s_nop 0\n"
"buffer_load_dwordx4 v9, s[12:15], s55 offen offset:0 lds\n"
"s_add_u32 m0, s54, 8064\n"
"s_nop 0\n"
"buffer_load_dwordx4 v9, s[12:15], s55 offen offset:128 lds\n"
"s_add_u32 m0, s54, 1024\n"
"s_nop 0\n"
"buffer_load_dwordx4 v9, s[12:15], s58 offen offset:0 lds\n"
"s_add_u32 m0, s54, 9088\n"
"s_nop 0\n"
"buffer_load_dwordx4 v9, s[12:15], s58 offen offset:128 lds\n"
"s_add_u32 s55, s55, 524288\n"
"s_add_u32 s58, s58, 524288\n"
"s_add_u32 m0, s54, 16384\n"
"s_nop 0\n"
"buffer_load_dwordx4 v9, s[12:15], s55 offen offset:0 lds\n"
"s_add_u32 m0, s54, 24448\n"
"s_nop 0\n"
"buffer_load_dwordx4 v9, s[12:15], s55 offen offset:128 lds\n"
"s_add_u32 m0, s54, 17408\n"
"s_nop 0\n"
"buffer_load_dwordx4 v9, s[12:15], s58 offen offset:0 lds\n"
"s_add_u32 m0, s54, 25472\n"
"s_nop 0\n"
"buffer_load_dwordx4 v9, s[12:15], s58 offen offset:128 lds\n"
"s_add_u32 s55, s55, 524288\n"
"s_add_u32 s58, s58, 524288\n"
"s_nop 9\n"
"s_add_u32 s36, s50, 55574528\n"
"s_addc_u32 s37, s51, 0\n"
"s_mov_b32 s16, s36\n"
"s_and_b32 s17, s37, 0xffff\n"
"s_mov_b32 s18, 2097152\n"
"s_mov_b32 s19, 0x20000\n"
"s_lshl_b32 s34, s41, 15\n"
"s_lshl_b32 s35, s42, 11\n"
"s_add_u32 s34, s34, s35\n"
"v_cvt_pk_bf16_f32 v144, v128, v129\n"
"buffer_store_dwordx4 v[120:123], v1, s[92:95], s86 offen sc1\n"
"v_accvgpr_read_b32 v112, a64\n"
"v_accvgpr_read_b32 v113, a65\n"
"v_accvgpr_read_b32 v114, a66\n"
"v_accvgpr_read_b32 v115, a67\n"
"v_accvgpr_read_b32 v116, a68\n"
"v_accvgpr_read_b32 v117, a69\n"
"v_cvt_pk_bf16_f32 v145, v130, v131\n"
"v_cvt_pk_bf16_f32 v146, v132, v133\n"
"v_cvt_pk_bf16_f32 v147, v134, v135\n"
"v_cvt_pk_bf16_f32 v148, v136, v137\n"
"v_cvt_pk_bf16_f32 v149, v138, v139\n"
"v_cvt_pk_bf16_f32 v150, v140, v141\n"
"v_cvt_pk_bf16_f32 v151, v142, v143\n"
"v_lshlrev_b32 v128, 16, v144\n"
"v_and_b32 v129, 0xffff0000, v144\n"
"v_lshlrev_b32 v130, 16, v145\n"
"v_accvgpr_read_b32 v118, a70\n"
"v_accvgpr_read_b32 v119, a71\n"
"v_and_b32 v131, 0xffff0000, v145\n"
"v_lshlrev_b32 v132, 16, v146\n"
"v_and_b32 v133, 0xffff0000, v146\n"
"v_lshlrev_b32 v134, 16, v147\n"
"v_and_b32 v135, 0xffff0000, v147\n"
"v_lshlrev_b32 v136, 16, v148\n"
"v_and_b32 v137, 0xffff0000, v148\n"
"v_lshlrev_b32 v138, 16, v149\n"
"v_and_b32 v139, 0xffff0000, v149\n"
"v_lshlrev_b32 v140, 16, v150\n"
"buffer_load_dwordx4 a[64:67], v1, s[96:99], s56 offen offset:0 sc1\n"
"buffer_load_dwordx4 a[68:71], v1, s[96:99], s57 offen offset:0 sc1\n"
"s_add_u32 s86, s91, 8192\n"
"v_cvt_pk_bf16_f32 v120, v112, v113\n"
"v_cvt_pk_bf16_f32 v121, v114, v115\n"
"v_cvt_pk_bf16_f32 v122, v116, v117\n"
"v_and_b32 v141, 0xffff0000, v150\n"
"v_lshlrev_b32 v142, 16, v151\n"
"v_and_b32 v143, 0xffff0000, v151\n"
"s_mov_b32 s35, 0x40135761\n"
"s_mov_b32 s36, 0x3dd2d3e8\n"
"v_mul_f32 v152, v128, v128\n"
"v_mul_f32 v153, v129, v129\n"
"v_mul_f32 v154, v130, v130\n"
"v_mul_f32 v155, v131, v131\n"
"v_mul_f32 v156, v132, v132\n"
"v_cvt_pk_bf16_f32 v123, v118, v119\n"
"v_mul_f32 v157, v133, v133\n"
"v_mul_f32 v158, v134, v134\n"
"v_mul_f32 v159, v135, v135\n"
"v_mul_f32 v152, s36, v152\n"
"v_mul_f32 v153, s36, v153\n"
"v_mul_f32 v154, s36, v154\n"
"v_mul_f32 v155, s36, v155\n"
"v_mul_f32 v156, s36, v156\n"
"v_mul_f32 v157, s36, v157\n"
"v_mul_f32 v158, s36, v158\n"
"buffer_store_dwordx4 v[120:123], v1, s[92:95], s86 offen sc1\n"
"v_accvgpr_read_b32 v112, a72\n"
"v_accvgpr_read_b32 v113, a73\n"
"v_accvgpr_read_b32 v114, a74\n"
"v_accvgpr_read_b32 v115, a75\n"
"v_accvgpr_read_b32 v116, a76\n"
"v_accvgpr_read_b32 v117, a77\n"
"v_mul_f32 v159, s36, v159\n"
"v_add_f32 v152, s35, v152\n"
"v_add_f32 v153, s35, v153\n"
"v_add_f32 v154, s35, v154\n"
"v_add_f32 v155, s35, v155\n"
"v_add_f32 v156, s35, v156\n"
"v_add_f32 v157, s35, v157\n"
"v_add_f32 v158, s35, v158\n"
"v_add_f32 v159, s35, v159\n"
"v_mul_f32 v152, v152, v128\n"
"v_accvgpr_read_b32 v118, a78\n"
"v_accvgpr_read_b32 v119, a79\n"
"v_mul_f32 v153, v153, v129\n"
"v_mul_f32 v154, v154, v130\n"
"v_mul_f32 v155, v155, v131\n"
"v_mul_f32 v156, v156, v132\n"
"v_mul_f32 v157, v157, v133\n"
"v_mul_f32 v158, v158, v134\n"
"v_mul_f32 v159, v159, v135\n"
"v_exp_f32 v152, -v152\n"
"v_exp_f32 v153, -v153\n"
"v_exp_f32 v154, -v154\n"
"buffer_load_dwordx4 a[72:75], v1, s[96:99], s56 offen offset:1024 sc1\n"
"buffer_load_dwordx4 a[76:79], v1, s[96:99], s57 offen offset:1024 sc1\n"
"s_add_u32 s86, s91, 8192\n"
"s_add_u32 s86, s86, s87\n"
"v_cvt_pk_bf16_f32 v120, v112, v113\n"
"v_cvt_pk_bf16_f32 v121, v114, v115\n"
"v_exp_f32 v155, -v155\n"
"v_exp_f32 v156, -v156\n"
"v_exp_f32 v157, -v157\n"
"v_exp_f32 v158, -v158\n"
"v_exp_f32 v159, -v159\n"
"v_add_f32 v152, 1.0, v152\n"
"v_add_f32 v153, 1.0, v153\n"
"v_add_f32 v154, 1.0, v154\n"
"v_add_f32 v155, 1.0, v155\n"
"v_add_f32 v156, 1.0, v156\n"
"v_cvt_pk_bf16_f32 v122, v116, v117\n"
"v_cvt_pk_bf16_f32 v123, v118, v119\n"
"v_add_f32 v157, 1.0, v157\n"
"v_add_f32 v158, 1.0, v158\n"
"v_add_f32 v159, 1.0, v159\n"
"v_rcp_f32 v152, v152\n"
"v_rcp_f32 v153, v153\n"
"v_rcp_f32 v154, v154\n"
"v_rcp_f32 v155, v155\n"
"v_rcp_f32 v156, v156\n"
"v_rcp_f32 v157, v157\n"
"v_rcp_f32 v158, v158\n"
"buffer_store_dwordx4 v[120:123], v1, s[92:95], s86 offen sc1\n"
"v_accvgpr_read_b32 v112, a80\n"
"v_accvgpr_read_b32 v113, a81\n"
"v_accvgpr_read_b32 v114, a82\n"
"v_accvgpr_read_b32 v115, a83\n"
"v_accvgpr_read_b32 v116, a84\n"
"v_accvgpr_read_b32 v117, a85\n"
"v_rcp_f32 v159, v159\n"
"v_mul_f32 v128, v128, v152\n"
"v_mul_f32 v129, v129, v153\n"
"v_mul_f32 v130, v130, v154\n"
"v_mul_f32 v131, v131, v155\n"
"v_mul_f32 v132, v132, v156\n"
"v_mul_f32 v133, v133, v157\n"
"v_mul_f32 v134, v134, v158\n"
"v_mul_f32 v135, v135, v159\n"
"v_mul_f32 v152, v136, v136\n"
"v_accvgpr_read_b32 v118, a86\n"
"v_accvgpr_read_b32 v119, a87\n"
"v_mul_f32 v153, v137, v137\n"
"v_mul_f32 v154, v138, v138\n"
"v_mul_f32 v155, v139, v139\n"
"v_mul_f32 v156, v140, v140\n"
"v_mul_f32 v157, v141, v141\n"
"v_mul_f32 v158, v142, v142\n"
"v_mul_f32 v159, v143, v143\n"
"v_mul_f32 v152, s36, v152\n"
"v_mul_f32 v153, s36, v153\n"
"v_mul_f32 v154, s36, v154\n"
"buffer_load_dwordx4 a[80:83], v1, s[96:99], s56 offen offset:2048 sc1\n"
"buffer_load_dwordx4 a[84:87], v1, s[96:99], s57 offen offset:2048 sc1\n"
"s_add_u32 s86, s91, 8192\n"
"s_add_u32 s86, s86, s88\n"
"v_cvt_pk_bf16_f32 v120, v112, v113\n"
"v_cvt_pk_bf16_f32 v121, v114, v115\n"
"v_mul_f32 v155, s36, v155\n"
"v_mul_f32 v156, s36, v156\n"
"v_mul_f32 v157, s36, v157\n"
"v_mul_f32 v158, s36, v158\n"
"v_mul_f32 v159, s36, v159\n"
"v_add_f32 v152, s35, v152\n"
"v_add_f32 v153, s35, v153\n"
"v_add_f32 v154, s35, v154\n"
"v_add_f32 v155, s35, v155\n"
"v_add_f32 v156, s35, v156\n"
"v_cvt_pk_bf16_f32 v122, v116, v117\n"
"v_cvt_pk_bf16_f32 v123, v118, v119\n"
"v_add_f32 v157, s35, v157\n"
"v_add_f32 v158, s35, v158\n"
"v_add_f32 v159, s35, v159\n"
"v_mul_f32 v152, v152, v136\n"
"v_mul_f32 v153, v153, v137\n"
"v_mul_f32 v154, v154, v138\n"
"v_mul_f32 v155, v155, v139\n"
"v_mul_f32 v156, v156, v140\n"
"v_mul_f32 v157, v157, v141\n"
"v_mul_f32 v158, v158, v142\n"
"buffer_store_dwordx4 v[120:123], v1, s[92:95], s86 offen sc1\n"
"v_accvgpr_read_b32 v112, a88\n"
"v_accvgpr_read_b32 v113, a89\n"
"v_accvgpr_read_b32 v114, a90\n"
"v_accvgpr_read_b32 v115, a91\n"
"v_accvgpr_read_b32 v116, a92\n"
"v_accvgpr_read_b32 v117, a93\n"
"v_mul_f32 v159, v159, v143\n"
"v_exp_f32 v152, -v152\n"
"v_exp_f32 v153, -v153\n"
"v_exp_f32 v154, -v154\n"
"v_exp_f32 v155, -v155\n"
"v_exp_f32 v156, -v156\n"
"v_exp_f32 v157, -v157\n"
"v_exp_f32 v158, -v158\n"
"v_exp_f32 v159, -v159\n"
"v_add_f32 v152, 1.0, v152\n"
"v_accvgpr_read_b32 v118, a94\n"
"v_accvgpr_read_b32 v119, a95\n"
"v_add_f32 v153, 1.0, v153\n"
"v_add_f32 v154, 1.0, v154\n"
"v_add_f32 v155, 1.0, v155\n"
"v_add_f32 v156, 1.0, v156\n"
"v_add_f32 v157, 1.0, v157\n"
"v_add_f32 v158, 1.0, v158\n"
"v_add_f32 v159, 1.0, v159\n"
"v_rcp_f32 v152, v152\n"
"v_rcp_f32 v153, v153\n"
"v_rcp_f32 v154, v154\n"
"buffer_load_dwordx4 a[88:91], v1, s[96:99], s56 offen offset:3072 sc1\n"
"buffer_load_dwordx4 a[92:95], v1, s[96:99], s57 offen offset:3072 sc1\n"
"s_add_u32 s56, s56, 4096\n"
"s_add_u32 s57, s57, 4096\n"
"s_add_u32 s86, s91, 8192\n"
"s_add_u32 s86, s86, s88\n"
"v_rcp_f32 v155, v155\n"
"v_rcp_f32 v156, v156\n"
"v_rcp_f32 v157, v157\n"
"v_rcp_f32 v158, v158\n"
"v_rcp_f32 v159, v159\n"
"v_mul_f32 v136, v136, v152\n"
"v_mul_f32 v137, v137, v153\n"
"v_mul_f32 v138, v138, v154\n"
"v_mul_f32 v139, v139, v155\n"
"v_mul_f32 v140, v140, v156\n"
"s_add_u32 s86, s86, s87\n"
"v_cvt_pk_bf16_f32 v120, v112, v113\n"
"v_cvt_pk_bf16_f32 v121, v114, v115\n"
"v_cvt_pk_bf16_f32 v122, v116, v117\n"
"v_cvt_pk_bf16_f32 v123, v118, v119\n"
"v_mul_f32 v141, v141, v157\n"
"v_mul_f32 v142, v142, v158\n"
"v_mul_f32 v143, v143, v159\n"
"v_cvt_pk_bf16_f32 v144, v128, v129\n"
"v_cvt_pk_bf16_f32 v145, v130, v131\n"
"v_cvt_pk_bf16_f32 v146, v132, v133\n"
"v_cvt_pk_bf16_f32 v147, v134, v135\n"
"v_cvt_pk_bf16_f32 v148, v136, v137\n"
"v_cvt_pk_bf16_f32 v149, v138, v139\n"
"v_cvt_pk_bf16_f32 v150, v140, v141\n"
"buffer_store_dwordx4 v[120:123], v1, s[92:95], s86 offen sc1\n"
"v_accvgpr_read_b32 v112, a96\n"
"v_accvgpr_read_b32 v113, a97\n"
"v_accvgpr_read_b32 v114, a98\n"
"v_accvgpr_read_b32 v115, a99\n"
"v_accvgpr_read_b32 v116, a100\n"
"v_accvgpr_read_b32 v117, a101\n"
"v_cvt_pk_bf16_f32 v151, v142, v143\n"
"buffer_store_dwordx4 v[144:147], v1, s[16:19], s34 offen offset:0 sc1\n"
"buffer_store_dwordx4 v[148:151], v1, s[16:19], s34 offen offset:1024 sc1\n"
"v_accvgpr_read_b32 v118, a102\n"
"v_accvgpr_read_b32 v119, a103\n"
"buffer_load_dwordx4 a[96:99], v1, s[96:99], s56 offen offset:0 sc1\n"
"buffer_load_dwordx4 a[100:103], v1, s[96:99], s57 offen offset:0 sc1\n"
"s_add_u32 s86, s91, 8192\n"
"s_add_u32 s86, s86, s89\n"
"v_cvt_pk_bf16_f32 v120, v112, v113\n"
"v_cvt_pk_bf16_f32 v121, v114, v115\n"
"v_cvt_pk_bf16_f32 v122, v116, v117\n"
"v_cvt_pk_bf16_f32 v123, v118, v119\n"
"buffer_store_dwordx4 v[120:123], v1, s[92:95], s86 offen sc1\n"
"v_accvgpr_read_b32 v112, a104\n"
"v_accvgpr_read_b32 v113, a105\n"
"v_accvgpr_read_b32 v114, a106\n"
"v_accvgpr_read_b32 v115, a107\n"
"v_accvgpr_read_b32 v116, a108\n"
"v_accvgpr_read_b32 v117, a109\n"
"v_accvgpr_read_b32 v118, a110\n"
"v_accvgpr_read_b32 v119, a111\n"
"buffer_load_dwordx4 a[104:107], v1, s[96:99], s56 offen offset:1024 sc1\n"
"buffer_load_dwordx4 a[108:111], v1, s[96:99], s57 offen offset:1024 sc1\n"
"s_add_u32 s86, s91, 8192\n"
"s_add_u32 s86, s86, s89\n"
"s_add_u32 s86, s86, s87\n"
"v_cvt_pk_bf16_f32 v120, v112, v113\n"
"v_cvt_pk_bf16_f32 v121, v114, v115\n"
"v_cvt_pk_bf16_f32 v122, v116, v117\n"
"v_cvt_pk_bf16_f32 v123, v118, v119\n"
"buffer_store_dwordx4 v[120:123], v1, s[92:95], s86 offen sc1\n"
"v_accvgpr_read_b32 v112, a112\n"
"v_accvgpr_read_b32 v113, a113\n"
"v_accvgpr_read_b32 v114, a114\n"
"v_accvgpr_read_b32 v115, a115\n"
"v_accvgpr_read_b32 v116, a116\n"
"v_accvgpr_read_b32 v117, a117\n"
"v_accvgpr_read_b32 v118, a118\n"
"v_accvgpr_read_b32 v119, a119\n"
"buffer_load_dwordx4 a[112:115], v1, s[96:99], s56 offen offset:2048 sc1\n"
"buffer_load_dwordx4 a[116:119], v1, s[96:99], s57 offen offset:2048 sc1\n"
"s_add_u32 s86, s91, 8192\n"
"s_add_u32 s86, s86, s90\n"
"v_cvt_pk_bf16_f32 v120, v112, v113\n"
"v_cvt_pk_bf16_f32 v121, v114, v115\n"
"v_cvt_pk_bf16_f32 v122, v116, v117\n"
"v_cvt_pk_bf16_f32 v123, v118, v119\n"
"buffer_store_dwordx4 v[120:123], v1, s[92:95], s86 offen sc1\n"
"v_accvgpr_read_b32 v112, a120\n"
"v_accvgpr_read_b32 v113, a121\n"
"v_accvgpr_read_b32 v114, a122\n"
"v_accvgpr_read_b32 v115, a123\n"
"v_accvgpr_read_b32 v116, a124\n"
"v_accvgpr_read_b32 v117, a125\n"
"v_accvgpr_read_b32 v118, a126\n"
"v_accvgpr_read_b32 v119, a127\n"
"buffer_load_dwordx4 a[120:123], v1, s[96:99], s56 offen offset:3072 sc1\n"
"buffer_load_dwordx4 a[124:127], v1, s[96:99], s57 offen offset:3072 sc1\n"
"s_add_u32 s56, s56, 4096\n"
"s_add_u32 s57, s57, 4096\n"
"s_add_u32 s86, s91, 8192\n"
"s_add_u32 s86, s86, s90\n"
"s_add_u32 s86, s86, s87\n"
"v_cvt_pk_bf16_f32 v120, v112, v113\n"
"v_cvt_pk_bf16_f32 v121, v114, v115\n"
"v_cvt_pk_bf16_f32 v122, v116, v117\n"
"v_cvt_pk_bf16_f32 v123, v118, v119\n"
"buffer_store_dwordx4 v[120:123], v1, s[92:95], s86 offen sc1\n"
"s_mov_b32 s47, s30\n"
"s_waitcnt vmcnt(31)\n"
"s_barrier\n"
"ds_read_b64_tr_b16 v[32:33], v10 offset:0\n"
"ds_read_b64_tr_b16 v[34:35], v10 offset:512\n"
"ds_read_b64_tr_b16 v[36:37], v11 offset:0\n"
"ds_read_b64_tr_b16 v[38:39], v11 offset:512\n"
"ds_read_b64_tr_b16 v[40:41], v10 offset:8192\n"
"ds_read_b64_tr_b16 v[42:43], v10 offset:8704\n"
"ds_read_b64_tr_b16 v[44:45], v11 offset:8192\n"
"ds_read_b64_tr_b16 v[46:47], v11 offset:8704\n"
"s_waitcnt lgkmcnt(0)\n"
"s_add_u32 m0, s54, 32768\n"
"v_mfma_f32_32x32x16_bf16 v[128:143], v[32:35], a[0:3], 0\n"
"buffer_load_dwordx4 v9, s[12:15], s55 offen offset:0 lds\n"
"ds_read_b64_tr_b16 v[48:49], v10 offset:2048\n"
"s_load_dwordx2 s[44:45], s[8:9], 48\n"
"v_mfma_f32_32x32x16_bf16 v[144:159], v[32:35], a[4:7], 0\n"
"ds_read_b64_tr_b16 v[50:51], v10 offset:2560\n"
"s_add_u32 m0, s54, 40832\n"
"v_mfma_f32_32x32x16_bf16 v[160:175], v[36:39], a[0:3], 0\n"
"buffer_load_dwordx4 v9, s[12:15], s55 offen offset:128 lds\n"
"ds_read_b64_tr_b16 v[52:53], v11 offset:2048\n"
"v_mfma_f32_32x32x16_bf16 v[176:191], v[36:39], a[4:7], 0\n"
"ds_read_b64_tr_b16 v[54:55], v11 offset:2560\n"
"s_add_u32 m0, s54, 33792\n"
"v_mfma_f32_32x32x16_bf16 v[192:207], v[40:43], a[0:3], 0\n"
"buffer_load_dwordx4 v9, s[12:15], s58 offen offset:0 lds\n"
"ds_read_b64_tr_b16 v[56:57], v10 offset:10240\n"
"v_mfma_f32_32x32x16_bf16 v[208:223], v[40:43], a[4:7], 0\n"
"ds_read_b64_tr_b16 v[58:59], v10 offset:10752\n"
"s_add_u32 m0, s54, 41856\n"
"v_mfma_f32_32x32x16_bf16 v[224:239], v[44:47], a[0:3], 0\n"
"buffer_load_dwordx4 v9, s[12:15], s58 offen offset:128 lds\n"
"ds_read_b64_tr_b16 v[60:61], v11 offset:10240\n"
"v_mfma_f32_32x32x16_bf16 v[240:255], v[44:47], a[4:7], 0\n"
"ds_read_b64_tr_b16 v[62:63], v11 offset:10752\n"
"s_add_u32 s55, s55, 524288\n"
"s_add_u32 s58, s58, 524288\n"
"s_waitcnt lgkmcnt(0)\n"
"v_mfma_f32_32x32x16_bf16 v[128:143], v[48:51], a[8:11], v[128:143]\n"
"ds_read_b64_tr_b16 v[32:33], v10 offset:4096\n"
"v_accvgpr_read_b32 v112, a128\n"
"v_accvgpr_read_b32 v113, a129\n"
"v_accvgpr_read_b32 v114, a130\n"
"v_accvgpr_read_b32 v115, a131\n"
"v_mfma_f32_32x32x16_bf16 v[144:159], v[48:51], a[12:15], v[144:159]\n"
"ds_read_b64_tr_b16 v[34:35], v10 offset:4608\n"
"v_accvgpr_read_b32 v116, a132\n"
"v_accvgpr_read_b32 v117, a133\n"
"v_accvgpr_read_b32 v118, a134\n"
"v_accvgpr_read_b32 v119, a135\n"
"v_mfma_f32_32x32x16_bf16 v[160:175], v[52:55], a[8:11], v[160:175]\n"
"ds_read_b64_tr_b16 v[36:37], v11 offset:4096\n"
"buffer_load_dwordx4 a[128:131], v1, s[96:99], s56 offen offset:0 sc1\n"
"buffer_load_dwordx4 a[132:135], v1, s[96:99], s57 offen offset:0 sc1\n"
"v_mfma_f32_32x32x16_bf16 v[176:191], v[52:55], a[12:15], v[176:191]\n"
"ds_read_b64_tr_b16 v[38:39], v11 offset:4608\n"
"s_add_u32 s86, s91, 16384\n"
"v_cvt_pk_bf16_f32 v120, v112, v113\n"
"v_cvt_pk_bf16_f32 v121, v114, v115\n"
"v_cvt_pk_bf16_f32 v122, v116, v117\n"
"v_mfma_f32_32x32x16_bf16 v[192:207], v[56:59], a[8:11], v[192:207]\n"
"ds_read_b64_tr_b16 v[40:41], v10 offset:12288\n"
"v_cvt_pk_bf16_f32 v123, v118, v119\n"
"v_mfma_f32_32x32x16_bf16 v[208:223], v[56:59], a[12:15], v[208:223]\n"
"ds_read_b64_tr_b16 v[42:43], v10 offset:12800\n"
"buffer_store_dwordx4 v[120:123], v1, s[92:95], s86 offen sc1\n"
"v_mfma_f32_32x32x16_bf16 v[224:239], v[60:63], a[8:11], v[224:239]\n"
"ds_read_b64_tr_b16 v[44:45], v11 offset:12288\n"
"v_mfma_f32_32x32x16_bf16 v[240:255], v[60:63], a[12:15], v[240:255]\n"
"ds_read_b64_tr_b16 v[46:47], v11 offset:12800\n"
"s_waitcnt lgkmcnt(0)\n"
"v_mfma_f32_32x32x16_bf16 v[128:143], v[32:35], a[16:19], v[128:143]\n"
"ds_read_b64_tr_b16 v[48:49], v10 offset:6144\n"
"v_accvgpr_read_b32 v112, a136\n"
"v_accvgpr_read_b32 v113, a137\n"
"v_accvgpr_read_b32 v114, a138\n"
"v_accvgpr_read_b32 v115, a139\n"
"v_mfma_f32_32x32x16_bf16 v[144:159], v[32:35], a[20:23], v[144:159]\n"
"ds_read_b64_tr_b16 v[50:51], v10 offset:6656\n"
"v_accvgpr_read_b32 v116, a140\n"
"v_accvgpr_read_b32 v117, a141\n"
"v_accvgpr_read_b32 v118, a142\n"
"v_accvgpr_read_b32 v119, a143\n"
"v_mfma_f32_32x32x16_bf16 v[160:175], v[36:39], a[16:19], v[160:175]\n"
"ds_read_b64_tr_b16 v[52:53], v11 offset:6144\n"
"buffer_load_dwordx4 a[136:139], v1, s[96:99], s56 offen offset:1024 sc1\n"
"buffer_load_dwordx4 a[140:143], v1, s[96:99], s57 offen offset:1024 sc1\n"
"v_mfma_f32_32x32x16_bf16 v[176:191], v[36:39], a[20:23], v[176:191]\n"
"ds_read_b64_tr_b16 v[54:55], v11 offset:6656\n"
"s_add_u32 s86, s91, 16384\n"
"s_add_u32 s86, s86, s87\n"
"v_cvt_pk_bf16_f32 v120, v112, v113\n"
"v_cvt_pk_bf16_f32 v121, v114, v115\n"
"v_mfma_f32_32x32x16_bf16 v[192:207], v[40:43], a[16:19], v[192:207]\n"
"ds_read_b64_tr_b16 v[56:57], v10 offset:14336\n"
"v_cvt_pk_bf16_f32 v122, v116, v117\n"
"v_cvt_pk_bf16_f32 v123, v118, v119\n"
"v_mfma_f32_32x32x16_bf16 v[208:223], v[40:43], a[20:23], v[208:223]\n"
"ds_read_b64_tr_b16 v[58:59], v10 offset:14848\n"
"buffer_store_dwordx4 v[120:123], v1, s[92:95], s86 offen sc1\n"
"v_mfma_f32_32x32x16_bf16 v[224:239], v[44:47], a[16:19], v[224:239]\n"
"ds_read_b64_tr_b16 v[60:61], v11 offset:14336\n"
"v_mfma_f32_32x32x16_bf16 v[240:255], v[44:47], a[20:23], v[240:255]\n"
"ds_read_b64_tr_b16 v[62:63], v11 offset:14848\n"
"s_waitcnt vmcnt(37)\n"
"s_barrier\n"
"s_waitcnt lgkmcnt(0)\n"
"v_mfma_f32_32x32x16_bf16 v[128:143], v[48:51], a[24:27], v[128:143]\n"
"ds_read_b64_tr_b16 v[32:33], v10 offset:16384\n"
"s_and_b32 s34, s28, 7\n"
"s_lshl_b32 s34, s34, 2\n"
"s_bfe_u32 s35, s28, 0x20003\n"
"s_add_u32 s30, s34, s35\n"
"v_mfma_f32_32x32x16_bf16 v[144:159], v[48:51], a[28:31], v[144:159]\n"
"ds_read_b64_tr_b16 v[34:35], v10 offset:16896\n"
"s_lshr_b32 s31, s28, 5\n"
"s_add_u32 s16, s50, 55574528\n"
"s_addc_u32 s17, s51, 0\n"
"s_and_b32 s17, s17, 0xffff\n"
"v_mfma_f32_32x32x16_bf16 v[160:175], v[52:55], a[24:27], v[160:175]\n"
"ds_read_b64_tr_b16 v[36:37], v11 offset:16384\n"
"s_mov_b32 s18, 2097152\n"
"s_mov_b32 s19, 0x20000\n"
"s_mov_b32 s20, s44\n"
"s_and_b32 s21, s45, 0xffff\n"
"v_mfma_f32_32x32x16_bf16 v[176:191], v[52:55], a[28:31], v[176:191]\n"
"ds_read_b64_tr_b16 v[38:39], v11 offset:16896\n"
"s_mov_b32 s22, 524288\n"
"s_mov_b32 s23, 0x20000\n"
"s_and_b32 s34, s29, 1\n"
"s_lshl_b32 s41, s30, 1\n"
"v_mfma_f32_32x32x16_bf16 v[192:207], v[56:59], a[24:27], v[192:207]\n"
"ds_read_b64_tr_b16 v[40:41], v10 offset:24576\n"
"s_add_u32 s41, s41, s34\n"
"s_lshl_b32 s40, s34, 15\n"
"s_lshr_b32 s35, s29, 1\n"
"s_lshl_b32 s35, s35, 12\n"
"v_mfma_f32_32x32x16_bf16 v[208:223], v[56:59], a[28:31], v[208:223]\n"
"ds_read_b64_tr_b16 v[42:43], v10 offset:25088\n"
"s_add_u32 s40, s40, s35\n"
"s_add_u32 s40, s40, 65536\n"
"s_mov_b32 s38, s35\n"
"s_lshl_b32 s34, s41, 5\n"
"v_mfma_f32_32x32x16_bf16 v[224:239], v[60:63], a[24:27], v[224:239]\n"
"ds_read_b64_tr_b16 v[44:45], v11 offset:24576\n"
"v_and_b32 v2, 31, v0\n"
"v_add_u32 v2, s34, v2\n"
"v_lshrrev_b32 v3, 5, v0\n"
"v_lshlrev_b32 v3, 9, v3\n"
"v_mfma_f32_32x32x16_bf16 v[240:255], v[60:63], a[28:31], v[240:255]\n"
"ds_read_b64_tr_b16 v[46:47], v11 offset:25088\n"
"v_subrev_u32 v4, 1, v2\n"
"v_lshrrev_b32 v5, 5, v4\n"
"s_waitcnt lgkmcnt(0)\n"
"s_add_u32 m0, s54, 49152\n"
"v_mfma_f32_32x32x16_bf16 v[128:143], v[32:35], a[32:35], v[128:143]\n"
"buffer_load_dwordx4 v9, s[12:15], s55 offen offset:0 lds\n"
"ds_read_b64_tr_b16 v[48:49], v10 offset:18432\n"
"v_lshlrev_b32 v5, 15, v5\n"
"v_and_b32 v24, 31, v4\n"
"v_mfma_f32_32x32x16_bf16 v[144:159], v[32:35], a[36:39], v[144:159]\n"
"ds_read_b64_tr_b16 v[50:51], v10 offset:18944\n"
"v_lshl_add_u32 v24, v24, 4, v5\n"
"v_add_u32 v24, v24, v3\n"
"s_add_u32 m0, s54, 57216\n"
"v_mfma_f32_32x32x16_bf16 v[160:175], v[36:39], a[32:35], v[160:175]\n"
"buffer_load_dwordx4 v9, s[12:15], s55 offen offset:128 lds\n"
"ds_read_b64_tr_b16 v[52:53], v11 offset:18432\n"
"v_mfma_f32_32x32x16_bf16 v[176:191], v[36:39], a[36:39], v[176:191]\n"
"ds_read_b64_tr_b16 v[54:55], v11 offset:18944\n"
"s_add_u32 m0, s54, 50176\n"
"v_mfma_f32_32x32x16_bf16 v[192:207], v[40:43], a[32:35], v[192:207]\n"
"buffer_load_dwordx4 v9, s[12:15], s58 offen offset:0 lds\n"
"ds_read_b64_tr_b16 v[56:57], v10 offset:26624\n"
"v_mfma_f32_32x32x16_bf16 v[208:223], v[40:43], a[36:39], v[208:223]\n"
"ds_read_b64_tr_b16 v[58:59], v10 offset:27136\n"
"s_add_u32 m0, s54, 58240\n"
"v_mfma_f32_32x32x16_bf16 v[224:239], v[44:47], a[32:35], v[224:239]\n"
"buffer_load_dwordx4 v9, s[12:15], s58 offen offset:128 lds\n"
"ds_read_b64_tr_b16 v[60:61], v11 offset:26624\n"
"v_mfma_f32_32x32x16_bf16 v[240:255], v[44:47], a[36:39], v[240:255]\n"
"ds_read_b64_tr_b16 v[62:63], v11 offset:27136\n"
"s_add_u32 s55, s55, 524288\n"
"s_add_u32 s58, s58, 524288\n"
"s_waitcnt lgkmcnt(0)\n"
"v_mfma_f32_32x32x16_bf16 v[128:143], v[48:51], a[40:43], v[128:143]\n"
"ds_read_b64_tr_b16 v[32:33], v10 offset:20480\n"
"v_cmp_gt_u32 vcc, 2048, v4\n"
"v_mov_b32 v5, 0x40000000\n"
"s_nop 0\n"
"v_cndmask_b32 v24, v5, v24, vcc\n"
"v_mfma_f32_32x32x16_bf16 v[144:159], v[48:51], a[44:47], v[144:159]\n"
"ds_read_b64_tr_b16 v[34:35], v10 offset:20992\n"
"v_add_u32 v4, 1, v2\n"
"v_lshrrev_b32 v5, 5, v4\n"
"v_lshlrev_b32 v5, 15, v5\n"
"v_and_b32 v25, 31, v4\n"
"v_mfma_f32_32x32x16_bf16 v[160:175], v[52:55], a[40:43], v[160:175]\n"
"ds_read_b64_tr_b16 v[36:37], v11 offset:20480\n"
"v_lshl_add_u32 v25, v25, 4, v5\n"
"v_add_u32 v25, v25, v3\n"
"v_mfma_f32_32x32x16_bf16 v[176:191], v[52:55], a[44:47], v[176:191]\n"
"ds_read_b64_tr_b16 v[38:39], v11 offset:20992\n"
"v_cmp_gt_u32 vcc, 2048, v4\n"
"v_mov_b32 v5, 0x40000000\n"
"s_nop 0\n"
"v_cndmask_b32 v25, v5, v25, vcc\n"
"v_mfma_f32_32x32x16_bf16 v[192:207], v[56:59], a[40:43], v[192:207]\n"
"ds_read_b64_tr_b16 v[40:41], v10 offset:28672\n"
"v_subrev_u32 v4, 2, v2\n"
"v_lshrrev_b32 v5, 5, v4\n"
"v_lshlrev_b32 v5, 15, v5\n"
"v_and_b32 v26, 31, v4\n"
"v_mfma_f32_32x32x16_bf16 v[208:223], v[56:59], a[44:47], v[208:223]\n"
"ds_read_b64_tr_b16 v[42:43], v10 offset:29184\n"
"v_lshl_add_u32 v26, v26, 4, v5\n"
"v_add_u32 v26, v26, v3\n"
"v_mfma_f32_32x32x16_bf16 v[224:239], v[60:63], a[40:43], v[224:239]\n"
"ds_read_b64_tr_b16 v[44:45], v11 offset:28672\n"
"v_cmp_gt_u32 vcc, 2048, v4\n"
"v_mov_b32 v5, 0x40000000\n"
"s_nop 0\n"
"v_cndmask_b32 v26, v5, v26, vcc\n"
"v_mfma_f32_32x32x16_bf16 v[240:255], v[60:63], a[44:47], v[240:255]\n"
"ds_read_b64_tr_b16 v[46:47], v11 offset:29184\n"
"v_add_u32 v4, 2, v2\n"
"v_lshrrev_b32 v5, 5, v4\n"
"s_waitcnt lgkmcnt(0)\n"
"v_mfma_f32_32x32x16_bf16 v[128:143], v[32:35], a[48:51], v[128:143]\n"
"ds_read_b64_tr_b16 v[48:49], v10 offset:22528\n"
"v_lshlrev_b32 v5, 15, v5\n"
"v_and_b32 v27, 31, v4\n"
"v_lshl_add_u32 v27, v27, 4, v5\n"
"v_add_u32 v27, v27, v3\n"
"v_mfma_f32_32x32x16_bf16 v[144:159], v[32:35], a[52:55], v[144:159]\n"
"ds_read_b64_tr_b16 v[50:51], v10 offset:23040\n"
"v_cmp_gt_u32 vcc, 2048, v4\n"
"v_mov_b32 v5, 0x40000000\n"
"s_nop 0\n"
"v_cndmask_b32 v27, v5, v27, vcc\n"
"v_mfma_f32_32x32x16_bf16 v[160:175], v[36:39], a[48:51], v[160:175]\n"
"ds_read_b64_tr_b16 v[52:53], v11 offset:22528\n"
"v_lshrrev_b32 v2, 3, v0\n"
"v_lshlrev_b32 v2, 10, v2\n"
"v_bfe_u32 v3, v0, 4, 1\n"
"v_lshlrev_b32 v3, 2, v3\n"
"v_mfma_f32_32x32x16_bf16 v[176:191], v[36:39], a[52:55], v[176:191]\n"
"ds_read_b64_tr_b16 v[54:55], v11 offset:23040\n"
"v_and_b32 v4, 7, v0\n"
"v_xor_b32 v3, v3, v4\n"
"v_lshl_add_u32 v12, v3, 4, v2\n"
"v_add_u32 v13, 8192, v12\n"
"v_mfma_f32_32x32x16_bf16 v[192:207], v[40:43], a[48:51], v[192:207]\n"
"ds_read_b64_tr_b16 v[56:57], v10 offset:30720\n"
"v_add_u32 v14, 16384, v12\n"
"v_add_u32 v15, 24576, v12\n"
"s_lshl_b32 s39, s29, 15\n"
"s_lshl_b32 s34, s31, 7\n"
"v_mfma_f32_32x32x16_bf16 v[208:223], v[40:43], a[52:55], v[208:223]\n"
"ds_read_b64_tr_b16 v[58:59], v10 offset:31232\n"
"s_add_u32 s39, s39, s34\n"
"s_lshl_b32 s43, s29, 12\n"
"s_lshr_b32 s34, s29, 1\n"
"s_lshl_b32 s35, s34, 15\n"
"v_mfma_f32_32x32x16_bf16 v[224:239], v[44:47], a[48:51], v[224:239]\n"
"ds_read_b64_tr_b16 v[60:61], v11 offset:30720\n"
"s_add_u32 s35, s35, 65536\n"
"v_add_u32 v28, s35, v1\n"
"v_lshrrev_b32 v2, 5, v0\n"
"v_bfe_u32 v3, v0, 2, 2\n"
"v_mfma_f32_32x32x16_bf16 v[240:255], v[44:47], a[52:55], v[240:255]\n"
"ds_read_b64_tr_b16 v[62:63], v11 offset:31232\n"
"v_lshl_add_u32 v2, v2, 2, v3\n"
"v_lshlrev_b32 v2, 7, v2\n"
"s_waitcnt vmcnt(10)\n"
"s_barrier\n"
"s_cmp_lg_u32 s29, 0\n"
"s_cbranch_scc1 .Lsig6\n"
"s_lshl_b32 s49, s47, 8\n"
"s_lshr_b32 s48, s28, 5\n"
"s_lshl_b32 s48, s48, 2\n"
"s_add_u32 s49, s49, s48\n"
"s_add_u32 s49, s49, 28928\n"
"v_mul_i32_i24 v124, 256, v0\n"
"v_add_u32 v124, s49, v124\n"
"v_mov_b32 v125, s11\n"
"s_mov_b64 exec, 1\n"
"global_store_dword v124, v125, s[50:51] sc1\n"
"s_mov_b64 exec, -1\n"
".Lsig6:\n"
"s_cmp_ge_u32 s28, 43\n"
"s_cbranch_scc1 .Lf47\n"
"s_cmp_lg_u32 s29, 0\n"
"s_cbranch_scc1 .Lsig8\n"
"s_lshl_b32 s49, s28, 2\n"
"s_add_u32 s49, s49, 41216\n"
"v_mul_i32_i24 v124, 256, v0\n"
"v_add_u32 v124, s49, v124\n"
"v_mov_b32 v125, s11\n"
"s_mov_b64 exec, 255\n"
"global_store_dword v124, v125, s[50:51] sc1\n"
"s_mov_b64 exec, -1\n"
".Lsig8:\n"
".Lf47:\n"
"s_waitcnt lgkmcnt(0)\n"
"v_mfma_f32_32x32x16_bf16 v[128:143], v[48:51], a[56:59], v[128:143]\n"
"ds_read_b64_tr_b16 v[32:33], v10 offset:32768\n"
"v_and_b32 v4, 1, v0\n"
"v_lshl_add_u32 v2, v4, 3, v2\n"
"v_bfe_u32 v4, v0, 4, 1\n"
"v_bfe_u32 v5, v0, 1, 1\n"
"v_mfma_f32_32x32x16_bf16 v[144:159], v[48:51], a[60:63], v[144:159]\n"
"ds_read_b64_tr_b16 v[34:35], v10 offset:33280\n"
"v_lshl_add_u32 v4, v4, 1, v5\n"
"s_and_b32 s34, s29, 1\n"
"s_lshl_b32 s34, s34, 2\n"
"v_add_u32 v4, s34, v4\n"
"v_mfma_f32_32x32x16_bf16 v[160:175], v[52:55], a[56:59], v[160:175]\n"
"ds_read_b64_tr_b16 v[36:37], v11 offset:32768\n"
"v_lshrrev_b32 v3, 1, v3\n"
"v_lshlrev_b32 v3, 2, v3\n"
"v_xor_b32 v4, v4, v3\n"
"v_lshl_add_u32 v29, v4, 4, v2\n"
"v_mfma_f32_32x32x16_bf16 v[176:191], v[52:55], a[60:63], v[176:191]\n"
"ds_read_b64_tr_b16 v[38:39], v11 offset:33280\n"
"v_lshrrev_b32 v30, 3, v0\n"
"v_min_u32 v30, 2, v30\n"
"v_add_u32 v30, s30, v30\n"
"v_subrev_u32 v30, 1, v30\n"
"v_mfma_f32_32x32x16_bf16 v[192:207], v[56:59], a[56:59], v[192:207]\n"
"ds_read_b64_tr_b16 v[40:41], v10 offset:40960\n"
"v_max_i32 v30, 0, v30\n"
"v_min_i32 v30, 31, v30\n"
"v_lshlrev_b32 v30, 8, v30\n"
"v_add_u32 v30, 28928, v30\n"
"v_mfma_f32_32x32x16_bf16 v[208:223], v[56:59], a[60:63], v[208:223]\n"
"ds_read_b64_tr_b16 v[42:43], v10 offset:41472\n"
"v_and_b32 v31, 7, v0\n"
"v_lshl_add_u32 v30, v31, 2, v30\n"
"v_mfma_f32_32x32x16_bf16 v[224:239], v[60:63], a[56:59], v[224:239]\n"
"ds_read_b64_tr_b16 v[44:45], v11 offset:40960\n"
"v_mfma_f32_32x32x16_bf16 v[240:255], v[60:63], a[60:63], v[240:255]\n"
"ds_read_b64_tr_b16 v[46:47], v11 offset:41472\n"
"s_waitcnt lgkmcnt(0)\n"
"s_add_u32 m0, s54, 65536\n"
"v_mfma_f32_32x32x16_bf16 v[128:143], v[32:35], a[64:67], v[128:143]\n"
"buffer_load_dwordx4 v9, s[12:15], s55 offen offset:0 lds\n"
"ds_read_b64_tr_b16 v[48:49], v10 offset:34816\n"
"v_accvgpr_read_b32 v112, a144\n"
"v_accvgpr_read_b32 v113, a145\n"
"v_mfma_f32_32x32x16_bf16 v[144:159], v[32:35], a[68:71], v[144:159]\n"
"ds_read_b64_tr_b16 v[50:51], v10 offset:35328\n"
"v_accvgpr_read_b32 v114, a146\n"
"v_accvgpr_read_b32 v115, a147\n"
"v_accvgpr_read_b32 v116, a148\n"
"s_add_u32 m0, s54, 73600\n"
"v_mfma_f32_32x32x16_bf16 v[160:175], v[36:39], a[64:67], v[160:175]\n"
"buffer_load_dwordx4 v9, s[12:15], s55 offen offset:128 lds\n"
"ds_read_b64_tr_b16 v[52:53], v11 offset:34816\n"
"v_accvgpr_read_b32 v117, a149\n"
"v_accvgpr_read_b32 v118, a150\n"
"v_mfma_f32_32x32x16_bf16 v[176:191], v[36:39], a[68:71], v[176:191]\n"
"ds_read_b64_tr_b16 v[54:55], v11 offset:35328\n"
"v_accvgpr_read_b32 v119, a151\n"
"s_add_u32 m0, s54, 66560\n"
"v_mfma_f32_32x32x16_bf16 v[192:207], v[40:43], a[64:67], v[192:207]\n"
"buffer_load_dwordx4 v9, s[12:15], s58 offen offset:0 lds\n"
"ds_read_b64_tr_b16 v[56:57], v10 offset:43008\n"
"buffer_load_dwordx4 a[144:147], v1, s[96:99], s56 offen offset:2048 sc1\n"
"v_mfma_f32_32x32x16_bf16 v[208:223], v[40:43], a[68:71], v[208:223]\n"
"ds_read_b64_tr_b16 v[58:59], v10 offset:43520\n"
"buffer_load_dwordx4 a[148:151], v1, s[96:99], s57 offen offset:2048 sc1\n"
"s_add_u32 s86, s91, 16384\n"
"s_add_u32 m0, s54, 74624\n"
"v_mfma_f32_32x32x16_bf16 v[224:239], v[44:47], a[64:67], v[224:239]\n"
"buffer_load_dwordx4 v9, s[12:15], s58 offen offset:128 lds\n"
"ds_read_b64_tr_b16 v[60:61], v11 offset:43008\n"
"s_add_u32 s86, s86, s88\n"
"v_cvt_pk_bf16_f32 v120, v112, v113\n"
"v_mfma_f32_32x32x16_bf16 v[240:255], v[44:47], a[68:71], v[240:255]\n"
"ds_read_b64_tr_b16 v[62:63], v11 offset:43520\n"
"v_cvt_pk_bf16_f32 v121, v114, v115\n"
"v_cvt_pk_bf16_f32 v122, v116, v117\n"
"s_add_u32 s55, s55, 524288\n"
"s_add_u32 s58, s58, 524288\n"
"s_waitcnt lgkmcnt(0)\n"
"v_mfma_f32_32x32x16_bf16 v[128:143], v[48:51], a[72:75], v[128:143]\n"
"ds_read_b64_tr_b16 v[32:33], v10 offset:36864\n"
"v_cvt_pk_bf16_f32 v123, v118, v119\n"
"v_mfma_f32_32x32x16_bf16 v[144:159], v[48:51], a[76:79], v[144:159]\n"
"ds_read_b64_tr_b16 v[34:35], v10 offset:37376\n"
"buffer_store_dwordx4 v[120:123], v1, s[92:95], s86 offen sc1\n"
"v_mfma_f32_32x32x16_bf16 v[160:175], v[52:55], a[72:75], v[160:175]\n"
"ds_read_b64_tr_b16 v[36:37], v11 offset:36864\n"
"v_mfma_f32_32x32x16_bf16 v[176:191], v[52:55], a[76:79], v[176:191]\n"
"ds_read_b64_tr_b16 v[38:39], v11 offset:37376\n"
"v_mfma_f32_32x32x16_bf16 v[192:207], v[56:59], a[72:75], v[192:207]\n"
"ds_read_b64_tr_b16 v[40:41], v10 offset:45056\n"
"v_mfma_f32_32x32x16_bf16 v[208:223], v[56:59], a[76:79], v[208:223]\n"
"ds_read_b64_tr_b16 v[42:43], v10 offset:45568\n"
"v_mfma_f32_32x32x16_bf16 v[224:239], v[60:63], a[72:75], v[224:239]\n"
"ds_read_b64_tr_b16 v[44:45], v11 offset:45056\n"
"v_mfma_f32_32x32x16_bf16 v[240:255], v[60:63], a[76:79], v[240:255]\n"
"ds_read_b64_tr_b16 v[46:47], v11 offset:45568\n"
"s_waitcnt lgkmcnt(0)\n"
"v_mfma_f32_32x32x16_bf16 v[128:143], v[32:35], a[80:83], v[128:143]\n"
"ds_read_b64_tr_b16 v[48:49], v10 offset:38912\n"
"v_accvgpr_read_b32 v112, a152\n"
"v_accvgpr_read_b32 v113, a153\n"
"v_accvgpr_read_b32 v114, a154\n"
"v_accvgpr_read_b32 v115, a155\n"
"v_mfma_f32_32x32x16_bf16 v[144:159], v[32:35], a[84:87], v[144:159]\n"
"ds_read_b64_tr_b16 v[50:51], v10 offset:39424\n"
"v_accvgpr_read_b32 v116, a156\n"
"v_accvgpr_read_b32 v117, a157\n"
"v_accvgpr_read_b32 v118, a158\n"
"v_accvgpr_read_b32 v119, a159\n"
"v_mfma_f32_32x32x16_bf16 v[160:175], v[36:39], a[80:83], v[160:175]\n"
"ds_read_b64_tr_b16 v[52:53], v11 offset:38912\n"
"buffer_load_dwordx4 a[152:155], v1, s[96:99], s56 offen offset:3072 sc1\n"
"buffer_load_dwordx4 a[156:159], v1, s[96:99], s57 offen offset:3072 sc1\n"
"v_mfma_f32_32x32x16_bf16 v[176:191], v[36:39], a[84:87], v[176:191]\n"
"ds_read_b64_tr_b16 v[54:55], v11 offset:39424\n"
"s_add_u32 s56, s56, 4096\n"
"s_add_u32 s57, s57, 4096\n"
"s_add_u32 s86, s91, 16384\n"
"s_add_u32 s86, s86, s88\n"
"v_mfma_f32_32x32x16_bf16 v[192:207], v[40:43], a[80:83], v[192:207]\n"
"ds_read_b64_tr_b16 v[56:57], v10 offset:47104\n"
"s_add_u32 s86, s86, s87\n"
"v_cvt_pk_bf16_f32 v120, v112, v113\n"
"v_cvt_pk_bf16_f32 v121, v114, v115\n"
"v_cvt_pk_bf16_f32 v122, v116, v117\n"
"v_mfma_f32_32x32x16_bf16 v[208:223], v[40:43], a[84:87], v[208:223]\n"
"ds_read_b64_tr_b16 v[58:59], v10 offset:47616\n"
"v_cvt_pk_bf16_f32 v123, v118, v119\n"
"v_mfma_f32_32x32x16_bf16 v[224:239], v[44:47], a[80:83], v[224:239]\n"
"ds_read_b64_tr_b16 v[60:61], v11 offset:47104\n"
"buffer_store_dwordx4 v[120:123], v1, s[92:95], s86 offen sc1\n"
"v_mfma_f32_32x32x16_bf16 v[240:255], v[44:47], a[84:87], v[240:255]\n"
"ds_read_b64_tr_b16 v[62:63], v11 offset:47616\n"
"s_waitcnt vmcnt(10)\n"
"s_barrier\n"
"s_waitcnt lgkmcnt(0)\n"
"v_mfma_f32_32x32x16_bf16 v[128:143], v[48:51], a[88:91], v[128:143]\n"
"ds_read_b64_tr_b16 v[32:33], v10 offset:49152\n"
"v_accvgpr_read_b32 v112, a160\n"
"v_accvgpr_read_b32 v113, a161\n"
"v_accvgpr_read_b32 v114, a162\n"
"v_accvgpr_read_b32 v115, a163\n"
"v_mfma_f32_32x32x16_bf16 v[144:159], v[48:51], a[92:95], v[144:159]\n"
"ds_read_b64_tr_b16 v[34:35], v10 offset:49664\n"
"v_accvgpr_read_b32 v116, a164\n"
"v_accvgpr_read_b32 v117, a165\n"
"v_accvgpr_read_b32 v118, a166\n"
"v_accvgpr_read_b32 v119, a167\n"
"v_mfma_f32_32x32x16_bf16 v[160:175], v[52:55], a[88:91], v[160:175]\n"
"ds_read_b64_tr_b16 v[36:37], v11 offset:49152\n"
"buffer_load_dwordx4 a[160:163], v1, s[96:99], s56 offen offset:0 sc1\n"
"buffer_load_dwordx4 a[164:167], v1, s[96:99], s57 offen offset:0 sc1\n"
"v_mfma_f32_32x32x16_bf16 v[176:191], v[52:55], a[92:95], v[176:191]\n"
"ds_read_b64_tr_b16 v[38:39], v11 offset:49664\n"
"s_add_u32 s86, s91, 16384\n"
"s_add_u32 s86, s86, s89\n"
"v_cvt_pk_bf16_f32 v120, v112, v113\n"
"v_cvt_pk_bf16_f32 v121, v114, v115\n"
"v_mfma_f32_32x32x16_bf16 v[192:207], v[56:59], a[88:91], v[192:207]\n"
"ds_read_b64_tr_b16 v[40:41], v10 offset:57344\n"
"v_cvt_pk_bf16_f32 v122, v116, v117\n"
"v_cvt_pk_bf16_f32 v123, v118, v119\n"
"v_mfma_f32_32x32x16_bf16 v[208:223], v[56:59], a[92:95], v[208:223]\n"
"ds_read_b64_tr_b16 v[42:43], v10 offset:57856\n"
"buffer_store_dwordx4 v[120:123], v1, s[92:95], s86 offen sc1\n"
"v_mfma_f32_32x32x16_bf16 v[224:239], v[60:63], a[88:91], v[224:239]\n"
"ds_read_b64_tr_b16 v[44:45], v11 offset:57344\n"
"v_mfma_f32_32x32x16_bf16 v[240:255], v[60:63], a[92:95], v[240:255]\n"
"ds_read_b64_tr_b16 v[46:47], v11 offset:57856\n"
"s_waitcnt lgkmcnt(0)\n"
"s_add_u32 m0, s54, 81920\n"
"v_mfma_f32_32x32x16_bf16 v[128:143], v[32:35], a[96:99], v[128:143]\n"
"buffer_load_dwordx4 v9, s[12:15], s55 offen offset:0 lds\n"
"ds_read_b64_tr_b16 v[48:49], v10 offset:51200\n"
"v_accvgpr_read_b32 v112, a168\n"
"v_accvgpr_read_b32 v113, a169\n"
"v_mfma_f32_32x32x16_bf16 v[144:159], v[32:35], a[100:103], v[144:159]\n"
"ds_read_b64_tr_b16 v[50:51], v10 offset:51712\n"
"v_accvgpr_read_b32 v114, a170\n"
"v_accvgpr_read_b32 v115, a171\n"
"v_accvgpr_read_b32 v116, a172\n"
"s_add_u32 m0, s54, 89984\n"
"v_mfma_f32_32x32x16_bf16 v[160:175], v[36:39], a[96:99], v[160:175]\n"
"buffer_load_dwordx4 v9, s[12:15], s55 offen offset:128 lds\n"
"ds_read_b64_tr_b16 v[52:53], v11 offset:51200\n"
"v_accvgpr_read_b32 v117, a173\n"
"v_accvgpr_read_b32 v118, a174\n"
"v_mfma_f32_32x32x16_bf16 v[176:191], v[36:39], a[100:103], v[176:191]\n"
"ds_read_b64_tr_b16 v[54:55], v11 offset:51712\n"
"v_accvgpr_read_b32 v119, a175\n"
"s_add_u32 m0, s54, 82944\n"
"v_mfma_f32_32x32x16_bf16 v[192:207], v[40:43], a[96:99], v[192:207]\n"
"buffer_load_dwordx4 v9, s[12:15], s58 offen offset:0 lds\n"
"ds_read_b64_tr_b16 v[56:57], v10 offset:59392\n"
"buffer_load_dwordx4 a[168:171], v1, s[96:99], s56 offen offset:1024 sc1\n"
"v_mfma_f32_32x32x16_bf16 v[208:223], v[40:43], a[100:103], v[208:223]\n"
"ds_read_b64_tr_b16 v[58:59], v10 offset:59904\n"
"buffer_load_dwordx4 a[172:175], v1, s[96:99], s57 offen offset:1024 sc1\n"
"s_add_u32 s86, s91, 16384\n"
"s_add_u32 m0, s54, 91008\n"
"v_mfma_f32_32x32x16_bf16 v[224:239], v[44:47], a[96:99], v[224:239]\n"
"buffer_load_dwordx4 v9, s[12:15], s58 offen offset:128 lds\n"
"ds_read_b64_tr_b16 v[60:61], v11 offset:59392\n"
"s_add_u32 s86, s86, s89\n"
"s_add_u32 s86, s86, s87\n"
"v_mfma_f32_32x32x16_bf16 v[240:255], v[44:47], a[100:103], v[240:255]\n"
"ds_read_b64_tr_b16 v[62:63], v11 offset:59904\n"
"v_cvt_pk_bf16_f32 v120, v112, v113\n"
"v_cvt_pk_bf16_f32 v121, v114, v115\n"
"s_add_u32 s55, s55, 524288\n"
"s_add_u32 s58, s58, 524288\n"
"s_waitcnt lgkmcnt(0)\n"
"v_mfma_f32_32x32x16_bf16 v[128:143], v[48:51], a[104:107], v[128:143]\n"
"ds_read_b64_tr_b16 v[32:33], v10 offset:53248\n"
"v_cvt_pk_bf16_f32 v122, v116, v117\n"
"v_cvt_pk_bf16_f32 v123, v118, v119\n"
"v_mfma_f32_32x32x16_bf16 v[144:159], v[48:51], a[108:111], v[144:159]\n"
"ds_read_b64_tr_b16 v[34:35], v10 offset:53760\n"
"buffer_store_dwordx4 v[120:123], v1, s[92:95], s86 offen sc1\n"
"v_mfma_f32_32x32x16_bf16 v[160:175], v[52:55], a[104:107], v[160:175]\n"
"ds_read_b64_tr_b16 v[36:37], v11 offset:53248\n"
"v_mfma_f32_32x32x16_bf16 v[176:191], v[52:55], a[108:111], v[176:191]\n"
"ds_read_b64_tr_b16 v[38:39], v11 offset:53760\n"
"v_mfma_f32_32x32x16_bf16 v[192:207], v[56:59], a[104:107], v[192:207]\n"
"ds_read_b64_tr_b16 v[40:41], v10 offset:61440\n"
"v_mfma_f32_32x32x16_bf16 v[208:223], v[56:59], a[108:111], v[208:223]\n"
"ds_read_b64_tr_b16 v[42:43], v10 offset:61952\n"
"v_mfma_f32_32x32x16_bf16 v[224:239], v[60:63], a[104:107], v[224:239]\n"
"ds_read_b64_tr_b16 v[44:45], v11 offset:61440\n"
"v_mfma_f32_32x32x16_bf16 v[240:255], v[60:63], a[108:111], v[240:255]\n"
"ds_read_b64_tr_b16 v[46:47], v11 offset:61952\n"
"s_waitcnt lgkmcnt(0)\n"
"v_mfma_f32_32x32x16_bf16 v[128:143], v[32:35], a[112:115], v[128:143]\n"
"ds_read_b64_tr_b16 v[48:49], v10 offset:55296\n"
"v_accvgpr_read_b32 v112, a176\n"
"v_accvgpr_read_b32 v113, a177\n"
"v_accvgpr_read_b32 v114, a178\n"
"v_accvgpr_read_b32 v115, a179\n"
"v_mfma_f32_32x32x16_bf16 v[144:159], v[32:35], a[116:119], v[144:159]\n"
"ds_read_b64_tr_b16 v[50:51], v10 offset:55808\n"
"v_accvgpr_read_b32 v116, a180\n"
"v_accvgpr_read_b32 v117, a181\n"
"v_accvgpr_read_b32 v118, a182\n"
"v_accvgpr_read_b32 v119, a183\n"
"v_mfma_f32_32x32x16_bf16 v[160:175], v[36:39], a[112:115], v[160:175]\n"
"ds_read_b64_tr_b16 v[52:53], v11 offset:55296\n"
"buffer_load_dwordx4 a[176:179], v1, s[96:99], s56 offen offset:2048 sc1\n"
"buffer_load_dwordx4 a[180:183], v1, s[96:99], s57 offen offset:2048 sc1\n"
"v_mfma_f32_32x32x16_bf16 v[176:191], v[36:39], a[116:119], v[176:191]\n"
"ds_read_b64_tr_b16 v[54:55], v11 offset:55808\n"
"s_add_u32 s86, s91, 16384\n"
"s_add_u32 s86, s86, s90\n"
"v_cvt_pk_bf16_f32 v120, v112, v113\n"
"v_cvt_pk_bf16_f32 v121, v114, v115\n"
"v_mfma_f32_32x32x16_bf16 v[192:207], v[40:43], a[112:115], v[192:207]\n"
"ds_read_b64_tr_b16 v[56:57], v10 offset:63488\n"
"v_cvt_pk_bf16_f32 v122, v116, v117\n"
"v_cvt_pk_bf16_f32 v123, v118, v119\n"
"v_mfma_f32_32x32x16_bf16 v[208:223], v[40:43], a[116:119], v[208:223]\n"
"ds_read_b64_tr_b16 v[58:59], v10 offset:64000\n"
"buffer_store_dwordx4 v[120:123], v1, s[92:95], s86 offen sc1\n"
"v_mfma_f32_32x32x16_bf16 v[224:239], v[44:47], a[112:115], v[224:239]\n"
"ds_read_b64_tr_b16 v[60:61], v11 offset:63488\n"
"v_mfma_f32_32x32x16_bf16 v[240:255], v[44:47], a[116:119], v[240:255]\n"
"ds_read_b64_tr_b16 v[62:63], v11 offset:64000\n"
"s_waitcnt vmcnt(17)\n"
"s_barrier\n"
"s_waitcnt lgkmcnt(0)\n"
"v_add_u32 v10, 0x10000, v10\n"
"v_add_u32 v11, 0x10000, v11\n"
"v_mfma_f32_32x32x16_bf16 v[128:143], v[48:51], a[120:123], v[128:143]\n"
"ds_read_b64_tr_b16 v[32:33], v10 offset:0\n"
"v_accvgpr_read_b32 v112, a184\n"
"v_accvgpr_read_b32 v113, a185\n"
"v_accvgpr_read_b32 v114, a186\n"
"v_accvgpr_read_b32 v115, a187\n"
"v_mfma_f32_32x32x16_bf16 v[144:159], v[48:51], a[124:127], v[144:159]\n"
"ds_read_b64_tr_b16 v[34:35], v10 offset:512\n"
"v_accvgpr_read_b32 v116, a188\n"
"v_accvgpr_read_b32 v117, a189\n"
"v_accvgpr_read_b32 v118, a190\n"
"v_accvgpr_read_b32 v119, a191\n"
"v_mfma_f32_32x32x16_bf16 v[160:175], v[52:55], a[120:123], v[160:175]\n"
"ds_read_b64_tr_b16 v[36:37], v11 offset:0\n"
"buffer_load_dwordx4 a[184:187], v1, s[96:99], s56 offen offset:3072 sc1\n"
"buffer_load_dwordx4 a[188:191], v1, s[96:99], s57 offen offset:3072 sc1\n"
"v_mfma_f32_32x32x16_bf16 v[176:191], v[52:55], a[124:127], v[176:191]\n"
"ds_read_b64_tr_b16 v[38:39], v11 offset:512\n"
"s_add_u32 s56, s56, 4096\n"
"s_add_u32 s57, s57, 4096\n"
"s_add_u32 s86, s91, 16384\n"
"s_add_u32 s86, s86, s90\n"
"v_mfma_f32_32x32x16_bf16 v[192:207], v[56:59], a[120:123], v[192:207]\n"
"ds_read_b64_tr_b16 v[40:41], v10 offset:8192\n"
"s_add_u32 s86, s86, s87\n"
"v_cvt_pk_bf16_f32 v120, v112, v113\n"
"v_cvt_pk_bf16_f32 v121, v114, v115\n"
"v_cvt_pk_bf16_f32 v122, v116, v117\n"
"v_mfma_f32_32x32x16_bf16 v[208:223], v[56:59], a[124:127], v[208:223]\n"
"ds_read_b64_tr_b16 v[42:43], v10 offset:8704\n"
"v_cvt_pk_bf16_f32 v123, v118, v119\n"
"v_mfma_f32_32x32x16_bf16 v[224:239], v[60:63], a[120:123], v[224:239]\n"
"ds_read_b64_tr_b16 v[44:45], v11 offset:8192\n"
"buffer_store_dwordx4 v[120:123], v1, s[92:95], s86 offen sc1\n"
"v_mfma_f32_32x32x16_bf16 v[240:255], v[60:63], a[124:127], v[240:255]\n"
"ds_read_b64_tr_b16 v[46:47], v11 offset:8704\n"
"s_waitcnt lgkmcnt(0)\n"
"s_add_u32 m0, s54, 98304\n"
"v_mfma_f32_32x32x16_bf16 v[128:143], v[32:35], a[128:131], v[128:143]\n"
"buffer_load_dwordx4 v9, s[12:15], s55 offen offset:0 lds\n"
"ds_read_b64_tr_b16 v[48:49], v10 offset:2048\n"
"v_accvgpr_read_b32 v112, a192\n"
"v_accvgpr_read_b32 v113, a193\n"
"v_mfma_f32_32x32x16_bf16 v[144:159], v[32:35], a[132:135], v[144:159]\n"
"ds_read_b64_tr_b16 v[50:51], v10 offset:2560\n"
"v_accvgpr_read_b32 v114, a194\n"
"v_accvgpr_read_b32 v115, a195\n"
"v_accvgpr_read_b32 v116, a196\n"
"s_add_u32 m0, s54, 106368\n"
"v_mfma_f32_32x32x16_bf16 v[160:175], v[36:39], a[128:131], v[160:175]\n"
"buffer_load_dwordx4 v9, s[12:15], s55 offen offset:128 lds\n"
"ds_read_b64_tr_b16 v[52:53], v11 offset:2048\n"
"v_accvgpr_read_b32 v117, a197\n"
"v_accvgpr_read_b32 v118, a198\n"
"v_mfma_f32_32x32x16_bf16 v[176:191], v[36:39], a[132:135], v[176:191]\n"
"ds_read_b64_tr_b16 v[54:55], v11 offset:2560\n"
"v_accvgpr_read_b32 v119, a199\n"
"s_add_u32 m0, s54, 99328\n"
"v_mfma_f32_32x32x16_bf16 v[192:207], v[40:43], a[128:131], v[192:207]\n"
"buffer_load_dwordx4 v9, s[12:15], s58 offen offset:0 lds\n"
"ds_read_b64_tr_b16 v[56:57], v10 offset:10240\n"
"buffer_load_dwordx4 a[192:195], v1, s[96:99], s56 offen offset:0 sc1\n"
"v_mfma_f32_32x32x16_bf16 v[208:223], v[40:43], a[132:135], v[208:223]\n"
"ds_read_b64_tr_b16 v[58:59], v10 offset:10752\n"
"buffer_load_dwordx4 a[196:199], v1, s[96:99], s57 offen offset:0 sc1\n"
"s_add_u32 s86, s91, 24576\n"
"s_add_u32 m0, s54, 107392\n"
"v_mfma_f32_32x32x16_bf16 v[224:239], v[44:47], a[128:131], v[224:239]\n"
"buffer_load_dwordx4 v9, s[12:15], s58 offen offset:128 lds\n"
"ds_read_b64_tr_b16 v[60:61], v11 offset:10240\n"
"v_cvt_pk_bf16_f32 v120, v112, v113\n"
"v_cvt_pk_bf16_f32 v121, v114, v115\n"
"v_mfma_f32_32x32x16_bf16 v[240:255], v[44:47], a[132:135], v[240:255]\n"
"ds_read_b64_tr_b16 v[62:63], v11 offset:10752\n"
"v_cvt_pk_bf16_f32 v122, v116, v117\n"
"v_cvt_pk_bf16_f32 v123, v118, v119\n"
"s_add_u32 s55, s55, 524288\n"
"s_add_u32 s58, s58, 524288\n"
"s_waitcnt lgkmcnt(0)\n"
"v_mfma_f32_32x32x16_bf16 v[128:143], v[48:51], a[136:139], v[128:143]\n"
"ds_read_b64_tr_b16 v[32:33], v10 offset:4096\n"
"buffer_store_dwordx4 v[120:123], v1, s[92:95], s86 offen sc1\n"
"v_mfma_f32_32x32x16_bf16 v[144:159], v[48:51], a[140:143], v[144:159]\n"
"ds_read_b64_tr_b16 v[34:35], v10 offset:4608\n"
"v_mfma_f32_32x32x16_bf16 v[160:175], v[52:55], a[136:139], v[160:175]\n"
"ds_read_b64_tr_b16 v[36:37], v11 offset:4096\n"
"v_mfma_f32_32x32x16_bf16 v[176:191], v[52:55], a[140:143], v[176:191]\n"
"ds_read_b64_tr_b16 v[38:39], v11 offset:4608\n"
"v_mfma_f32_32x32x16_bf16 v[192:207], v[56:59], a[136:139], v[192:207]\n"
"ds_read_b64_tr_b16 v[40:41], v10 offset:12288\n"
"v_mfma_f32_32x32x16_bf16 v[208:223], v[56:59], a[140:143], v[208:223]\n"
"ds_read_b64_tr_b16 v[42:43], v10 offset:12800\n"
"v_mfma_f32_32x32x16_bf16 v[224:239], v[60:63], a[136:139], v[224:239]\n"
"ds_read_b64_tr_b16 v[44:45], v11 offset:12288\n"
"v_mfma_f32_32x32x16_bf16 v[240:255], v[60:63], a[140:143], v[240:255]\n"
"ds_read_b64_tr_b16 v[46:47], v11 offset:12800\n"
"s_waitcnt lgkmcnt(0)\n"
"v_mfma_f32_32x32x16_bf16 v[128:143], v[32:35], a[144:147], v[128:143]\n"
"ds_read_b64_tr_b16 v[48:49], v10 offset:6144\n"
"v_accvgpr_read_b32 v112, a200\n"
"v_accvgpr_read_b32 v113, a201\n"
"v_accvgpr_read_b32 v114, a202\n"
"v_accvgpr_read_b32 v115, a203\n"
"v_mfma_f32_32x32x16_bf16 v[144:159], v[32:35], a[148:151], v[144:159]\n"
"ds_read_b64_tr_b16 v[50:51], v10 offset:6656\n"
"v_accvgpr_read_b32 v116, a204\n"
"v_accvgpr_read_b32 v117, a205\n"
"v_accvgpr_read_b32 v118, a206\n"
"v_accvgpr_read_b32 v119, a207\n"
"v_mfma_f32_32x32x16_bf16 v[160:175], v[36:39], a[144:147], v[160:175]\n"
"ds_read_b64_tr_b16 v[52:53], v11 offset:6144\n"
"buffer_load_dwordx4 a[200:203], v1, s[96:99], s56 offen offset:1024 sc1\n"
"buffer_load_dwordx4 a[204:207], v1, s[96:99], s57 offen offset:1024 sc1\n"
"v_mfma_f32_32x32x16_bf16 v[176:191], v[36:39], a[148:151], v[176:191]\n"
"ds_read_b64_tr_b16 v[54:55], v11 offset:6656\n"
"s_add_u32 s86, s91, 24576\n"
"s_add_u32 s86, s86, s87\n"
"v_cvt_pk_bf16_f32 v120, v112, v113\n"
"v_cvt_pk_bf16_f32 v121, v114, v115\n"
"v_mfma_f32_32x32x16_bf16 v[192:207], v[40:43], a[144:147], v[192:207]\n"
"ds_read_b64_tr_b16 v[56:57], v10 offset:14336\n"
"v_cvt_pk_bf16_f32 v122, v116, v117\n"
"v_cvt_pk_bf16_f32 v123, v118, v119\n"
"v_mfma_f32_32x32x16_bf16 v[208:223], v[40:43], a[148:151], v[208:223]\n"
"ds_read_b64_tr_b16 v[58:59], v10 offset:14848\n"
"buffer_store_dwordx4 v[120:123], v1, s[92:95], s86 offen sc1\n"
"v_mfma_f32_32x32x16_bf16 v[224:239], v[44:47], a[144:147], v[224:239]\n"
"ds_read_b64_tr_b16 v[60:61], v11 offset:14336\n"
"s_cmp_lg_u32 s29, 0\n"
"s_cbranch_scc1 .Lpi5\n"
"global_load_dword v31, v30, s[50:51] sc1\n"
".Lpi5:\n"
"v_mfma_f32_32x32x16_bf16 v[240:255], v[44:47], a[148:151], v[240:255]\n"
"ds_read_b64_tr_b16 v[62:63], v11 offset:14848\n"
"s_waitcnt vmcnt(17)\n"
"s_barrier\n"
"s_waitcnt lgkmcnt(0)\n"
"v_mfma_f32_32x32x16_bf16 v[128:143], v[48:51], a[152:155], v[128:143]\n"
"ds_read_b64_tr_b16 v[32:33], v10 offset:16384\n"
"v_accvgpr_read_b32 v112, a208\n"
"v_accvgpr_read_b32 v113, a209\n"
"v_accvgpr_read_b32 v114, a210\n"
"v_accvgpr_read_b32 v115, a211\n"
"v_mfma_f32_32x32x16_bf16 v[144:159], v[48:51], a[156:159], v[144:159]\n"
"ds_read_b64_tr_b16 v[34:35], v10 offset:16896\n"
"v_accvgpr_read_b32 v116, a212\n"
"v_accvgpr_read_b32 v117, a213\n"
"v_accvgpr_read_b32 v118, a214\n"
"v_accvgpr_read_b32 v119, a215\n"
"v_mfma_f32_32x32x16_bf16 v[160:175], v[52:55], a[152:155], v[160:175]\n"
"ds_read_b64_tr_b16 v[36:37], v11 offset:16384\n"
"buffer_load_dwordx4 a[208:211], v1, s[96:99], s56 offen offset:2048 sc1\n"
"buffer_load_dwordx4 a[212:215], v1, s[96:99], s57 offen offset:2048 sc1\n"
"v_mfma_f32_32x32x16_bf16 v[176:191], v[52:55], a[156:159], v[176:191]\n"
"ds_read_b64_tr_b16 v[38:39], v11 offset:16896\n"
"s_add_u32 s86, s91, 24576\n"
"s_add_u32 s86, s86, s88\n"
"v_cvt_pk_bf16_f32 v120, v112, v113\n"
"v_cvt_pk_bf16_f32 v121, v114, v115\n"
"v_mfma_f32_32x32x16_bf16 v[192:207], v[56:59], a[152:155], v[192:207]\n"
"ds_read_b64_tr_b16 v[40:41], v10 offset:24576\n"
"v_cvt_pk_bf16_f32 v122, v116, v117\n"
"v_cvt_pk_bf16_f32 v123, v118, v119\n"
"v_mfma_f32_32x32x16_bf16 v[208:223], v[56:59], a[156:159], v[208:223]\n"
"ds_read_b64_tr_b16 v[42:43], v10 offset:25088\n"
"buffer_store_dwordx4 v[120:123], v1, s[92:95], s86 offen sc1\n"
"v_mfma_f32_32x32x16_bf16 v[224:239], v[60:63], a[152:155], v[224:239]\n"
"ds_read_b64_tr_b16 v[44:45], v11 offset:24576\n"
"v_mfma_f32_32x32x16_bf16 v[240:255], v[60:63], a[156:159], v[240:255]\n"
"ds_read_b64_tr_b16 v[46:47], v11 offset:25088\n"
"s_waitcnt lgkmcnt(0)\n"
"s_add_u32 m0, s54, 114688\n"
"v_mfma_f32_32x32x16_bf16 v[128:143], v[32:35], a[160:163], v[128:143]\n"
"buffer_load_dwordx4 v9, s[12:15], s55 offen offset:0 lds\n"
"ds_read_b64_tr_b16 v[48:49], v10 offset:18432\n"
"v_accvgpr_read_b32 v112, a216\n"
"v_accvgpr_read_b32 v113, a217\n"
"v_mfma_f32_32x32x16_bf16 v[144:159], v[32:35], a[164:167], v[144:159]\n"
"ds_read_b64_tr_b16 v[50:51], v10 offset:18944\n"
"v_accvgpr_read_b32 v114, a218\n"
"v_accvgpr_read_b32 v115, a219\n"
"v_accvgpr_read_b32 v116, a220\n"
"s_add_u32 m0, s54, 122752\n"
"v_mfma_f32_32x32x16_bf16 v[160:175], v[36:39], a[160:163], v[160:175]\n"
"buffer_load_dwordx4 v9, s[12:15], s55 offen offset:128 lds\n"
"ds_read_b64_tr_b16 v[52:53], v11 offset:18432\n"
"v_accvgpr_read_b32 v117, a221\n"
"v_accvgpr_read_b32 v118, a222\n"
"v_mfma_f32_32x32x16_bf16 v[176:191], v[36:39], a[164:167], v[176:191]\n"
"ds_read_b64_tr_b16 v[54:55], v11 offset:18944\n"
"v_accvgpr_read_b32 v119, a223\n"
"s_add_u32 m0, s54, 115712\n"
"v_mfma_f32_32x32x16_bf16 v[192:207], v[40:43], a[160:163], v[192:207]\n"
"buffer_load_dwordx4 v9, s[12:15], s58 offen offset:0 lds\n"
"ds_read_b64_tr_b16 v[56:57], v10 offset:26624\n"
"buffer_load_dwordx4 a[216:219], v1, s[96:99], s56 offen offset:3072 sc1\n"
"v_mfma_f32_32x32x16_bf16 v[208:223], v[40:43], a[164:167], v[208:223]\n"
"ds_read_b64_tr_b16 v[58:59], v10 offset:27136\n"
"buffer_load_dwordx4 a[220:223], v1, s[96:99], s57 offen offset:3072 sc1\n"
"s_add_u32 s56, s56, 4096\n"
"s_add_u32 m0, s54, 123776\n"
"v_mfma_f32_32x32x16_bf16 v[224:239], v[44:47], a[160:163], v[224:239]\n"
"buffer_load_dwordx4 v9, s[12:15], s58 offen offset:128 lds\n"
"ds_read_b64_tr_b16 v[60:61], v11 offset:26624\n"
"s_add_u32 s57, s57, 4096\n"
"s_add_u32 s86, s91, 24576\n"
"v_mfma_f32_32x32x16_bf16 v[240:255], v[44:47], a[164:167], v[240:255]\n"
"ds_read_b64_tr_b16 v[62:63], v11 offset:27136\n"
"s_add_u32 s86, s86, s88\n"
"s_add_u32 s86, s86, s87\n"
"s_add_u32 s55, s55, 524288\n"
"s_add_u32 s58, s58, 524288\n"
"s_waitcnt lgkmcnt(0)\n"
"s_add_u32 m0, s43, 0\n"
"v_mfma_f32_32x32x16_bf16 v[128:143], v[48:51], a[168:171], v[128:143]\n"
"buffer_load_dwordx4 v12, s[20:23], s39 offen lds\n"
"ds_read_b64_tr_b16 v[32:33], v10 offset:20480\n"
"v_cvt_pk_bf16_f32 v120, v112, v113\n"
"v_cvt_pk_bf16_f32 v121, v114, v115\n"
"v_mfma_f32_32x32x16_bf16 v[144:159], v[48:51], a[172:175], v[144:159]\n"
"ds_read_b64_tr_b16 v[34:35], v10 offset:20992\n"
"v_cvt_pk_bf16_f32 v122, v116, v117\n"
"v_cvt_pk_bf16_f32 v123, v118, v119\n"
"s_add_u32 m0, s43, 1024\n"
"v_mfma_f32_32x32x16_bf16 v[160:175], v[52:55], a[168:171], v[160:175]\n"
"buffer_load_dwordx4 v13, s[20:23], s39 offen lds\n"
"ds_read_b64_tr_b16 v[36:37], v11 offset:20480\n"
"buffer_store_dwordx4 v[120:123], v1, s[92:95], s86 offen sc1\n"
"v_mfma_f32_32x32x16_bf16 v[176:191], v[52:55], a[172:175], v[176:191]\n"
"ds_read_b64_tr_b16 v[38:39], v11 offset:20992\n"
"v_mfma_f32_32x32x16_bf16 v[192:207], v[56:59], a[168:171], v[192:207]\n"
"ds_read_b64_tr_b16 v[40:41], v10 offset:28672\n"
"v_mfma_f32_32x32x16_bf16 v[208:223], v[56:59], a[172:175], v[208:223]\n"
"ds_read_b64_tr_b16 v[42:43], v10 offset:29184\n"
"v_mfma_f32_32x32x16_bf16 v[224:239], v[60:63], a[168:171], v[224:239]\n"
"ds_read_b64_tr_b16 v[44:45], v11 offset:28672\n"
"v_mfma_f32_32x32x16_bf16 v[240:255], v[60:63], a[172:175], v[240:255]\n"
"ds_read_b64_tr_b16 v[46:47], v11 offset:29184\n"
"s_waitcnt vmcnt(26)\n"
"s_waitcnt lgkmcnt(0)\n"
"s_add_u32 m0, s43, 2048\n"
"v_mfma_f32_32x32x16_bf16 v[128:143], v[32:35], a[176:179], v[128:143]\n"
"buffer_load_dwordx4 v14, s[20:23], s39 offen lds\n"
"ds_read_b64_tr_b16 v[48:49], v10 offset:22528\n"
"v_accvgpr_read_b32 v112, a224\n"
"v_accvgpr_read_b32 v113, a225\n"
"v_mfma_f32_32x32x16_bf16 v[144:159], v[32:35], a[180:183], v[144:159]\n"
"ds_read_b64_tr_b16 v[50:51], v10 offset:23040\n"
"v_accvgpr_read_b32 v114, a226\n"
"v_accvgpr_read_b32 v115, a227\n"
"v_accvgpr_read_b32 v116, a228\n"
"s_add_u32 m0, s43, 3072\n"
"v_mfma_f32_32x32x16_bf16 v[160:175], v[36:39], a[176:179], v[160:175]\n"
"buffer_load_dwordx4 v15, s[20:23], s39 offen lds\n"
"ds_read_b64_tr_b16 v[52:53], v11 offset:22528\n"
"v_accvgpr_read_b32 v117, a229\n"
"v_accvgpr_read_b32 v118, a230\n"
"v_mfma_f32_32x32x16_bf16 v[176:191], v[36:39], a[180:183], v[176:191]\n"
"ds_read_b64_tr_b16 v[54:55], v11 offset:23040\n"
"v_accvgpr_read_b32 v119, a231\n"
"v_mfma_f32_32x32x16_bf16 v[192:207], v[40:43], a[176:179], v[192:207]\n"
"ds_read_b64_tr_b16 v[56:57], v10 offset:30720\n"
"buffer_load_dwordx4 a[224:227], v1, s[96:99], s56 offen offset:0 sc1\n"
"buffer_load_dwordx4 a[228:231], v1, s[96:99], s57 offen offset:0 sc1\n"
"v_mfma_f32_32x32x16_bf16 v[208:223], v[40:43], a[180:183], v[208:223]\n"
"ds_read_b64_tr_b16 v[58:59], v10 offset:31232\n"
"s_add_u32 s86, s91, 24576\n"
"s_add_u32 s86, s86, s89\n"
"v_cvt_pk_bf16_f32 v120, v112, v113\n"
"v_cvt_pk_bf16_f32 v121, v114, v115\n"
"v_mfma_f32_32x32x16_bf16 v[224:239], v[44:47], a[176:179], v[224:239]\n"
"ds_read_b64_tr_b16 v[60:61], v11 offset:30720\n"
"v_cvt_pk_bf16_f32 v122, v116, v117\n"
"v_cvt_pk_bf16_f32 v123, v118, v119\n"
"v_mfma_f32_32x32x16_bf16 v[240:255], v[44:47], a[180:183], v[240:255]\n"
"ds_read_b64_tr_b16 v[62:63], v11 offset:31232\n"
"buffer_store_dwordx4 v[120:123], v1, s[92:95], s86 offen sc1\n"
"s_waitcnt vmcnt(21)\n"
"s_barrier\n"
"s_waitcnt lgkmcnt(0)\n"
"s_add_u32 s39, s39, 131072\n"
"s_add_u32 m0, s43, 16384\n"
"v_mfma_f32_32x32x16_bf16 v[128:143], v[48:51], a[184:187], v[128:143]\n"
"buffer_load_dwordx4 v12, s[20:23], s39 offen lds\n"
"ds_read_b64_tr_b16 v[32:33], v10 offset:32768\n"
"v_accvgpr_read_b32 v112, a232\n"
"v_accvgpr_read_b32 v113, a233\n"
"v_mfma_f32_32x32x16_bf16 v[144:159], v[48:51], a[188:191], v[144:159]\n"
"ds_read_b64_tr_b16 v[34:35], v10 offset:33280\n"
"v_accvgpr_read_b32 v114, a234\n"
"v_accvgpr_read_b32 v115, a235\n"
"v_accvgpr_read_b32 v116, a236\n"
"s_add_u32 m0, s43, 17408\n"
"v_mfma_f32_32x32x16_bf16 v[160:175], v[52:55], a[184:187], v[160:175]\n"
"buffer_load_dwordx4 v13, s[20:23], s39 offen lds\n"
"ds_read_b64_tr_b16 v[36:37], v11 offset:32768\n"
"v_accvgpr_read_b32 v117, a237\n"
"v_accvgpr_read_b32 v118, a238\n"
"v_mfma_f32_32x32x16_bf16 v[176:191], v[52:55], a[188:191], v[176:191]\n"
"ds_read_b64_tr_b16 v[38:39], v11 offset:33280\n"
"v_accvgpr_read_b32 v119, a239\n"
"v_mfma_f32_32x32x16_bf16 v[192:207], v[56:59], a[184:187], v[192:207]\n"
"ds_read_b64_tr_b16 v[40:41], v10 offset:40960\n"
"buffer_load_dwordx4 a[232:235], v1, s[96:99], s56 offen offset:1024 sc1\n"
"buffer_load_dwordx4 a[236:239], v1, s[96:99], s57 offen offset:1024 sc1\n"
"v_mfma_f32_32x32x16_bf16 v[208:223], v[56:59], a[188:191], v[208:223]\n"
"ds_read_b64_tr_b16 v[42:43], v10 offset:41472\n"
"s_add_u32 s86, s91, 24576\n"
"s_add_u32 s86, s86, s89\n"
"s_add_u32 s86, s86, s87\n"
"v_cvt_pk_bf16_f32 v120, v112, v113\n"
"v_mfma_f32_32x32x16_bf16 v[224:239], v[60:63], a[184:187], v[224:239]\n"
"ds_read_b64_tr_b16 v[44:45], v11 offset:40960\n"
"v_cvt_pk_bf16_f32 v121, v114, v115\n"
"v_cvt_pk_bf16_f32 v122, v116, v117\n"
"v_cvt_pk_bf16_f32 v123, v118, v119\n"
"v_mfma_f32_32x32x16_bf16 v[240:255], v[60:63], a[188:191], v[240:255]\n"
"ds_read_b64_tr_b16 v[46:47], v11 offset:41472\n"
"buffer_store_dwordx4 v[120:123], v1, s[92:95], s86 offen sc1\n"
"s_waitcnt lgkmcnt(0)\n"
"s_add_u32 m0, s43, 18432\n"
"v_mfma_f32_32x32x16_bf16 v[128:143], v[32:35], a[192:195], v[128:143]\n"
"buffer_load_dwordx4 v14, s[20:23], s39 offen lds\n"
"ds_read_b64_tr_b16 v[48:49], v10 offset:34816\n"
"v_accvgpr_read_b32 v112, a240\n"
"v_accvgpr_read_b32 v113, a241\n"
"v_mfma_f32_32x32x16_bf16 v[144:159], v[32:35], a[196:199], v[144:159]\n"
"ds_read_b64_tr_b16 v[50:51], v10 offset:35328\n"
"v_accvgpr_read_b32 v114, a242\n"
"v_accvgpr_read_b32 v115, a243\n"
"v_accvgpr_read_b32 v116, a244\n"
"s_add_u32 m0, s43, 19456\n"
"v_mfma_f32_32x32x16_bf16 v[160:175], v[36:39], a[192:195], v[160:175]\n"
"buffer_load_dwordx4 v15, s[20:23], s39 offen lds\n"
"ds_read_b64_tr_b16 v[52:53], v11 offset:34816\n"
"v_accvgpr_read_b32 v117, a245\n"
"v_accvgpr_read_b32 v118, a246\n"
"v_mfma_f32_32x32x16_bf16 v[176:191], v[36:39], a[196:199], v[176:191]\n"
"ds_read_b64_tr_b16 v[54:55], v11 offset:35328\n"
"v_accvgpr_read_b32 v119, a247\n"
"v_mfma_f32_32x32x16_bf16 v[192:207], v[40:43], a[192:195], v[192:207]\n"
"ds_read_b64_tr_b16 v[56:57], v10 offset:43008\n"
"buffer_load_dwordx4 a[240:243], v1, s[96:99], s56 offen offset:2048 sc1\n"
"buffer_load_dwordx4 a[244:247], v1, s[96:99], s57 offen offset:2048 sc1\n"
"v_mfma_f32_32x32x16_bf16 v[208:223], v[40:43], a[196:199], v[208:223]\n"
"ds_read_b64_tr_b16 v[58:59], v10 offset:43520\n"
"s_add_u32 s86, s91, 24576\n"
"s_add_u32 s86, s86, s90\n"
"v_cvt_pk_bf16_f32 v120, v112, v113\n"
"v_cvt_pk_bf16_f32 v121, v114, v115\n"
"v_mfma_f32_32x32x16_bf16 v[224:239], v[44:47], a[192:195], v[224:239]\n"
"ds_read_b64_tr_b16 v[60:61], v11 offset:43008\n"
"v_cvt_pk_bf16_f32 v122, v116, v117\n"
"v_cvt_pk_bf16_f32 v123, v118, v119\n"
"v_mfma_f32_32x32x16_bf16 v[240:255], v[44:47], a[196:199], v[240:255]\n"
"ds_read_b64_tr_b16 v[62:63], v11 offset:43520\n"
"buffer_store_dwordx4 v[120:123], v1, s[92:95], s86 offen sc1\n"
"s_waitcnt vmcnt(28)\n"
"s_waitcnt lgkmcnt(0)\n"
"s_add_u32 s39, s39, 131072\n"
"s_add_u32 m0, s43, 32768\n"
"v_mfma_f32_32x32x16_bf16 v[128:143], v[48:51], a[200:203], v[128:143]\n"
"buffer_load_dwordx4 v12, s[20:23], s39 offen lds\n"
"ds_read_b64_tr_b16 v[32:33], v10 offset:36864\n"
"v_accvgpr_read_b32 v112, a248\n"
"v_accvgpr_read_b32 v113, a249\n"
"v_mfma_f32_32x32x16_bf16 v[144:159], v[48:51], a[204:207], v[144:159]\n"
"ds_read_b64_tr_b16 v[34:35], v10 offset:37376\n"
"v_accvgpr_read_b32 v114, a250\n"
"v_accvgpr_read_b32 v115, a251\n"
"v_accvgpr_read_b32 v116, a252\n"
"s_add_u32 m0, s43, 33792\n"
"v_mfma_f32_32x32x16_bf16 v[160:175], v[52:55], a[200:203], v[160:175]\n"
"buffer_load_dwordx4 v13, s[20:23], s39 offen lds\n"
"ds_read_b64_tr_b16 v[36:37], v11 offset:36864\n"
"v_accvgpr_read_b32 v117, a253\n"
"v_accvgpr_read_b32 v118, a254\n"
"v_mfma_f32_32x32x16_bf16 v[176:191], v[52:55], a[204:207], v[176:191]\n"
"ds_read_b64_tr_b16 v[38:39], v11 offset:37376\n"
"v_accvgpr_read_b32 v119, a255\n"
"v_mfma_f32_32x32x16_bf16 v[192:207], v[56:59], a[200:203], v[192:207]\n"
"ds_read_b64_tr_b16 v[40:41], v10 offset:45056\n"
"buffer_load_dwordx4 a[248:251], v1, s[96:99], s56 offen offset:3072 sc1\n"
"buffer_load_dwordx4 a[252:255], v1, s[96:99], s57 offen offset:3072 sc1\n"
"v_mfma_f32_32x32x16_bf16 v[208:223], v[56:59], a[204:207], v[208:223]\n"
"ds_read_b64_tr_b16 v[42:43], v10 offset:45568\n"
"s_add_u32 s56, s56, 4096\n"
"s_add_u32 s57, s57, 4096\n"
"s_add_u32 s86, s91, 24576\n"
"s_add_u32 s86, s86, s90\n"
"v_mfma_f32_32x32x16_bf16 v[224:239], v[60:63], a[200:203], v[224:239]\n"
"ds_read_b64_tr_b16 v[44:45], v11 offset:45056\n"
"s_add_u32 s86, s86, s87\n"
"v_cvt_pk_bf16_f32 v120, v112, v113\n"
"v_cvt_pk_bf16_f32 v121, v114, v115\n"
"v_cvt_pk_bf16_f32 v122, v116, v117\n"
"v_mfma_f32_32x32x16_bf16 v[240:255], v[60:63], a[204:207], v[240:255]\n"
"ds_read_b64_tr_b16 v[46:47], v11 offset:45568\n"
"v_cvt_pk_bf16_f32 v123, v118, v119\n"
"s_waitcnt vmcnt(29)\n"
"s_waitcnt lgkmcnt(0)\n"
"s_add_u32 m0, s43, 34816\n"
"v_mfma_f32_32x32x16_bf16 v[128:143], v[32:35], a[208:211], v[128:143]\n"
"buffer_load_dwordx4 v14, s[20:23], s39 offen lds\n"
"ds_read_b64_tr_b16 v[48:49], v10 offset:38912\n"
"buffer_store_dwordx4 v[120:123], v1, s[92:95], s86 offen sc1\n"
"v_mfma_f32_32x32x16_bf16 v[144:159], v[32:35], a[212:215], v[144:159]\n"
"ds_read_b64_tr_b16 v[50:51], v10 offset:39424\n"
"s_add_u32 m0, s43, 35840\n"
"v_mfma_f32_32x32x16_bf16 v[160:175], v[36:39], a[208:211], v[160:175]\n"
"buffer_load_dwordx4 v15, s[20:23], s39 offen lds\n"
"ds_read_b64_tr_b16 v[52:53], v11 offset:38912\n"
"v_mfma_f32_32x32x16_bf16 v[176:191], v[36:39], a[212:215], v[176:191]\n"
"ds_read_b64_tr_b16 v[54:55], v11 offset:39424\n"
"v_mfma_f32_32x32x16_bf16 v[192:207], v[40:43], a[208:211], v[192:207]\n"
"ds_read_b64_tr_b16 v[56:57], v10 offset:47104\n"
"v_mfma_f32_32x32x16_bf16 v[208:223], v[40:43], a[212:215], v[208:223]\n"
"ds_read_b64_tr_b16 v[58:59], v10 offset:47616\n"
"v_mfma_f32_32x32x16_bf16 v[224:239], v[44:47], a[208:211], v[224:239]\n"
"ds_read_b64_tr_b16 v[60:61], v11 offset:47104\n"
"v_mfma_f32_32x32x16_bf16 v[240:255], v[44:47], a[212:215], v[240:255]\n"
"ds_read_b64_tr_b16 v[62:63], v11 offset:47616\n"
"s_waitcnt vmcnt(25)\n"
"s_barrier\n"
"s_waitcnt lgkmcnt(0)\n"
"s_add_u32 s39, s39, 131072\n"
"s_add_u32 m0, s43, 49152\n"
"v_mfma_f32_32x32x16_bf16 v[128:143], v[48:51], a[216:219], v[128:143]\n"
"buffer_load_dwordx4 v12, s[20:23], s39 offen lds\n"
"ds_read_b64_tr_b16 v[32:33], v10 offset:49152\n"
"v_mfma_f32_32x32x16_bf16 v[144:159], v[48:51], a[220:223], v[144:159]\n"
"ds_read_b64_tr_b16 v[34:35], v10 offset:49664\n"
"s_add_u32 m0, s43, 50176\n"
"v_mfma_f32_32x32x16_bf16 v[160:175], v[52:55], a[216:219], v[160:175]\n"
"buffer_load_dwordx4 v13, s[20:23], s39 offen lds\n"
"ds_read_b64_tr_b16 v[36:37], v11 offset:49152\n"
"v_mfma_f32_32x32x16_bf16 v[176:191], v[52:55], a[220:223], v[176:191]\n"
"ds_read_b64_tr_b16 v[38:39], v11 offset:49664\n"
"v_mfma_f32_32x32x16_bf16 v[192:207], v[56:59], a[216:219], v[192:207]\n"
"ds_read_b64_tr_b16 v[40:41], v10 offset:57344\n"
"v_mfma_f32_32x32x16_bf16 v[208:223], v[56:59], a[220:223], v[208:223]\n"
"ds_read_b64_tr_b16 v[42:43], v10 offset:57856\n"
"v_mfma_f32_32x32x16_bf16 v[224:239], v[60:63], a[216:219], v[224:239]\n"
"ds_read_b64_tr_b16 v[44:45], v11 offset:57344\n"
"v_mfma_f32_32x32x16_bf16 v[240:255], v[60:63], a[220:223], v[240:255]\n"
"ds_read_b64_tr_b16 v[46:47], v11 offset:57856\n"
"s_waitcnt vmcnt(20)\n"
"s_waitcnt lgkmcnt(0)\n"
"s_add_u32 m0, s43, 51200\n"
"v_mfma_f32_32x32x16_bf16 v[128:143], v[32:35], a[224:227], v[128:143]\n"
"buffer_load_dwordx4 v14, s[20:23], s39 offen lds\n"
"ds_read_b64_tr_b16 v[48:49], v10 offset:51200\n"
"v_mfma_f32_32x32x16_bf16 v[144:159], v[32:35], a[228:231], v[144:159]\n"
"ds_read_b64_tr_b16 v[50:51], v10 offset:51712\n"
"s_add_u32 m0, s43, 52224\n"
"v_mfma_f32_32x32x16_bf16 v[160:175], v[36:39], a[224:227], v[160:175]\n"
"buffer_load_dwordx4 v15, s[20:23], s39 offen lds\n"
"ds_read_b64_tr_b16 v[52:53], v11 offset:51200\n"
"v_mfma_f32_32x32x16_bf16 v[176:191], v[36:39], a[228:231], v[176:191]\n"
"ds_read_b64_tr_b16 v[54:55], v11 offset:51712\n"
"v_mfma_f32_32x32x16_bf16 v[192:207], v[40:43], a[224:227], v[192:207]\n"
"ds_read_b64_tr_b16 v[56:57], v10 offset:59392\n"
"v_mfma_f32_32x32x16_bf16 v[208:223], v[40:43], a[228:231], v[208:223]\n"
"ds_read_b64_tr_b16 v[58:59], v10 offset:59904\n"
"v_mfma_f32_32x32x16_bf16 v[224:239], v[44:47], a[224:227], v[224:239]\n"
"ds_read_b64_tr_b16 v[60:61], v11 offset:59392\n"
"v_mfma_f32_32x32x16_bf16 v[240:255], v[44:47], a[228:231], v[240:255]\n"
"ds_read_b64_tr_b16 v[62:63], v11 offset:59904\n"
"s_waitcnt vmcnt(17)\n"
"s_waitcnt lgkmcnt(0)\n"
"v_mfma_f32_32x32x16_bf16 v[128:143], v[48:51], a[232:235], v[128:143]\n"
"ds_read_b64_tr_b16 v[32:33], v10 offset:53248\n"
"v_mfma_f32_32x32x16_bf16 v[144:159], v[48:51], a[236:239], v[144:159]\n"
"ds_read_b64_tr_b16 v[34:35], v10 offset:53760\n"
"v_mfma_f32_32x32x16_bf16 v[160:175], v[52:55], a[232:235], v[160:175]\n"
"ds_read_b64_tr_b16 v[36:37], v11 offset:53248\n"
"v_mfma_f32_32x32x16_bf16 v[176:191], v[52:55], a[236:239], v[176:191]\n"
"ds_read_b64_tr_b16 v[38:39], v11 offset:53760\n"
"v_mfma_f32_32x32x16_bf16 v[192:207], v[56:59], a[232:235], v[192:207]\n"
"ds_read_b64_tr_b16 v[40:41], v10 offset:61440\n"
"v_mfma_f32_32x32x16_bf16 v[208:223], v[56:59], a[236:239], v[208:223]\n"
"ds_read_b64_tr_b16 v[42:43], v10 offset:61952\n"
"v_mfma_f32_32x32x16_bf16 v[224:239], v[60:63], a[232:235], v[224:239]\n"
"ds_read_b64_tr_b16 v[44:45], v11 offset:61440\n"
"v_mfma_f32_32x32x16_bf16 v[240:255], v[60:63], a[236:239], v[240:255]\n"
"ds_read_b64_tr_b16 v[46:47], v11 offset:61952\n"
"s_waitcnt vmcnt(12)\n"
"s_waitcnt lgkmcnt(0)\n"
"v_mfma_f32_32x32x16_bf16 v[128:143], v[32:35], a[240:243], v[128:143]\n"
"ds_read_b64_tr_b16 v[48:49], v10 offset:55296\n"
"v_mfma_f32_32x32x16_bf16 v[144:159], v[32:35], a[244:247], v[144:159]\n"
"ds_read_b64_tr_b16 v[50:51], v10 offset:55808\n"
"v_mfma_f32_32x32x16_bf16 v[160:175], v[36:39], a[240:243], v[160:175]\n"
"ds_read_b64_tr_b16 v[52:53], v11 offset:55296\n"
"v_mfma_f32_32x32x16_bf16 v[176:191], v[36:39], a[244:247], v[176:191]\n"
"ds_read_b64_tr_b16 v[54:55], v11 offset:55808\n"
"v_mfma_f32_32x32x16_bf16 v[192:207], v[40:43], a[240:243], v[192:207]\n"
"ds_read_b64_tr_b16 v[56:57], v10 offset:63488\n"
"v_mfma_f32_32x32x16_bf16 v[208:223], v[40:43], a[244:247], v[208:223]\n"
"ds_read_b64_tr_b16 v[58:59], v10 offset:64000\n"
"v_mfma_f32_32x32x16_bf16 v[224:239], v[44:47], a[240:243], v[224:239]\n"
"ds_read_b64_tr_b16 v[60:61], v11 offset:63488\n"
"v_mfma_f32_32x32x16_bf16 v[240:255], v[44:47], a[244:247], v[240:255]\n"
"ds_read_b64_tr_b16 v[62:63], v11 offset:64000\n"
"s_waitcnt vmcnt(7)\n"
"s_waitcnt lgkmcnt(0)\n"
"v_mfma_f32_32x32x16_bf16 v[128:143], v[48:51], a[248:251], v[128:143]\n"
"v_mfma_f32_32x32x16_bf16 v[144:159], v[48:51], a[252:255], v[144:159]\n"
"v_mfma_f32_32x32x16_bf16 v[160:175], v[52:55], a[248:251], v[160:175]\n"
"v_mfma_f32_32x32x16_bf16 v[176:191], v[52:55], a[252:255], v[176:191]\n"
"v_mfma_f32_32x32x16_bf16 v[192:207], v[56:59], a[248:251], v[192:207]\n"
"v_mfma_f32_32x32x16_bf16 v[208:223], v[56:59], a[252:255], v[208:223]\n"
"v_mfma_f32_32x32x16_bf16 v[224:239], v[60:63], a[248:251], v[224:239]\n"
"v_mfma_f32_32x32x16_bf16 v[240:255], v[60:63], a[252:255], v[240:255]\n"
"s_cmp_lg_u32 s29, 0\n"
"s_cbranch_scc1 .Lwok9\n"
"v_cmp_ne_u32 vcc, s11, v31\n"
"s_nop 1\n"
"s_cbranch_vccz .Lwok9\n"
"s_mov_b32 s49, 0\n"
".Lwait9:\n"
"s_sleep 2\n"
"global_load_dword v31, v30, s[50:51] sc1\n"
"s_waitcnt vmcnt(0)\n"
"v_cmp_ne_u32 vcc, s11, v31\n"
"s_nop 1\n"
"s_cbranch_vccz .Lwok9\n"
"s_add_u32 s49, s49, 1\n"
"s_cmp_lt_u32 s49, 32768\n"
"s_cbranch_scc1 .Lwait9\n"
".Lwok9:\n"
"s_barrier\n"
"s_add_u32 m0, s40, 0\n"
"s_nop 0\n"
"buffer_load_dwordx4 v24, s[16:19], s38 offen offset:0 sc1 lds\n"
"buffer_load_dwordx4 v24, s[16:19], s38 offen offset:1024 sc1 lds\n"
"buffer_load_dwordx4 v24, s[16:19], s38 offen offset:2048 sc1 lds\n"
"buffer_load_dwordx4 v24, s[16:19], s38 offen offset:3072 sc1 lds\n"
"s_nop 5\n"
"s_mov_b32 s64, 0x3e0293ee\n"
"s_mov_b32 s65, s64\n"
"v_pk_mul_f32 v[128:129], v[128:129], s[64:65]\n"
"v_pk_mul_f32 v[130:131], v[130:131], s[64:65]\n"
"v_pk_mul_f32 v[132:133], v[132:133], s[64:65]\n"
"v_pk_mul_f32 v[134:135], v[134:135], s[64:65]\n"
"v_cvt_pk_bf16_f32 v16, v128, v129\n"
"v_cvt_pk_bf16_f32 v17, v130, v131\n"
"v_cvt_pk_bf16_f32 v18, v132, v133\n"
"v_cvt_pk_bf16_f32 v19, v134, v135\n"
"v_accvgpr_write_b32 a0, v16\n"
"v_accvgpr_write_b32 a1, v17\n"
"v_accvgpr_write_b32 a2, v18\n"
"v_accvgpr_write_b32 a3, v19\n"
"s_add_u32 s38, s38, 8192\n"
"s_add_u32 m0, s40, 8192\n"
"s_nop 0\n"
"buffer_load_dwordx4 v25, s[16:19], s38 offen offset:0 sc1 lds\n"
"v_pk_mul_f32 v[136:137], v[136:137], s[64:65]\n"
"v_pk_mul_f32 v[138:139], v[138:139], s[64:65]\n"
"v_pk_mul_f32 v[140:141], v[140:141], s[64:65]\n"
"v_pk_mul_f32 v[142:143], v[142:143], s[64:65]\n"
"v_cvt_pk_bf16_f32 v20, v136, v137\n"
"v_cvt_pk_bf16_f32 v21, v138, v139\n"
"v_cvt_pk_bf16_f32 v22, v140, v141\n"
"v_cvt_pk_bf16_f32 v23, v142, v143\n"
"v_accvgpr_write_b32 a4, v20\n"
"v_accvgpr_write_b32 a5, v21\n"
"v_accvgpr_write_b32 a6, v22\n"
"v_accvgpr_write_b32 a7, v23\n"
"buffer_load_dwordx4 v25, s[16:19], s38 offen offset:1024 sc1 lds\n"
"buffer_load_dwordx4 v25, s[16:19], s38 offen offset:2048 sc1 lds\n"
"buffer_load_dwordx4 v25, s[16:19], s38 offen offset:3072 sc1 lds\n"
"v_pk_mul_f32 v[144:145], v[144:145], s[64:65]\n"
"v_pk_mul_f32 v[146:147], v[146:147], s[64:65]\n"
"v_pk_mul_f32 v[148:149], v[148:149], s[64:65]\n"
"v_pk_mul_f32 v[150:151], v[150:151], s[64:65]\n"
"v_cvt_pk_bf16_f32 v16, v144, v145\n"
"v_cvt_pk_bf16_f32 v17, v146, v147\n"
"v_cvt_pk_bf16_f32 v18, v148, v149\n"
"v_cvt_pk_bf16_f32 v19, v150, v151\n"
"v_accvgpr_write_b32 a32, v16\n"
"v_accvgpr_write_b32 a33, v17\n"
"v_accvgpr_write_b32 a34, v18\n"
"v_accvgpr_write_b32 a35, v19\n"
"s_add_u32 s38, s38, 8192\n"
"s_add_u32 m0, s40, 16384\n"
"s_nop 0\n"
"buffer_load_dwordx4 v26, s[16:19], s38 offen offset:0 sc1 lds\n"
"v_pk_mul_f32 v[152:153], v[152:153], s[64:65]\n"
"v_pk_mul_f32 v[154:155], v[154:155], s[64:65]\n"
"v_pk_mul_f32 v[156:157], v[156:157], s[64:65]\n"
"v_pk_mul_f32 v[158:159], v[158:159], s[64:65]\n"
"v_cvt_pk_bf16_f32 v20, v152, v153\n"
"v_cvt_pk_bf16_f32 v21, v154, v155\n"
"v_cvt_pk_bf16_f32 v22, v156, v157\n"
"v_cvt_pk_bf16_f32 v23, v158, v159\n"
"v_accvgpr_write_b32 a36, v20\n"
"v_accvgpr_write_b32 a37, v21\n"
"v_accvgpr_write_b32 a38, v22\n"
"v_accvgpr_write_b32 a39, v23\n"
"buffer_load_dwordx4 v26, s[16:19], s38 offen offset:1024 sc1 lds\n"
"buffer_load_dwordx4 v26, s[16:19], s38 offen offset:2048 sc1 lds\n"
"buffer_load_dwordx4 v26, s[16:19], s38 offen offset:3072 sc1 lds\n"
"v_pk_mul_f32 v[160:161], v[160:161], s[64:65]\n"
"v_pk_mul_f32 v[162:163], v[162:163], s[64:65]\n"
"v_pk_mul_f32 v[164:165], v[164:165], s[64:65]\n"
"v_pk_mul_f32 v[166:167], v[166:167], s[64:65]\n"
"v_cvt_pk_bf16_f32 v16, v160, v161\n"
"v_cvt_pk_bf16_f32 v17, v162, v163\n"
"v_cvt_pk_bf16_f32 v18, v164, v165\n"
"v_cvt_pk_bf16_f32 v19, v166, v167\n"
"v_accvgpr_write_b32 a8, v16\n"
"v_accvgpr_write_b32 a9, v17\n"
"v_accvgpr_write_b32 a10, v18\n"
"v_accvgpr_write_b32 a11, v19\n"
"s_add_u32 s38, s38, 8192\n"
"s_add_u32 m0, s40, 24576\n"
"s_nop 0\n"
"buffer_load_dwordx4 v27, s[16:19], s38 offen offset:0 sc1 lds\n"
"v_pk_mul_f32 v[168:169], v[168:169], s[64:65]\n"
"v_pk_mul_f32 v[170:171], v[170:171], s[64:65]\n"
"v_pk_mul_f32 v[172:173], v[172:173], s[64:65]\n"
"v_pk_mul_f32 v[174:175], v[174:175], s[64:65]\n"
"v_cvt_pk_bf16_f32 v20, v168, v169\n"
"v_cvt_pk_bf16_f32 v21, v170, v171\n"
"v_cvt_pk_bf16_f32 v22, v172, v173\n"
"v_cvt_pk_bf16_f32 v23, v174, v175\n"
"v_accvgpr_write_b32 a12, v20\n"
"v_accvgpr_write_b32 a13, v21\n"
"v_accvgpr_write_b32 a14, v22\n"
"v_accvgpr_write_b32 a15, v23\n"
"buffer_load_dwordx4 v27, s[16:19], s38 offen offset:1024 sc1 lds\n"
"buffer_load_dwordx4 v27, s[16:19], s38 offen offset:2048 sc1 lds\n"
"buffer_load_dwordx4 v27, s[16:19], s38 offen offset:3072 sc1 lds\n"
"v_pk_mul_f32 v[176:177], v[176:177], s[64:65]\n"
"v_pk_mul_f32 v[178:179], v[178:179], s[64:65]\n"
"v_pk_mul_f32 v[180:181], v[180:181], s[64:65]\n"
"v_pk_mul_f32 v[182:183], v[182:183], s[64:65]\n"
"v_cvt_pk_bf16_f32 v16, v176, v177\n"
"v_cvt_pk_bf16_f32 v17, v178, v179\n"
"v_cvt_pk_bf16_f32 v18, v180, v181\n"
"v_cvt_pk_bf16_f32 v19, v182, v183\n"
"v_accvgpr_write_b32 a40, v16\n"
"v_accvgpr_write_b32 a41, v17\n"
"v_accvgpr_write_b32 a42, v18\n"
"v_accvgpr_write_b32 a43, v19\n"
"s_lshr_b32 s34, s29, 1\n"
"s_lshl_b32 s41, s30, 1\n"
"s_add_u32 s41, s41, s34\n"
"s_and_b32 s34, s29, 1\n"
"s_lshl_b32 s42, s31, 1\n"
"s_add_u32 s42, s42, s34\n"
"v_pk_mul_f32 v[184:185], v[184:185], s[64:65]\n"
"v_pk_mul_f32 v[186:187], v[186:187], s[64:65]\n"
"v_pk_mul_f32 v[188:189], v[188:189], s[64:65]\n"
"v_pk_mul_f32 v[190:191], v[190:191], s[64:65]\n"
"v_cvt_pk_bf16_f32 v20, v184, v185\n"
"v_cvt_pk_bf16_f32 v21, v186, v187\n"
"v_cvt_pk_bf16_f32 v22, v188, v189\n"
"v_cvt_pk_bf16_f32 v23, v190, v191\n"
"v_accvgpr_write_b32 a44, v20\n"
"v_accvgpr_write_b32 a45, v21\n"
"v_accvgpr_write_b32 a46, v22\n"
"v_accvgpr_write_b32 a47, v23\n"
"s_add_u32 s24, s50, 51380224\n"
"s_addc_u32 s25, s51, 0\n"
"s_and_b32 s25, s25, 0xffff\n"
"s_mov_b32 s26, 2097152\n"
"s_mov_b32 s27, 0x20000\n"
"v_and_b32 v2, 31, v0\n"
"v_pk_mul_f32 v[192:193], v[192:193], s[64:65]\n"
"v_pk_mul_f32 v[194:195], v[194:195], s[64:65]\n"
"v_pk_mul_f32 v[196:197], v[196:197], s[64:65]\n"
"v_pk_mul_f32 v[198:199], v[198:199], s[64:65]\n"
"v_cvt_pk_bf16_f32 v16, v192, v193\n"
"v_cvt_pk_bf16_f32 v17, v194, v195\n"
"v_cvt_pk_bf16_f32 v18, v196, v197\n"
"v_cvt_pk_bf16_f32 v19, v198, v199\n"
"v_accvgpr_write_b32 a16, v16\n"
"v_accvgpr_write_b32 a17, v17\n"
"v_accvgpr_write_b32 a18, v18\n"
"v_accvgpr_write_b32 a19, v19\n"
"v_lshlrev_b32 v2, 4, v2\n"
"v_lshrrev_b32 v3, 5, v0\n"
"v_lshl_add_u32 v2, v3, 3, v2\n"
"s_lshl_b32 s34, s41, 15\n"
"s_lshl_b32 s35, s42, 11\n"
"s_add_u32 s34, s34, s35\n"
"v_pk_mul_f32 v[200:201], v[200:201], s[64:65]\n"
"v_pk_mul_f32 v[202:203], v[202:203], s[64:65]\n"
"v_pk_mul_f32 v[204:205], v[204:205], s[64:65]\n"
"v_pk_mul_f32 v[206:207], v[206:207], s[64:65]\n"
"v_cvt_pk_bf16_f32 v20, v200, v201\n"
"v_cvt_pk_bf16_f32 v21, v202, v203\n"
"v_cvt_pk_bf16_f32 v22, v204, v205\n"
"v_cvt_pk_bf16_f32 v23, v206, v207\n"
"v_accvgpr_write_b32 a20, v20\n"
"v_accvgpr_write_b32 a21, v21\n"
"v_accvgpr_write_b32 a22, v22\n"
"v_accvgpr_write_b32 a23, v23\n"
"buffer_load_dwordx2 v[112:113], v2, s[24:27], s34 offen offset:0 sc1\n"
"buffer_load_dwordx2 v[114:115], v2, s[24:27], s34 offen offset:512 sc1\n"
"buffer_load_dwordx2 v[116:117], v2, s[24:27], s34 offen offset:1024 sc1\n"
"v_pk_mul_f32 v[208:209], v[208:209], s[64:65]\n"
"v_pk_mul_f32 v[210:211], v[210:211], s[64:65]\n"
"v_pk_mul_f32 v[212:213], v[212:213], s[64:65]\n"
"v_pk_mul_f32 v[214:215], v[214:215], s[64:65]\n"
"v_cvt_pk_bf16_f32 v16, v208, v209\n"
"v_cvt_pk_bf16_f32 v17, v210, v211\n"
"v_cvt_pk_bf16_f32 v18, v212, v213\n"
"v_cvt_pk_bf16_f32 v19, v214, v215\n"
"v_accvgpr_write_b32 a48, v16\n"
"v_accvgpr_write_b32 a49, v17\n"
"v_accvgpr_write_b32 a50, v18\n"
"v_accvgpr_write_b32 a51, v19\n"
"buffer_load_dwordx2 v[118:119], v2, s[24:27], s34 offen offset:1536 sc1\n"
"v_pk_mul_f32 v[216:217], v[216:217], s[64:65]\n"
"v_pk_mul_f32 v[218:219], v[218:219], s[64:65]\n"
"v_pk_mul_f32 v[220:221], v[220:221], s[64:65]\n"
"v_pk_mul_f32 v[222:223], v[222:223], s[64:65]\n"
"v_cvt_pk_bf16_f32 v20, v216, v217\n"
"v_cvt_pk_bf16_f32 v21, v218, v219\n"
"v_cvt_pk_bf16_f32 v22, v220, v221\n"
"v_cvt_pk_bf16_f32 v23, v222, v223\n"
"v_accvgpr_write_b32 a52, v20\n"
"v_accvgpr_write_b32 a53, v21\n"
"v_accvgpr_write_b32 a54, v22\n"
"v_accvgpr_write_b32 a55, v23\n"
"v_pk_mul_f32 v[224:225], v[224:225], s[64:65]\n"
"v_pk_mul_f32 v[226:227], v[226:227], s[64:65]\n"
"v_pk_mul_f32 v[228:229], v[228:229], s[64:65]\n"
"v_pk_mul_f32 v[230:231], v[230:231], s[64:65]\n"
"v_cvt_pk_bf16_f32 v16, v224, v225\n"
"v_cvt_pk_bf16_f32 v17, v226, v227\n"
"v_cvt_pk_bf16_f32 v18, v228, v229\n"
"v_cvt_pk_bf16_f32 v19, v230, v231\n"
"v_accvgpr_write_b32 a24, v16\n"
"v_accvgpr_write_b32 a25, v17\n"
"v_accvgpr_write_b32 a26, v18\n"
"v_accvgpr_write_b32 a27, v19\n"
"v_pk_mul_f32 v[232:233], v[232:233], s[64:65]\n"
"v_pk_mul_f32 v[234:235], v[234:235], s[64:65]\n"
"v_pk_mul_f32 v[236:237], v[236:237], s[64:65]\n"
"v_pk_mul_f32 v[238:239], v[238:239], s[64:65]\n"
"v_cvt_pk_bf16_f32 v20, v232, v233\n"
"v_cvt_pk_bf16_f32 v21, v234, v235\n"
"v_cvt_pk_bf16_f32 v22, v236, v237\n"
"v_cvt_pk_bf16_f32 v23, v238, v239\n"
"v_accvgpr_write_b32 a28, v20\n"
"v_accvgpr_write_b32 a29, v21\n"
"v_accvgpr_write_b32 a30, v22\n"
"v_accvgpr_write_b32 a31, v23\n"
"v_pk_mul_f32 v[240:241], v[240:241], s[64:65]\n"
"v_pk_mul_f32 v[242:243], v[242:243], s[64:65]\n"
"v_pk_mul_f32 v[244:245], v[244:245], s[64:65]\n"
"v_pk_mul_f32 v[246:247], v[246:247], s[64:65]\n"
"v_cvt_pk_bf16_f32 v16, v240, v241\n"
"v_cvt_pk_bf16_f32 v17, v242, v243\n"
"v_cvt_pk_bf16_f32 v18, v244, v245\n"
"v_cvt_pk_bf16_f32 v19, v246, v247\n"
"v_accvgpr_write_b32 a56, v16\n"
"v_accvgpr_write_b32 a57, v17\n"
"v_accvgpr_write_b32 a58, v18\n"
"v_accvgpr_write_b32 a59, v19\n"
"v_pk_mul_f32 v[248:249], v[248:249], s[64:65]\n"
"v_pk_mul_f32 v[250:251], v[250:251], s[64:65]\n"
"v_pk_mul_f32 v[252:253], v[252:253], s[64:65]\n"
"v_pk_mul_f32 v[254:255], v[254:255], s[64:65]\n"
"v_cvt_pk_bf16_f32 v20, v248, v249\n"
"v_cvt_pk_bf16_f32 v21, v250, v251\n"
"v_cvt_pk_bf16_f32 v22, v252, v253\n"
"v_cvt_pk_bf16_f32 v23, v254, v255\n"
"v_accvgpr_write_b32 a60, v20\n"
"v_accvgpr_write_b32 a61, v21\n"
"v_accvgpr_write_b32 a62, v22\n"
"v_accvgpr_write_b32 a63, v23\n"
"s_cmp_lt_u32 s29, 2\n"
"s_cbranch_scc1 .Lq_nostash\n"
"s_sub_u32 s64, s29, 2\n"
"s_lshl_b32 s64, s64, 14\n"
"s_add_u32 s64, s64, 131072\n"
"v_add_u32 v16, s64, v1\n"
"ds_write_b128 v16, a[0:3] offset:0\n"
"ds_write_b128 v16, a[4:7] offset:1024\n"
"ds_write_b128 v16, a[8:11] offset:2048\n"
"ds_write_b128 v16, a[12:15] offset:3072\n"
"ds_write_b128 v16, a[16:19] offset:4096\n"
"ds_write_b128 v16, a[20:23] offset:5120\n"
"ds_write_b128 v16, a[24:27] offset:6144\n"
"ds_write_b128 v16, a[28:31] offset:7168\n"
"ds_write_b128 v16, a[32:35] offset:8192\n"
"ds_write_b128 v16, a[36:39] offset:9216\n"
"ds_write_b128 v16, a[40:43] offset:10240\n"
"ds_write_b128 v16, a[44:47] offset:11264\n"
"ds_write_b128 v16, a[48:51] offset:12288\n"
"ds_write_b128 v16, a[52:55] offset:13312\n"
"ds_write_b128 v16, a[56:59] offset:14336\n"
"ds_write_b128 v16, a[60:63] offset:15360\n"
".Lq_nostash:\n"
"s_lshr_b32 s47, s30, 1\n"
"s_lshl_b32 s47, s47, 8\n"
"s_and_b32 s46, s30, 1\n"
"s_lshl_b32 s46, s46, 5\n"
"s_add_u32 s47, s47, s46\n"
"s_lshl_b32 s46, s31, 2\n"
"s_add_u32 s47, s47, s46\n"
"s_mov_b32 s44, s41\n"
"s_mov_b32 s45, s42\n"
"s_and_b32 s34, s28, 7\n"
"s_lshr_b32 s35, s28, 3\n"
"s_and_b32 s36, s35, 3\n"
"s_lshl_b32 s34, s34, 2\n"
"s_add_u32 s30, s34, s36\n"
"s_lshr_b32 s31, s35, 2\n"
"s_lshl_b32 s36, s30, 19\n"
"s_add_u32 s36, s36, 1048576\n"
"s_add_u32 s34, s50, s36\n"
"s_addc_u32 s35, s51, 0\n"
"s_mov_b32 s16, s34\n"
"s_and_b32 s17, s35, 0xffff\n"
"s_mov_b32 s18, 524288\n"
"s_mov_b32 s19, 0x20000\n"
"s_add_u32 s34, s34, 16777216\n"
"s_addc_u32 s35, s35, 0\n"
"s_mov_b32 s20, s34\n"
"s_and_b32 s21, s35, 0xffff\n"
"s_mov_b32 s22, 524288\n"
"s_mov_b32 s23, 0x20000\n"
"s_add_u32 s34, s50, 34603008\n"
"s_addc_u32 s35, s51, 0\n"
"s_mov_b32 s24, s34\n"
"s_and_b32 s25, s35, 0xffff\n"
"s_mov_b32 s26, 16777216\n"
"s_mov_b32 s27, 0x20000\n"
"s_lshl_b32 s38, s29, 13\n"
"s_sub_u32 s53, 16, s31\n"
"s_cmp_lt_u32 s29, 2\n"
"s_cselect_b32 s55, 16384, 0\n"
"s_and_b32 s34, s29, 1\n"
"s_lshl_b32 s34, s34, 1\n"
"s_lshl_b32 s35, s31, 2\n"
"s_add_u32 s35, s35, s34\n"
"s_sub_u32 s36, 15, s31\n"
"s_lshl_b32 s36, s36, 2\n"
"s_add_u32 s36, s36, s34\n"
"s_lshl_b32 s37, s30, 13\n"
"s_lshl_b32 s58, s35, 18\n"
"s_add_u32 s58, s58, s37\n"
"s_lshl_b32 s59, s36, 18\n"
"s_add_u32 s59, s59, s37\n"
"v_and_b32 v2, 31, v0\n"
"v_lshrrev_b32 v3, 5, v0\n"
"v_lshlrev_b32 v3, 2, v3\n"
"v_sub_u32 v2, v2, v3\n"
"v_cmp_le_i32 s[64:65], 0, v2\n"
"v_cmp_le_i32 s[66:67], 1, v2\n"
"v_cmp_le_i32 s[68:69], 2, v2\n"
"v_cmp_le_i32 s[70:71], 3, v2\n"
"v_cmp_le_i32 s[72:73], 8, v2\n"
"v_cmp_le_i32 s[74:75], 9, v2\n"
"v_cmp_le_i32 s[76:77], 10, v2\n"
"v_cmp_le_i32 s[78:79], 11, v2\n"
"v_cmp_le_i32 s[80:81], 16, v2\n"
"v_cmp_le_i32 s[82:83], 17, v2\n"
"v_cmp_le_i32 s[84:85], 18, v2\n"
"v_cmp_le_i32 s[86:87], 19, v2\n"
"v_cmp_le_i32 s[88:89], 24, v2\n"
"v_cmp_le_i32 s[90:91], 25, v2\n"
"s_waitcnt vmcnt(12)\n"
"s_barrier\n"
"s_cmp_lg_u32 s29, 0\n"
"s_cbranch_scc1 .Lsig10\n"
"s_and_b32 s49, s28, 7\n"
"s_lshl_b32 s49, s49, 2\n"
"s_bfe_u32 s48, s28, 0x10003\n"
"s_lshl_b32 s48, s48, 1\n"
"s_add_u32 s49, s49, s48\n"
"s_lshl_b32 s49, s49, 8\n"
"s_bfe_u32 s48, s28, 0x10004\n"
"s_lshl_b32 s48, s48, 5\n"
"s_add_u32 s49, s49, s48\n"
"s_lshr_b32 s48, s28, 5\n"
"s_lshl_b32 s48, s48, 2\n"
"s_add_u32 s49, s49, s48\n"
"s_add_u32 s49, s49, 20736\n"
"v_mul_i32_i24 v126, 256, v0\n"
"v_add_u32 v126, s49, v126\n"
"v_mov_b32 v127, s11\n"
"s_mov_b64 exec, 3\n"
"global_store_dword v126, v127, s[50:51] sc1\n"
"s_mov_b64 exec, -1\n"
".Lsig10:\n"
"ds_read_b128 v[48:51], v28 offset:0\n"
"ds_read_b64_tr_b16 v[52:53], v29 offset:0\n"
"ds_read_b64_tr_b16 v[54:55], v29 offset:1024\n"
"ds_read_b128 v[56:59], v28 offset:1024\n"
"ds_read_b64_tr_b16 v[60:61], v29 offset:2048\n"
"ds_read_b64_tr_b16 v[62:63], v29 offset:3072\n"
"ds_read_b128 v[64:67], v28 offset:2048\n"
"ds_read_b64_tr_b16 v[68:69], v29 offset:4096\n"
"ds_read_b64_tr_b16 v[70:71], v29 offset:5120\n"
"ds_read_b128 v[72:75], v28 offset:3072\n"
"ds_read_b64_tr_b16 v[76:77], v29 offset:6144\n"
"ds_read_b64_tr_b16 v[78:79], v29 offset:7168\n"
"ds_read_b128 v[80:83], v28 offset:4096\n"
"ds_read_b64_tr_b16 v[84:85], v29 offset:8192\n"
"ds_read_b64_tr_b16 v[86:87], v29 offset:9216\n"
"ds_read_b128 v[88:91], v28 offset:5120\n"
"ds_read_b64_tr_b16 v[92:93], v29 offset:10240\n"
"ds_read_b64_tr_b16 v[94:95], v29 offset:11264\n"
"ds_read_b128 v[96:99], v28 offset:6144\n"
"ds_read_b64_tr_b16 v[100:101], v29 offset:12288\n"
"ds_read_b64_tr_b16 v[102:103], v29 offset:13312\n"
"ds_read_b128 v[104:107], v28 offset:7168\n"
"ds_read_b64_tr_b16 v[108:109], v29 offset:14336\n"
"ds_read_b64_tr_b16 v[110:111], v29 offset:15360\n"
"s_waitcnt lgkmcnt(12)\n"
"v_mfma_f32_32x32x16_bf16 v[128:143], v[52:55], v[48:51], 0\n"
"v_cmp_le_i32 s[92:93], 26, v2\n"
"v_cmp_le_i32 s[94:95], 27, v2\n"
"s_mov_b32 s60, 0\n"
"v_mfma_f32_32x32x16_bf16 v[128:143], v[60:63], v[56:59], v[128:143]\n"
"v_mfma_f32_32x32x16_bf16 v[128:143], v[68:71], v[64:67], v[128:143]\n"
"v_mfma_f32_32x32x16_bf16 v[128:143], v[76:79], v[72:75], v[128:143]\n"
"ds_read_b128 v[48:51], v28 offset:8192\n"
"ds_read_b64_tr_b16 v[52:53], v29 offset:16384\n"
"ds_read_b64_tr_b16 v[54:55], v29 offset:17408\n"
"ds_read_b128 v[56:59], v28 offset:9216\n"
"ds_read_b64_tr_b16 v[60:61], v29 offset:18432\n"
"ds_read_b64_tr_b16 v[62:63], v29 offset:19456\n"
"ds_read_b128 v[64:67], v28 offset:10240\n"
"ds_read_b64_tr_b16 v[68:69], v29 offset:20480\n"
"ds_read_b64_tr_b16 v[70:71], v29 offset:21504\n"
"ds_read_b128 v[72:75], v28 offset:11264\n"
"ds_read_b64_tr_b16 v[76:77], v29 offset:22528\n"
"ds_read_b64_tr_b16 v[78:79], v29 offset:23552\n"
"s_waitcnt lgkmcnt(12)\n"
"v_mfma_f32_32x32x16_bf16 v[128:143], v[84:87], v[80:83], v[128:143]\n"
"v_mfma_f32_32x32x16_bf16 v[128:143], v[92:95], v[88:91], v[128:143]\n"
"v_mfma_f32_32x32x16_bf16 v[128:143], v[100:103], v[96:99], v[128:143]\n"
"v_mfma_f32_32x32x16_bf16 v[128:143], v[108:111], v[104:107], v[128:143]\n"
"ds_read_b128 v[80:83], v28 offset:12288\n"
"ds_read_b64_tr_b16 v[84:85], v29 offset:24576\n"
"ds_read_b64_tr_b16 v[86:87], v29 offset:25600\n"
"ds_read_b128 v[88:91], v28 offset:13312\n"
"ds_read_b64_tr_b16 v[92:93], v29 offset:26624\n"
"ds_read_b64_tr_b16 v[94:95], v29 offset:27648\n"
"ds_read_b128 v[96:99], v28 offset:14336\n"
"ds_read_b64_tr_b16 v[100:101], v29 offset:28672\n"
"ds_read_b64_tr_b16 v[102:103], v29 offset:29696\n"
"ds_read_b128 v[104:107], v28 offset:15360\n"
"ds_read_b64_tr_b16 v[108:109], v29 offset:30720\n"
"ds_read_b64_tr_b16 v[110:111], v29 offset:31744\n"
"s_waitcnt lgkmcnt(12)\n"
"v_mfma_f32_32x32x16_bf16 v[128:143], v[52:55], v[48:51], v[128:143]\n"
"v_mfma_f32_32x32x16_bf16 v[128:143], v[60:63], v[56:59], v[128:143]\n"
"v_mfma_f32_32x32x16_bf16 v[128:143], v[68:71], v[64:67], v[128:143]\n"
"v_mfma_f32_32x32x16_bf16 v[128:143], v[76:79], v[72:75], v[128:143]\n"
"s_waitcnt lgkmcnt(0)\n"
"v_mfma_f32_32x32x16_bf16 v[128:143], v[84:87], v[80:83], v[128:143]\n"
"v_mfma_f32_32x32x16_bf16 v[128:143], v[92:95], v[88:91], v[128:143]\n"
"v_mfma_f32_32x32x16_bf16 v[128:143], v[100:103], v[96:99], v[128:143]\n"
"v_mfma_f32_32x32x16_bf16 v[128:143], v[108:111], v[104:107], v[128:143]\n"
"s_waitcnt vmcnt(4)\n"
"s_barrier\n"
"s_and_b32 s46, s28, 7\n"
"s_lshl_b32 s46, s46, 2\n"
"s_bfe_u32 s48, s28, 0x20003\n"
"s_add_u32 s46, s46, s48\n"
"s_lshl_b32 s46, s46, 8\n"
"s_add_u32 s46, s46, 20736\n"
"v_and_b32 v30, 15, v0\n"
"v_lshlrev_b32 v30, 2, v30\n"
"v_add_u32 v30, s46, v30\n"
"s_cmp_lg_u32 s29, 0\n"
"s_cbranch_scc1 .Lpi11\n"
"global_load_dword v31, v30, s[50:51] sc1\n"
".Lpi11:\n"
"ds_read_b128 v[48:51], v28 offset:16384\n"
"ds_read_b64_tr_b16 v[52:53], v29 offset:32768\n"
"ds_read_b64_tr_b16 v[54:55], v29 offset:33792\n"
"ds_read_b128 v[56:59], v28 offset:17408\n"
"ds_read_b64_tr_b16 v[60:61], v29 offset:34816\n"
"ds_read_b64_tr_b16 v[62:63], v29 offset:35840\n"
"ds_read_b128 v[64:67], v28 offset:18432\n"
"ds_read_b64_tr_b16 v[68:69], v29 offset:36864\n"
"ds_read_b64_tr_b16 v[70:71], v29 offset:37888\n"
"ds_read_b128 v[72:75], v28 offset:19456\n"
"ds_read_b64_tr_b16 v[76:77], v29 offset:38912\n"
"ds_read_b64_tr_b16 v[78:79], v29 offset:39936\n"
"ds_read_b128 v[80:83], v28 offset:20480\n"
"ds_read_b64_tr_b16 v[84:85], v29 offset:40960\n"
"ds_read_b64_tr_b16 v[86:87], v29 offset:41984\n"
"ds_read_b128 v[88:91], v28 offset:21504\n"
"ds_read_b64_tr_b16 v[92:93], v29 offset:43008\n"
"ds_read_b64_tr_b16 v[94:95], v29 offset:44032\n"
"ds_read_b128 v[96:99], v28 offset:22528\n"
"ds_read_b64_tr_b16 v[100:101], v29 offset:45056\n"
"ds_read_b64_tr_b16 v[102:103], v29 offset:46080\n"
"ds_read_b128 v[104:107], v28 offset:23552\n"
"ds_read_b64_tr_b16 v[108:109], v29 offset:47104\n"
"ds_read_b64_tr_b16 v[110:111], v29 offset:48128\n"
"s_waitcnt lgkmcnt(12)\n"
"v_mfma_f32_32x32x16_bf16 v[128:143], v[52:55], v[48:51], v[128:143]\n"
"v_mfma_f32_32x32x16_bf16 v[128:143], v[60:63], v[56:59], v[128:143]\n"
"v_mfma_f32_32x32x16_bf16 v[128:143], v[68:71], v[64:67], v[128:143]\n"
"v_mfma_f32_32x32x16_bf16 v[128:143], v[76:79], v[72:75], v[128:143]\n"
"ds_read_b128 v[48:51], v28 offset:24576\n"
"ds_read_b64_tr_b16 v[52:53], v29 offset:49152\n"
"ds_read_b64_tr_b16 v[54:55], v29 offset:50176\n"
"ds_read_b128 v[56:59], v28 offset:25600\n"
"ds_read_b64_tr_b16 v[60:61], v29 offset:51200\n"
"ds_read_b64_tr_b16 v[62:63], v29 offset:52224\n"
"ds_read_b128 v[64:67], v28 offset:26624\n"
"ds_read_b64_tr_b16 v[68:69], v29 offset:53248\n"
"ds_read_b64_tr_b16 v[70:71], v29 offset:54272\n"
"ds_read_b128 v[72:75], v28 offset:27648\n"
"ds_read_b64_tr_b16 v[76:77], v29 offset:55296\n"
"ds_read_b64_tr_b16 v[78:79], v29 offset:56320\n"
"s_waitcnt lgkmcnt(12)\n"
"v_mfma_f32_32x32x16_bf16 v[128:143], v[84:87], v[80:83], v[128:143]\n"
"v_mfma_f32_32x32x16_bf16 v[128:143], v[92:95], v[88:91], v[128:143]\n"
"v_mfma_f32_32x32x16_bf16 v[128:143], v[100:103], v[96:99], v[128:143]\n"
"v_mfma_f32_32x32x16_bf16 v[128:143], v[108:111], v[104:107], v[128:143]\n"
"ds_read_b128 v[80:83], v28 offset:28672\n"
"ds_read_b64_tr_b16 v[84:85], v29 offset:57344\n"
"ds_read_b64_tr_b16 v[86:87], v29 offset:58368\n"
"ds_read_b128 v[88:91], v28 offset:29696\n"
"ds_read_b64_tr_b16 v[92:93], v29 offset:59392\n"
"ds_read_b64_tr_b16 v[94:95], v29 offset:60416\n"
"ds_read_b128 v[96:99], v28 offset:30720\n"
"ds_read_b64_tr_b16 v[100:101], v29 offset:61440\n"
"ds_read_b64_tr_b16 v[102:103], v29 offset:62464\n"
"ds_read_b128 v[104:107], v28 offset:31744\n"
"ds_read_b64_tr_b16 v[108:109], v29 offset:63488\n"
"ds_read_b64_tr_b16 v[110:111], v29 offset:64512\n"
"s_waitcnt lgkmcnt(12)\n"
"v_mfma_f32_32x32x16_bf16 v[128:143], v[52:55], v[48:51], v[128:143]\n"
"v_mfma_f32_32x32x16_bf16 v[128:143], v[60:63], v[56:59], v[128:143]\n"
"v_mfma_f32_32x32x16_bf16 v[128:143], v[68:71], v[64:67], v[128:143]\n"
"v_mfma_f32_32x32x16_bf16 v[128:143], v[76:79], v[72:75], v[128:143]\n"
"s_waitcnt lgkmcnt(0)\n"
"v_mfma_f32_32x32x16_bf16 v[128:143], v[84:87], v[80:83], v[128:143]\n"
"v_mfma_f32_32x32x16_bf16 v[128:143], v[92:95], v[88:91], v[128:143]\n"
"v_mfma_f32_32x32x16_bf16 v[128:143], v[100:103], v[96:99], v[128:143]\n"
"v_mfma_f32_32x32x16_bf16 v[128:143], v[108:111], v[104:107], v[128:143]\n"
"s_waitcnt vmcnt(0)\n"
"s_cmp_lg_u32 s29, 0\n"
"s_cbranch_scc1 .Lwok12\n"
"v_cmp_ne_u32 vcc, s11, v31\n"
"s_nop 1\n"
"s_cbranch_vccz .Lwok12\n"
"s_mov_b32 s49, 0\n"
".Lwait12:\n"
"s_sleep 2\n"
"global_load_dword v31, v30, s[50:51] sc1\n"
"s_waitcnt vmcnt(0)\n"
"v_cmp_ne_u32 vcc, s11, v31\n"
"s_nop 1\n"
"s_cbranch_vccz .Lwok12\n"
"s_add_u32 s49, s49, 1\n"
"s_cmp_lt_u32 s49, 32768\n"
"s_cbranch_scc1 .Lwait12\n"
".Lwok12:\n"
"s_add_u32 s36, s50, 57671680\n"
"s_addc_u32 s37, s51, 0\n"
"s_mov_b32 s12, s36\n"
"s_and_b32 s13, s37, 0xffff\n"
"s_mov_b32 s14, 2097152\n"
"s_mov_b32 s15, 0x20000\n"
"s_lshl_b32 s34, s44, 15\n"
"s_lshl_b32 s35, s45, 11\n"
"s_add_u32 s34, s34, s35\n"
"v_cvt_pk_bf16_f32 v144, v128, v129\n"
"v_cvt_pk_bf16_f32 v145, v130, v131\n"
"v_cvt_pk_bf16_f32 v146, v132, v133\n"
"v_cvt_pk_bf16_f32 v147, v134, v135\n"
"v_cvt_pk_bf16_f32 v148, v136, v137\n"
"v_cvt_pk_bf16_f32 v149, v138, v139\n"
"v_cvt_pk_bf16_f32 v150, v140, v141\n"
"v_cvt_pk_bf16_f32 v151, v142, v143\n"
"v_lshlrev_b32 v128, 16, v144\n"
"v_and_b32 v129, 0xffff0000, v144\n"
"v_lshlrev_b32 v2, 16, v112\n"
"v_and_b32 v3, 0xffff0000, v112\n"
"v_add_f32 v128, v128, v2\n"
"v_add_f32 v129, v129, v3\n"
"v_cvt_pk_bf16_f32 v144, v128, v129\n"
"v_lshlrev_b32 v130, 16, v145\n"
"v_and_b32 v131, 0xffff0000, v145\n"
"v_lshlrev_b32 v2, 16, v113\n"
"v_and_b32 v3, 0xffff0000, v113\n"
"v_add_f32 v130, v130, v2\n"
"v_add_f32 v131, v131, v3\n"
"v_cvt_pk_bf16_f32 v145, v130, v131\n"
"v_lshlrev_b32 v132, 16, v146\n"
"v_and_b32 v133, 0xffff0000, v146\n"
"v_lshlrev_b32 v2, 16, v114\n"
"v_and_b32 v3, 0xffff0000, v114\n"
"v_add_f32 v132, v132, v2\n"
"v_add_f32 v133, v133, v3\n"
"v_cvt_pk_bf16_f32 v146, v132, v133\n"
"v_lshlrev_b32 v134, 16, v147\n"
"v_and_b32 v135, 0xffff0000, v147\n"
"v_lshlrev_b32 v2, 16, v115\n"
"v_and_b32 v3, 0xffff0000, v115\n"
"v_add_f32 v134, v134, v2\n"
"v_add_f32 v135, v135, v3\n"
"v_cvt_pk_bf16_f32 v147, v134, v135\n"
"v_lshlrev_b32 v136, 16, v148\n"
"v_and_b32 v137, 0xffff0000, v148\n"
"v_lshlrev_b32 v2, 16, v116\n"
"v_and_b32 v3, 0xffff0000, v116\n"
"v_add_f32 v136, v136, v2\n"
"v_add_f32 v137, v137, v3\n"
"v_cvt_pk_bf16_f32 v148, v136, v137\n"
"v_lshlrev_b32 v138, 16, v149\n"
"v_and_b32 v139, 0xffff0000, v149\n"
"v_lshlrev_b32 v2, 16, v117\n"
"v_and_b32 v3, 0xffff0000, v117\n"
"v_add_f32 v138, v138, v2\n"
"v_add_f32 v139, v139, v3\n"
"v_cvt_pk_bf16_f32 v149, v138, v139\n"
"v_lshlrev_b32 v140, 16, v150\n"
"v_and_b32 v141, 0xffff0000, v150\n"
"v_lshlrev_b32 v2, 16, v118\n"
"v_and_b32 v3, 0xffff0000, v118\n"
"v_add_f32 v140, v140, v2\n"
"v_add_f32 v141, v141, v3\n"
"v_cvt_pk_bf16_f32 v150, v140, v141\n"
"v_lshlrev_b32 v142, 16, v151\n"
"v_and_b32 v143, 0xffff0000, v151\n"
"v_lshlrev_b32 v2, 16, v119\n"
"v_and_b32 v3, 0xffff0000, v119\n"
"v_add_f32 v142, v142, v2\n"
"v_add_f32 v143, v143, v3\n"
"v_cvt_pk_bf16_f32 v151, v142, v143\n"
"v_mov_b32 v4, 0\n"
"v_mov_b32 v5, 0\n"
"v_lshlrev_b32 v2, 16, v144\n"
"v_and_b32 v3, 0xffff0000, v144\n"
"v_add_f32 v4, v4, v2\n"
"v_fmac_f32 v5, v2, v2\n"
"v_add_f32 v4, v4, v3\n"
"v_fmac_f32 v5, v3, v3\n"
"v_lshlrev_b32 v2, 16, v145\n"
"v_and_b32 v3, 0xffff0000, v145\n"
"v_add_f32 v4, v4, v2\n"
"v_fmac_f32 v5, v2, v2\n"
"v_add_f32 v4, v4, v3\n"
"v_fmac_f32 v5, v3, v3\n"
"v_lshlrev_b32 v2, 16, v146\n"
"v_and_b32 v3, 0xffff0000, v146\n"
"v_add_f32 v4, v4, v2\n"
"v_fmac_f32 v5, v2, v2\n"
"v_add_f32 v4, v4, v3\n"
"v_fmac_f32 v5, v3, v3\n"
"v_lshlrev_b32 v2, 16, v147\n"
"v_and_b32 v3, 0xffff0000, v147\n"
"v_add_f32 v4, v4, v2\n"
"v_fmac_f32 v5, v2, v2\n"
"v_add_f32 v4, v4, v3\n"
"v_fmac_f32 v5, v3, v3\n"
"v_lshlrev_b32 v2, 16, v148\n"
"v_and_b32 v3, 0xffff0000, v148\n"
"v_add_f32 v4, v4, v2\n"
"v_fmac_f32 v5, v2, v2\n"
"v_add_f32 v4, v4, v3\n"
"v_fmac_f32 v5, v3, v3\n"
"v_lshlrev_b32 v2, 16, v149\n"
"v_and_b32 v3, 0xffff0000, v149\n"
"v_add_f32 v4, v4, v2\n"
"v_fmac_f32 v5, v2, v2\n"
"v_add_f32 v4, v4, v3\n"
"v_fmac_f32 v5, v3, v3\n"
"v_lshlrev_b32 v2, 16, v150\n"
"v_and_b32 v3, 0xffff0000, v150\n"
"v_add_f32 v4, v4, v2\n"
"v_fmac_f32 v5, v2, v2\n"
"v_add_f32 v4, v4, v3\n"
"v_fmac_f32 v5, v3, v3\n"
"v_lshlrev_b32 v2, 16, v151\n"
"v_and_b32 v3, 0xffff0000, v151\n"
"v_add_f32 v4, v4, v2\n"
"v_fmac_f32 v5, v2, v2\n"
"v_add_f32 v4, v4, v3\n"
"v_fmac_f32 v5, v3, v3\n"
"v_mov_b32 v2, v4\n"
"v_mov_b32 v3, v5\n"
"s_nop 1\n"
"v_permlane32_swap_b32 v4, v2\n"
"v_permlane32_swap_b32 v5, v3\n"
"v_add_f32 v4, v4, v2\n"
"v_add_f32 v5, v5, v3\n"
"s_add_u32 s36, s50, 74448896\n"
"s_addc_u32 s37, s51, 0\n"
"s_mov_b32 s96, s36\n"
"s_and_b32 s97, s37, 0xffff\n"
"s_mov_b32 s98, 262144\n"
"s_mov_b32 s99, 0x20000\n"
"s_lshl_b32 s35, s44, 12\n"
"s_lshl_b32 s36, s45, 8\n"
"s_add_u32 s35, s35, s36\n"
"v_lshlrev_b32 v2, 3, v0\n"
"s_mov_b64 exec, 0xffffffff\n"
"buffer_store_dwordx2 v[4:5], v2, s[96:99], s35 offen sc1\n"
"s_mov_b64 exec, -1\n"
"buffer_store_dwordx4 v[144:147], v1, s[12:15], s34 offen offset:0 sc1\n"
"buffer_store_dwordx4 v[148:151], v1, s[12:15], s34 offen offset:1024 sc1\n"
"s_barrier\n"
"s_mov_b32 s52, 0\n"
"s_lshl_b32 s39, s52, 15\n"
"s_add_u32 s39, s39, s38\n"
"s_cmp_lt_u32 s52, s53\n"
"s_cselect_b32 s39, s39, 0x40000000\n"
"s_add_u32 s40, s39, 4096\n"
"s_and_b32 s41, s52, 1\n"
"s_lshl_b32 s41, s41, 16\n"
"s_add_u32 s41, s41, s38\n"
"s_add_u32 s52, s52, 1\n"
"s_add_u32 m0, s41, 0\n"
"s_nop 0\n"
"buffer_load_dwordx4 v1, s[16:19], s39 offen offset:0 sc1 lds\n"
"buffer_load_dwordx4 v1, s[16:19], s39 offen offset:1024 sc1 lds\n"
"buffer_load_dwordx4 v1, s[16:19], s39 offen offset:2048 sc1 lds\n"
"buffer_load_dwordx4 v1, s[16:19], s39 offen offset:3072 sc1 lds\n"
"s_add_u32 m0, s41, 4096\n"
"s_nop 0\n"
"buffer_load_dwordx4 v1, s[16:19], s40 offen offset:0 sc1 lds\n"
"buffer_load_dwordx4 v1, s[16:19], s40 offen offset:1024 sc1 lds\n"
"buffer_load_dwordx4 v1, s[16:19], s40 offen offset:2048 sc1 lds\n"
"buffer_load_dwordx4 v1, s[16:19], s40 offen offset:3072 sc1 lds\n"
"s_add_u32 m0, s41, 32768\n"
"s_nop 0\n"
"buffer_load_dwordx4 v1, s[20:23], s39 offen offset:0 sc1 lds\n"
"buffer_load_dwordx4 v1, s[20:23], s39 offen offset:1024 sc1 lds\n"
"buffer_load_dwordx4 v1, s[20:23], s39 offen offset:2048 sc1 lds\n"
"buffer_load_dwordx4 v1, s[20:23], s39 offen offset:3072 sc1 lds\n"
"s_add_u32 m0, s41, 36864\n"
"s_nop 0\n"
"buffer_load_dwordx4 v1, s[20:23], s40 offen offset:0 sc1 lds\n"
"buffer_load_dwordx4 v1, s[20:23], s40 offen offset:1024 sc1 lds\n"
"buffer_load_dwordx4 v1, s[20:23], s40 offen offset:2048 sc1 lds\n"
"buffer_load_dwordx4 v1, s[20:23], s40 offen offset:3072 sc1 lds\n"
"s_lshl_b32 s39, s52, 15\n"
"s_add_u32 s39, s39, s38\n"
"s_cmp_lt_u32 s52, s53\n"
"s_cselect_b32 s39, s39, 0x40000000\n"
"s_add_u32 s40, s39, 4096\n"
"s_and_b32 s41, s52, 1\n"
"s_lshl_b32 s41, s41, 16\n"
"s_add_u32 s41, s41, s38\n"
"s_add_u32 s52, s52, 1\n"
"s_add_u32 m0, s41, 0\n"
"s_nop 0\n"
"buffer_load_dwordx4 v1, s[16:19], s39 offen offset:0 sc1 lds\n"
"buffer_load_dwordx4 v1, s[16:19], s39 offen offset:1024 sc1 lds\n"
"buffer_load_dwordx4 v1, s[16:19], s39 offen offset:2048 sc1 lds\n"
"buffer_load_dwordx4 v1, s[16:19], s39 offen offset:3072 sc1 lds\n"
"s_add_u32 m0, s41, 4096\n"
"s_nop 0\n"
"buffer_load_dwordx4 v1, s[16:19], s40 offen offset:0 sc1 lds\n"
"buffer_load_dwordx4 v1, s[16:19], s40 offen offset:1024 sc1 lds\n"
"v_mov_b32 v96, 0\n"
"v_mov_b32 v97, 0\n"
"v_mov_b32 v98, 0\n"
"v_mov_b32 v99, 0\n"
"v_mov_b32 v12, 0\n"
"v_mov_b32 v13, 0\n"
"v_mov_b32 v14, 0\n"
"v_mov_b32 v15, 0\n"
"v_mfma_f32_32x32x16_bf16 v[128:143], v[96:99], v[96:99], 0\n"
"v_mfma_f32_32x32x16_bf16 v[144:159], v[96:99], v[96:99], 0\n"
"v_mfma_f32_32x32x16_bf16 v[160:175], v[96:99], v[96:99], 0\n"
"v_mfma_f32_32x32x16_bf16 v[176:191], v[96:99], v[96:99], 0\n"
"v_mfma_f32_32x32x16_bf16 v[192:207], v[96:99], v[96:99], 0\n"
"v_mfma_f32_32x32x16_bf16 v[208:223], v[96:99], v[96:99], 0\n"
"v_mfma_f32_32x32x16_bf16 v[224:239], v[96:99], v[96:99], 0\n"
"v_mfma_f32_32x32x16_bf16 v[240:255], v[96:99], v[96:99], 0\n"
"s_mov_b32 s43, 0\n"
"v_mov_b32 v8, v1\n"
"s_waitcnt vmcnt(6)\n"
"s_barrier\n"
"ds_read_b128 a[64:67], v8 offset:0\n"
"ds_read_b128 a[68:71], v8 offset:1024\n"
"ds_read_b128 a[72:75], v8 offset:2048\n"
"ds_read_b128 a[76:79], v8 offset:3072\n"
"ds_read_b128 a[80:83], v8 offset:4096\n"
"ds_read_b128 a[84:87], v8 offset:5120\n"
"ds_read_b128 a[88:91], v8 offset:6144\n"
"ds_read_b128 a[92:95], v8 offset:7168\n"
"s_cmp_lg_u32 s29, 0\n"
"s_cbranch_scc1 .Lsig13\n"
"s_add_u32 s49, s47, 37120\n"
"v_mul_i32_i24 v2, 256, v0\n"
"v_add_u32 v2, s49, v2\n"
"v_mov_b32 v3, s11\n"
"s_mov_b64 exec, 1\n"
"global_store_dword v2, v3, s[50:51] sc1\n"
"s_mov_b64 exec, -1\n"
".Lsig13:\n"
"s_load_dwordx2 s[44:45], s[8:9], 56\n"
"s_mov_b32 s54, s31\n"
"s_cmp_eq_u32 s54, 0\n"
"s_cbranch_scc1 .Lp1_done\n"
".Lp1_loop:\n"
"s_waitcnt lgkmcnt(0)\n"
"v_mfma_f32_32x32x16_bf16 v[32:47], a[64:67], a[0:3], 0\n"
"v_mfma_f32_32x32x16_bf16 v[32:47], a[68:71], a[4:7], v[32:47]\n"
"buffer_load_dwordx4 v1, s[16:19], s40 offen offset:2048 sc1 lds\n"
"v_mfma_f32_32x32x16_bf16 v[32:47], a[72:75], a[8:11], v[32:47]\n"
"v_mfma_f32_32x32x16_bf16 v[32:47], a[76:79], a[12:15], v[32:47]\n"
"buffer_load_dwordx4 v1, s[16:19], s40 offen offset:3072 sc1 lds\n"
"v_mfma_f32_32x32x16_bf16 v[32:47], a[80:83], a[16:19], v[32:47]\n"
"ds_read_b128 a[96:99], v8 offset:8192\n"
"s_add_u32 m0, s41, 32768\n"
"v_mfma_f32_32x32x16_bf16 v[32:47], a[84:87], a[20:23], v[32:47]\n"
"buffer_load_dwordx4 v1, s[20:23], s39 offen offset:0 sc1 lds\n"
"ds_read_b128 a[100:103], v8 offset:9216\n"
"v_mfma_f32_32x32x16_bf16 v[32:47], a[88:91], a[24:27], v[32:47]\n"
"ds_read_b128 a[104:107], v8 offset:10240\n"
"v_mfma_f32_32x32x16_bf16 v[32:47], a[92:95], a[28:31], v[32:47]\n"
"buffer_load_dwordx4 v1, s[20:23], s39 offen offset:1024 sc1 lds\n"
"ds_read_b128 a[108:111], v8 offset:11264\n"
"v_mfma_f32_32x32x16_bf16 v[48:63], a[64:67], a[32:35], 0\n"
"ds_read_b128 a[112:115], v8 offset:12288\n"
"v_mfma_f32_32x32x16_bf16 v[48:63], a[68:71], a[36:39], v[48:63]\n"
"buffer_load_dwordx4 v1, s[20:23], s39 offen offset:2048 sc1 lds\n"
"ds_read_b128 a[116:119], v8 offset:13312\n"
"v_exp_f32 v32, v32\n"
"v_mfma_f32_32x32x16_bf16 v[48:63], a[72:75], a[40:43], v[48:63]\n"
"ds_read_b128 a[120:123], v8 offset:14336\n"
"v_exp_f32 v33, v33\n"
"v_exp_f32 v34, v34\n"
"v_mfma_f32_32x32x16_bf16 v[48:63], a[76:79], a[44:47], v[48:63]\n"
"buffer_load_dwordx4 v1, s[20:23], s39 offen offset:3072 sc1 lds\n"
"ds_read_b128 a[124:127], v8 offset:15360\n"
"v_exp_f32 v35, v35\n"
"v_cvt_pk_bf16_f32 v96, v32, v33\n"
"v_mfma_f32_32x32x16_bf16 v[48:63], a[80:83], a[48:51], v[48:63]\n"
"v_add_f32 v12, v12, v32\n"
"v_add_f32 v13, v13, v33\n"
"v_exp_f32 v36, v36\n"
"s_add_u32 m0, s41, 36864\n"
"v_mfma_f32_32x32x16_bf16 v[48:63], a[84:87], a[52:55], v[48:63]\n"
"buffer_load_dwordx4 v1, s[20:23], s40 offen offset:0 sc1 lds\n"
"v_exp_f32 v37, v37\n"
"v_cvt_pk_bf16_f32 v97, v34, v35\n"
"v_add_f32 v12, v12, v34\n"
"v_mfma_f32_32x32x16_bf16 v[48:63], a[88:91], a[56:59], v[48:63]\n"
"ds_read_b128 a[128:131], v8 offset:32768\n"
"v_add_f32 v13, v13, v35\n"
"v_exp_f32 v38, v38\n"
"v_exp_f32 v39, v39\n"
"v_mfma_f32_32x32x16_bf16 v[48:63], a[92:95], a[60:63], v[48:63]\n"
"buffer_load_dwordx4 v1, s[20:23], s40 offen offset:1024 sc1 lds\n"
"v_cvt_pk_bf16_f32 v98, v36, v37\n"
"v_add_f32 v12, v12, v36\n"
"v_add_f32 v13, v13, v37\n"
"s_waitcnt lgkmcnt(1)\n"
"v_mfma_f32_32x32x16_bf16 v[64:79], a[96:99], a[0:3], 0\n"
"ds_read_b128 a[132:135], v8 offset:33792\n"
"v_exp_f32 v40, v40\n"
"v_exp_f32 v41, v41\n"
"v_cvt_pk_bf16_f32 v99, v38, v39\n"
"v_mfma_f32_32x32x16_bf16 v[64:79], a[100:103], a[4:7], v[64:79]\n"
"buffer_load_dwordx4 v1, s[20:23], s40 offen offset:2048 sc1 lds\n"
"v_add_f32 v12, v12, v38\n"
"v_add_f32 v13, v13, v39\n"
"v_exp_f32 v42, v42\n"
"v_mfma_f32_32x32x16_bf16 v[64:79], a[104:107], a[8:11], v[64:79]\n"
"ds_read_b128 a[136:139], v8 offset:34816\n"
"v_exp_f32 v43, v43\n"
"v_cvt_pk_bf16_f32 v100, v40, v41\n"
"v_add_f32 v12, v12, v40\n"
"v_add_f32 v13, v13, v41\n"
"v_mfma_f32_32x32x16_bf16 v[64:79], a[108:111], a[12:15], v[64:79]\n"
"buffer_load_dwordx4 v1, s[20:23], s40 offen offset:3072 sc1 lds\n"
"v_exp_f32 v44, v44\n"
"v_exp_f32 v45, v45\n"
"v_mfma_f32_32x32x16_bf16 v[64:79], a[112:115], a[16:19], v[64:79]\n"
"ds_read_b128 a[140:143], v8 offset:35840\n"
"v_cvt_pk_bf16_f32 v101, v42, v43\n"
"v_add_f32 v12, v12, v42\n"
"v_add_f32 v13, v13, v43\n"
"v_exp_f32 v46, v46\n"
"v_mfma_f32_32x32x16_bf16 v[64:79], a[116:119], a[20:23], v[64:79]\n"
"v_exp_f32 v47, v47\n"
"v_cvt_pk_bf16_f32 v102, v44, v45\n"
"v_add_f32 v12, v12, v44\n"
"v_add_f32 v13, v13, v45\n"
"v_mfma_f32_32x32x16_bf16 v[64:79], a[120:123], a[24:27], v[64:79]\n"
"ds_read_b128 a[144:147], v8 offset:36864\n"
"v_exp_f32 v48, v48\n"
"v_exp_f32 v49, v49\n"
"v_cvt_pk_bf16_f32 v103, v46, v47\n"
"v_mfma_f32_32x32x16_bf16 v[64:79], a[124:127], a[28:31], v[64:79]\n"
"v_add_f32 v12, v12, v46\n"
"v_add_f32 v13, v13, v47\n"
"v_exp_f32 v50, v50\n"
"v_exp_f32 v51, v51\n"
"v_mfma_f32_32x32x16_bf16 v[80:95], a[96:99], a[32:35], 0\n"
"ds_read_b128 a[148:151], v8 offset:37888\n"
"v_cvt_pk_bf16_f32 v112, v48, v49\n"
"v_add_f32 v14, v14, v48\n"
"v_add_f32 v15, v15, v49\n"
"v_exp_f32 v52, v52\n"
"v_mfma_f32_32x32x16_bf16 v[80:95], a[100:103], a[36:39], v[80:95]\n"
"v_exp_f32 v53, v53\n"
"v_cvt_pk_bf16_f32 v113, v50, v51\n"
"v_add_f32 v14, v14, v50\n"
"v_add_f32 v15, v15, v51\n"
"v_mfma_f32_32x32x16_bf16 v[80:95], a[104:107], a[40:43], v[80:95]\n"
"ds_read_b128 a[152:155], v8 offset:38912\n"
"v_exp_f32 v54, v54\n"
"v_exp_f32 v55, v55\n"
"v_cvt_pk_bf16_f32 v114, v52, v53\n"
"v_mfma_f32_32x32x16_bf16 v[80:95], a[108:111], a[44:47], v[80:95]\n"
"v_add_f32 v14, v14, v52\n"
"v_add_f32 v15, v15, v53\n"
"v_exp_f32 v56, v56\n"
"v_exp_f32 v57, v57\n"
"v_mfma_f32_32x32x16_bf16 v[80:95], a[112:115], a[48:51], v[80:95]\n"
"ds_read_b128 a[156:159], v8 offset:39936\n"
"v_cvt_pk_bf16_f32 v115, v54, v55\n"
"v_add_f32 v14, v14, v54\n"
"v_add_f32 v15, v15, v55\n"
"v_exp_f32 v58, v58\n"
"v_mfma_f32_32x32x16_bf16 v[80:95], a[116:119], a[52:55], v[80:95]\n"
"v_exp_f32 v59, v59\n"
"v_cvt_pk_bf16_f32 v116, v56, v57\n"
"v_add_f32 v14, v14, v56\n"
"v_add_f32 v15, v15, v57\n"
"v_mfma_f32_32x32x16_bf16 v[80:95], a[120:123], a[56:59], v[80:95]\n"
"ds_read_b128 a[160:163], v8 offset:40960\n"
"v_exp_f32 v60, v60\n"
"v_exp_f32 v61, v61\n"
"v_cvt_pk_bf16_f32 v117, v58, v59\n"
"v_mfma_f32_32x32x16_bf16 v[80:95], a[124:127], a[60:63], v[80:95]\n"
"v_add_f32 v14, v14, v58\n"
"v_add_f32 v15, v15, v59\n"
"v_exp_f32 v62, v62\n"
"s_waitcnt lgkmcnt(5)\n"
"v_mfma_f32_32x32x16_bf16 v[128:143], a[128:131], v[96:99], v[128:143]\n"
"ds_read_b128 a[164:167], v8 offset:41984\n"
"v_exp_f32 v63, v63\n"
"v_cvt_pk_bf16_f32 v118, v60, v61\n"
"v_add_f32 v14, v14, v60\n"
"v_add_f32 v15, v15, v61\n"
"v_mfma_f32_32x32x16_bf16 v[160:175], a[132:135], v[96:99], v[160:175]\n"
"v_exp_f32 v64, v64\n"
"v_exp_f32 v65, v65\n"
"v_cvt_pk_bf16_f32 v119, v62, v63\n"
"v_add_f32 v14, v14, v62\n"
"v_mfma_f32_32x32x16_bf16 v[192:207], a[136:139], v[96:99], v[192:207]\n"
"ds_read_b128 a[168:171], v8 offset:43008\n"
"v_add_f32 v15, v15, v63\n"
"v_exp_f32 v66, v66\n"
"v_exp_f32 v67, v67\n"
"v_mfma_f32_32x32x16_bf16 v[224:239], a[140:143], v[96:99], v[224:239]\n"
"v_cvt_pk_bf16_f32 v104, v64, v65\n"
"v_add_f32 v12, v12, v64\n"
"v_add_f32 v13, v13, v65\n"
"v_exp_f32 v68, v68\n"
"v_mfma_f32_32x32x16_bf16 v[144:159], a[128:131], v[112:115], v[144:159]\n"
"ds_read_b128 a[172:175], v8 offset:44032\n"
"v_exp_f32 v69, v69\n"
"v_cvt_pk_bf16_f32 v105, v66, v67\n"
"v_add_f32 v12, v12, v66\n"
"v_add_f32 v13, v13, v67\n"
"v_mfma_f32_32x32x16_bf16 v[176:191], a[132:135], v[112:115], v[176:191]\n"
"v_exp_f32 v70, v70\n"
"v_exp_f32 v71, v71\n"
"v_cvt_pk_bf16_f32 v106, v68, v69\n"
"v_add_f32 v12, v12, v68\n"
"v_mfma_f32_32x32x16_bf16 v[208:223], a[136:139], v[112:115], v[208:223]\n"
"ds_read_b128 a[176:179], v8 offset:45056\n"
"v_add_f32 v13, v13, v69\n"
"v_exp_f32 v80, v80\n"
"v_exp_f32 v81, v81\n"
"v_mfma_f32_32x32x16_bf16 v[240:255], a[140:143], v[112:115], v[240:255]\n"
"v_cvt_pk_bf16_f32 v107, v70, v71\n"
"v_add_f32 v12, v12, v70\n"
"v_add_f32 v13, v13, v71\n"
"v_exp_f32 v82, v82\n"
"s_waitcnt lgkmcnt(5)\n"
"v_mfma_f32_32x32x16_bf16 v[128:143], a[144:147], v[100:103], v[128:143]\n"
"ds_read_b128 a[180:183], v8 offset:46080\n"
"v_exp_f32 v83, v83\n"
"v_cvt_pk_bf16_f32 v120, v80, v81\n"
"v_add_f32 v14, v14, v80\n"
"v_add_f32 v15, v15, v81\n"
"v_mfma_f32_32x32x16_bf16 v[160:175], a[148:151], v[100:103], v[160:175]\n"
"v_exp_f32 v84, v84\n"
"v_exp_f32 v85, v85\n"
"v_cvt_pk_bf16_f32 v121, v82, v83\n"
"v_add_f32 v14, v14, v82\n"
"v_mfma_f32_32x32x16_bf16 v[192:207], a[152:155], v[100:103], v[192:207]\n"
"ds_read_b128 a[184:187], v8 offset:47104\n"
"v_add_f32 v15, v15, v83\n"
"v_exp_f32 v86, v86\n"
"v_exp_f32 v87, v87\n"
"v_mfma_f32_32x32x16_bf16 v[224:239], a[156:159], v[100:103], v[224:239]\n"
"v_cvt_pk_bf16_f32 v122, v84, v85\n"
"v_add_f32 v14, v14, v84\n"
"v_add_f32 v15, v15, v85\n"
"v_exp_f32 v72, v72\n"
"v_mfma_f32_32x32x16_bf16 v[144:159], a[144:147], v[116:119], v[144:159]\n"
"ds_read_b128 a[188:191], v8 offset:48128\n"
"v_exp_f32 v73, v73\n"
"v_cvt_pk_bf16_f32 v123, v86, v87\n"
"v_add_f32 v14, v14, v86\n"
"v_add_f32 v15, v15, v87\n"
"v_mfma_f32_32x32x16_bf16 v[176:191], a[148:151], v[116:119], v[176:191]\n"
"v_exp_f32 v74, v74\n"
"v_exp_f32 v75, v75\n"
"v_cvt_pk_bf16_f32 v108, v72, v73\n"
"v_add_f32 v12, v12, v72\n"
"v_mfma_f32_32x32x16_bf16 v[208:223], a[152:155], v[116:119], v[208:223]\n"
"s_add_u32 s42, s43, 16384\n"
"v_add_u32 v8, s42, v1\n"
"v_add_f32 v13, v13, v73\n"
"v_exp_f32 v76, v76\n"
"v_mfma_f32_32x32x16_bf16 v[240:255], a[156:159], v[116:119], v[240:255]\n"
"v_exp_f32 v77, v77\n"
"v_cvt_pk_bf16_f32 v109, v74, v75\n"
"v_add_f32 v12, v12, v74\n"
"v_add_f32 v13, v13, v75\n"
"s_waitcnt lgkmcnt(4)\n"
"v_mfma_f32_32x32x16_bf16 v[128:143], a[160:163], v[104:107], v[128:143]\n"
"v_exp_f32 v78, v78\n"
"v_exp_f32 v79, v79\n"
"v_cvt_pk_bf16_f32 v110, v76, v77\n"
"v_add_f32 v12, v12, v76\n"
"v_mfma_f32_32x32x16_bf16 v[160:175], a[164:167], v[104:107], v[160:175]\n"
"v_add_f32 v13, v13, v77\n"
"v_exp_f32 v88, v88\n"
"v_exp_f32 v89, v89\n"
"v_cvt_pk_bf16_f32 v111, v78, v79\n"
"v_mfma_f32_32x32x16_bf16 v[192:207], a[168:171], v[104:107], v[192:207]\n"
"v_add_f32 v12, v12, v78\n"
"v_add_f32 v13, v13, v79\n"
"v_exp_f32 v90, v90\n"
"v_exp_f32 v91, v91\n"
"v_mfma_f32_32x32x16_bf16 v[224:239], a[172:175], v[104:107], v[224:239]\n"
"v_cvt_pk_bf16_f32 v124, v88, v89\n"
"v_add_f32 v14, v14, v88\n"
"v_add_f32 v15, v15, v89\n"
"v_exp_f32 v92, v92\n"
"s_waitcnt lgkmcnt(0)\n"
"v_mfma_f32_32x32x16_bf16 v[144:159], a[160:163], v[120:123], v[144:159]\n"
"ds_read_b128 a[64:67], v8 offset:0\n"
"v_exp_f32 v93, v93\n"
"v_cvt_pk_bf16_f32 v125, v90, v91\n"
"v_add_f32 v14, v14, v90\n"
"v_add_f32 v15, v15, v91\n"
"v_mfma_f32_32x32x16_bf16 v[176:191], a[164:167], v[120:123], v[176:191]\n"
"ds_read_b128 a[68:71], v8 offset:1024\n"
"v_exp_f32 v94, v94\n"
"v_exp_f32 v95, v95\n"
"v_cvt_pk_bf16_f32 v126, v92, v93\n"
"v_mfma_f32_32x32x16_bf16 v[208:223], a[168:171], v[120:123], v[208:223]\n"
"ds_read_b128 a[72:75], v8 offset:2048\n"
"v_add_f32 v14, v14, v92\n"
"v_add_f32 v15, v15, v93\n"
"v_cvt_pk_bf16_f32 v127, v94, v95\n"
"v_add_f32 v14, v14, v94\n"
"v_add_f32 v15, v15, v95\n"
"v_mfma_f32_32x32x16_bf16 v[240:255], a[172:175], v[120:123], v[240:255]\n"
"ds_read_b128 a[76:79], v8 offset:3072\n"
"v_mfma_f32_32x32x16_bf16 v[128:143], a[176:179], v[108:111], v[128:143]\n"
"ds_read_b128 a[80:83], v8 offset:4096\n"
"v_mfma_f32_32x32x16_bf16 v[160:175], a[180:183], v[108:111], v[160:175]\n"
"ds_read_b128 a[84:87], v8 offset:5120\n"
"v_mfma_f32_32x32x16_bf16 v[192:207], a[184:187], v[108:111], v[192:207]\n"
"ds_read_b128 a[88:91], v8 offset:6144\n"
"v_mfma_f32_32x32x16_bf16 v[224:239], a[188:191], v[108:111], v[224:239]\n"
"ds_read_b128 a[92:95], v8 offset:7168\n"
"v_mfma_f32_32x32x16_bf16 v[144:159], a[176:179], v[124:127], v[144:159]\n"
"v_mfma_f32_32x32x16_bf16 v[176:191], a[180:183], v[124:127], v[176:191]\n"
"v_mfma_f32_32x32x16_bf16 v[208:223], a[184:187], v[124:127], v[208:223]\n"
"v_mfma_f32_32x32x16_bf16 v[240:255], a[188:191], v[124:127], v[240:255]\n"
"s_mov_b32 s43, s42\n"
"s_waitcnt lgkmcnt(0)\n"
"v_mfma_f32_32x32x16_bf16 v[32:47], a[64:67], a[0:3], 0\n"
"v_mfma_f32_32x32x16_bf16 v[32:47], a[68:71], a[4:7], v[32:47]\n"
"v_mfma_f32_32x32x16_bf16 v[32:47], a[72:75], a[8:11], v[32:47]\n"
"v_mfma_f32_32x32x16_bf16 v[32:47], a[76:79], a[12:15], v[32:47]\n"
"v_mfma_f32_32x32x16_bf16 v[32:47], a[80:83], a[16:19], v[32:47]\n"
"ds_read_b128 a[96:99], v8 offset:8192\n"
"v_mfma_f32_32x32x16_bf16 v[32:47], a[84:87], a[20:23], v[32:47]\n"
"ds_read_b128 a[100:103], v8 offset:9216\n"
"v_mfma_f32_32x32x16_bf16 v[32:47], a[88:91], a[24:27], v[32:47]\n"
"ds_read_b128 a[104:107], v8 offset:10240\n"
"v_mfma_f32_32x32x16_bf16 v[32:47], a[92:95], a[28:31], v[32:47]\n"
"ds_read_b128 a[108:111], v8 offset:11264\n"
"v_mfma_f32_32x32x16_bf16 v[48:63], a[64:67], a[32:35], 0\n"
"ds_read_b128 a[112:115], v8 offset:12288\n"
"v_mfma_f32_32x32x16_bf16 v[48:63], a[68:71], a[36:39], v[48:63]\n"
"ds_read_b128 a[116:119], v8 offset:13312\n"
"v_exp_f32 v32, v32\n"
"v_exp_f32 v33, v33\n"
"v_mfma_f32_32x32x16_bf16 v[48:63], a[72:75], a[40:43], v[48:63]\n"
"ds_read_b128 a[120:123], v8 offset:14336\n"
"v_exp_f32 v34, v34\n"
"v_exp_f32 v35, v35\n"
"v_cvt_pk_bf16_f32 v96, v32, v33\n"
"v_mfma_f32_32x32x16_bf16 v[48:63], a[76:79], a[44:47], v[48:63]\n"
"ds_read_b128 a[124:127], v8 offset:15360\n"
"v_add_f32 v12, v12, v32\n"
"v_add_f32 v13, v13, v33\n"
"v_exp_f32 v36, v36\n"
"v_mfma_f32_32x32x16_bf16 v[48:63], a[80:83], a[48:51], v[48:63]\n"
"v_exp_f32 v37, v37\n"
"v_cvt_pk_bf16_f32 v97, v34, v35\n"
"v_add_f32 v12, v12, v34\n"
"v_add_f32 v13, v13, v35\n"
"v_mfma_f32_32x32x16_bf16 v[48:63], a[84:87], a[52:55], v[48:63]\n"
"v_exp_f32 v38, v38\n"
"v_exp_f32 v39, v39\n"
"v_cvt_pk_bf16_f32 v98, v36, v37\n"
"v_add_f32 v12, v12, v36\n"
"v_mfma_f32_32x32x16_bf16 v[48:63], a[88:91], a[56:59], v[48:63]\n"
"ds_read_b128 a[128:131], v8 offset:32768\n"
"v_add_f32 v13, v13, v37\n"
"v_exp_f32 v40, v40\n"
"v_exp_f32 v41, v41\n"
"v_mfma_f32_32x32x16_bf16 v[48:63], a[92:95], a[60:63], v[48:63]\n"
"v_cvt_pk_bf16_f32 v99, v38, v39\n"
"v_add_f32 v12, v12, v38\n"
"v_add_f32 v13, v13, v39\n"
"v_exp_f32 v42, v42\n"
"s_waitcnt lgkmcnt(1)\n"
"v_mfma_f32_32x32x16_bf16 v[64:79], a[96:99], a[0:3], 0\n"
"ds_read_b128 a[132:135], v8 offset:33792\n"
"v_exp_f32 v43, v43\n"
"v_cvt_pk_bf16_f32 v100, v40, v41\n"
"v_add_f32 v12, v12, v40\n"
"v_add_f32 v13, v13, v41\n"
"v_mfma_f32_32x32x16_bf16 v[64:79], a[100:103], a[4:7], v[64:79]\n"
"v_exp_f32 v44, v44\n"
"v_exp_f32 v45, v45\n"
"v_cvt_pk_bf16_f32 v101, v42, v43\n"
"v_add_f32 v12, v12, v42\n"
"v_mfma_f32_32x32x16_bf16 v[64:79], a[104:107], a[8:11], v[64:79]\n"
"ds_read_b128 a[136:139], v8 offset:34816\n"
"v_add_f32 v13, v13, v43\n"
"v_exp_f32 v46, v46\n"
"v_exp_f32 v47, v47\n"
"v_mfma_f32_32x32x16_bf16 v[64:79], a[108:111], a[12:15], v[64:79]\n"
"v_cvt_pk_bf16_f32 v102, v44, v45\n"
"v_add_f32 v12, v12, v44\n"
"v_add_f32 v13, v13, v45\n"
"v_exp_f32 v48, v48\n"
"v_mfma_f32_32x32x16_bf16 v[64:79], a[112:115], a[16:19], v[64:79]\n"
"ds_read_b128 a[140:143], v8 offset:35840\n"
"v_exp_f32 v49, v49\n"
"v_cvt_pk_bf16_f32 v103, v46, v47\n"
"v_add_f32 v12, v12, v46\n"
"v_add_f32 v13, v13, v47\n"
"v_mfma_f32_32x32x16_bf16 v[64:79], a[116:119], a[20:23], v[64:79]\n"
"v_exp_f32 v50, v50\n"
"v_exp_f32 v51, v51\n"
"v_cvt_pk_bf16_f32 v112, v48, v49\n"
"v_add_f32 v14, v14, v48\n"
"v_mfma_f32_32x32x16_bf16 v[64:79], a[120:123], a[24:27], v[64:79]\n"
"ds_read_b128 a[144:147], v8 offset:36864\n"
"v_add_f32 v15, v15, v49\n"
"v_exp_f32 v52, v52\n"
"v_exp_f32 v53, v53\n"
"v_mfma_f32_32x32x16_bf16 v[64:79], a[124:127], a[28:31], v[64:79]\n"
"v_cvt_pk_bf16_f32 v113, v50, v51\n"
"v_add_f32 v14, v14, v50\n"
"v_add_f32 v15, v15, v51\n"
"v_exp_f32 v54, v54\n"
"v_mfma_f32_32x32x16_bf16 v[80:95], a[96:99], a[32:35], 0\n"
"ds_read_b128 a[148:151], v8 offset:37888\n"
"v_exp_f32 v55, v55\n"
"v_cvt_pk_bf16_f32 v114, v52, v53\n"
"v_add_f32 v14, v14, v52\n"
"v_add_f32 v15, v15, v53\n"
"v_mfma_f32_32x32x16_bf16 v[80:95], a[100:103], a[36:39], v[80:95]\n"
"v_exp_f32 v56, v56\n"
"v_exp_f32 v57, v57\n"
"v_cvt_pk_bf16_f32 v115, v54, v55\n"
"v_add_f32 v14, v14, v54\n"
"v_mfma_f32_32x32x16_bf16 v[80:95], a[104:107], a[40:43], v[80:95]\n"
"ds_read_b128 a[152:155], v8 offset:38912\n"
"v_add_f32 v15, v15, v55\n"
"v_exp_f32 v58, v58\n"
"v_exp_f32 v59, v59\n"
"v_mfma_f32_32x32x16_bf16 v[80:95], a[108:111], a[44:47], v[80:95]\n"
"v_cvt_pk_bf16_f32 v116, v56, v57\n"
"v_add_f32 v14, v14, v56\n"
"v_add_f32 v15, v15, v57\n"
"v_exp_f32 v60, v60\n"
"v_mfma_f32_32x32x16_bf16 v[80:95], a[112:115], a[48:51], v[80:95]\n"
"ds_read_b128 a[156:159], v8 offset:39936\n"
"v_exp_f32 v61, v61\n"
"v_cvt_pk_bf16_f32 v117, v58, v59\n"
"v_add_f32 v14, v14, v58\n"
"v_add_f32 v15, v15, v59\n"
"v_mfma_f32_32x32x16_bf16 v[80:95], a[116:119], a[52:55], v[80:95]\n"
"v_exp_f32 v62, v62\n"
"v_exp_f32 v63, v63\n"
"v_cvt_pk_bf16_f32 v118, v60, v61\n"
"v_add_f32 v14, v14, v60\n"
"v_mfma_f32_32x32x16_bf16 v[80:95], a[120:123], a[56:59], v[80:95]\n"
"ds_read_b128 a[160:163], v8 offset:40960\n"
"v_add_f32 v15, v15, v61\n"
"v_exp_f32 v64, v64\n"
"v_exp_f32 v65, v65\n"
"v_mfma_f32_32x32x16_bf16 v[80:95], a[124:127], a[60:63], v[80:95]\n"
"v_cvt_pk_bf16_f32 v119, v62, v63\n"
"v_add_f32 v14, v14, v62\n"
"v_add_f32 v15, v15, v63\n"
"v_exp_f32 v66, v66\n"
"s_waitcnt lgkmcnt(5)\n"
"v_mfma_f32_32x32x16_bf16 v[128:143], a[128:131], v[96:99], v[128:143]\n"
"ds_read_b128 a[164:167], v8 offset:41984\n"
"v_exp_f32 v67, v67\n"
"v_cvt_pk_bf16_f32 v104, v64, v65\n"
"v_add_f32 v12, v12, v64\n"
"v_add_f32 v13, v13, v65\n"
"v_mfma_f32_32x32x16_bf16 v[160:175], a[132:135], v[96:99], v[160:175]\n"
"v_exp_f32 v68, v68\n"
"v_exp_f32 v69, v69\n"
"v_cvt_pk_bf16_f32 v105, v66, v67\n"
"v_add_f32 v12, v12, v66\n"
"v_mfma_f32_32x32x16_bf16 v[192:207], a[136:139], v[96:99], v[192:207]\n"
"ds_read_b128 a[168:171], v8 offset:43008\n"
"v_add_f32 v13, v13, v67\n"
"v_exp_f32 v70, v70\n"
"v_exp_f32 v71, v71\n"
"v_mfma_f32_32x32x16_bf16 v[224:239], a[140:143], v[96:99], v[224:239]\n"
"v_cvt_pk_bf16_f32 v106, v68, v69\n"
"v_add_f32 v12, v12, v68\n"
"v_add_f32 v13, v13, v69\n"
"v_exp_f32 v80, v80\n"
"v_mfma_f32_32x32x16_bf16 v[144:159], a[128:131], v[112:115], v[144:159]\n"
"ds_read_b128 a[172:175], v8 offset:44032\n"
"v_exp_f32 v81, v81\n"
"v_cvt_pk_bf16_f32 v107, v70, v71\n"
"v_add_f32 v12, v12, v70\n"
"v_add_f32 v13, v13, v71\n"
"v_mfma_f32_32x32x16_bf16 v[176:191], a[132:135], v[112:115], v[176:191]\n"
"v_exp_f32 v82, v82\n"
"v_exp_f32 v83, v83\n"
"v_cvt_pk_bf16_f32 v120, v80, v81\n"
"v_add_f32 v14, v14, v80\n"
"v_mfma_f32_32x32x16_bf16 v[208:223], a[136:139], v[112:115], v[208:223]\n"
"ds_read_b128 a[176:179], v8 offset:45056\n"
"v_add_f32 v15, v15, v81\n"
"v_exp_f32 v84, v84\n"
"v_exp_f32 v85, v85\n"
"v_mfma_f32_32x32x16_bf16 v[240:255], a[140:143], v[112:115], v[240:255]\n"
"v_cvt_pk_bf16_f32 v121, v82, v83\n"
"v_add_f32 v14, v14, v82\n"
"v_add_f32 v15, v15, v83\n"
"v_exp_f32 v86, v86\n"
"s_waitcnt lgkmcnt(5)\n"
"v_mfma_f32_32x32x16_bf16 v[128:143], a[144:147], v[100:103], v[128:143]\n"
"ds_read_b128 a[180:183], v8 offset:46080\n"
"v_exp_f32 v87, v87\n"
"v_cvt_pk_bf16_f32 v122, v84, v85\n"
"v_add_f32 v14, v14, v84\n"
"v_add_f32 v15, v15, v85\n"
"v_mfma_f32_32x32x16_bf16 v[160:175], a[148:151], v[100:103], v[160:175]\n"
"v_exp_f32 v72, v72\n"
"v_exp_f32 v73, v73\n"
"v_cvt_pk_bf16_f32 v123, v86, v87\n"
"v_add_f32 v14, v14, v86\n"
"v_mfma_f32_32x32x16_bf16 v[192:207], a[152:155], v[100:103], v[192:207]\n"
"ds_read_b128 a[184:187], v8 offset:47104\n"
"v_add_f32 v15, v15, v87\n"
"v_exp_f32 v74, v74\n"
"v_exp_f32 v75, v75\n"
"v_mfma_f32_32x32x16_bf16 v[224:239], a[156:159], v[100:103], v[224:239]\n"
"v_cvt_pk_bf16_f32 v108, v72, v73\n"
"v_add_f32 v12, v12, v72\n"
"v_add_f32 v13, v13, v73\n"
"v_exp_f32 v76, v76\n"
"v_mfma_f32_32x32x16_bf16 v[144:159], a[144:147], v[116:119], v[144:159]\n"
"ds_read_b128 a[188:191], v8 offset:48128\n"
"v_exp_f32 v77, v77\n"
"v_cvt_pk_bf16_f32 v109, v74, v75\n"
"v_add_f32 v12, v12, v74\n"
"v_add_f32 v13, v13, v75\n"
"v_mfma_f32_32x32x16_bf16 v[176:191], a[148:151], v[116:119], v[176:191]\n"
"v_exp_f32 v78, v78\n"
"v_exp_f32 v79, v79\n"
"v_cvt_pk_bf16_f32 v110, v76, v77\n"
"v_add_f32 v12, v12, v76\n"
"v_mfma_f32_32x32x16_bf16 v[208:223], a[152:155], v[116:119], v[208:223]\n"
"s_xor_b32 s42, s43, 0x10000\n"
"s_sub_u32 s42, s42, 16384\n"
"v_add_u32 v8, s42, v1\n"
"v_add_f32 v13, v13, v77\n"
"v_exp_f32 v88, v88\n"
"v_mfma_f32_32x32x16_bf16 v[240:255], a[156:159], v[116:119], v[240:255]\n"
"s_lshl_b32 s39, s52, 15\n"
"s_add_u32 s39, s39, s38\n"
"v_exp_f32 v89, v89\n"
"v_cvt_pk_bf16_f32 v111, v78, v79\n"
"s_waitcnt lgkmcnt(4)\n"
"v_mfma_f32_32x32x16_bf16 v[128:143], a[160:163], v[104:107], v[128:143]\n"
"s_cmp_lt_u32 s52, s53\n"
"s_cselect_b32 s39, s39, 0x40000000\n"
"v_add_f32 v12, v12, v78\n"
"v_add_f32 v13, v13, v79\n"
"v_exp_f32 v90, v90\n"
"v_mfma_f32_32x32x16_bf16 v[160:175], a[164:167], v[104:107], v[160:175]\n"
"s_add_u32 s40, s39, 4096\n"
"s_and_b32 s41, s52, 1\n"
"v_exp_f32 v91, v91\n"
"v_cvt_pk_bf16_f32 v124, v88, v89\n"
"v_add_f32 v14, v14, v88\n"
"v_mfma_f32_32x32x16_bf16 v[192:207], a[168:171], v[104:107], v[192:207]\n"
"s_lshl_b32 s41, s41, 16\n"
"s_add_u32 s41, s41, s38\n"
"v_add_f32 v15, v15, v89\n"
"v_exp_f32 v92, v92\n"
"v_mfma_f32_32x32x16_bf16 v[224:239], a[172:175], v[104:107], v[224:239]\n"
"s_add_u32 s52, s52, 1\n"
"v_exp_f32 v93, v93\n"
"v_cvt_pk_bf16_f32 v125, v90, v91\n"
"s_waitcnt vmcnt(0) lgkmcnt(0)\n"
"s_barrier\n"
"v_mfma_f32_32x32x16_bf16 v[144:159], a[160:163], v[120:123], v[144:159]\n"
"ds_read_b128 a[64:67], v8 offset:0\n"
"v_add_f32 v14, v14, v90\n"
"v_add_f32 v15, v15, v91\n"
"v_exp_f32 v94, v94\n"
"s_add_u32 m0, s41, 0\n"
"v_mfma_f32_32x32x16_bf16 v[176:191], a[164:167], v[120:123], v[176:191]\n"
"ds_read_b128 a[68:71], v8 offset:1024\n"
"buffer_load_dwordx4 v1, s[16:19], s39 offen offset:0 sc1 lds\n"
"v_exp_f32 v95, v95\n"
"v_cvt_pk_bf16_f32 v126, v92, v93\n"
"v_mfma_f32_32x32x16_bf16 v[208:223], a[168:171], v[120:123], v[208:223]\n"
"ds_read_b128 a[72:75], v8 offset:2048\n"
"v_add_f32 v14, v14, v92\n"
"v_add_f32 v15, v15, v93\n"
"v_cvt_pk_bf16_f32 v127, v94, v95\n"
"v_add_f32 v14, v14, v94\n"
"v_add_f32 v15, v15, v95\n"
"v_mfma_f32_32x32x16_bf16 v[240:255], a[172:175], v[120:123], v[240:255]\n"
"ds_read_b128 a[76:79], v8 offset:3072\n"
"buffer_load_dwordx4 v1, s[16:19], s39 offen offset:1024 sc1 lds\n"
"v_mfma_f32_32x32x16_bf16 v[128:143], a[176:179], v[108:111], v[128:143]\n"
"ds_read_b128 a[80:83], v8 offset:4096\n"
"v_mfma_f32_32x32x16_bf16 v[160:175], a[180:183], v[108:111], v[160:175]\n"
"ds_read_b128 a[84:87], v8 offset:5120\n"
"buffer_load_dwordx4 v1, s[16:19], s39 offen offset:2048 sc1 lds\n"
"v_mfma_f32_32x32x16_bf16 v[192:207], a[184:187], v[108:111], v[192:207]\n"
"ds_read_b128 a[88:91], v8 offset:6144\n"
"v_mfma_f32_32x32x16_bf16 v[224:239], a[188:191], v[108:111], v[224:239]\n"
"ds_read_b128 a[92:95], v8 offset:7168\n"
"buffer_load_dwordx4 v1, s[16:19], s39 offen offset:3072 sc1 lds\n"
"v_mfma_f32_32x32x16_bf16 v[144:159], a[176:179], v[124:127], v[144:159]\n"
"s_add_u32 m0, s41, 4096\n"
"v_mfma_f32_32x32x16_bf16 v[176:191], a[180:183], v[124:127], v[176:191]\n"
"buffer_load_dwordx4 v1, s[16:19], s40 offen offset:0 sc1 lds\n"
"v_mfma_f32_32x32x16_bf16 v[208:223], a[184:187], v[124:127], v[208:223]\n"
"v_mfma_f32_32x32x16_bf16 v[240:255], a[188:191], v[124:127], v[240:255]\n"
"buffer_load_dwordx4 v1, s[16:19], s40 offen offset:1024 sc1 lds\n"
"s_mov_b32 s43, s42\n"
"s_sub_u32 s54, s54, 1\n"
"s_cmp_lg_u32 s54, 0\n"
"s_cbranch_scc1 .Lp1_loop\n"
".Lp1_done:\n"
"s_cmp_eq_u32 s29, 0\n"
"s_cbranch_scc1 .Lp1_w0\n"
"s_cmp_eq_u32 s29, 1\n"
"s_cbranch_scc1 .Lp1_w1\n"
"s_waitcnt lgkmcnt(0)\n"
"v_mfma_f32_32x32x16_bf16 v[32:47], a[64:67], a[0:3], 0\n"
"v_mfma_f32_32x32x16_bf16 v[32:47], a[68:71], a[4:7], v[32:47]\n"
"buffer_load_dwordx4 v1, s[16:19], s40 offen offset:2048 sc1 lds\n"
"v_mfma_f32_32x32x16_bf16 v[32:47], a[72:75], a[8:11], v[32:47]\n"
"v_mfma_f32_32x32x16_bf16 v[32:47], a[76:79], a[12:15], v[32:47]\n"
"buffer_load_dwordx4 v1, s[16:19], s40 offen offset:3072 sc1 lds\n"
"v_mfma_f32_32x32x16_bf16 v[32:47], a[80:83], a[16:19], v[32:47]\n"
"ds_read_b128 a[96:99], v8 offset:8192\n"
"s_add_u32 m0, s41, 32768\n"
"v_mfma_f32_32x32x16_bf16 v[32:47], a[84:87], a[20:23], v[32:47]\n"
"buffer_load_dwordx4 v1, s[20:23], s39 offen offset:0 sc1 lds\n"
"ds_read_b128 a[100:103], v8 offset:9216\n"
"v_mfma_f32_32x32x16_bf16 v[32:47], a[88:91], a[24:27], v[32:47]\n"
"ds_read_b128 a[104:107], v8 offset:10240\n"
"v_mfma_f32_32x32x16_bf16 v[32:47], a[92:95], a[28:31], v[32:47]\n"
"buffer_load_dwordx4 v1, s[20:23], s39 offen offset:1024 sc1 lds\n"
"ds_read_b128 a[108:111], v8 offset:11264\n"
"v_mfma_f32_32x32x16_bf16 v[48:63], a[64:67], a[32:35], 0\n"
"ds_read_b128 a[112:115], v8 offset:12288\n"
"v_mfma_f32_32x32x16_bf16 v[48:63], a[68:71], a[36:39], v[48:63]\n"
"buffer_load_dwordx4 v1, s[20:23], s39 offen offset:2048 sc1 lds\n"
"ds_read_b128 a[116:119], v8 offset:13312\n"
"v_exp_f32 v32, v32\n"
"v_mfma_f32_32x32x16_bf16 v[48:63], a[72:75], a[40:43], v[48:63]\n"
"ds_read_b128 a[120:123], v8 offset:14336\n"
"v_exp_f32 v33, v33\n"
"v_exp_f32 v34, v34\n"
"v_mfma_f32_32x32x16_bf16 v[48:63], a[76:79], a[44:47], v[48:63]\n"
"buffer_load_dwordx4 v1, s[20:23], s39 offen offset:3072 sc1 lds\n"
"ds_read_b128 a[124:127], v8 offset:15360\n"
"v_exp_f32 v35, v35\n"
"v_cvt_pk_bf16_f32 v96, v32, v33\n"
"v_mfma_f32_32x32x16_bf16 v[48:63], a[80:83], a[48:51], v[48:63]\n"
"v_add_f32 v12, v12, v32\n"
"v_add_f32 v13, v13, v33\n"
"v_exp_f32 v36, v36\n"
"s_add_u32 m0, s41, 36864\n"
"v_mfma_f32_32x32x16_bf16 v[48:63], a[84:87], a[52:55], v[48:63]\n"
"buffer_load_dwordx4 v1, s[20:23], s40 offen offset:0 sc1 lds\n"
"v_exp_f32 v37, v37\n"
"v_cvt_pk_bf16_f32 v97, v34, v35\n"
"v_add_f32 v12, v12, v34\n"
"v_mfma_f32_32x32x16_bf16 v[48:63], a[88:91], a[56:59], v[48:63]\n"
"ds_read_b128 a[128:131], v8 offset:32768\n"
"v_add_f32 v13, v13, v35\n"
"v_exp_f32 v38, v38\n"
"v_exp_f32 v39, v39\n"
"v_mfma_f32_32x32x16_bf16 v[48:63], a[92:95], a[60:63], v[48:63]\n"
"buffer_load_dwordx4 v1, s[20:23], s40 offen offset:1024 sc1 lds\n"
"v_cvt_pk_bf16_f32 v98, v36, v37\n"
"v_add_f32 v12, v12, v36\n"
"v_add_f32 v13, v13, v37\n"
"s_waitcnt lgkmcnt(1)\n"
"v_mfma_f32_32x32x16_bf16 v[64:79], a[96:99], a[0:3], 0\n"
"ds_read_b128 a[132:135], v8 offset:33792\n"
"v_exp_f32 v40, v40\n"
"v_exp_f32 v41, v41\n"
"v_cvt_pk_bf16_f32 v99, v38, v39\n"
"v_mfma_f32_32x32x16_bf16 v[64:79], a[100:103], a[4:7], v[64:79]\n"
"buffer_load_dwordx4 v1, s[20:23], s40 offen offset:2048 sc1 lds\n"
"v_add_f32 v12, v12, v38\n"
"v_add_f32 v13, v13, v39\n"
"v_exp_f32 v42, v42\n"
"v_mfma_f32_32x32x16_bf16 v[64:79], a[104:107], a[8:11], v[64:79]\n"
"ds_read_b128 a[136:139], v8 offset:34816\n"
"v_exp_f32 v43, v43\n"
"v_cvt_pk_bf16_f32 v100, v40, v41\n"
"v_add_f32 v12, v12, v40\n"
"v_add_f32 v13, v13, v41\n"
"v_mfma_f32_32x32x16_bf16 v[64:79], a[108:111], a[12:15], v[64:79]\n"
"buffer_load_dwordx4 v1, s[20:23], s40 offen offset:3072 sc1 lds\n"
"v_exp_f32 v44, v44\n"
"v_exp_f32 v45, v45\n"
"v_mfma_f32_32x32x16_bf16 v[64:79], a[112:115], a[16:19], v[64:79]\n"
"ds_read_b128 a[140:143], v8 offset:35840\n"
"v_cvt_pk_bf16_f32 v101, v42, v43\n"
"v_add_f32 v12, v12, v42\n"
"v_add_f32 v13, v13, v43\n"
"v_exp_f32 v46, v46\n"
"v_mfma_f32_32x32x16_bf16 v[64:79], a[116:119], a[20:23], v[64:79]\n"
"v_exp_f32 v47, v47\n"
"v_cvt_pk_bf16_f32 v102, v44, v45\n"
"v_add_f32 v12, v12, v44\n"
"v_add_f32 v13, v13, v45\n"
"v_mfma_f32_32x32x16_bf16 v[64:79], a[120:123], a[24:27], v[64:79]\n"
"ds_read_b128 a[144:147], v8 offset:36864\n"
"v_exp_f32 v48, v48\n"
"v_exp_f32 v49, v49\n"
"v_cvt_pk_bf16_f32 v103, v46, v47\n"
"v_mfma_f32_32x32x16_bf16 v[64:79], a[124:127], a[28:31], v[64:79]\n"
"v_add_f32 v12, v12, v46\n"
"v_add_f32 v13, v13, v47\n"
"v_exp_f32 v50, v50\n"
"v_exp_f32 v51, v51\n"
"v_mfma_f32_32x32x16_bf16 v[80:95], a[96:99], a[32:35], 0\n"
"ds_read_b128 a[148:151], v8 offset:37888\n"
"v_cvt_pk_bf16_f32 v112, v48, v49\n"
"v_add_f32 v14, v14, v48\n"
"v_add_f32 v15, v15, v49\n"
"v_exp_f32 v52, v52\n"
"v_mfma_f32_32x32x16_bf16 v[80:95], a[100:103], a[36:39], v[80:95]\n"
"v_exp_f32 v53, v53\n"
"v_cvt_pk_bf16_f32 v113, v50, v51\n"
"v_add_f32 v14, v14, v50\n"
"v_add_f32 v15, v15, v51\n"
"v_mfma_f32_32x32x16_bf16 v[80:95], a[104:107], a[40:43], v[80:95]\n"
"ds_read_b128 a[152:155], v8 offset:38912\n"
"v_exp_f32 v54, v54\n"
"v_exp_f32 v55, v55\n"
"v_cvt_pk_bf16_f32 v114, v52, v53\n"
"v_mfma_f32_32x32x16_bf16 v[80:95], a[108:111], a[44:47], v[80:95]\n"
"v_add_f32 v14, v14, v52\n"
"v_add_f32 v15, v15, v53\n"
"v_exp_f32 v56, v56\n"
"v_exp_f32 v57, v57\n"
"v_mfma_f32_32x32x16_bf16 v[80:95], a[112:115], a[48:51], v[80:95]\n"
"ds_read_b128 a[156:159], v8 offset:39936\n"
"v_cvt_pk_bf16_f32 v115, v54, v55\n"
"v_add_f32 v14, v14, v54\n"
"v_add_f32 v15, v15, v55\n"
"v_exp_f32 v58, v58\n"
"v_mfma_f32_32x32x16_bf16 v[80:95], a[116:119], a[52:55], v[80:95]\n"
"v_exp_f32 v59, v59\n"
"v_cvt_pk_bf16_f32 v116, v56, v57\n"
"v_add_f32 v14, v14, v56\n"
"v_add_f32 v15, v15, v57\n"
"v_mfma_f32_32x32x16_bf16 v[80:95], a[120:123], a[56:59], v[80:95]\n"
"ds_read_b128 a[160:163], v8 offset:40960\n"
"v_exp_f32 v60, v60\n"
"v_exp_f32 v61, v61\n"
"v_cvt_pk_bf16_f32 v117, v58, v59\n"
"v_mfma_f32_32x32x16_bf16 v[80:95], a[124:127], a[60:63], v[80:95]\n"
"v_add_f32 v14, v14, v58\n"
"v_add_f32 v15, v15, v59\n"
"v_exp_f32 v62, v62\n"
"s_waitcnt lgkmcnt(5)\n"
"v_mfma_f32_32x32x16_bf16 v[128:143], a[128:131], v[96:99], v[128:143]\n"
"ds_read_b128 a[164:167], v8 offset:41984\n"
"v_exp_f32 v63, v63\n"
"v_cvt_pk_bf16_f32 v118, v60, v61\n"
"v_add_f32 v14, v14, v60\n"
"v_add_f32 v15, v15, v61\n"
"v_mfma_f32_32x32x16_bf16 v[160:175], a[132:135], v[96:99], v[160:175]\n"
"v_exp_f32 v64, v64\n"
"v_exp_f32 v65, v65\n"
"v_cvt_pk_bf16_f32 v119, v62, v63\n"
"v_add_f32 v14, v14, v62\n"
"v_mfma_f32_32x32x16_bf16 v[192:207], a[136:139], v[96:99], v[192:207]\n"
"ds_read_b128 a[168:171], v8 offset:43008\n"
"v_add_f32 v15, v15, v63\n"
"v_exp_f32 v66, v66\n"
"v_exp_f32 v67, v67\n"
"v_mfma_f32_32x32x16_bf16 v[224:239], a[140:143], v[96:99], v[224:239]\n"
"v_cvt_pk_bf16_f32 v104, v64, v65\n"
"v_add_f32 v12, v12, v64\n"
"v_add_f32 v13, v13, v65\n"
"v_exp_f32 v68, v68\n"
"v_mfma_f32_32x32x16_bf16 v[144:159], a[128:131], v[112:115], v[144:159]\n"
"ds_read_b128 a[172:175], v8 offset:44032\n"
"v_exp_f32 v69, v69\n"
"v_cvt_pk_bf16_f32 v105, v66, v67\n"
"v_add_f32 v12, v12, v66\n"
"v_add_f32 v13, v13, v67\n"
"v_mfma_f32_32x32x16_bf16 v[176:191], a[132:135], v[112:115], v[176:191]\n"
"v_exp_f32 v70, v70\n"
"v_exp_f32 v71, v71\n"
"v_cvt_pk_bf16_f32 v106, v68, v69\n"
"v_add_f32 v12, v12, v68\n"
"v_mfma_f32_32x32x16_bf16 v[208:223], a[136:139], v[112:115], v[208:223]\n"
"ds_read_b128 a[176:179], v8 offset:45056\n"
"v_add_f32 v13, v13, v69\n"
"v_exp_f32 v80, v80\n"
"v_exp_f32 v81, v81\n"
"v_mfma_f32_32x32x16_bf16 v[240:255], a[140:143], v[112:115], v[240:255]\n"
"v_cvt_pk_bf16_f32 v107, v70, v71\n"
"v_add_f32 v12, v12, v70\n"
"v_add_f32 v13, v13, v71\n"
"v_exp_f32 v82, v82\n"
"s_waitcnt lgkmcnt(5)\n"
"v_mfma_f32_32x32x16_bf16 v[128:143], a[144:147], v[100:103], v[128:143]\n"
"ds_read_b128 a[180:183], v8 offset:46080\n"
"v_exp_f32 v83, v83\n"
"v_cvt_pk_bf16_f32 v120, v80, v81\n"
"v_add_f32 v14, v14, v80\n"
"v_add_f32 v15, v15, v81\n"
"v_mfma_f32_32x32x16_bf16 v[160:175], a[148:151], v[100:103], v[160:175]\n"
"v_exp_f32 v84, v84\n"
"v_exp_f32 v85, v85\n"
"v_cvt_pk_bf16_f32 v121, v82, v83\n"
"v_add_f32 v14, v14, v82\n"
"v_mfma_f32_32x32x16_bf16 v[192:207], a[152:155], v[100:103], v[192:207]\n"
"ds_read_b128 a[184:187], v8 offset:47104\n"
"v_add_f32 v15, v15, v83\n"
"v_exp_f32 v86, v86\n"
"v_exp_f32 v87, v87\n"
"v_mfma_f32_32x32x16_bf16 v[224:239], a[156:159], v[100:103], v[224:239]\n"
"v_cvt_pk_bf16_f32 v122, v84, v85\n"
"v_add_f32 v14, v14, v84\n"
"v_add_f32 v15, v15, v85\n"
"v_exp_f32 v72, v72\n"
"v_mfma_f32_32x32x16_bf16 v[144:159], a[144:147], v[116:119], v[144:159]\n"
"ds_read_b128 a[188:191], v8 offset:48128\n"
"v_exp_f32 v73, v73\n"
"v_cvt_pk_bf16_f32 v123, v86, v87\n"
"v_add_f32 v14, v14, v86\n"
"v_add_f32 v15, v15, v87\n"
"v_mfma_f32_32x32x16_bf16 v[176:191], a[148:151], v[116:119], v[176:191]\n"
"v_exp_f32 v74, v74\n"
"v_exp_f32 v75, v75\n"
"v_cvt_pk_bf16_f32 v108, v72, v73\n"
"v_add_f32 v12, v12, v72\n"
"v_mfma_f32_32x32x16_bf16 v[208:223], a[152:155], v[116:119], v[208:223]\n"
"s_add_u32 s42, s43, 16384\n"
"v_add_u32 v8, s42, v1\n"
"v_add_f32 v13, v13, v73\n"
"v_exp_f32 v76, v76\n"
"v_mfma_f32_32x32x16_bf16 v[240:255], a[156:159], v[116:119], v[240:255]\n"
"v_exp_f32 v77, v77\n"
"v_cvt_pk_bf16_f32 v109, v74, v75\n"
"v_add_f32 v12, v12, v74\n"
"v_add_f32 v13, v13, v75\n"
"s_waitcnt lgkmcnt(4)\n"
"v_mfma_f32_32x32x16_bf16 v[128:143], a[160:163], v[104:107], v[128:143]\n"
"v_exp_f32 v78, v78\n"
"v_exp_f32 v79, v79\n"
"v_cvt_pk_bf16_f32 v110, v76, v77\n"
"v_add_f32 v12, v12, v76\n"
"v_mfma_f32_32x32x16_bf16 v[160:175], a[164:167], v[104:107], v[160:175]\n"
"v_add_f32 v13, v13, v77\n"
"v_exp_f32 v88, v88\n"
"v_exp_f32 v89, v89\n"
"v_cvt_pk_bf16_f32 v111, v78, v79\n"
"v_mfma_f32_32x32x16_bf16 v[192:207], a[168:171], v[104:107], v[192:207]\n"
"v_add_f32 v12, v12, v78\n"
"v_add_f32 v13, v13, v79\n"
"v_exp_f32 v90, v90\n"
"v_exp_f32 v91, v91\n"
"v_mfma_f32_32x32x16_bf16 v[224:239], a[172:175], v[104:107], v[224:239]\n"
"v_cvt_pk_bf16_f32 v124, v88, v89\n"
"v_add_f32 v14, v14, v88\n"
"v_add_f32 v15, v15, v89\n"
"v_exp_f32 v92, v92\n"
"s_waitcnt lgkmcnt(0)\n"
"v_mfma_f32_32x32x16_bf16 v[144:159], a[160:163], v[120:123], v[144:159]\n"
"ds_read_b128 a[64:67], v8 offset:0\n"
"v_exp_f32 v93, v93\n"
"v_cvt_pk_bf16_f32 v125, v90, v91\n"
"v_add_f32 v14, v14, v90\n"
"v_add_f32 v15, v15, v91\n"
"v_mfma_f32_32x32x16_bf16 v[176:191], a[164:167], v[120:123], v[176:191]\n"
"ds_read_b128 a[68:71], v8 offset:1024\n"
"v_exp_f32 v94, v94\n"
"v_exp_f32 v95, v95\n"
"v_cvt_pk_bf16_f32 v126, v92, v93\n"
"v_mfma_f32_32x32x16_bf16 v[208:223], a[168:171], v[120:123], v[208:223]\n"
"ds_read_b128 a[72:75], v8 offset:2048\n"
"v_add_f32 v14, v14, v92\n"
"v_add_f32 v15, v15, v93\n"
"v_cvt_pk_bf16_f32 v127, v94, v95\n"
"v_add_f32 v14, v14, v94\n"
"v_add_f32 v15, v15, v95\n"
"v_mfma_f32_32x32x16_bf16 v[240:255], a[172:175], v[120:123], v[240:255]\n"
"ds_read_b128 a[76:79], v8 offset:3072\n"
"v_mfma_f32_32x32x16_bf16 v[128:143], a[176:179], v[108:111], v[128:143]\n"
"ds_read_b128 a[80:83], v8 offset:4096\n"
"v_mfma_f32_32x32x16_bf16 v[160:175], a[180:183], v[108:111], v[160:175]\n"
"ds_read_b128 a[84:87], v8 offset:5120\n"
"v_mfma_f32_32x32x16_bf16 v[192:207], a[184:187], v[108:111], v[192:207]\n"
"ds_read_b128 a[88:91], v8 offset:6144\n"
"v_mfma_f32_32x32x16_bf16 v[224:239], a[188:191], v[108:111], v[224:239]\n"
"ds_read_b128 a[92:95], v8 offset:7168\n"
"v_mfma_f32_32x32x16_bf16 v[144:159], a[176:179], v[124:127], v[144:159]\n"
"v_mfma_f32_32x32x16_bf16 v[176:191], a[180:183], v[124:127], v[176:191]\n"
"v_mfma_f32_32x32x16_bf16 v[208:223], a[184:187], v[124:127], v[208:223]\n"
"v_mfma_f32_32x32x16_bf16 v[240:255], a[188:191], v[124:127], v[240:255]\n"
"s_mov_b32 s43, s42\n"
"s_waitcnt lgkmcnt(0)\n"
"v_mfma_f32_32x32x16_bf16 v[32:47], a[64:67], a[0:3], 0\n"
"v_mfma_f32_32x32x16_bf16 v[32:47], a[68:71], a[4:7], v[32:47]\n"
"v_mfma_f32_32x32x16_bf16 v[32:47], a[72:75], a[8:11], v[32:47]\n"
"v_mfma_f32_32x32x16_bf16 v[32:47], a[76:79], a[12:15], v[32:47]\n"
"v_mfma_f32_32x32x16_bf16 v[32:47], a[80:83], a[16:19], v[32:47]\n"
"ds_read_b128 a[96:99], v8 offset:8192\n"
"v_mfma_f32_32x32x16_bf16 v[32:47], a[84:87], a[20:23], v[32:47]\n"
"ds_read_b128 a[100:103], v8 offset:9216\n"
"v_mfma_f32_32x32x16_bf16 v[32:47], a[88:91], a[24:27], v[32:47]\n"
"ds_read_b128 a[104:107], v8 offset:10240\n"
"v_mfma_f32_32x32x16_bf16 v[32:47], a[92:95], a[28:31], v[32:47]\n"
"ds_read_b128 a[108:111], v8 offset:11264\n"
"v_mfma_f32_32x32x16_bf16 v[48:63], a[64:67], a[32:35], 0\n"
"ds_read_b128 a[112:115], v8 offset:12288\n"
"v_mfma_f32_32x32x16_bf16 v[48:63], a[68:71], a[36:39], v[48:63]\n"
"ds_read_b128 a[116:119], v8 offset:13312\n"
"v_exp_f32 v32, v32\n"
"v_exp_f32 v33, v33\n"
"v_mfma_f32_32x32x16_bf16 v[48:63], a[72:75], a[40:43], v[48:63]\n"
"ds_read_b128 a[120:123], v8 offset:14336\n"
"v_exp_f32 v34, v34\n"
"v_exp_f32 v35, v35\n"
"v_cvt_pk_bf16_f32 v96, v32, v33\n"
"v_mfma_f32_32x32x16_bf16 v[48:63], a[76:79], a[44:47], v[48:63]\n"
"ds_read_b128 a[124:127], v8 offset:15360\n"
"v_add_f32 v12, v12, v32\n"
"v_add_f32 v13, v13, v33\n"
"v_exp_f32 v36, v36\n"
"v_mfma_f32_32x32x16_bf16 v[48:63], a[80:83], a[48:51], v[48:63]\n"
"v_exp_f32 v37, v37\n"
"v_cvt_pk_bf16_f32 v97, v34, v35\n"
"v_add_f32 v12, v12, v34\n"
"v_add_f32 v13, v13, v35\n"
"v_mfma_f32_32x32x16_bf16 v[48:63], a[84:87], a[52:55], v[48:63]\n"
"v_exp_f32 v38, v38\n"
"v_exp_f32 v39, v39\n"
"v_cvt_pk_bf16_f32 v98, v36, v37\n"
"v_add_f32 v12, v12, v36\n"
"v_mfma_f32_32x32x16_bf16 v[48:63], a[88:91], a[56:59], v[48:63]\n"
"ds_read_b128 a[128:131], v8 offset:32768\n"
"v_add_f32 v13, v13, v37\n"
"v_exp_f32 v40, v40\n"
"v_exp_f32 v41, v41\n"
"v_mfma_f32_32x32x16_bf16 v[48:63], a[92:95], a[60:63], v[48:63]\n"
"v_cvt_pk_bf16_f32 v99, v38, v39\n"
"v_add_f32 v12, v12, v38\n"
"v_add_f32 v13, v13, v39\n"
"v_exp_f32 v42, v42\n"
"s_waitcnt lgkmcnt(1)\n"
"v_mfma_f32_32x32x16_bf16 v[64:79], a[96:99], a[0:3], 0\n"
"ds_read_b128 a[132:135], v8 offset:33792\n"
"v_exp_f32 v43, v43\n"
"v_cvt_pk_bf16_f32 v100, v40, v41\n"
"v_add_f32 v12, v12, v40\n"
"v_add_f32 v13, v13, v41\n"
"v_mfma_f32_32x32x16_bf16 v[64:79], a[100:103], a[4:7], v[64:79]\n"
"v_exp_f32 v44, v44\n"
"v_exp_f32 v45, v45\n"
"v_cvt_pk_bf16_f32 v101, v42, v43\n"
"v_add_f32 v12, v12, v42\n"
"v_mfma_f32_32x32x16_bf16 v[64:79], a[104:107], a[8:11], v[64:79]\n"
"ds_read_b128 a[136:139], v8 offset:34816\n"
"v_add_f32 v13, v13, v43\n"
"v_exp_f32 v46, v46\n"
"v_exp_f32 v47, v47\n"
"v_mfma_f32_32x32x16_bf16 v[64:79], a[108:111], a[12:15], v[64:79]\n"
"v_cvt_pk_bf16_f32 v102, v44, v45\n"
"v_add_f32 v12, v12, v44\n"
"v_add_f32 v13, v13, v45\n"
"v_exp_f32 v48, v48\n"
"v_mfma_f32_32x32x16_bf16 v[64:79], a[112:115], a[16:19], v[64:79]\n"
"ds_read_b128 a[140:143], v8 offset:35840\n"
"v_exp_f32 v49, v49\n"
"v_cvt_pk_bf16_f32 v103, v46, v47\n"
"v_add_f32 v12, v12, v46\n"
"v_add_f32 v13, v13, v47\n"
"v_mfma_f32_32x32x16_bf16 v[64:79], a[116:119], a[20:23], v[64:79]\n"
"v_exp_f32 v50, v50\n"
"v_exp_f32 v51, v51\n"
"v_cvt_pk_bf16_f32 v112, v48, v49\n"
"v_add_f32 v14, v14, v48\n"
"v_mfma_f32_32x32x16_bf16 v[64:79], a[120:123], a[24:27], v[64:79]\n"
"ds_read_b128 a[144:147], v8 offset:36864\n"
"v_add_f32 v15, v15, v49\n"
"v_exp_f32 v52, v52\n"
"v_exp_f32 v53, v53\n"
"v_mfma_f32_32x32x16_bf16 v[64:79], a[124:127], a[28:31], v[64:79]\n"
"v_cvt_pk_bf16_f32 v113, v50, v51\n"
"v_add_f32 v14, v14, v50\n"
"v_add_f32 v15, v15, v51\n"
"v_exp_f32 v54, v54\n"
"v_mfma_f32_32x32x16_bf16 v[80:95], a[96:99], a[32:35], 0\n"
"ds_read_b128 a[148:151], v8 offset:37888\n"
"v_exp_f32 v55, v55\n"
"v_cvt_pk_bf16_f32 v114, v52, v53\n"
"v_add_f32 v14, v14, v52\n"
"v_add_f32 v15, v15, v53\n"
"v_mfma_f32_32x32x16_bf16 v[80:95], a[100:103], a[36:39], v[80:95]\n"
"v_exp_f32 v56, v56\n"
"v_exp_f32 v57, v57\n"
"v_cvt_pk_bf16_f32 v115, v54, v55\n"
"v_add_f32 v14, v14, v54\n"
"v_mfma_f32_32x32x16_bf16 v[80:95], a[104:107], a[40:43], v[80:95]\n"
"ds_read_b128 a[152:155], v8 offset:38912\n"
"v_add_f32 v15, v15, v55\n"
"v_exp_f32 v58, v58\n"
"v_exp_f32 v59, v59\n"
"v_mfma_f32_32x32x16_bf16 v[80:95], a[108:111], a[44:47], v[80:95]\n"
"v_cvt_pk_bf16_f32 v116, v56, v57\n"
"v_add_f32 v14, v14, v56\n"
"v_add_f32 v15, v15, v57\n"
"v_exp_f32 v60, v60\n"
"v_mfma_f32_32x32x16_bf16 v[80:95], a[112:115], a[48:51], v[80:95]\n"
"ds_read_b128 a[156:159], v8 offset:39936\n"
"v_exp_f32 v61, v61\n"
"v_cvt_pk_bf16_f32 v117, v58, v59\n"
"v_add_f32 v14, v14, v58\n"
"v_add_f32 v15, v15, v59\n"
"v_mfma_f32_32x32x16_bf16 v[80:95], a[116:119], a[52:55], v[80:95]\n"
"v_exp_f32 v62, v62\n"
"v_exp_f32 v63, v63\n"
"v_cvt_pk_bf16_f32 v118, v60, v61\n"
"v_add_f32 v14, v14, v60\n"
"v_mfma_f32_32x32x16_bf16 v[80:95], a[120:123], a[56:59], v[80:95]\n"
"ds_read_b128 a[160:163], v8 offset:40960\n"
"v_add_f32 v15, v15, v61\n"
"v_exp_f32 v64, v64\n"
"v_exp_f32 v65, v65\n"
"v_mfma_f32_32x32x16_bf16 v[80:95], a[124:127], a[60:63], v[80:95]\n"
"v_cvt_pk_bf16_f32 v119, v62, v63\n"
"v_add_f32 v14, v14, v62\n"
"v_add_f32 v15, v15, v63\n"
"v_exp_f32 v66, v66\n"
"s_waitcnt lgkmcnt(5)\n"
"v_mfma_f32_32x32x16_bf16 v[128:143], a[128:131], v[96:99], v[128:143]\n"
"ds_read_b128 a[164:167], v8 offset:41984\n"
"v_exp_f32 v67, v67\n"
"v_cvt_pk_bf16_f32 v104, v64, v65\n"
"v_add_f32 v12, v12, v64\n"
"v_add_f32 v13, v13, v65\n"
"v_mfma_f32_32x32x16_bf16 v[160:175], a[132:135], v[96:99], v[160:175]\n"
"v_exp_f32 v68, v68\n"
"v_exp_f32 v69, v69\n"
"v_cvt_pk_bf16_f32 v105, v66, v67\n"
"v_add_f32 v12, v12, v66\n"
"v_mfma_f32_32x32x16_bf16 v[192:207], a[136:139], v[96:99], v[192:207]\n"
"ds_read_b128 a[168:171], v8 offset:43008\n"
"v_add_f32 v13, v13, v67\n"
"v_exp_f32 v70, v70\n"
"v_exp_f32 v71, v71\n"
"v_mfma_f32_32x32x16_bf16 v[224:239], a[140:143], v[96:99], v[224:239]\n"
"v_cvt_pk_bf16_f32 v106, v68, v69\n"
"v_add_f32 v12, v12, v68\n"
"v_add_f32 v13, v13, v69\n"
"v_exp_f32 v80, v80\n"
"v_mfma_f32_32x32x16_bf16 v[144:159], a[128:131], v[112:115], v[144:159]\n"
"ds_read_b128 a[172:175], v8 offset:44032\n"
"v_exp_f32 v81, v81\n"
"v_cvt_pk_bf16_f32 v107, v70, v71\n"
"v_add_f32 v12, v12, v70\n"
"v_add_f32 v13, v13, v71\n"
"v_mfma_f32_32x32x16_bf16 v[176:191], a[132:135], v[112:115], v[176:191]\n"
"v_exp_f32 v82, v82\n"
"v_exp_f32 v83, v83\n"
"v_cvt_pk_bf16_f32 v120, v80, v81\n"
"v_add_f32 v14, v14, v80\n"
"v_mfma_f32_32x32x16_bf16 v[208:223], a[136:139], v[112:115], v[208:223]\n"
"ds_read_b128 a[176:179], v8 offset:45056\n"
"v_add_f32 v15, v15, v81\n"
"v_exp_f32 v84, v84\n"
"v_exp_f32 v85, v85\n"
"v_mfma_f32_32x32x16_bf16 v[240:255], a[140:143], v[112:115], v[240:255]\n"
"v_cvt_pk_bf16_f32 v121, v82, v83\n"
"v_add_f32 v14, v14, v82\n"
"v_add_f32 v15, v15, v83\n"
"v_exp_f32 v86, v86\n"
"s_waitcnt lgkmcnt(5)\n"
"v_mfma_f32_32x32x16_bf16 v[128:143], a[144:147], v[100:103], v[128:143]\n"
"ds_read_b128 a[180:183], v8 offset:46080\n"
"v_exp_f32 v87, v87\n"
"v_cvt_pk_bf16_f32 v122, v84, v85\n"
"v_add_f32 v14, v14, v84\n"
"v_add_f32 v15, v15, v85\n"
"v_mfma_f32_32x32x16_bf16 v[160:175], a[148:151], v[100:103], v[160:175]\n"
"v_exp_f32 v72, v72\n"
"v_exp_f32 v73, v73\n"
"v_cvt_pk_bf16_f32 v123, v86, v87\n"
"v_add_f32 v14, v14, v86\n"
"v_mfma_f32_32x32x16_bf16 v[192:207], a[152:155], v[100:103], v[192:207]\n"
"ds_read_b128 a[184:187], v8 offset:47104\n"
"v_add_f32 v15, v15, v87\n"
"v_exp_f32 v74, v74\n"
"v_exp_f32 v75, v75\n"
"v_mfma_f32_32x32x16_bf16 v[224:239], a[156:159], v[100:103], v[224:239]\n"
"v_cvt_pk_bf16_f32 v108, v72, v73\n"
"v_add_f32 v12, v12, v72\n"
"v_add_f32 v13, v13, v73\n"
"v_exp_f32 v76, v76\n"
"v_mfma_f32_32x32x16_bf16 v[144:159], a[144:147], v[116:119], v[144:159]\n"
"ds_read_b128 a[188:191], v8 offset:48128\n"
"v_exp_f32 v77, v77\n"
"v_cvt_pk_bf16_f32 v109, v74, v75\n"
"v_add_f32 v12, v12, v74\n"
"v_add_f32 v13, v13, v75\n"
"v_mfma_f32_32x32x16_bf16 v[176:191], a[148:151], v[116:119], v[176:191]\n"
"v_exp_f32 v78, v78\n"
"v_exp_f32 v79, v79\n"
"v_cvt_pk_bf16_f32 v110, v76, v77\n"
"v_add_f32 v12, v12, v76\n"
"v_mfma_f32_32x32x16_bf16 v[208:223], a[152:155], v[116:119], v[208:223]\n"
"s_xor_b32 s42, s43, 0x10000\n"
"s_sub_u32 s42, s42, 16384\n"
"s_add_u32 s42, s42, s55\n"
"v_add_u32 v8, s42, v1\n"
"v_add_f32 v13, v13, v77\n"
"v_mfma_f32_32x32x16_bf16 v[240:255], a[156:159], v[116:119], v[240:255]\n"
"s_lshl_b32 s39, s52, 15\n"
"s_add_u32 s39, s39, s38\n"
"v_exp_f32 v88, v88\n"
"s_waitcnt lgkmcnt(4)\n"
"v_mfma_f32_32x32x16_bf16 v[128:143], a[160:163], v[104:107], v[128:143]\n"
"s_cmp_lt_u32 s52, s53\n"
"s_cselect_b32 s39, s39, 0x40000000\n"
"v_exp_f32 v89, v89\n"
"v_cvt_pk_bf16_f32 v111, v78, v79\n"
"v_add_f32 v12, v12, v78\n"
"v_mfma_f32_32x32x16_bf16 v[160:175], a[164:167], v[104:107], v[160:175]\n"
"s_add_u32 s40, s39, 4096\n"
"s_and_b32 s41, s52, 1\n"
"v_add_f32 v13, v13, v79\n"
"v_exp_f32 v90, v90\n"
"v_mfma_f32_32x32x16_bf16 v[192:207], a[168:171], v[104:107], v[192:207]\n"
"s_lshl_b32 s41, s41, 16\n"
"s_add_u32 s41, s41, s38\n"
"v_exp_f32 v91, v91\n"
"v_cvt_pk_bf16_f32 v124, v88, v89\n"
"v_add_f32 v14, v14, v88\n"
"v_mfma_f32_32x32x16_bf16 v[224:239], a[172:175], v[104:107], v[224:239]\n"
"s_add_u32 s52, s52, 1\n"
"v_add_f32 v15, v15, v89\n"
"v_exp_f32 v92, v92\n"
"s_waitcnt vmcnt(0) lgkmcnt(0)\n"
"s_barrier\n"
"v_mfma_f32_32x32x16_bf16 v[144:159], a[160:163], v[120:123], v[144:159]\n"
"ds_read_b128 a[64:67], v8 offset:0\n"
"v_exp_f32 v93, v93\n"
"v_cvt_pk_bf16_f32 v125, v90, v91\n"
"v_add_f32 v14, v14, v90\n"
"s_add_u32 m0, s41, 0\n"
"v_mfma_f32_32x32x16_bf16 v[176:191], a[164:167], v[120:123], v[176:191]\n"
"ds_read_b128 a[68:71], v8 offset:1024\n"
"buffer_load_dwordx4 v1, s[16:19], s39 offen offset:0 sc1 lds\n"
"v_add_f32 v15, v15, v91\n"
"v_exp_f32 v94, v94\n"
"v_mfma_f32_32x32x16_bf16 v[208:223], a[168:171], v[120:123], v[208:223]\n"
"ds_read_b128 a[72:75], v8 offset:2048\n"
"v_exp_f32 v95, v95\n"
"v_cvt_pk_bf16_f32 v126, v92, v93\n"
"v_add_f32 v14, v14, v92\n"
"v_add_f32 v15, v15, v93\n"
"v_mfma_f32_32x32x16_bf16 v[240:255], a[172:175], v[120:123], v[240:255]\n"
"ds_read_b128 a[76:79], v8 offset:3072\n"
"buffer_load_dwordx4 v1, s[16:19], s39 offen offset:1024 sc1 lds\n"
"v_cvt_pk_bf16_f32 v127, v94, v95\n"
"v_add_f32 v14, v14, v94\n"
"v_add_f32 v15, v15, v95\n"
"v_mfma_f32_32x32x16_bf16 v[128:143], a[176:179], v[108:111], v[128:143]\n"
"ds_read_b128 a[80:83], v8 offset:4096\n"
"v_mfma_f32_32x32x16_bf16 v[160:175], a[180:183], v[108:111], v[160:175]\n"
"ds_read_b128 a[84:87], v8 offset:5120\n"
"buffer_load_dwordx4 v1, s[16:19], s39 offen offset:2048 sc1 lds\n"
"v_mfma_f32_32x32x16_bf16 v[192:207], a[184:187], v[108:111], v[192:207]\n"
"ds_read_b128 a[88:91], v8 offset:6144\n"
"v_mfma_f32_32x32x16_bf16 v[224:239], a[188:191], v[108:111], v[224:239]\n"
"ds_read_b128 a[92:95], v8 offset:7168\n"
"buffer_load_dwordx4 v1, s[16:19], s39 offen offset:3072 sc1 lds\n"
"v_mfma_f32_32x32x16_bf16 v[144:159], a[176:179], v[124:127], v[144:159]\n"
"s_add_u32 m0, s41, 4096\n"
"v_mfma_f32_32x32x16_bf16 v[176:191], a[180:183], v[124:127], v[176:191]\n"
"buffer_load_dwordx4 v1, s[16:19], s40 offen offset:0 sc1 lds\n"
"v_mfma_f32_32x32x16_bf16 v[208:223], a[184:187], v[124:127], v[208:223]\n"
"v_mfma_f32_32x32x16_bf16 v[240:255], a[188:191], v[124:127], v[240:255]\n"
"buffer_load_dwordx4 v1, s[16:19], s40 offen offset:1024 sc1 lds\n"
"s_mov_b32 s43, s42\n"
"s_branch .Lp2\n"
".Lp1_w0:\n"
"s_waitcnt lgkmcnt(0)\n"
"v_mfma_f32_32x32x16_bf16 v[32:47], a[64:67], a[0:3], 0\n"
"v_mfma_f32_32x32x16_bf16 v[32:47], a[68:71], a[4:7], v[32:47]\n"
"buffer_load_dwordx4 v1, s[16:19], s40 offen offset:2048 sc1 lds\n"
"v_mfma_f32_32x32x16_bf16 v[32:47], a[72:75], a[8:11], v[32:47]\n"
"v_mfma_f32_32x32x16_bf16 v[32:47], a[76:79], a[12:15], v[32:47]\n"
"buffer_load_dwordx4 v1, s[16:19], s40 offen offset:3072 sc1 lds\n"
"v_mfma_f32_32x32x16_bf16 v[32:47], a[80:83], a[16:19], v[32:47]\n"
"ds_read_b128 a[96:99], v8 offset:8192\n"
"s_add_u32 m0, s41, 32768\n"
"v_mfma_f32_32x32x16_bf16 v[32:47], a[84:87], a[20:23], v[32:47]\n"
"buffer_load_dwordx4 v1, s[20:23], s39 offen offset:0 sc1 lds\n"
"ds_read_b128 a[100:103], v8 offset:9216\n"
"v_mfma_f32_32x32x16_bf16 v[32:47], a[88:91], a[24:27], v[32:47]\n"
"ds_read_b128 a[104:107], v8 offset:10240\n"
"v_mfma_f32_32x32x16_bf16 v[32:47], a[92:95], a[28:31], v[32:47]\n"
"buffer_load_dwordx4 v1, s[20:23], s39 offen offset:1024 sc1 lds\n"
"ds_read_b128 a[108:111], v8 offset:11264\n"
"v_mfma_f32_32x32x16_bf16 v[48:63], a[64:67], a[32:35], 0\n"
"ds_read_b128 a[112:115], v8 offset:12288\n"
"v_mfma_f32_32x32x16_bf16 v[48:63], a[68:71], a[36:39], v[48:63]\n"
"buffer_load_dwordx4 v1, s[20:23], s39 offen offset:2048 sc1 lds\n"
"ds_read_b128 a[116:119], v8 offset:13312\n"
"v_exp_f32 v32, v32\n"
"v_mfma_f32_32x32x16_bf16 v[48:63], a[72:75], a[40:43], v[48:63]\n"
"ds_read_b128 a[120:123], v8 offset:14336\n"
"v_exp_f32 v33, v33\n"
"v_exp_f32 v34, v34\n"
"v_mfma_f32_32x32x16_bf16 v[48:63], a[76:79], a[44:47], v[48:63]\n"
"buffer_load_dwordx4 v1, s[20:23], s39 offen offset:3072 sc1 lds\n"
"ds_read_b128 a[124:127], v8 offset:15360\n"
"v_exp_f32 v35, v35\n"
"v_cndmask_b32 v32, 0, v32, s[64:65]\n"
"v_mfma_f32_32x32x16_bf16 v[48:63], a[80:83], a[48:51], v[48:63]\n"
"v_cndmask_b32 v33, 0, v33, s[66:67]\n"
"v_cvt_pk_bf16_f32 v96, v32, v33\n"
"v_add_f32 v12, v12, v32\n"
"v_add_f32 v13, v13, v33\n"
"s_add_u32 m0, s41, 36864\n"
"v_mfma_f32_32x32x16_bf16 v[48:63], a[84:87], a[52:55], v[48:63]\n"
"buffer_load_dwordx4 v1, s[20:23], s40 offen offset:0 sc1 lds\n"
"v_exp_f32 v36, v36\n"
"v_exp_f32 v37, v37\n"
"v_mfma_f32_32x32x16_bf16 v[48:63], a[88:91], a[56:59], v[48:63]\n"
"ds_read_b128 a[128:131], v8 offset:32768\n"
"v_cndmask_b32 v34, 0, v34, s[68:69]\n"
"v_cndmask_b32 v35, 0, v35, s[70:71]\n"
"v_cvt_pk_bf16_f32 v97, v34, v35\n"
"v_add_f32 v12, v12, v34\n"
"v_add_f32 v13, v13, v35\n"
"v_mfma_f32_32x32x16_bf16 v[48:63], a[92:95], a[60:63], v[48:63]\n"
"buffer_load_dwordx4 v1, s[20:23], s40 offen offset:1024 sc1 lds\n"
"v_exp_f32 v38, v38\n"
"s_waitcnt lgkmcnt(1)\n"
"v_mfma_f32_32x32x16_bf16 v[80:95], a[96:99], a[32:35], 0\n"
"ds_read_b128 a[132:135], v8 offset:33792\n"
"v_exp_f32 v39, v39\n"
"v_cndmask_b32 v36, 0, v36, s[72:73]\n"
"v_cndmask_b32 v37, 0, v37, s[74:75]\n"
"v_cvt_pk_bf16_f32 v98, v36, v37\n"
"v_mfma_f32_32x32x16_bf16 v[80:95], a[100:103], a[36:39], v[80:95]\n"
"buffer_load_dwordx4 v1, s[20:23], s40 offen offset:2048 sc1 lds\n"
"v_add_f32 v12, v12, v36\n"
"v_add_f32 v13, v13, v37\n"
"v_exp_f32 v40, v40\n"
"v_mfma_f32_32x32x16_bf16 v[80:95], a[104:107], a[40:43], v[80:95]\n"
"ds_read_b128 a[136:139], v8 offset:34816\n"
"v_exp_f32 v41, v41\n"
"v_cndmask_b32 v38, 0, v38, s[76:77]\n"
"v_cndmask_b32 v39, 0, v39, s[78:79]\n"
"v_cvt_pk_bf16_f32 v99, v38, v39\n"
"v_mfma_f32_32x32x16_bf16 v[80:95], a[108:111], a[44:47], v[80:95]\n"
"buffer_load_dwordx4 v1, s[20:23], s40 offen offset:3072 sc1 lds\n"
"v_add_f32 v12, v12, v38\n"
"v_add_f32 v13, v13, v39\n"
"v_exp_f32 v42, v42\n"
"v_mfma_f32_32x32x16_bf16 v[80:95], a[112:115], a[48:51], v[80:95]\n"
"ds_read_b128 a[140:143], v8 offset:35840\n"
"v_exp_f32 v43, v43\n"
"v_cndmask_b32 v40, 0, v40, s[80:81]\n"
"v_cndmask_b32 v41, 0, v41, s[82:83]\n"
"v_cvt_pk_bf16_f32 v100, v40, v41\n"
"v_mfma_f32_32x32x16_bf16 v[80:95], a[116:119], a[52:55], v[80:95]\n"
"v_add_f32 v12, v12, v40\n"
"v_add_f32 v13, v13, v41\n"
"v_exp_f32 v44, v44\n"
"v_exp_f32 v45, v45\n"
"v_mfma_f32_32x32x16_bf16 v[80:95], a[120:123], a[56:59], v[80:95]\n"
"ds_read_b128 a[144:147], v8 offset:36864\n"
"v_cndmask_b32 v42, 0, v42, s[84:85]\n"
"v_cndmask_b32 v43, 0, v43, s[86:87]\n"
"v_cvt_pk_bf16_f32 v101, v42, v43\n"
"v_add_f32 v12, v12, v42\n"
"v_add_f32 v13, v13, v43\n"
"v_mfma_f32_32x32x16_bf16 v[80:95], a[124:127], a[60:63], v[80:95]\n"
"v_exp_f32 v46, v46\n"
"v_exp_f32 v47, v47\n"
"v_cndmask_b32 v44, 0, v44, s[88:89]\n"
"v_cndmask_b32 v45, 0, v45, s[90:91]\n"
"ds_read_b128 a[148:151], v8 offset:37888\n"
"v_cvt_pk_bf16_f32 v102, v44, v45\n"
"v_add_f32 v12, v12, v44\n"
"v_add_f32 v13, v13, v45\n"
"v_exp_f32 v48, v48\n"
"v_exp_f32 v49, v49\n"
"v_cndmask_b32 v46, 0, v46, s[92:93]\n"
"v_cndmask_b32 v47, 0, v47, s[94:95]\n"
"v_cvt_pk_bf16_f32 v103, v46, v47\n"
"v_add_f32 v12, v12, v46\n"
"v_add_f32 v13, v13, v47\n"
"v_exp_f32 v50, v50\n"
"v_exp_f32 v51, v51\n"
"v_cvt_pk_bf16_f32 v112, v48, v49\n"
"v_add_f32 v14, v14, v48\n"
"v_add_f32 v15, v15, v49\n"
"v_exp_f32 v52, v52\n"
"v_exp_f32 v53, v53\n"
"v_cvt_pk_bf16_f32 v113, v50, v51\n"
"v_add_f32 v14, v14, v50\n"
"v_add_f32 v15, v15, v51\n"
"v_exp_f32 v54, v54\n"
"v_exp_f32 v55, v55\n"
"v_cvt_pk_bf16_f32 v114, v52, v53\n"
"v_add_f32 v14, v14, v52\n"
"v_add_f32 v15, v15, v53\n"
"v_exp_f32 v56, v56\n"
"v_exp_f32 v57, v57\n"
"v_cvt_pk_bf16_f32 v115, v54, v55\n"
"v_add_f32 v14, v14, v54\n"
"v_add_f32 v15, v15, v55\n"
"v_exp_f32 v58, v58\n"
"v_exp_f32 v59, v59\n"
"v_cvt_pk_bf16_f32 v116, v56, v57\n"
"v_add_f32 v14, v14, v56\n"
"v_add_f32 v15, v15, v57\n"
"v_exp_f32 v60, v60\n"
"v_exp_f32 v61, v61\n"
"v_cvt_pk_bf16_f32 v117, v58, v59\n"
"v_add_f32 v14, v14, v58\n"
"v_add_f32 v15, v15, v59\n"
"v_exp_f32 v62, v62\n"
"v_exp_f32 v63, v63\n"
"v_cvt_pk_bf16_f32 v118, v60, v61\n"
"v_add_f32 v14, v14, v60\n"
"v_add_f32 v15, v15, v61\n"
"ds_read_b128 a[152:155], v8 offset:38912\n"
"v_exp_f32 v80, v80\n"
"v_exp_f32 v81, v81\n"
"v_cvt_pk_bf16_f32 v119, v62, v63\n"
"v_add_f32 v14, v14, v62\n"
"v_add_f32 v15, v15, v63\n"
"v_exp_f32 v82, v82\n"
"v_exp_f32 v83, v83\n"
"v_cndmask_b32 v80, 0, v80, s[64:65]\n"
"v_cndmask_b32 v81, 0, v81, s[66:67]\n"
"v_cvt_pk_bf16_f32 v120, v80, v81\n"
"v_add_f32 v14, v14, v80\n"
"v_add_f32 v15, v15, v81\n"
"v_exp_f32 v84, v84\n"
"v_exp_f32 v85, v85\n"
"v_cndmask_b32 v82, 0, v82, s[68:69]\n"
"v_cndmask_b32 v83, 0, v83, s[70:71]\n"
"v_cvt_pk_bf16_f32 v121, v82, v83\n"
"v_add_f32 v14, v14, v82\n"
"v_add_f32 v15, v15, v83\n"
"v_exp_f32 v86, v86\n"
"v_exp_f32 v87, v87\n"
"v_cndmask_b32 v84, 0, v84, s[72:73]\n"
"v_cndmask_b32 v85, 0, v85, s[74:75]\n"
"v_cvt_pk_bf16_f32 v122, v84, v85\n"
"v_add_f32 v14, v14, v84\n"
"v_add_f32 v15, v15, v85\n"
"v_exp_f32 v88, v88\n"
"v_exp_f32 v89, v89\n"
"v_cndmask_b32 v86, 0, v86, s[76:77]\n"
"v_cndmask_b32 v87, 0, v87, s[78:79]\n"
"v_cvt_pk_bf16_f32 v123, v86, v87\n"
"v_add_f32 v14, v14, v86\n"
"v_add_f32 v15, v15, v87\n"
"v_exp_f32 v90, v90\n"
"v_exp_f32 v91, v91\n"
"v_cndmask_b32 v88, 0, v88, s[80:81]\n"
"v_cndmask_b32 v89, 0, v89, s[82:83]\n"
"v_cvt_pk_bf16_f32 v124, v88, v89\n"
"v_add_f32 v14, v14, v88\n"
"v_add_f32 v15, v15, v89\n"
"v_exp_f32 v92, v92\n"
"v_exp_f32 v93, v93\n"
"v_cndmask_b32 v90, 0, v90, s[84:85]\n"
"v_cndmask_b32 v91, 0, v91, s[86:87]\n"
"v_cvt_pk_bf16_f32 v125, v90, v91\n"
"v_add_f32 v14, v14, v90\n"
"v_add_f32 v15, v15, v91\n"
"v_exp_f32 v94, v94\n"
"v_exp_f32 v95, v95\n"
"v_cndmask_b32 v92, 0, v92, s[88:89]\n"
"v_cndmask_b32 v93, 0, v93, s[90:91]\n"
"v_cvt_pk_bf16_f32 v126, v92, v93\n"
"v_add_f32 v14, v14, v92\n"
"v_add_f32 v15, v15, v93\n"
"v_cndmask_b32 v94, 0, v94, s[92:93]\n"
"v_cndmask_b32 v95, 0, v95, s[94:95]\n"
"v_cvt_pk_bf16_f32 v127, v94, v95\n"
"v_add_f32 v14, v14, v94\n"
"v_add_f32 v15, v15, v95\n"
"ds_read_b128 a[156:159], v8 offset:39936\n"
"ds_read_b128 a[160:163], v8 offset:40960\n"
"s_waitcnt lgkmcnt(5)\n"
"v_mfma_f32_32x32x16_bf16 v[128:143], a[128:131], v[96:99], v[128:143]\n"
"ds_read_b128 a[164:167], v8 offset:41984\n"
"v_mfma_f32_32x32x16_bf16 v[160:175], a[132:135], v[96:99], v[160:175]\n"
"v_mfma_f32_32x32x16_bf16 v[192:207], a[136:139], v[96:99], v[192:207]\n"
"ds_read_b128 a[168:171], v8 offset:43008\n"
"v_mfma_f32_32x32x16_bf16 v[224:239], a[140:143], v[96:99], v[224:239]\n"
"v_mfma_f32_32x32x16_bf16 v[144:159], a[128:131], v[112:115], v[144:159]\n"
"ds_read_b128 a[172:175], v8 offset:44032\n"
"v_mfma_f32_32x32x16_bf16 v[176:191], a[132:135], v[112:115], v[176:191]\n"
"v_mfma_f32_32x32x16_bf16 v[208:223], a[136:139], v[112:115], v[208:223]\n"
"ds_read_b128 a[176:179], v8 offset:45056\n"
"v_mfma_f32_32x32x16_bf16 v[240:255], a[140:143], v[112:115], v[240:255]\n"
"s_waitcnt lgkmcnt(5)\n"
"v_mfma_f32_32x32x16_bf16 v[128:143], a[144:147], v[100:103], v[128:143]\n"
"ds_read_b128 a[180:183], v8 offset:46080\n"
"v_mfma_f32_32x32x16_bf16 v[160:175], a[148:151], v[100:103], v[160:175]\n"
"v_mfma_f32_32x32x16_bf16 v[192:207], a[152:155], v[100:103], v[192:207]\n"
"ds_read_b128 a[184:187], v8 offset:47104\n"
"v_mfma_f32_32x32x16_bf16 v[224:239], a[156:159], v[100:103], v[224:239]\n"
"v_mfma_f32_32x32x16_bf16 v[144:159], a[144:147], v[116:119], v[144:159]\n"
"ds_read_b128 a[188:191], v8 offset:48128\n"
"v_mfma_f32_32x32x16_bf16 v[176:191], a[148:151], v[116:119], v[176:191]\n"
"v_mfma_f32_32x32x16_bf16 v[208:223], a[152:155], v[116:119], v[208:223]\n"
"s_add_u32 s42, s43, 16384\n"
"v_add_u32 v8, s42, v1\n"
"v_mfma_f32_32x32x16_bf16 v[240:255], a[156:159], v[116:119], v[240:255]\n"
"s_waitcnt lgkmcnt(4)\n"
"s_waitcnt lgkmcnt(0)\n"
"v_mfma_f32_32x32x16_bf16 v[144:159], a[160:163], v[120:123], v[144:159]\n"
"ds_read_b128 a[64:67], v8 offset:0\n"
"v_mfma_f32_32x32x16_bf16 v[176:191], a[164:167], v[120:123], v[176:191]\n"
"ds_read_b128 a[68:71], v8 offset:1024\n"
"v_mfma_f32_32x32x16_bf16 v[208:223], a[168:171], v[120:123], v[208:223]\n"
"ds_read_b128 a[72:75], v8 offset:2048\n"
"v_mfma_f32_32x32x16_bf16 v[240:255], a[172:175], v[120:123], v[240:255]\n"
"ds_read_b128 a[76:79], v8 offset:3072\n"
"ds_read_b128 a[80:83], v8 offset:4096\n"
"ds_read_b128 a[84:87], v8 offset:5120\n"
"ds_read_b128 a[88:91], v8 offset:6144\n"
"ds_read_b128 a[92:95], v8 offset:7168\n"
"v_mfma_f32_32x32x16_bf16 v[144:159], a[176:179], v[124:127], v[144:159]\n"
"v_mfma_f32_32x32x16_bf16 v[176:191], a[180:183], v[124:127], v[176:191]\n"
"v_mfma_f32_32x32x16_bf16 v[208:223], a[184:187], v[124:127], v[208:223]\n"
"v_mfma_f32_32x32x16_bf16 v[240:255], a[188:191], v[124:127], v[240:255]\n"
"s_mov_b32 s43, s42\n"
"s_lshl_b32 s34, s29, 14\n"
"s_add_u32 s34, s34, 131072\n"
"v_add_u32 v16, s34, v1\n"
"ds_read_b128 a[0:3], v16 offset:0\n"
"ds_read_b128 a[4:7], v16 offset:1024\n"
"ds_read_b128 a[8:11], v16 offset:2048\n"
"ds_read_b128 a[12:15], v16 offset:3072\n"
"ds_read_b128 a[16:19], v16 offset:4096\n"
"ds_read_b128 a[20:23], v16 offset:5120\n"
"ds_read_b128 a[24:27], v16 offset:6144\n"
"ds_read_b128 a[28:31], v16 offset:7168\n"
"ds_read_b128 a[32:35], v16 offset:8192\n"
"ds_read_b128 a[36:39], v16 offset:9216\n"
"ds_read_b128 a[40:43], v16 offset:10240\n"
"ds_read_b128 a[44:47], v16 offset:11264\n"
"ds_read_b128 a[48:51], v16 offset:12288\n"
"ds_read_b128 a[52:55], v16 offset:13312\n"
"ds_read_b128 a[56:59], v16 offset:14336\n"
"ds_read_b128 a[60:63], v16 offset:15360\n"
"v_add_f32 v2, v12, v13\n"
"v_mov_b32 v3, v2\n"
"s_nop 1\n"
"v_permlane32_swap_b32 v2, v3\n"
"v_add_f32 v2, v2, v3\n"
"v_cmp_le_f32 vcc, 0x2b800000, v2\n"
"s_mov_b32 s36, 0x53800000\n"
"v_cmp_ge_f32 s[34:35], s36, v2\n"
"s_nop 0\n"
"s_and_b64 vcc, vcc, s[34:35]\n"
"s_cmp_eq_u64 vcc, -1\n"
"s_cselect_b32 s36, 0, 1\n"
"s_or_b32 s60, s60, s36\n"
"v_rcp_f32 v2, v2\n"
"v_add_f32 v4, v14, v15\n"
"v_mov_b32 v5, v4\n"
"s_nop 1\n"
"v_permlane32_swap_b32 v4, v5\n"
"v_add_f32 v4, v4, v5\n"
"v_cmp_le_f32 vcc, 0x2b800000, v4\n"
"s_mov_b32 s36, 0x53800000\n"
"v_cmp_ge_f32 s[34:35], s36, v4\n"
"s_nop 0\n"
"s_and_b64 vcc, vcc, s[34:35]\n"
"s_cmp_eq_u64 vcc, -1\n"
"s_cselect_b32 s36, 0, 1\n"
"s_or_b32 s60, s60, s36\n"
"v_rcp_f32 v4, v4\n"
"v_mov_b32 v96, 0\n"
"v_mov_b32 v97, 0\n"
"v_mov_b32 v98, 0\n"
"v_mov_b32 v99, 0\n"
"v_mov_b32 v12, 0\n"
"v_mov_b32 v13, 0\n"
"v_mov_b32 v14, 0\n"
"v_mov_b32 v15, 0\n"
"v_pk_mul_f32 v[128:129], v[128:129], v[2:3] op_sel_hi:[1,0]\n"
"v_pk_mul_f32 v[130:131], v[130:131], v[2:3] op_sel_hi:[1,0]\n"
"v_pk_mul_f32 v[132:133], v[132:133], v[2:3] op_sel_hi:[1,0]\n"
"v_pk_mul_f32 v[134:135], v[134:135], v[2:3] op_sel_hi:[1,0]\n"
"v_cvt_pk_bf16_f32 v16, v128, v129\n"
"v_cvt_pk_bf16_f32 v17, v130, v131\n"
"v_cvt_pk_bf16_f32 v18, v132, v133\n"
"v_cvt_pk_bf16_f32 v19, v134, v135\n"
"s_add_u32 s37, s58, 0\n"
"buffer_store_dwordx4 v[16:19], v1, s[24:27], s37 offen offset:0 sc1\n"
"v_pk_mul_f32 v[136:137], v[136:137], v[2:3] op_sel_hi:[1,0]\n"
"v_pk_mul_f32 v[138:139], v[138:139], v[2:3] op_sel_hi:[1,0]\n"
"v_pk_mul_f32 v[140:141], v[140:141], v[2:3] op_sel_hi:[1,0]\n"
"v_pk_mul_f32 v[142:143], v[142:143], v[2:3] op_sel_hi:[1,0]\n"
"v_cvt_pk_bf16_f32 v20, v136, v137\n"
"v_cvt_pk_bf16_f32 v21, v138, v139\n"
"v_cvt_pk_bf16_f32 v22, v140, v141\n"
"v_cvt_pk_bf16_f32 v23, v142, v143\n"
"buffer_store_dwordx4 v[20:23], v1, s[24:27], s37 offen offset:1024 sc1\n"
"v_mfma_f32_32x32x16_bf16 v[128:143], v[96:99], v[96:99], 0\n"
"v_pk_mul_f32 v[160:161], v[160:161], v[2:3] op_sel_hi:[1,0]\n"
"v_pk_mul_f32 v[162:163], v[162:163], v[2:3] op_sel_hi:[1,0]\n"
"v_pk_mul_f32 v[164:165], v[164:165], v[2:3] op_sel_hi:[1,0]\n"
"v_pk_mul_f32 v[166:167], v[166:167], v[2:3] op_sel_hi:[1,0]\n"
"v_cvt_pk_bf16_f32 v24, v160, v161\n"
"v_cvt_pk_bf16_f32 v25, v162, v163\n"
"v_cvt_pk_bf16_f32 v26, v164, v165\n"
"v_cvt_pk_bf16_f32 v27, v166, v167\n"
"buffer_store_dwordx4 v[24:27], v1, s[24:27], s37 offen offset:2048 sc1\n"
"v_pk_mul_f32 v[168:169], v[168:169], v[2:3] op_sel_hi:[1,0]\n"
"v_pk_mul_f32 v[170:171], v[170:171], v[2:3] op_sel_hi:[1,0]\n"
"v_pk_mul_f32 v[172:173], v[172:173], v[2:3] op_sel_hi:[1,0]\n"
"v_pk_mul_f32 v[174:175], v[174:175], v[2:3] op_sel_hi:[1,0]\n"
"v_cvt_pk_bf16_f32 v28, v168, v169\n"
"v_cvt_pk_bf16_f32 v29, v170, v171\n"
"v_cvt_pk_bf16_f32 v30, v172, v173\n"
"v_cvt_pk_bf16_f32 v31, v174, v175\n"
"buffer_store_dwordx4 v[28:31], v1, s[24:27], s37 offen offset:3072 sc1\n"
"v_mfma_f32_32x32x16_bf16 v[160:175], v[96:99], v[96:99], 0\n"
"v_pk_mul_f32 v[192:193], v[192:193], v[2:3] op_sel_hi:[1,0]\n"
"v_pk_mul_f32 v[194:195], v[194:195], v[2:3] op_sel_hi:[1,0]\n"
"v_pk_mul_f32 v[196:197], v[196:197], v[2:3] op_sel_hi:[1,0]\n"
"v_pk_mul_f32 v[198:199], v[198:199], v[2:3] op_sel_hi:[1,0]\n"
"v_cvt_pk_bf16_f32 v16, v192, v193\n"
"v_cvt_pk_bf16_f32 v17, v194, v195\n"
"v_cvt_pk_bf16_f32 v18, v196, v197\n"
"v_cvt_pk_bf16_f32 v19, v198, v199\n"
"s_add_u32 s37, s58, 4096\n"
"buffer_store_dwordx4 v[16:19], v1, s[24:27], s37 offen offset:0 sc1\n"
"v_pk_mul_f32 v[200:201], v[200:201], v[2:3] op_sel_hi:[1,0]\n"
"v_pk_mul_f32 v[202:203], v[202:203], v[2:3] op_sel_hi:[1,0]\n"
"v_pk_mul_f32 v[204:205], v[204:205], v[2:3] op_sel_hi:[1,0]\n"
"v_pk_mul_f32 v[206:207], v[206:207], v[2:3] op_sel_hi:[1,0]\n"
"v_cvt_pk_bf16_f32 v20, v200, v201\n"
"v_cvt_pk_bf16_f32 v21, v202, v203\n"
"v_cvt_pk_bf16_f32 v22, v204, v205\n"
"v_cvt_pk_bf16_f32 v23, v206, v207\n"
"buffer_store_dwordx4 v[20:23], v1, s[24:27], s37 offen offset:1024 sc1\n"
"v_mfma_f32_32x32x16_bf16 v[192:207], v[96:99], v[96:99], 0\n"
"v_pk_mul_f32 v[224:225], v[224:225], v[2:3] op_sel_hi:[1,0]\n"
"v_pk_mul_f32 v[226:227], v[226:227], v[2:3] op_sel_hi:[1,0]\n"
"v_pk_mul_f32 v[228:229], v[228:229], v[2:3] op_sel_hi:[1,0]\n"
"v_pk_mul_f32 v[230:231], v[230:231], v[2:3] op_sel_hi:[1,0]\n"
"v_cvt_pk_bf16_f32 v24, v224, v225\n"
"v_cvt_pk_bf16_f32 v25, v226, v227\n"
"v_cvt_pk_bf16_f32 v26, v228, v229\n"
"v_cvt_pk_bf16_f32 v27, v230, v231\n"
"buffer_store_dwordx4 v[24:27], v1, s[24:27], s37 offen offset:2048 sc1\n"
"v_pk_mul_f32 v[232:233], v[232:233], v[2:3] op_sel_hi:[1,0]\n"
"v_pk_mul_f32 v[234:235], v[234:235], v[2:3] op_sel_hi:[1,0]\n"
"v_pk_mul_f32 v[236:237], v[236:237], v[2:3] op_sel_hi:[1,0]\n"
"v_pk_mul_f32 v[238:239], v[238:239], v[2:3] op_sel_hi:[1,0]\n"
"v_cvt_pk_bf16_f32 v28, v232, v233\n"
"v_cvt_pk_bf16_f32 v29, v234, v235\n"
"v_cvt_pk_bf16_f32 v30, v236, v237\n"
"v_cvt_pk_bf16_f32 v31, v238, v239\n"
"buffer_store_dwordx4 v[28:31], v1, s[24:27], s37 offen offset:3072 sc1\n"
"v_mfma_f32_32x32x16_bf16 v[224:239], v[96:99], v[96:99], 0\n"
"v_pk_mul_f32 v[144:145], v[144:145], v[4:5] op_sel_hi:[1,0]\n"
"v_pk_mul_f32 v[146:147], v[146:147], v[4:5] op_sel_hi:[1,0]\n"
"v_pk_mul_f32 v[148:149], v[148:149], v[4:5] op_sel_hi:[1,0]\n"
"v_pk_mul_f32 v[150:151], v[150:151], v[4:5] op_sel_hi:[1,0]\n"
"v_cvt_pk_bf16_f32 v16, v144, v145\n"
"v_cvt_pk_bf16_f32 v17, v146, v147\n"
"v_cvt_pk_bf16_f32 v18, v148, v149\n"
"v_cvt_pk_bf16_f32 v19, v150, v151\n"
"s_add_u32 s37, s58, 262144\n"
"buffer_store_dwordx4 v[16:19], v1, s[24:27], s37 offen offset:0 sc1\n"
"v_pk_mul_f32 v[152:153], v[152:153], v[4:5] op_sel_hi:[1,0]\n"
"v_pk_mul_f32 v[154:155], v[154:155], v[4:5] op_sel_hi:[1,0]\n"
"v_pk_mul_f32 v[156:157], v[156:157], v[4:5] op_sel_hi:[1,0]\n"
"v_pk_mul_f32 v[158:159], v[158:159], v[4:5] op_sel_hi:[1,0]\n"
"v_cvt_pk_bf16_f32 v20, v152, v153\n"
"v_cvt_pk_bf16_f32 v21, v154, v155\n"
"v_cvt_pk_bf16_f32 v22, v156, v157\n"
"v_cvt_pk_bf16_f32 v23, v158, v159\n"
"buffer_store_dwordx4 v[20:23], v1, s[24:27], s37 offen offset:1024 sc1\n"
"v_mfma_f32_32x32x16_bf16 v[144:159], v[96:99], v[96:99], 0\n"
"v_pk_mul_f32 v[176:177], v[176:177], v[4:5] op_sel_hi:[1,0]\n"
"v_pk_mul_f32 v[178:179], v[178:179], v[4:5] op_sel_hi:[1,0]\n"
"v_pk_mul_f32 v[180:181], v[180:181], v[4:5] op_sel_hi:[1,0]\n"
"v_pk_mul_f32 v[182:183], v[182:183], v[4:5] op_sel_hi:[1,0]\n"
"v_cvt_pk_bf16_f32 v24, v176, v177\n"
"v_cvt_pk_bf16_f32 v25, v178, v179\n"
"v_cvt_pk_bf16_f32 v26, v180, v181\n"
"v_cvt_pk_bf16_f32 v27, v182, v183\n"
"buffer_store_dwordx4 v[24:27], v1, s[24:27], s37 offen offset:2048 sc1\n"
"v_pk_mul_f32 v[184:185], v[184:185], v[4:5] op_sel_hi:[1,0]\n"
"v_pk_mul_f32 v[186:187], v[186:187], v[4:5] op_sel_hi:[1,0]\n"
"v_pk_mul_f32 v[188:189], v[188:189], v[4:5] op_sel_hi:[1,0]\n"
"v_pk_mul_f32 v[190:191], v[190:191], v[4:5] op_sel_hi:[1,0]\n"
"v_cvt_pk_bf16_f32 v28, v184, v185\n"
"v_cvt_pk_bf16_f32 v29, v186, v187\n"
"v_cvt_pk_bf16_f32 v30, v188, v189\n"
"v_cvt_pk_bf16_f32 v31, v190, v191\n"
"buffer_store_dwordx4 v[28:31], v1, s[24:27], s37 offen offset:3072 sc1\n"
"v_mfma_f32_32x32x16_bf16 v[176:191], v[96:99], v[96:99], 0\n"
"v_pk_mul_f32 v[208:209], v[208:209], v[4:5] op_sel_hi:[1,0]\n"
"v_pk_mul_f32 v[210:211], v[210:211], v[4:5] op_sel_hi:[1,0]\n"
"v_pk_mul_f32 v[212:213], v[212:213], v[4:5] op_sel_hi:[1,0]\n"
"v_pk_mul_f32 v[214:215], v[214:215], v[4:5] op_sel_hi:[1,0]\n"
"v_cvt_pk_bf16_f32 v16, v208, v209\n"
"v_cvt_pk_bf16_f32 v17, v210, v211\n"
"v_cvt_pk_bf16_f32 v18, v212, v213\n"
"v_cvt_pk_bf16_f32 v19, v214, v215\n"
"s_add_u32 s37, s58, 266240\n"
"buffer_store_dwordx4 v[16:19], v1, s[24:27], s37 offen offset:0 sc1\n"
"v_pk_mul_f32 v[216:217], v[216:217], v[4:5] op_sel_hi:[1,0]\n"
"v_pk_mul_f32 v[218:219], v[218:219], v[4:5] op_sel_hi:[1,0]\n"
"v_pk_mul_f32 v[220:221], v[220:221], v[4:5] op_sel_hi:[1,0]\n"
"v_pk_mul_f32 v[222:223], v[222:223], v[4:5] op_sel_hi:[1,0]\n"
"v_cvt_pk_bf16_f32 v20, v216, v217\n"
"v_cvt_pk_bf16_f32 v21, v218, v219\n"
"v_cvt_pk_bf16_f32 v22, v220, v221\n"
"v_cvt_pk_bf16_f32 v23, v222, v223\n"
"buffer_store_dwordx4 v[20:23], v1, s[24:27], s37 offen offset:1024 sc1\n"
"v_mfma_f32_32x32x16_bf16 v[208:223], v[96:99], v[96:99], 0\n"
"v_pk_mul_f32 v[240:241], v[240:241], v[4:5] op_sel_hi:[1,0]\n"
"v_pk_mul_f32 v[242:243], v[242:243], v[4:5] op_sel_hi:[1,0]\n"
"v_pk_mul_f32 v[244:245], v[244:245], v[4:5] op_sel_hi:[1,0]\n"
"v_pk_mul_f32 v[246:247], v[246:247], v[4:5] op_sel_hi:[1,0]\n"
"v_cvt_pk_bf16_f32 v24, v240, v241\n"
"v_cvt_pk_bf16_f32 v25, v242, v243\n"
"v_cvt_pk_bf16_f32 v26, v244, v245\n"
"v_cvt_pk_bf16_f32 v27, v246, v247\n"
"buffer_store_dwordx4 v[24:27], v1, s[24:27], s37 offen offset:2048 sc1\n"
"v_pk_mul_f32 v[248:249], v[248:249], v[4:5] op_sel_hi:[1,0]\n"
"v_pk_mul_f32 v[250:251], v[250:251], v[4:5] op_sel_hi:[1,0]\n"
"v_pk_mul_f32 v[252:253], v[252:253], v[4:5] op_sel_hi:[1,0]\n"
"v_pk_mul_f32 v[254:255], v[254:255], v[4:5] op_sel_hi:[1,0]\n"
"v_cvt_pk_bf16_f32 v28, v248, v249\n"
"v_cvt_pk_bf16_f32 v29, v250, v251\n"
"v_cvt_pk_bf16_f32 v30, v252, v253\n"
"v_cvt_pk_bf16_f32 v31, v254, v255\n"
"buffer_store_dwordx4 v[28:31], v1, s[24:27], s37 offen offset:3072 sc1\n"
"v_mfma_f32_32x32x16_bf16 v[240:255], v[96:99], v[96:99], 0\n"
"s_waitcnt lgkmcnt(0)\n"
"s_bfe_u32 s34, s28, 0x20001\n"
"s_lshl_b32 s34, s34, 2\n"
"s_bfe_u32 s35, s28, 0x20003\n"
"s_add_u32 s34, s34, s35\n"
"s_lshl_b32 s34, s34, 8\n"
"s_add_u32 s34, s34, 37120\n"
"v_lshlrev_b32 v4, 2, v0\n"
"v_add_u32 v4, s34, v4\n"
"s_and_b32 s34, s28, 7\n"
"s_lshl_b32 s34, s34, 8\n"
"s_add_u32 s34, s34, 41216\n"
"v_subrev_u32 v5, 16, v0\n"
"v_min_u32 v5, 42, v5\n"
"v_lshlrev_b32 v5, 2, v5\n"
"v_add_u32 v5, s34, v5\n"
"v_cmp_le_u32 vcc, 16, v0\n"
"s_nop 1\n"
"v_cndmask_b32 v4, v4, v5, vcc\n"
"global_load_dword v5, v4, s[50:51] sc1\n"
"s_load_dwordx4 s[12:15], s[8:9], 72\n"
"v_lshlrev_b32 v2, 7, v0\n"
"v_cmp_gt_u32 vcc, 43, v0\n"
"v_mov_b32 v3, 0x40000000\n"
"s_waitcnt lgkmcnt(0)\n"
"v_cndmask_b32 v3, v3, v2, vcc\n"
"s_mov_b64 s[46:47], s[14:15]\n"
"s_and_b32 s13, s13, 0xffff\n"
"s_mov_b32 s14, 4194304\n"
"s_mov_b32 s15, 0x20000\n"
"s_lshl_b32 s34, s28, 14\n"
"buffer_load_dword v16, v2, s[12:15], s34 offen\n"
"s_add_u32 s34, s34, 8192\n"
"buffer_load_dword v17, v2, s[12:15], s34 offen\n"
"s_nop 4\n"
"s_mov_b32 s12, s46\n"
"s_and_b32 s13, s47, 0xffff\n"
"s_mov_b32 s14, 1409024\n"
"s_mul_i32 s34, s28, 5504\n"
"buffer_load_dword v18, v3, s[12:15], s34 offen\n"
"s_xor_b32 s42, s43, 0x10000\n"
"s_sub_u32 s42, s42, 16384\n"
"s_add_u32 s42, s42, s55\n"
"v_add_u32 v8, s42, v1\n"
"s_lshl_b32 s39, s52, 15\n"
"s_add_u32 s39, s39, s38\n"
"s_cmp_lt_u32 s52, s53\n"
"s_cselect_b32 s39, s39, 0x40000000\n"
"s_add_u32 s40, s39, 4096\n"
"s_and_b32 s41, s52, 1\n"
"s_lshl_b32 s41, s41, 16\n"
"s_add_u32 s41, s41, s38\n"
"s_add_u32 s52, s52, 1\n"
"s_waitcnt vmcnt(3) lgkmcnt(0)\n"
"s_cmp_lg_u32 s29, 0\n"
"s_cbranch_scc1 .Lwok14\n"
"v_cmp_ne_u32 vcc, s11, v5\n"
"s_nop 1\n"
"s_cbranch_vccz .Lwok14\n"
"s_mov_b32 s49, 0\n"
".Lwait14:\n"
"s_sleep 2\n"
"global_load_dword v5, v4, s[50:51] sc1\n"
"s_waitcnt vmcnt(0)\n"
"v_cmp_ne_u32 vcc, s11, v5\n"
"s_nop 1\n"
"s_cbranch_vccz .Lwok14\n"
"s_add_u32 s49, s49, 1\n"
"s_cmp_lt_u32 s49, 32768\n"
"s_cbranch_scc1 .Lwait14\n"
".Lwok14:\n"
"s_barrier\n"
"ds_read_b128 a[64:67], v8 offset:0\n"
"s_add_u32 m0, s41, 0\n"
"s_nop 0\n"
"ds_read_b128 a[68:71], v8 offset:1024\n"
"buffer_load_dwordx4 v1, s[16:19], s39 offen offset:0 sc1 lds\n"
"ds_read_b128 a[72:75], v8 offset:2048\n"
"ds_read_b128 a[76:79], v8 offset:3072\n"
"buffer_load_dwordx4 v1, s[16:19], s39 offen offset:1024 sc1 lds\n"
"ds_read_b128 a[80:83], v8 offset:4096\n"
"ds_read_b128 a[84:87], v8 offset:5120\n"
"buffer_load_dwordx4 v1, s[16:19], s39 offen offset:2048 sc1 lds\n"
"ds_read_b128 a[88:91], v8 offset:6144\n"
"ds_read_b128 a[92:95], v8 offset:7168\n"
"buffer_load_dwordx4 v1, s[16:19], s39 offen offset:3072 sc1 lds\n"
"s_add_u32 m0, s41, 4096\n"
"s_nop 0\n"
"buffer_load_dwordx4 v1, s[16:19], s40 offen offset:0 sc1 lds\n"
"buffer_load_dwordx4 v1, s[16:19], s40 offen offset:1024 sc1 lds\n"
"s_mov_b32 s43, s42\n"
"s_branch .Lp2\n"
".Lp1_w1:\n"
"s_waitcnt lgkmcnt(0)\n"
"v_mfma_f32_32x32x16_bf16 v[32:47], a[64:67], a[0:3], 0\n"
"v_mfma_f32_32x32x16_bf16 v[32:47], a[68:71], a[4:7], v[32:47]\n"
"buffer_load_dwordx4 v1, s[16:19], s40 offen offset:2048 sc1 lds\n"
"v_mfma_f32_32x32x16_bf16 v[32:47], a[72:75], a[8:11], v[32:47]\n"
"v_mfma_f32_32x32x16_bf16 v[32:47], a[76:79], a[12:15], v[32:47]\n"
"buffer_load_dwordx4 v1, s[16:19], s40 offen offset:3072 sc1 lds\n"
"v_mfma_f32_32x32x16_bf16 v[32:47], a[80:83], a[16:19], v[32:47]\n"
"ds_read_b128 a[96:99], v8 offset:8192\n"
"s_add_u32 m0, s41, 32768\n"
"v_mfma_f32_32x32x16_bf16 v[32:47], a[84:87], a[20:23], v[32:47]\n"
"buffer_load_dwordx4 v1, s[20:23], s39 offen offset:0 sc1 lds\n"
"ds_read_b128 a[100:103], v8 offset:9216\n"
"v_mfma_f32_32x32x16_bf16 v[32:47], a[88:91], a[24:27], v[32:47]\n"
"ds_read_b128 a[104:107], v8 offset:10240\n"
"v_mfma_f32_32x32x16_bf16 v[32:47], a[92:95], a[28:31], v[32:47]\n"
"buffer_load_dwordx4 v1, s[20:23], s39 offen offset:1024 sc1 lds\n"
"ds_read_b128 a[108:111], v8 offset:11264\n"
"v_mfma_f32_32x32x16_bf16 v[48:63], a[64:67], a[32:35], 0\n"
"ds_read_b128 a[112:115], v8 offset:12288\n"
"v_mfma_f32_32x32x16_bf16 v[48:63], a[68:71], a[36:39], v[48:63]\n"
"buffer_load_dwordx4 v1, s[20:23], s39 offen offset:2048 sc1 lds\n"
"ds_read_b128 a[116:119], v8 offset:13312\n"
"v_exp_f32 v32, v32\n"
"v_mfma_f32_32x32x16_bf16 v[48:63], a[72:75], a[40:43], v[48:63]\n"
"ds_read_b128 a[120:123], v8 offset:14336\n"
"v_exp_f32 v33, v33\n"
"v_exp_f32 v34, v34\n"
"v_mfma_f32_32x32x16_bf16 v[48:63], a[76:79], a[44:47], v[48:63]\n"
"buffer_load_dwordx4 v1, s[20:23], s39 offen offset:3072 sc1 lds\n"
"ds_read_b128 a[124:127], v8 offset:15360\n"
"v_exp_f32 v35, v35\n"
"v_cvt_pk_bf16_f32 v96, v32, v33\n"
"v_mfma_f32_32x32x16_bf16 v[48:63], a[80:83], a[48:51], v[48:63]\n"
"v_add_f32 v12, v12, v32\n"
"v_add_f32 v13, v13, v33\n"
"v_exp_f32 v36, v36\n"
"s_add_u32 m0, s41, 36864\n"
"v_mfma_f32_32x32x16_bf16 v[48:63], a[84:87], a[52:55], v[48:63]\n"
"buffer_load_dwordx4 v1, s[20:23], s40 offen offset:0 sc1 lds\n"
"v_exp_f32 v37, v37\n"
"v_cvt_pk_bf16_f32 v97, v34, v35\n"
"v_add_f32 v12, v12, v34\n"
"v_mfma_f32_32x32x16_bf16 v[48:63], a[88:91], a[56:59], v[48:63]\n"
"ds_read_b128 a[128:131], v8 offset:32768\n"
"v_add_f32 v13, v13, v35\n"
"v_exp_f32 v38, v38\n"
"v_exp_f32 v39, v39\n"
"v_mfma_f32_32x32x16_bf16 v[48:63], a[92:95], a[60:63], v[48:63]\n"
"buffer_load_dwordx4 v1, s[20:23], s40 offen offset:1024 sc1 lds\n"
"v_cvt_pk_bf16_f32 v98, v36, v37\n"
"v_add_f32 v12, v12, v36\n"
"v_add_f32 v13, v13, v37\n"
"s_waitcnt lgkmcnt(1)\n"
"v_mfma_f32_32x32x16_bf16 v[64:79], a[96:99], a[0:3], 0\n"
"ds_read_b128 a[132:135], v8 offset:33792\n"
"v_exp_f32 v40, v40\n"
"v_exp_f32 v41, v41\n"
"v_cvt_pk_bf16_f32 v99, v38, v39\n"
"v_mfma_f32_32x32x16_bf16 v[64:79], a[100:103], a[4:7], v[64:79]\n"
"buffer_load_dwordx4 v1, s[20:23], s40 offen offset:2048 sc1 lds\n"
"v_add_f32 v12, v12, v38\n"
"v_add_f32 v13, v13, v39\n"
"v_exp_f32 v42, v42\n"
"v_mfma_f32_32x32x16_bf16 v[64:79], a[104:107], a[8:11], v[64:79]\n"
"ds_read_b128 a[136:139], v8 offset:34816\n"
"v_exp_f32 v43, v43\n"
"v_cvt_pk_bf16_f32 v100, v40, v41\n"
"v_add_f32 v12, v12, v40\n"
"v_add_f32 v13, v13, v41\n"
"v_mfma_f32_32x32x16_bf16 v[64:79], a[108:111], a[12:15], v[64:79]\n"
"buffer_load_dwordx4 v1, s[20:23], s40 offen offset:3072 sc1 lds\n"
"v_exp_f32 v44, v44\n"
"v_exp_f32 v45, v45\n"
"v_mfma_f32_32x32x16_bf16 v[64:79], a[112:115], a[16:19], v[64:79]\n"
"ds_read_b128 a[140:143], v8 offset:35840\n"
"v_cvt_pk_bf16_f32 v101, v42, v43\n"
"v_add_f32 v12, v12, v42\n"
"v_add_f32 v13, v13, v43\n"
"v_exp_f32 v46, v46\n"
"v_mfma_f32_32x32x16_bf16 v[64:79], a[116:119], a[20:23], v[64:79]\n"
"v_exp_f32 v47, v47\n"
"v_cvt_pk_bf16_f32 v102, v44, v45\n"
"v_add_f32 v12, v12, v44\n"
"v_add_f32 v13, v13, v45\n"
"v_mfma_f32_32x32x16_bf16 v[64:79], a[120:123], a[24:27], v[64:79]\n"
"ds_read_b128 a[144:147], v8 offset:36864\n"
"v_exp_f32 v48, v48\n"
"v_exp_f32 v49, v49\n"
"v_cvt_pk_bf16_f32 v103, v46, v47\n"
"v_mfma_f32_32x32x16_bf16 v[64:79], a[124:127], a[28:31], v[64:79]\n"
"v_add_f32 v12, v12, v46\n"
"v_add_f32 v13, v13, v47\n"
"v_exp_f32 v50, v50\n"
"v_exp_f32 v51, v51\n"
"v_mfma_f32_32x32x16_bf16 v[80:95], a[96:99], a[32:35], 0\n"
"ds_read_b128 a[148:151], v8 offset:37888\n"
"v_cvt_pk_bf16_f32 v112, v48, v49\n"
"v_add_f32 v14, v14, v48\n"
"v_add_f32 v15, v15, v49\n"
"v_exp_f32 v52, v52\n"
"v_mfma_f32_32x32x16_bf16 v[80:95], a[100:103], a[36:39], v[80:95]\n"
"v_exp_f32 v53, v53\n"
"v_cvt_pk_bf16_f32 v113, v50, v51\n"
"v_add_f32 v14, v14, v50\n"
"v_add_f32 v15, v15, v51\n"
"v_mfma_f32_32x32x16_bf16 v[80:95], a[104:107], a[40:43], v[80:95]\n"
"ds_read_b128 a[152:155], v8 offset:38912\n"
"v_exp_f32 v54, v54\n"
"v_exp_f32 v55, v55\n"
"v_cvt_pk_bf16_f32 v114, v52, v53\n"
"v_mfma_f32_32x32x16_bf16 v[80:95], a[108:111], a[44:47], v[80:95]\n"
"v_add_f32 v14, v14, v52\n"
"v_add_f32 v15, v15, v53\n"
"v_exp_f32 v56, v56\n"
"v_exp_f32 v57, v57\n"
"v_mfma_f32_32x32x16_bf16 v[80:95], a[112:115], a[48:51], v[80:95]\n"
"ds_read_b128 a[156:159], v8 offset:39936\n"
"v_cvt_pk_bf16_f32 v115, v54, v55\n"
"v_add_f32 v14, v14, v54\n"
"v_add_f32 v15, v15, v55\n"
"v_exp_f32 v58, v58\n"
"v_mfma_f32_32x32x16_bf16 v[80:95], a[116:119], a[52:55], v[80:95]\n"
"v_exp_f32 v59, v59\n"
"v_cvt_pk_bf16_f32 v116, v56, v57\n"
"v_add_f32 v14, v14, v56\n"
"v_add_f32 v15, v15, v57\n"
"v_mfma_f32_32x32x16_bf16 v[80:95], a[120:123], a[56:59], v[80:95]\n"
"ds_read_b128 a[160:163], v8 offset:40960\n"
"v_exp_f32 v60, v60\n"
"v_exp_f32 v61, v61\n"
"v_cvt_pk_bf16_f32 v117, v58, v59\n"
"v_mfma_f32_32x32x16_bf16 v[80:95], a[124:127], a[60:63], v[80:95]\n"
"v_add_f32 v14, v14, v58\n"
"v_add_f32 v15, v15, v59\n"
"v_exp_f32 v62, v62\n"
"s_waitcnt lgkmcnt(5)\n"
"v_mfma_f32_32x32x16_bf16 v[128:143], a[128:131], v[96:99], v[128:143]\n"
"ds_read_b128 a[164:167], v8 offset:41984\n"
"v_exp_f32 v63, v63\n"
"v_cvt_pk_bf16_f32 v118, v60, v61\n"
"v_add_f32 v14, v14, v60\n"
"v_add_f32 v15, v15, v61\n"
"v_mfma_f32_32x32x16_bf16 v[160:175], a[132:135], v[96:99], v[160:175]\n"
"v_exp_f32 v64, v64\n"
"v_exp_f32 v65, v65\n"
"v_cvt_pk_bf16_f32 v119, v62, v63\n"
"v_add_f32 v14, v14, v62\n"
"v_mfma_f32_32x32x16_bf16 v[192:207], a[136:139], v[96:99], v[192:207]\n"
"ds_read_b128 a[168:171], v8 offset:43008\n"
"v_add_f32 v15, v15, v63\n"
"v_exp_f32 v66, v66\n"
"v_exp_f32 v67, v67\n"
"v_mfma_f32_32x32x16_bf16 v[224:239], a[140:143], v[96:99], v[224:239]\n"
"v_cvt_pk_bf16_f32 v104, v64, v65\n"
"v_add_f32 v12, v12, v64\n"
"v_add_f32 v13, v13, v65\n"
"v_exp_f32 v68, v68\n"
"v_mfma_f32_32x32x16_bf16 v[144:159], a[128:131], v[112:115], v[144:159]\n"
"ds_read_b128 a[172:175], v8 offset:44032\n"
"v_exp_f32 v69, v69\n"
"v_cvt_pk_bf16_f32 v105, v66, v67\n"
"v_add_f32 v12, v12, v66\n"
"v_add_f32 v13, v13, v67\n"
"v_mfma_f32_32x32x16_bf16 v[176:191], a[132:135], v[112:115], v[176:191]\n"
"v_exp_f32 v70, v70\n"
"v_exp_f32 v71, v71\n"
"v_cvt_pk_bf16_f32 v106, v68, v69\n"
"v_add_f32 v12, v12, v68\n"
"v_mfma_f32_32x32x16_bf16 v[208:223], a[136:139], v[112:115], v[208:223]\n"
"ds_read_b128 a[176:179], v8 offset:45056\n"
"v_add_f32 v13, v13, v69\n"
"v_exp_f32 v80, v80\n"
"v_exp_f32 v81, v81\n"
"v_mfma_f32_32x32x16_bf16 v[240:255], a[140:143], v[112:115], v[240:255]\n"
"v_cvt_pk_bf16_f32 v107, v70, v71\n"
"v_add_f32 v12, v12, v70\n"
"v_add_f32 v13, v13, v71\n"
"v_exp_f32 v82, v82\n"
"s_waitcnt lgkmcnt(5)\n"
"v_mfma_f32_32x32x16_bf16 v[128:143], a[144:147], v[100:103], v[128:143]\n"
"ds_read_b128 a[180:183], v8 offset:46080\n"
"v_exp_f32 v83, v83\n"
"v_cvt_pk_bf16_f32 v120, v80, v81\n"
"v_add_f32 v14, v14, v80\n"
"v_add_f32 v15, v15, v81\n"
"v_mfma_f32_32x32x16_bf16 v[160:175], a[148:151], v[100:103], v[160:175]\n"
"v_exp_f32 v84, v84\n"
"v_exp_f32 v85, v85\n"
"v_cvt_pk_bf16_f32 v121, v82, v83\n"
"v_add_f32 v14, v14, v82\n"
"v_mfma_f32_32x32x16_bf16 v[192:207], a[152:155], v[100:103], v[192:207]\n"
"ds_read_b128 a[184:187], v8 offset:47104\n"
"v_add_f32 v15, v15, v83\n"
"v_exp_f32 v86, v86\n"
"v_exp_f32 v87, v87\n"
"v_mfma_f32_32x32x16_bf16 v[224:239], a[156:159], v[100:103], v[224:239]\n"
"v_cvt_pk_bf16_f32 v122, v84, v85\n"
"v_add_f32 v14, v14, v84\n"
"v_add_f32 v15, v15, v85\n"
"v_exp_f32 v72, v72\n"
"v_mfma_f32_32x32x16_bf16 v[144:159], a[144:147], v[116:119], v[144:159]\n"
"ds_read_b128 a[188:191], v8 offset:48128\n"
"v_exp_f32 v73, v73\n"
"v_cvt_pk_bf16_f32 v123, v86, v87\n"
"v_add_f32 v14, v14, v86\n"
"v_add_f32 v15, v15, v87\n"
"v_mfma_f32_32x32x16_bf16 v[176:191], a[148:151], v[116:119], v[176:191]\n"
"v_exp_f32 v74, v74\n"
"v_exp_f32 v75, v75\n"
"v_cvt_pk_bf16_f32 v108, v72, v73\n"
"v_add_f32 v12, v12, v72\n"
"v_mfma_f32_32x32x16_bf16 v[208:223], a[152:155], v[116:119], v[208:223]\n"
"s_add_u32 s42, s43, 16384\n"
"v_add_u32 v8, s42, v1\n"
"v_add_f32 v13, v13, v73\n"
"v_exp_f32 v76, v76\n"
"v_mfma_f32_32x32x16_bf16 v[240:255], a[156:159], v[116:119], v[240:255]\n"
"v_exp_f32 v77, v77\n"
"v_cvt_pk_bf16_f32 v109, v74, v75\n"
"v_add_f32 v12, v12, v74\n"
"v_add_f32 v13, v13, v75\n"
"s_waitcnt lgkmcnt(4)\n"
"v_mfma_f32_32x32x16_bf16 v[128:143], a[160:163], v[104:107], v[128:143]\n"
"v_exp_f32 v78, v78\n"
"v_exp_f32 v79, v79\n"
"v_cvt_pk_bf16_f32 v110, v76, v77\n"
"v_add_f32 v12, v12, v76\n"
"v_mfma_f32_32x32x16_bf16 v[160:175], a[164:167], v[104:107], v[160:175]\n"
"v_add_f32 v13, v13, v77\n"
"v_exp_f32 v88, v88\n"
"v_exp_f32 v89, v89\n"
"v_cvt_pk_bf16_f32 v111, v78, v79\n"
"v_mfma_f32_32x32x16_bf16 v[192:207], a[168:171], v[104:107], v[192:207]\n"
"v_add_f32 v12, v12, v78\n"
"v_add_f32 v13, v13, v79\n"
"v_exp_f32 v90, v90\n"
"v_exp_f32 v91, v91\n"
"v_mfma_f32_32x32x16_bf16 v[224:239], a[172:175], v[104:107], v[224:239]\n"
"v_cvt_pk_bf16_f32 v124, v88, v89\n"
"v_add_f32 v14, v14, v88\n"
"v_add_f32 v15, v15, v89\n"
"v_exp_f32 v92, v92\n"
"s_waitcnt lgkmcnt(0)\n"
"v_mfma_f32_32x32x16_bf16 v[144:159], a[160:163], v[120:123], v[144:159]\n"
"ds_read_b128 a[64:67], v8 offset:0\n"
"v_exp_f32 v93, v93\n"
"v_cvt_pk_bf16_f32 v125, v90, v91\n"
"v_add_f32 v14, v14, v90\n"
"v_add_f32 v15, v15, v91\n"
"v_mfma_f32_32x32x16_bf16 v[176:191], a[164:167], v[120:123], v[176:191]\n"
"ds_read_b128 a[68:71], v8 offset:1024\n"
"v_exp_f32 v94, v94\n"
"v_exp_f32 v95, v95\n"
"v_cvt_pk_bf16_f32 v126, v92, v93\n"
"v_mfma_f32_32x32x16_bf16 v[208:223], a[168:171], v[120:123], v[208:223]\n"
"ds_read_b128 a[72:75], v8 offset:2048\n"
"v_add_f32 v14, v14, v92\n"
"v_add_f32 v15, v15, v93\n"
"v_cvt_pk_bf16_f32 v127, v94, v95\n"
"v_add_f32 v14, v14, v94\n"
"v_add_f32 v15, v15, v95\n"
"v_mfma_f32_32x32x16_bf16 v[240:255], a[172:175], v[120:123], v[240:255]\n"
"ds_read_b128 a[76:79], v8 offset:3072\n"
"v_mfma_f32_32x32x16_bf16 v[128:143], a[176:179], v[108:111], v[128:143]\n"
"ds_read_b128 a[80:83], v8 offset:4096\n"
"v_mfma_f32_32x32x16_bf16 v[160:175], a[180:183], v[108:111], v[160:175]\n"
"ds_read_b128 a[84:87], v8 offset:5120\n"
"v_mfma_f32_32x32x16_bf16 v[192:207], a[184:187], v[108:111], v[192:207]\n"
"ds_read_b128 a[88:91], v8 offset:6144\n"
"v_mfma_f32_32x32x16_bf16 v[224:239], a[188:191], v[108:111], v[224:239]\n"
"ds_read_b128 a[92:95], v8 offset:7168\n"
"v_mfma_f32_32x32x16_bf16 v[144:159], a[176:179], v[124:127], v[144:159]\n"
"v_mfma_f32_32x32x16_bf16 v[176:191], a[180:183], v[124:127], v[176:191]\n"
"v_mfma_f32_32x32x16_bf16 v[208:223], a[184:187], v[124:127], v[208:223]\n"
"v_mfma_f32_32x32x16_bf16 v[240:255], a[188:191], v[124:127], v[240:255]\n"
"s_mov_b32 s43, s42\n"
"s_waitcnt lgkmcnt(0)\n"
"v_mfma_f32_32x32x16_bf16 v[32:47], a[64:67], a[0:3], 0\n"
"v_mfma_f32_32x32x16_bf16 v[32:47], a[68:71], a[4:7], v[32:47]\n"
"v_mfma_f32_32x32x16_bf16 v[32:47], a[72:75], a[8:11], v[32:47]\n"
"v_mfma_f32_32x32x16_bf16 v[32:47], a[76:79], a[12:15], v[32:47]\n"
"v_mfma_f32_32x32x16_bf16 v[32:47], a[80:83], a[16:19], v[32:47]\n"
"ds_read_b128 a[96:99], v8 offset:8192\n"
"v_mfma_f32_32x32x16_bf16 v[32:47], a[84:87], a[20:23], v[32:47]\n"
"ds_read_b128 a[100:103], v8 offset:9216\n"
"v_mfma_f32_32x32x16_bf16 v[32:47], a[88:91], a[24:27], v[32:47]\n"
"ds_read_b128 a[104:107], v8 offset:10240\n"
"v_mfma_f32_32x32x16_bf16 v[32:47], a[92:95], a[28:31], v[32:47]\n"
"ds_read_b128 a[108:111], v8 offset:11264\n"
"v_mfma_f32_32x32x16_bf16 v[48:63], a[64:67], a[32:35], 0\n"
"ds_read_b128 a[112:115], v8 offset:12288\n"
"v_mfma_f32_32x32x16_bf16 v[48:63], a[68:71], a[36:39], v[48:63]\n"
"ds_read_b128 a[116:119], v8 offset:13312\n"
"v_exp_f32 v32, v32\n"
"v_exp_f32 v33, v33\n"
"v_mfma_f32_32x32x16_bf16 v[48:63], a[72:75], a[40:43], v[48:63]\n"
"ds_read_b128 a[120:123], v8 offset:14336\n"
"v_exp_f32 v34, v34\n"
"v_exp_f32 v35, v35\n"
"v_cndmask_b32 v32, 0, v32, s[64:65]\n"
"v_mfma_f32_32x32x16_bf16 v[48:63], a[76:79], a[44:47], v[48:63]\n"
"ds_read_b128 a[124:127], v8 offset:15360\n"
"v_cndmask_b32 v33, 0, v33, s[66:67]\n"
"v_cvt_pk_bf16_f32 v96, v32, v33\n"
"v_add_f32 v12, v12, v32\n"
"v_add_f32 v13, v13, v33\n"
"v_mfma_f32_32x32x16_bf16 v[48:63], a[80:83], a[48:51], v[48:63]\n"
"v_exp_f32 v36, v36\n"
"v_exp_f32 v37, v37\n"
"v_cndmask_b32 v34, 0, v34, s[68:69]\n"
"v_cndmask_b32 v35, 0, v35, s[70:71]\n"
"v_mfma_f32_32x32x16_bf16 v[48:63], a[84:87], a[52:55], v[48:63]\n"
"v_cvt_pk_bf16_f32 v97, v34, v35\n"
"v_add_f32 v12, v12, v34\n"
"v_add_f32 v13, v13, v35\n"
"v_exp_f32 v38, v38\n"
"v_mfma_f32_32x32x16_bf16 v[48:63], a[88:91], a[56:59], v[48:63]\n"
"ds_read_b128 a[128:131], v8 offset:32768\n"
"v_exp_f32 v39, v39\n"
"v_cndmask_b32 v36, 0, v36, s[72:73]\n"
"v_cndmask_b32 v37, 0, v37, s[74:75]\n"
"v_cvt_pk_bf16_f32 v98, v36, v37\n"
"v_mfma_f32_32x32x16_bf16 v[48:63], a[92:95], a[60:63], v[48:63]\n"
"v_add_f32 v12, v12, v36\n"
"v_add_f32 v13, v13, v37\n"
"v_exp_f32 v40, v40\n"
"s_waitcnt lgkmcnt(1)\n"
"v_mfma_f32_32x32x16_bf16 v[80:95], a[96:99], a[32:35], 0\n"
"ds_read_b128 a[132:135], v8 offset:33792\n"
"v_exp_f32 v41, v41\n"
"v_cndmask_b32 v38, 0, v38, s[76:77]\n"
"v_cndmask_b32 v39, 0, v39, s[78:79]\n"
"v_cvt_pk_bf16_f32 v99, v38, v39\n"
"v_mfma_f32_32x32x16_bf16 v[80:95], a[100:103], a[36:39], v[80:95]\n"
"v_add_f32 v12, v12, v38\n"
"v_add_f32 v13, v13, v39\n"
"v_exp_f32 v42, v42\n"
"v_exp_f32 v43, v43\n"
"v_mfma_f32_32x32x16_bf16 v[80:95], a[104:107], a[40:43], v[80:95]\n"
"ds_read_b128 a[136:139], v8 offset:34816\n"
"v_cndmask_b32 v40, 0, v40, s[80:81]\n"
"v_cndmask_b32 v41, 0, v41, s[82:83]\n"
"v_cvt_pk_bf16_f32 v100, v40, v41\n"
"v_add_f32 v12, v12, v40\n"
"v_add_f32 v13, v13, v41\n"
"v_mfma_f32_32x32x16_bf16 v[80:95], a[108:111], a[44:47], v[80:95]\n"
"v_exp_f32 v44, v44\n"
"v_exp_f32 v45, v45\n"
"v_cndmask_b32 v42, 0, v42, s[84:85]\n"
"v_cndmask_b32 v43, 0, v43, s[86:87]\n"
"v_mfma_f32_32x32x16_bf16 v[80:95], a[112:115], a[48:51], v[80:95]\n"
"ds_read_b128 a[140:143], v8 offset:35840\n"
"v_cvt_pk_bf16_f32 v101, v42, v43\n"
"v_add_f32 v12, v12, v42\n"
"v_add_f32 v13, v13, v43\n"
"v_exp_f32 v46, v46\n"
"v_mfma_f32_32x32x16_bf16 v[80:95], a[116:119], a[52:55], v[80:95]\n"
"v_exp_f32 v47, v47\n"
"v_cndmask_b32 v44, 0, v44, s[88:89]\n"
"v_cndmask_b32 v45, 0, v45, s[90:91]\n"
"v_cvt_pk_bf16_f32 v102, v44, v45\n"
"v_add_f32 v12, v12, v44\n"
"v_mfma_f32_32x32x16_bf16 v[80:95], a[120:123], a[56:59], v[80:95]\n"
"ds_read_b128 a[144:147], v8 offset:36864\n"
"v_add_f32 v13, v13, v45\n"
"v_exp_f32 v48, v48\n"
"v_exp_f32 v49, v49\n"
"v_mfma_f32_32x32x16_bf16 v[80:95], a[124:127], a[60:63], v[80:95]\n"
"v_cndmask_b32 v46, 0, v46, s[92:93]\n"
"v_cndmask_b32 v47, 0, v47, s[94:95]\n"
"v_cvt_pk_bf16_f32 v103, v46, v47\n"
"v_add_f32 v12, v12, v46\n"
"v_add_f32 v13, v13, v47\n"
"ds_read_b128 a[148:151], v8 offset:37888\n"
"v_exp_f32 v50, v50\n"
"v_exp_f32 v51, v51\n"
"v_cvt_pk_bf16_f32 v112, v48, v49\n"
"v_add_f32 v14, v14, v48\n"
"v_add_f32 v15, v15, v49\n"
"v_exp_f32 v52, v52\n"
"v_exp_f32 v53, v53\n"
"v_cvt_pk_bf16_f32 v113, v50, v51\n"
"v_add_f32 v14, v14, v50\n"
"v_add_f32 v15, v15, v51\n"
"v_exp_f32 v54, v54\n"
"v_exp_f32 v55, v55\n"
"v_cvt_pk_bf16_f32 v114, v52, v53\n"
"v_add_f32 v14, v14, v52\n"
"v_add_f32 v15, v15, v53\n"
"v_exp_f32 v56, v56\n"
"v_exp_f32 v57, v57\n"
"v_cvt_pk_bf16_f32 v115, v54, v55\n"
"v_add_f32 v14, v14, v54\n"
"v_add_f32 v15, v15, v55\n"
"v_exp_f32 v58, v58\n"
"v_exp_f32 v59, v59\n"
"v_cvt_pk_bf16_f32 v116, v56, v57\n"
"v_add_f32 v14, v14, v56\n"
"v_add_f32 v15, v15, v57\n"
"v_exp_f32 v60, v60\n"
"v_exp_f32 v61, v61\n"
"v_cvt_pk_bf16_f32 v117, v58, v59\n"
"v_add_f32 v14, v14, v58\n"
"v_add_f32 v15, v15, v59\n"
"v_exp_f32 v62, v62\n"
"v_exp_f32 v63, v63\n"
"v_cvt_pk_bf16_f32 v118, v60, v61\n"
"v_add_f32 v14, v14, v60\n"
"v_add_f32 v15, v15, v61\n"
"ds_read_b128 a[152:155], v8 offset:38912\n"
"v_exp_f32 v80, v80\n"
"v_exp_f32 v81, v81\n"
"v_cvt_pk_bf16_f32 v119, v62, v63\n"
"v_add_f32 v14, v14, v62\n"
"v_add_f32 v15, v15, v63\n"
"v_exp_f32 v82, v82\n"
"v_exp_f32 v83, v83\n"
"v_cndmask_b32 v80, 0, v80, s[64:65]\n"
"v_cndmask_b32 v81, 0, v81, s[66:67]\n"
"v_cvt_pk_bf16_f32 v120, v80, v81\n"
"v_add_f32 v14, v14, v80\n"
"v_add_f32 v15, v15, v81\n"
"v_exp_f32 v84, v84\n"
"v_exp_f32 v85, v85\n"
"v_cndmask_b32 v82, 0, v82, s[68:69]\n"
"v_cndmask_b32 v83, 0, v83, s[70:71]\n"
"v_cvt_pk_bf16_f32 v121, v82, v83\n"
"v_add_f32 v14, v14, v82\n"
"v_add_f32 v15, v15, v83\n"
"v_exp_f32 v86, v86\n"
"v_exp_f32 v87, v87\n"
"v_cndmask_b32 v84, 0, v84, s[72:73]\n"
"v_cndmask_b32 v85, 0, v85, s[74:75]\n"
"v_cvt_pk_bf16_f32 v122, v84, v85\n"
"v_add_f32 v14, v14, v84\n"
"v_add_f32 v15, v15, v85\n"
"v_exp_f32 v88, v88\n"
"v_exp_f32 v89, v89\n"
"v_cndmask_b32 v86, 0, v86, s[76:77]\n"
"v_cndmask_b32 v87, 0, v87, s[78:79]\n"
"v_cvt_pk_bf16_f32 v123, v86, v87\n"
"v_add_f32 v14, v14, v86\n"
"v_add_f32 v15, v15, v87\n"
"v_exp_f32 v90, v90\n"
"v_exp_f32 v91, v91\n"
"v_cndmask_b32 v88, 0, v88, s[80:81]\n"
"v_cndmask_b32 v89, 0, v89, s[82:83]\n"
"v_cvt_pk_bf16_f32 v124, v88, v89\n"
"v_add_f32 v14, v14, v88\n"
"v_add_f32 v15, v15, v89\n"
"v_exp_f32 v92, v92\n"
"v_exp_f32 v93, v93\n"
"v_cndmask_b32 v90, 0, v90, s[84:85]\n"
"v_cndmask_b32 v91, 0, v91, s[86:87]\n"
"v_cvt_pk_bf16_f32 v125, v90, v91\n"
"v_add_f32 v14, v14, v90\n"
"v_add_f32 v15, v15, v91\n"
"v_exp_f32 v94, v94\n"
"v_exp_f32 v95, v95\n"
"v_cndmask_b32 v92, 0, v92, s[88:89]\n"
"v_cndmask_b32 v93, 0, v93, s[90:91]\n"
"v_cvt_pk_bf16_f32 v126, v92, v93\n"
"v_add_f32 v14, v14, v92\n"
"v_add_f32 v15, v15, v93\n"
"v_cndmask_b32 v94, 0, v94, s[92:93]\n"
"v_cndmask_b32 v95, 0, v95, s[94:95]\n"
"v_cvt_pk_bf16_f32 v127, v94, v95\n"
"v_add_f32 v14, v14, v94\n"
"v_add_f32 v15, v15, v95\n"
"ds_read_b128 a[156:159], v8 offset:39936\n"
"ds_read_b128 a[160:163], v8 offset:40960\n"
"s_waitcnt lgkmcnt(5)\n"
"v_mfma_f32_32x32x16_bf16 v[128:143], a[128:131], v[96:99], v[128:143]\n"
"ds_read_b128 a[164:167], v8 offset:41984\n"
"v_mfma_f32_32x32x16_bf16 v[160:175], a[132:135], v[96:99], v[160:175]\n"
"v_mfma_f32_32x32x16_bf16 v[192:207], a[136:139], v[96:99], v[192:207]\n"
"ds_read_b128 a[168:171], v8 offset:43008\n"
"v_mfma_f32_32x32x16_bf16 v[224:239], a[140:143], v[96:99], v[224:239]\n"
"v_mfma_f32_32x32x16_bf16 v[144:159], a[128:131], v[112:115], v[144:159]\n"
"ds_read_b128 a[172:175], v8 offset:44032\n"
"v_mfma_f32_32x32x16_bf16 v[176:191], a[132:135], v[112:115], v[176:191]\n"
"v_mfma_f32_32x32x16_bf16 v[208:223], a[136:139], v[112:115], v[208:223]\n"
"ds_read_b128 a[176:179], v8 offset:45056\n"
"v_mfma_f32_32x32x16_bf16 v[240:255], a[140:143], v[112:115], v[240:255]\n"
"s_waitcnt lgkmcnt(5)\n"
"v_mfma_f32_32x32x16_bf16 v[128:143], a[144:147], v[100:103], v[128:143]\n"
"ds_read_b128 a[180:183], v8 offset:46080\n"
"v_mfma_f32_32x32x16_bf16 v[160:175], a[148:151], v[100:103], v[160:175]\n"
"v_mfma_f32_32x32x16_bf16 v[192:207], a[152:155], v[100:103], v[192:207]\n"
"ds_read_b128 a[184:187], v8 offset:47104\n"
"v_mfma_f32_32x32x16_bf16 v[224:239], a[156:159], v[100:103], v[224:239]\n"
"v_mfma_f32_32x32x16_bf16 v[144:159], a[144:147], v[116:119], v[144:159]\n"
"ds_read_b128 a[188:191], v8 offset:48128\n"
"v_mfma_f32_32x32x16_bf16 v[176:191], a[148:151], v[116:119], v[176:191]\n"
"v_mfma_f32_32x32x16_bf16 v[208:223], a[152:155], v[116:119], v[208:223]\n"
"s_xor_b32 s42, s43, 0x10000\n"
"s_sub_u32 s42, s42, 16384\n"
"s_add_u32 s42, s42, s55\n"
"v_add_u32 v8, s42, v1\n"
"v_mfma_f32_32x32x16_bf16 v[240:255], a[156:159], v[116:119], v[240:255]\n"
"s_lshl_b32 s39, s52, 15\n"
"s_add_u32 s39, s39, s38\n"
"s_waitcnt lgkmcnt(4)\n"
"s_cmp_lt_u32 s52, s53\n"
"s_cselect_b32 s39, s39, 0x40000000\n"
"s_add_u32 s40, s39, 4096\n"
"s_and_b32 s41, s52, 1\n"
"s_lshl_b32 s41, s41, 16\n"
"s_add_u32 s41, s41, s38\n"
"s_add_u32 s52, s52, 1\n"
"s_waitcnt vmcnt(0) lgkmcnt(0)\n"
"s_barrier\n"
"v_mfma_f32_32x32x16_bf16 v[144:159], a[160:163], v[120:123], v[144:159]\n"
"ds_read_b128 a[64:67], v8 offset:0\n"
"s_add_u32 m0, s41, 0\n"
"v_mfma_f32_32x32x16_bf16 v[176:191], a[164:167], v[120:123], v[176:191]\n"
"ds_read_b128 a[68:71], v8 offset:1024\n"
"buffer_load_dwordx4 v1, s[16:19], s39 offen offset:0 sc1 lds\n"
"v_mfma_f32_32x32x16_bf16 v[208:223], a[168:171], v[120:123], v[208:223]\n"
"ds_read_b128 a[72:75], v8 offset:2048\n"
"v_mfma_f32_32x32x16_bf16 v[240:255], a[172:175], v[120:123], v[240:255]\n"
"ds_read_b128 a[76:79], v8 offset:3072\n"
"buffer_load_dwordx4 v1, s[16:19], s39 offen offset:1024 sc1 lds\n"
"ds_read_b128 a[80:83], v8 offset:4096\n"
"ds_read_b128 a[84:87], v8 offset:5120\n"
"buffer_load_dwordx4 v1, s[16:19], s39 offen offset:2048 sc1 lds\n"
"ds_read_b128 a[88:91], v8 offset:6144\n"
"ds_read_b128 a[92:95], v8 offset:7168\n"
"buffer_load_dwordx4 v1, s[16:19], s39 offen offset:3072 sc1 lds\n"
"v_mfma_f32_32x32x16_bf16 v[144:159], a[176:179], v[124:127], v[144:159]\n"
"s_add_u32 m0, s41, 4096\n"
"v_mfma_f32_32x32x16_bf16 v[176:191], a[180:183], v[124:127], v[176:191]\n"
"buffer_load_dwordx4 v1, s[16:19], s40 offen offset:0 sc1 lds\n"
"v_mfma_f32_32x32x16_bf16 v[208:223], a[184:187], v[124:127], v[208:223]\n"
"v_mfma_f32_32x32x16_bf16 v[240:255], a[188:191], v[124:127], v[240:255]\n"
"buffer_load_dwordx4 v1, s[16:19], s40 offen offset:1024 sc1 lds\n"
"s_mov_b32 s43, s42\n"
"s_lshl_b32 s34, s29, 14\n"
"s_add_u32 s34, s34, 131072\n"
"v_add_u32 v16, s34, v1\n"
"ds_read_b128 a[0:3], v16 offset:0\n"
"ds_read_b128 a[4:7], v16 offset:1024\n"
"ds_read_b128 a[8:11], v16 offset:2048\n"
"ds_read_b128 a[12:15], v16 offset:3072\n"
"ds_read_b128 a[16:19], v16 offset:4096\n"
"ds_read_b128 a[20:23], v16 offset:5120\n"
"ds_read_b128 a[24:27], v16 offset:6144\n"
"ds_read_b128 a[28:31], v16 offset:7168\n"
"ds_read_b128 a[32:35], v16 offset:8192\n"
"ds_read_b128 a[36:39], v16 offset:9216\n"
"ds_read_b128 a[40:43], v16 offset:10240\n"
"ds_read_b128 a[44:47], v16 offset:11264\n"
"ds_read_b128 a[48:51], v16 offset:12288\n"
"ds_read_b128 a[52:55], v16 offset:13312\n"
"ds_read_b128 a[56:59], v16 offset:14336\n"
"ds_read_b128 a[60:63], v16 offset:15360\n"
"v_add_f32 v2, v12, v13\n"
"v_mov_b32 v3, v2\n"
"s_nop 1\n"
"v_permlane32_swap_b32 v2, v3\n"
"v_add_f32 v2, v2, v3\n"
"v_cmp_le_f32 vcc, 0x2b800000, v2\n"
"s_mov_b32 s36, 0x53800000\n"
"v_cmp_ge_f32 s[34:35], s36, v2\n"
"s_nop 0\n"
"s_and_b64 vcc, vcc, s[34:35]\n"
"s_cmp_eq_u64 vcc, -1\n"
"s_cselect_b32 s36, 0, 1\n"
"s_or_b32 s60, s60, s36\n"
"v_rcp_f32 v2, v2\n"
"v_add_f32 v4, v14, v15\n"
"v_mov_b32 v5, v4\n"
"s_nop 1\n"
"v_permlane32_swap_b32 v4, v5\n"
"v_add_f32 v4, v4, v5\n"
"v_cmp_le_f32 vcc, 0x2b800000, v4\n"
"s_mov_b32 s36, 0x53800000\n"
"v_cmp_ge_f32 s[34:35], s36, v4\n"
"s_nop 0\n"
"s_and_b64 vcc, vcc, s[34:35]\n"
"s_cmp_eq_u64 vcc, -1\n"
"s_cselect_b32 s36, 0, 1\n"
"s_or_b32 s60, s60, s36\n"
"v_rcp_f32 v4, v4\n"
"v_mov_b32 v96, 0\n"
"v_mov_b32 v97, 0\n"
"v_mov_b32 v98, 0\n"
"v_mov_b32 v99, 0\n"
"v_mov_b32 v12, 0\n"
"v_mov_b32 v13, 0\n"
"v_mov_b32 v14, 0\n"
"v_mov_b32 v15, 0\n"
"v_pk_mul_f32 v[128:129], v[128:129], v[2:3] op_sel_hi:[1,0]\n"
"v_pk_mul_f32 v[130:131], v[130:131], v[2:3] op_sel_hi:[1,0]\n"
"v_pk_mul_f32 v[132:133], v[132:133], v[2:3] op_sel_hi:[1,0]\n"
"v_pk_mul_f32 v[134:135], v[134:135], v[2:3] op_sel_hi:[1,0]\n"
"v_cvt_pk_bf16_f32 v16, v128, v129\n"
"v_cvt_pk_bf16_f32 v17, v130, v131\n"
"v_cvt_pk_bf16_f32 v18, v132, v133\n"
"v_cvt_pk_bf16_f32 v19, v134, v135\n"
"s_add_u32 s37, s58, 0\n"
"buffer_store_dwordx4 v[16:19], v1, s[24:27], s37 offen offset:0 sc1\n"
"v_pk_mul_f32 v[136:137], v[136:137], v[2:3] op_sel_hi:[1,0]\n"
"v_pk_mul_f32 v[138:139], v[138:139], v[2:3] op_sel_hi:[1,0]\n"
"v_pk_mul_f32 v[140:141], v[140:141], v[2:3] op_sel_hi:[1,0]\n"
"v_pk_mul_f32 v[142:143], v[142:143], v[2:3] op_sel_hi:[1,0]\n"
"v_cvt_pk_bf16_f32 v20, v136, v137\n"
"v_cvt_pk_bf16_f32 v21, v138, v139\n"
"v_cvt_pk_bf16_f32 v22, v140, v141\n"
"v_cvt_pk_bf16_f32 v23, v142, v143\n"
"buffer_store_dwordx4 v[20:23], v1, s[24:27], s37 offen offset:1024 sc1\n"
"v_mfma_f32_32x32x16_bf16 v[128:143], v[96:99], v[96:99], 0\n"
"v_pk_mul_f32 v[160:161], v[160:161], v[2:3] op_sel_hi:[1,0]\n"
"v_pk_mul_f32 v[162:163], v[162:163], v[2:3] op_sel_hi:[1,0]\n"
"v_pk_mul_f32 v[164:165], v[164:165], v[2:3] op_sel_hi:[1,0]\n"
"v_pk_mul_f32 v[166:167], v[166:167], v[2:3] op_sel_hi:[1,0]\n"
"v_cvt_pk_bf16_f32 v24, v160, v161\n"
"v_cvt_pk_bf16_f32 v25, v162, v163\n"
"v_cvt_pk_bf16_f32 v26, v164, v165\n"
"v_cvt_pk_bf16_f32 v27, v166, v167\n"
"buffer_store_dwordx4 v[24:27], v1, s[24:27], s37 offen offset:2048 sc1\n"
"v_pk_mul_f32 v[168:169], v[168:169], v[2:3] op_sel_hi:[1,0]\n"
"v_pk_mul_f32 v[170:171], v[170:171], v[2:3] op_sel_hi:[1,0]\n"
"v_pk_mul_f32 v[172:173], v[172:173], v[2:3] op_sel_hi:[1,0]\n"
"v_pk_mul_f32 v[174:175], v[174:175], v[2:3] op_sel_hi:[1,0]\n"
"v_cvt_pk_bf16_f32 v28, v168, v169\n"
"v_cvt_pk_bf16_f32 v29, v170, v171\n"
"v_cvt_pk_bf16_f32 v30, v172, v173\n"
"v_cvt_pk_bf16_f32 v31, v174, v175\n"
"buffer_store_dwordx4 v[28:31], v1, s[24:27], s37 offen offset:3072 sc1\n"
"v_mfma_f32_32x32x16_bf16 v[160:175], v[96:99], v[96:99], 0\n"
"v_pk_mul_f32 v[192:193], v[192:193], v[2:3] op_sel_hi:[1,0]\n"
"v_pk_mul_f32 v[194:195], v[194:195], v[2:3] op_sel_hi:[1,0]\n"
"v_pk_mul_f32 v[196:197], v[196:197], v[2:3] op_sel_hi:[1,0]\n"
"v_pk_mul_f32 v[198:199], v[198:199], v[2:3] op_sel_hi:[1,0]\n"
"v_cvt_pk_bf16_f32 v16, v192, v193\n"
"v_cvt_pk_bf16_f32 v17, v194, v195\n"
"v_cvt_pk_bf16_f32 v18, v196, v197\n"
"v_cvt_pk_bf16_f32 v19, v198, v199\n"
"s_add_u32 s37, s58, 4096\n"
"buffer_store_dwordx4 v[16:19], v1, s[24:27], s37 offen offset:0 sc1\n"
"v_pk_mul_f32 v[200:201], v[200:201], v[2:3] op_sel_hi:[1,0]\n"
"v_pk_mul_f32 v[202:203], v[202:203], v[2:3] op_sel_hi:[1,0]\n"
"v_pk_mul_f32 v[204:205], v[204:205], v[2:3] op_sel_hi:[1,0]\n"
"v_pk_mul_f32 v[206:207], v[206:207], v[2:3] op_sel_hi:[1,0]\n"
"v_cvt_pk_bf16_f32 v20, v200, v201\n"
"v_cvt_pk_bf16_f32 v21, v202, v203\n"
"v_cvt_pk_bf16_f32 v22, v204, v205\n"
"v_cvt_pk_bf16_f32 v23, v206, v207\n"
"buffer_store_dwordx4 v[20:23], v1, s[24:27], s37 offen offset:1024 sc1\n"
"v_mfma_f32_32x32x16_bf16 v[192:207], v[96:99], v[96:99], 0\n"
"v_pk_mul_f32 v[224:225], v[224:225], v[2:3] op_sel_hi:[1,0]\n"
"v_pk_mul_f32 v[226:227], v[226:227], v[2:3] op_sel_hi:[1,0]\n"
"v_pk_mul_f32 v[228:229], v[228:229], v[2:3] op_sel_hi:[1,0]\n"
"v_pk_mul_f32 v[230:231], v[230:231], v[2:3] op_sel_hi:[1,0]\n"
"v_cvt_pk_bf16_f32 v24, v224, v225\n"
"v_cvt_pk_bf16_f32 v25, v226, v227\n"
"v_cvt_pk_bf16_f32 v26, v228, v229\n"
"v_cvt_pk_bf16_f32 v27, v230, v231\n"
"buffer_store_dwordx4 v[24:27], v1, s[24:27], s37 offen offset:2048 sc1\n"
"v_pk_mul_f32 v[232:233], v[232:233], v[2:3] op_sel_hi:[1,0]\n"
"v_pk_mul_f32 v[234:235], v[234:235], v[2:3] op_sel_hi:[1,0]\n"
"v_pk_mul_f32 v[236:237], v[236:237], v[2:3] op_sel_hi:[1,0]\n"
"v_pk_mul_f32 v[238:239], v[238:239], v[2:3] op_sel_hi:[1,0]\n"
"v_cvt_pk_bf16_f32 v28, v232, v233\n"
"v_cvt_pk_bf16_f32 v29, v234, v235\n"
"v_cvt_pk_bf16_f32 v30, v236, v237\n"
"v_cvt_pk_bf16_f32 v31, v238, v239\n"
"buffer_store_dwordx4 v[28:31], v1, s[24:27], s37 offen offset:3072 sc1\n"
"v_mfma_f32_32x32x16_bf16 v[224:239], v[96:99], v[96:99], 0\n"
"v_pk_mul_f32 v[144:145], v[144:145], v[4:5] op_sel_hi:[1,0]\n"
"v_pk_mul_f32 v[146:147], v[146:147], v[4:5] op_sel_hi:[1,0]\n"
"v_pk_mul_f32 v[148:149], v[148:149], v[4:5] op_sel_hi:[1,0]\n"
"v_pk_mul_f32 v[150:151], v[150:151], v[4:5] op_sel_hi:[1,0]\n"
"v_cvt_pk_bf16_f32 v16, v144, v145\n"
"v_cvt_pk_bf16_f32 v17, v146, v147\n"
"v_cvt_pk_bf16_f32 v18, v148, v149\n"
"v_cvt_pk_bf16_f32 v19, v150, v151\n"
"s_add_u32 s37, s58, 262144\n"
"buffer_store_dwordx4 v[16:19], v1, s[24:27], s37 offen offset:0 sc1\n"
"v_pk_mul_f32 v[152:153], v[152:153], v[4:5] op_sel_hi:[1,0]\n"
"v_pk_mul_f32 v[154:155], v[154:155], v[4:5] op_sel_hi:[1,0]\n"
"v_pk_mul_f32 v[156:157], v[156:157], v[4:5] op_sel_hi:[1,0]\n"
"v_pk_mul_f32 v[158:159], v[158:159], v[4:5] op_sel_hi:[1,0]\n"
"v_cvt_pk_bf16_f32 v20, v152, v153\n"
"v_cvt_pk_bf16_f32 v21, v154, v155\n"
"v_cvt_pk_bf16_f32 v22, v156, v157\n"
"v_cvt_pk_bf16_f32 v23, v158, v159\n"
"buffer_store_dwordx4 v[20:23], v1, s[24:27], s37 offen offset:1024 sc1\n"
"v_mfma_f32_32x32x16_bf16 v[144:159], v[96:99], v[96:99], 0\n"
"v_pk_mul_f32 v[176:177], v[176:177], v[4:5] op_sel_hi:[1,0]\n"
"v_pk_mul_f32 v[178:179], v[178:179], v[4:5] op_sel_hi:[1,0]\n"
"v_pk_mul_f32 v[180:181], v[180:181], v[4:5] op_sel_hi:[1,0]\n"
"v_pk_mul_f32 v[182:183], v[182:183], v[4:5] op_sel_hi:[1,0]\n"
"v_cvt_pk_bf16_f32 v24, v176, v177\n"
"v_cvt_pk_bf16_f32 v25, v178, v179\n"
"v_cvt_pk_bf16_f32 v26, v180, v181\n"
"v_cvt_pk_bf16_f32 v27, v182, v183\n"
"buffer_store_dwordx4 v[24:27], v1, s[24:27], s37 offen offset:2048 sc1\n"
"v_pk_mul_f32 v[184:185], v[184:185], v[4:5] op_sel_hi:[1,0]\n"
"v_pk_mul_f32 v[186:187], v[186:187], v[4:5] op_sel_hi:[1,0]\n"
"v_pk_mul_f32 v[188:189], v[188:189], v[4:5] op_sel_hi:[1,0]\n"
"v_pk_mul_f32 v[190:191], v[190:191], v[4:5] op_sel_hi:[1,0]\n"
"v_cvt_pk_bf16_f32 v28, v184, v185\n"
"v_cvt_pk_bf16_f32 v29, v186, v187\n"
"v_cvt_pk_bf16_f32 v30, v188, v189\n"
"v_cvt_pk_bf16_f32 v31, v190, v191\n"
"buffer_store_dwordx4 v[28:31], v1, s[24:27], s37 offen offset:3072 sc1\n"
"v_mfma_f32_32x32x16_bf16 v[176:191], v[96:99], v[96:99], 0\n"
"v_pk_mul_f32 v[208:209], v[208:209], v[4:5] op_sel_hi:[1,0]\n"
"v_pk_mul_f32 v[210:211], v[210:211], v[4:5] op_sel_hi:[1,0]\n"
"v_pk_mul_f32 v[212:213], v[212:213], v[4:5] op_sel_hi:[1,0]\n"
"v_pk_mul_f32 v[214:215], v[214:215], v[4:5] op_sel_hi:[1,0]\n"
"v_cvt_pk_bf16_f32 v16, v208, v209\n"
"v_cvt_pk_bf16_f32 v17, v210, v211\n"
"v_cvt_pk_bf16_f32 v18, v212, v213\n"
"v_cvt_pk_bf16_f32 v19, v214, v215\n"
"s_add_u32 s37, s58, 266240\n"
"buffer_store_dwordx4 v[16:19], v1, s[24:27], s37 offen offset:0 sc1\n"
"v_pk_mul_f32 v[216:217], v[216:217], v[4:5] op_sel_hi:[1,0]\n"
"v_pk_mul_f32 v[218:219], v[218:219], v[4:5] op_sel_hi:[1,0]\n"
"v_pk_mul_f32 v[220:221], v[220:221], v[4:5] op_sel_hi:[1,0]\n"
"v_pk_mul_f32 v[222:223], v[222:223], v[4:5] op_sel_hi:[1,0]\n"
"v_cvt_pk_bf16_f32 v20, v216, v217\n"
"v_cvt_pk_bf16_f32 v21, v218, v219\n"
"v_cvt_pk_bf16_f32 v22, v220, v221\n"
"v_cvt_pk_bf16_f32 v23, v222, v223\n"
"buffer_store_dwordx4 v[20:23], v1, s[24:27], s37 offen offset:1024 sc1\n"
"v_mfma_f32_32x32x16_bf16 v[208:223], v[96:99], v[96:99], 0\n"
"v_pk_mul_f32 v[240:241], v[240:241], v[4:5] op_sel_hi:[1,0]\n"
"v_pk_mul_f32 v[242:243], v[242:243], v[4:5] op_sel_hi:[1,0]\n"
"v_pk_mul_f32 v[244:245], v[244:245], v[4:5] op_sel_hi:[1,0]\n"
"v_pk_mul_f32 v[246:247], v[246:247], v[4:5] op_sel_hi:[1,0]\n"
"v_cvt_pk_bf16_f32 v24, v240, v241\n"
"v_cvt_pk_bf16_f32 v25, v242, v243\n"
"v_cvt_pk_bf16_f32 v26, v244, v245\n"
"v_cvt_pk_bf16_f32 v27, v246, v247\n"
"buffer_store_dwordx4 v[24:27], v1, s[24:27], s37 offen offset:2048 sc1\n"
"v_pk_mul_f32 v[248:249], v[248:249], v[4:5] op_sel_hi:[1,0]\n"
"v_pk_mul_f32 v[250:251], v[250:251], v[4:5] op_sel_hi:[1,0]\n"
"v_pk_mul_f32 v[252:253], v[252:253], v[4:5] op_sel_hi:[1,0]\n"
"v_pk_mul_f32 v[254:255], v[254:255], v[4:5] op_sel_hi:[1,0]\n"
"v_cvt_pk_bf16_f32 v28, v248, v249\n"
"v_cvt_pk_bf16_f32 v29, v250, v251\n"
"v_cvt_pk_bf16_f32 v30, v252, v253\n"
"v_cvt_pk_bf16_f32 v31, v254, v255\n"
"buffer_store_dwordx4 v[28:31], v1, s[24:27], s37 offen offset:3072 sc1\n"
"v_mfma_f32_32x32x16_bf16 v[240:255], v[96:99], v[96:99], 0\n"
"s_waitcnt lgkmcnt(0)\n"
".Lp2:\n"
"s_lshl_b32 s54, s31, 1\n"
"s_sub_u32 s54, 14, s54\n"
"s_cmp_eq_u32 s54, 0\n"
"s_cbranch_scc1 .Lp2_last\n"
".Lp2_loop:\n"
"s_waitcnt lgkmcnt(0)\n"
"v_mfma_f32_32x32x16_bf16 v[32:47], a[64:67], a[0:3], 0\n"
"v_mfma_f32_32x32x16_bf16 v[32:47], a[68:71], a[4:7], v[32:47]\n"
"buffer_load_dwordx4 v1, s[16:19], s40 offen offset:2048 sc1 lds\n"
"v_mfma_f32_32x32x16_bf16 v[32:47], a[72:75], a[8:11], v[32:47]\n"
"v_mfma_f32_32x32x16_bf16 v[32:47], a[76:79], a[12:15], v[32:47]\n"
"buffer_load_dwordx4 v1, s[16:19], s40 offen offset:3072 sc1 lds\n"
"v_mfma_f32_32x32x16_bf16 v[32:47], a[80:83], a[16:19], v[32:47]\n"
"ds_read_b128 a[96:99], v8 offset:8192\n"
"s_add_u32 m0, s41, 32768\n"
"v_mfma_f32_32x32x16_bf16 v[32:47], a[84:87], a[20:23], v[32:47]\n"
"buffer_load_dwordx4 v1, s[20:23], s39 offen offset:0 sc1 lds\n"
"ds_read_b128 a[100:103], v8 offset:9216\n"
"v_mfma_f32_32x32x16_bf16 v[32:47], a[88:91], a[24:27], v[32:47]\n"
"ds_read_b128 a[104:107], v8 offset:10240\n"
"v_mfma_f32_32x32x16_bf16 v[32:47], a[92:95], a[28:31], v[32:47]\n"
"buffer_load_dwordx4 v1, s[20:23], s39 offen offset:1024 sc1 lds\n"
"ds_read_b128 a[108:111], v8 offset:11264\n"
"v_mfma_f32_32x32x16_bf16 v[48:63], a[64:67], a[32:35], 0\n"
"ds_read_b128 a[112:115], v8 offset:12288\n"
"v_mfma_f32_32x32x16_bf16 v[48:63], a[68:71], a[36:39], v[48:63]\n"
"buffer_load_dwordx4 v1, s[20:23], s39 offen offset:2048 sc1 lds\n"
"ds_read_b128 a[116:119], v8 offset:13312\n"
"v_exp_f32 v32, v32\n"
"v_mfma_f32_32x32x16_bf16 v[48:63], a[72:75], a[40:43], v[48:63]\n"
"ds_read_b128 a[120:123], v8 offset:14336\n"
"v_exp_f32 v33, v33\n"
"v_exp_f32 v34, v34\n"
"v_mfma_f32_32x32x16_bf16 v[48:63], a[76:79], a[44:47], v[48:63]\n"
"buffer_load_dwordx4 v1, s[20:23], s39 offen offset:3072 sc1 lds\n"
"ds_read_b128 a[124:127], v8 offset:15360\n"
"v_exp_f32 v35, v35\n"
"v_cvt_pk_bf16_f32 v96, v32, v33\n"
"v_mfma_f32_32x32x16_bf16 v[48:63], a[80:83], a[48:51], v[48:63]\n"
"v_add_f32 v12, v12, v32\n"
"v_add_f32 v13, v13, v33\n"
"v_exp_f32 v36, v36\n"
"s_add_u32 m0, s41, 36864\n"
"v_mfma_f32_32x32x16_bf16 v[48:63], a[84:87], a[52:55], v[48:63]\n"
"buffer_load_dwordx4 v1, s[20:23], s40 offen offset:0 sc1 lds\n"
"v_exp_f32 v37, v37\n"
"v_cvt_pk_bf16_f32 v97, v34, v35\n"
"v_add_f32 v12, v12, v34\n"
"v_mfma_f32_32x32x16_bf16 v[48:63], a[88:91], a[56:59], v[48:63]\n"
"ds_read_b128 a[128:131], v8 offset:32768\n"
"v_add_f32 v13, v13, v35\n"
"v_exp_f32 v38, v38\n"
"v_exp_f32 v39, v39\n"
"v_mfma_f32_32x32x16_bf16 v[48:63], a[92:95], a[60:63], v[48:63]\n"
"buffer_load_dwordx4 v1, s[20:23], s40 offen offset:1024 sc1 lds\n"
"v_cvt_pk_bf16_f32 v98, v36, v37\n"
"v_add_f32 v12, v12, v36\n"
"v_add_f32 v13, v13, v37\n"
"s_waitcnt lgkmcnt(1)\n"
"v_mfma_f32_32x32x16_bf16 v[64:79], a[96:99], a[0:3], 0\n"
"ds_read_b128 a[132:135], v8 offset:33792\n"
"v_exp_f32 v40, v40\n"
"v_exp_f32 v41, v41\n"
"v_cvt_pk_bf16_f32 v99, v38, v39\n"
"v_mfma_f32_32x32x16_bf16 v[64:79], a[100:103], a[4:7], v[64:79]\n"
"buffer_load_dwordx4 v1, s[20:23], s40 offen offset:2048 sc1 lds\n"
"v_add_f32 v12, v12, v38\n"
"v_add_f32 v13, v13, v39\n"
"v_exp_f32 v42, v42\n"
"v_mfma_f32_32x32x16_bf16 v[64:79], a[104:107], a[8:11], v[64:79]\n"
"ds_read_b128 a[136:139], v8 offset:34816\n"
"v_exp_f32 v43, v43\n"
"v_cvt_pk_bf16_f32 v100, v40, v41\n"
"v_add_f32 v12, v12, v40\n"
"v_add_f32 v13, v13, v41\n"
"v_mfma_f32_32x32x16_bf16 v[64:79], a[108:111], a[12:15], v[64:79]\n"
"buffer_load_dwordx4 v1, s[20:23], s40 offen offset:3072 sc1 lds\n"
"v_exp_f32 v44, v44\n"
"v_exp_f32 v45, v45\n"
"v_mfma_f32_32x32x16_bf16 v[64:79], a[112:115], a[16:19], v[64:79]\n"
"ds_read_b128 a[140:143], v8 offset:35840\n"
"v_cvt_pk_bf16_f32 v101, v42, v43\n"
"v_add_f32 v12, v12, v42\n"
"v_add_f32 v13, v13, v43\n"
"v_exp_f32 v46, v46\n"
"v_mfma_f32_32x32x16_bf16 v[64:79], a[116:119], a[20:23], v[64:79]\n"
"v_exp_f32 v47, v47\n"
"v_cvt_pk_bf16_f32 v102, v44, v45\n"
"v_add_f32 v12, v12, v44\n"
"v_add_f32 v13, v13, v45\n"
"v_mfma_f32_32x32x16_bf16 v[64:79], a[120:123], a[24:27], v[64:79]\n"
"ds_read_b128 a[144:147], v8 offset:36864\n"
"v_exp_f32 v48, v48\n"
"v_exp_f32 v49, v49\n"
"v_cvt_pk_bf16_f32 v103, v46, v47\n"
"v_mfma_f32_32x32x16_bf16 v[64:79], a[124:127], a[28:31], v[64:79]\n"
"v_add_f32 v12, v12, v46\n"
"v_add_f32 v13, v13, v47\n"
"v_exp_f32 v50, v50\n"
"v_exp_f32 v51, v51\n"
"v_mfma_f32_32x32x16_bf16 v[80:95], a[96:99], a[32:35], 0\n"
"ds_read_b128 a[148:151], v8 offset:37888\n"
"v_cvt_pk_bf16_f32 v112, v48, v49\n"
"v_add_f32 v14, v14, v48\n"
"v_add_f32 v15, v15, v49\n"
"v_exp_f32 v52, v52\n"
"v_mfma_f32_32x32x16_bf16 v[80:95], a[100:103], a[36:39], v[80:95]\n"
"v_exp_f32 v53, v53\n"
"v_cvt_pk_bf16_f32 v113, v50, v51\n"
"v_add_f32 v14, v14, v50\n"
"v_add_f32 v15, v15, v51\n"
"v_mfma_f32_32x32x16_bf16 v[80:95], a[104:107], a[40:43], v[80:95]\n"
"ds_read_b128 a[152:155], v8 offset:38912\n"
"v_exp_f32 v54, v54\n"
"v_exp_f32 v55, v55\n"
"v_cvt_pk_bf16_f32 v114, v52, v53\n"
"v_mfma_f32_32x32x16_bf16 v[80:95], a[108:111], a[44:47], v[80:95]\n"
"v_add_f32 v14, v14, v52\n"
"v_add_f32 v15, v15, v53\n"
"v_exp_f32 v56, v56\n"
"v_exp_f32 v57, v57\n"
"v_mfma_f32_32x32x16_bf16 v[80:95], a[112:115], a[48:51], v[80:95]\n"
"ds_read_b128 a[156:159], v8 offset:39936\n"
"v_cvt_pk_bf16_f32 v115, v54, v55\n"
"v_add_f32 v14, v14, v54\n"
"v_add_f32 v15, v15, v55\n"
"v_exp_f32 v58, v58\n"
"v_mfma_f32_32x32x16_bf16 v[80:95], a[116:119], a[52:55], v[80:95]\n"
"v_exp_f32 v59, v59\n"
"v_cvt_pk_bf16_f32 v116, v56, v57\n"
"v_add_f32 v14, v14, v56\n"
"v_add_f32 v15, v15, v57\n"
"v_mfma_f32_32x32x16_bf16 v[80:95], a[120:123], a[56:59], v[80:95]\n"
"ds_read_b128 a[160:163], v8 offset:40960\n"
"v_exp_f32 v60, v60\n"
"v_exp_f32 v61, v61\n"
"v_cvt_pk_bf16_f32 v117, v58, v59\n"
"v_mfma_f32_32x32x16_bf16 v[80:95], a[124:127], a[60:63], v[80:95]\n"
"v_add_f32 v14, v14, v58\n"
"v_add_f32 v15, v15, v59\n"
"v_exp_f32 v62, v62\n"
"s_waitcnt lgkmcnt(5)\n"
"v_mfma_f32_32x32x16_bf16 v[128:143], a[128:131], v[96:99], v[128:143]\n"
"ds_read_b128 a[164:167], v8 offset:41984\n"
"v_exp_f32 v63, v63\n"
"v_cvt_pk_bf16_f32 v118, v60, v61\n"
"v_add_f32 v14, v14, v60\n"
"v_add_f32 v15, v15, v61\n"
"v_mfma_f32_32x32x16_bf16 v[160:175], a[132:135], v[96:99], v[160:175]\n"
"v_exp_f32 v64, v64\n"
"v_exp_f32 v65, v65\n"
"v_cvt_pk_bf16_f32 v119, v62, v63\n"
"v_add_f32 v14, v14, v62\n"
"v_mfma_f32_32x32x16_bf16 v[192:207], a[136:139], v[96:99], v[192:207]\n"
"ds_read_b128 a[168:171], v8 offset:43008\n"
"v_add_f32 v15, v15, v63\n"
"v_exp_f32 v66, v66\n"
"v_exp_f32 v67, v67\n"
"v_mfma_f32_32x32x16_bf16 v[224:239], a[140:143], v[96:99], v[224:239]\n"
"v_cvt_pk_bf16_f32 v104, v64, v65\n"
"v_add_f32 v12, v12, v64\n"
"v_add_f32 v13, v13, v65\n"
"v_exp_f32 v68, v68\n"
"v_mfma_f32_32x32x16_bf16 v[144:159], a[128:131], v[112:115], v[144:159]\n"
"ds_read_b128 a[172:175], v8 offset:44032\n"
"v_exp_f32 v69, v69\n"
"v_cvt_pk_bf16_f32 v105, v66, v67\n"
"v_add_f32 v12, v12, v66\n"
"v_add_f32 v13, v13, v67\n"
"v_mfma_f32_32x32x16_bf16 v[176:191], a[132:135], v[112:115], v[176:191]\n"
"v_exp_f32 v70, v70\n"
"v_exp_f32 v71, v71\n"
"v_cvt_pk_bf16_f32 v106, v68, v69\n"
"v_add_f32 v12, v12, v68\n"
"v_mfma_f32_32x32x16_bf16 v[208:223], a[136:139], v[112:115], v[208:223]\n"
"ds_read_b128 a[176:179], v8 offset:45056\n"
"v_add_f32 v13, v13, v69\n"
"v_exp_f32 v80, v80\n"
"v_exp_f32 v81, v81\n"
"v_mfma_f32_32x32x16_bf16 v[240:255], a[140:143], v[112:115], v[240:255]\n"
"v_cvt_pk_bf16_f32 v107, v70, v71\n"
"v_add_f32 v12, v12, v70\n"
"v_add_f32 v13, v13, v71\n"
"v_exp_f32 v82, v82\n"
"s_waitcnt lgkmcnt(5)\n"
"v_mfma_f32_32x32x16_bf16 v[128:143], a[144:147], v[100:103], v[128:143]\n"
"ds_read_b128 a[180:183], v8 offset:46080\n"
"v_exp_f32 v83, v83\n"
"v_cvt_pk_bf16_f32 v120, v80, v81\n"
"v_add_f32 v14, v14, v80\n"
"v_add_f32 v15, v15, v81\n"
"v_mfma_f32_32x32x16_bf16 v[160:175], a[148:151], v[100:103], v[160:175]\n"
"v_exp_f32 v84, v84\n"
"v_exp_f32 v85, v85\n"
"v_cvt_pk_bf16_f32 v121, v82, v83\n"
"v_add_f32 v14, v14, v82\n"
"v_mfma_f32_32x32x16_bf16 v[192:207], a[152:155], v[100:103], v[192:207]\n"
"ds_read_b128 a[184:187], v8 offset:47104\n"
"v_add_f32 v15, v15, v83\n"
"v_exp_f32 v86, v86\n"
"v_exp_f32 v87, v87\n"
"v_mfma_f32_32x32x16_bf16 v[224:239], a[156:159], v[100:103], v[224:239]\n"
"v_cvt_pk_bf16_f32 v122, v84, v85\n"
"v_add_f32 v14, v14, v84\n"
"v_add_f32 v15, v15, v85\n"
"v_exp_f32 v72, v72\n"
"v_mfma_f32_32x32x16_bf16 v[144:159], a[144:147], v[116:119], v[144:159]\n"
"ds_read_b128 a[188:191], v8 offset:48128\n"
"v_exp_f32 v73, v73\n"
"v_cvt_pk_bf16_f32 v123, v86, v87\n"
"v_add_f32 v14, v14, v86\n"
"v_add_f32 v15, v15, v87\n"
"v_mfma_f32_32x32x16_bf16 v[176:191], a[148:151], v[116:119], v[176:191]\n"
"v_exp_f32 v74, v74\n"
"v_exp_f32 v75, v75\n"
"v_cvt_pk_bf16_f32 v108, v72, v73\n"
"v_add_f32 v12, v12, v72\n"
"v_mfma_f32_32x32x16_bf16 v[208:223], a[152:155], v[116:119], v[208:223]\n"
"s_xor_b32 s42, s43, 0x10000\n"
"v_add_u32 v8, s42, v1\n"
"v_add_f32 v13, v13, v73\n"
"v_exp_f32 v76, v76\n"
"v_mfma_f32_32x32x16_bf16 v[240:255], a[156:159], v[116:119], v[240:255]\n"
"s_lshl_b32 s39, s52, 15\n"
"s_add_u32 s39, s39, s38\n"
"v_exp_f32 v77, v77\n"
"v_cvt_pk_bf16_f32 v109, v74, v75\n"
"s_waitcnt lgkmcnt(4)\n"
"v_mfma_f32_32x32x16_bf16 v[128:143], a[160:163], v[104:107], v[128:143]\n"
"s_cmp_lt_u32 s52, s53\n"
"s_cselect_b32 s39, s39, 0x40000000\n"
"v_add_f32 v12, v12, v74\n"
"v_add_f32 v13, v13, v75\n"
"v_exp_f32 v78, v78\n"
"v_mfma_f32_32x32x16_bf16 v[160:175], a[164:167], v[104:107], v[160:175]\n"
"s_add_u32 s40, s39, 4096\n"
"s_and_b32 s41, s52, 1\n"
"v_exp_f32 v79, v79\n"
"v_cvt_pk_bf16_f32 v110, v76, v77\n"
"v_add_f32 v12, v12, v76\n"
"v_mfma_f32_32x32x16_bf16 v[192:207], a[168:171], v[104:107], v[192:207]\n"
"s_lshl_b32 s41, s41, 16\n"
"s_add_u32 s41, s41, s38\n"
"v_add_f32 v13, v13, v77\n"
"v_exp_f32 v88, v88\n"
"v_mfma_f32_32x32x16_bf16 v[224:239], a[172:175], v[104:107], v[224:239]\n"
"s_add_u32 s52, s52, 1\n"
"v_exp_f32 v89, v89\n"
"v_cvt_pk_bf16_f32 v111, v78, v79\n"
"s_waitcnt vmcnt(0) lgkmcnt(0)\n"
"s_barrier\n"
"v_mfma_f32_32x32x16_bf16 v[144:159], a[160:163], v[120:123], v[144:159]\n"
"ds_read_b128 a[64:67], v8 offset:0\n"
"v_add_f32 v12, v12, v78\n"
"v_add_f32 v13, v13, v79\n"
"v_exp_f32 v90, v90\n"
"s_add_u32 m0, s41, 0\n"
"v_mfma_f32_32x32x16_bf16 v[176:191], a[164:167], v[120:123], v[176:191]\n"
"ds_read_b128 a[68:71], v8 offset:1024\n"
"buffer_load_dwordx4 v1, s[16:19], s39 offen offset:0 sc1 lds\n"
"v_exp_f32 v91, v91\n"
"v_cvt_pk_bf16_f32 v124, v88, v89\n"
"v_mfma_f32_32x32x16_bf16 v[208:223], a[168:171], v[120:123], v[208:223]\n"
"ds_read_b128 a[72:75], v8 offset:2048\n"
"v_add_f32 v14, v14, v88\n"
"v_add_f32 v15, v15, v89\n"
"v_exp_f32 v92, v92\n"
"v_mfma_f32_32x32x16_bf16 v[240:255], a[172:175], v[120:123], v[240:255]\n"
"ds_read_b128 a[76:79], v8 offset:3072\n"
"buffer_load_dwordx4 v1, s[16:19], s39 offen offset:1024 sc1 lds\n"
"v_exp_f32 v93, v93\n"
"v_cvt_pk_bf16_f32 v125, v90, v91\n"
"v_mfma_f32_32x32x16_bf16 v[128:143], a[176:179], v[108:111], v[128:143]\n"
"ds_read_b128 a[80:83], v8 offset:4096\n"
"v_add_f32 v14, v14, v90\n"
"v_add_f32 v15, v15, v91\n"
"v_exp_f32 v94, v94\n"
"v_mfma_f32_32x32x16_bf16 v[160:175], a[180:183], v[108:111], v[160:175]\n"
"ds_read_b128 a[84:87], v8 offset:5120\n"
"buffer_load_dwordx4 v1, s[16:19], s39 offen offset:2048 sc1 lds\n"
"v_exp_f32 v95, v95\n"
"v_cvt_pk_bf16_f32 v126, v92, v93\n"
"v_mfma_f32_32x32x16_bf16 v[192:207], a[184:187], v[108:111], v[192:207]\n"
"ds_read_b128 a[88:91], v8 offset:6144\n"
"v_add_f32 v14, v14, v92\n"
"v_add_f32 v15, v15, v93\n"
"v_cvt_pk_bf16_f32 v127, v94, v95\n"
"v_add_f32 v14, v14, v94\n"
"v_add_f32 v15, v15, v95\n"
"v_mfma_f32_32x32x16_bf16 v[224:239], a[188:191], v[108:111], v[224:239]\n"
"ds_read_b128 a[92:95], v8 offset:7168\n"
"buffer_load_dwordx4 v1, s[16:19], s39 offen offset:3072 sc1 lds\n"
"v_mfma_f32_32x32x16_bf16 v[144:159], a[176:179], v[124:127], v[144:159]\n"
"s_add_u32 m0, s41, 4096\n"
"v_mfma_f32_32x32x16_bf16 v[176:191], a[180:183], v[124:127], v[176:191]\n"
"buffer_load_dwordx4 v1, s[16:19], s40 offen offset:0 sc1 lds\n"
"v_mfma_f32_32x32x16_bf16 v[208:223], a[184:187], v[124:127], v[208:223]\n"
"v_mfma_f32_32x32x16_bf16 v[240:255], a[188:191], v[124:127], v[240:255]\n"
"buffer_load_dwordx4 v1, s[16:19], s40 offen offset:1024 sc1 lds\n"
"s_mov_b32 s43, s42\n"
"s_sub_u32 s54, s54, 1\n"
"s_cmp_lg_u32 s54, 0\n"
"s_cbranch_scc1 .Lp2_loop\n"
".Lp2_last:\n"
"s_barrier\n"
"s_bfe_u32 s34, s28, 0x20001\n"
"s_lshl_b32 s34, s34, 2\n"
"s_bfe_u32 s35, s28, 0x20003\n"
"s_add_u32 s30, s34, s35\n"
"s_and_b32 s34, s28, 1\n"
"s_lshl_b32 s34, s34, 3\n"
"s_lshr_b32 s35, s28, 5\n"
"s_add_u32 s31, s34, s35\n"
"s_lshl_b32 s46, s30, 2\n"
"s_add_u32 s46, s46, s29\n"
"s_mov_b32 s12, s44\n"
"s_and_b32 s13, s45, 0xffff\n"
"s_mov_b32 s14, 1409024\n"
"s_mov_b32 s15, 0x20000\n"
"s_add_u32 s36, s50, 57671680\n"
"s_addc_u32 s37, s51, 0\n"
"s_mov_b32 s16, s36\n"
"s_and_b32 s17, s37, 0xffff\n"
"s_mov_b32 s18, 2097152\n"
"s_mov_b32 s19, 0x20000\n"
"v_lshrrev_b32 v9, 2, v0\n"
"v_mul_u32_u24 v9, 2752, v9\n"
"v_and_b32 v10, 3, v0\n"
"v_lshl_add_u32 v9, v10, 4, v9\n"
"s_mul_i32 s47, s31, 192\n"
"s_mul_i32 s34, s29, 44032\n"
"s_add_u32 s47, s47, s34\n"
"s_lshl_b32 s52, s46, 15\n"
"s_cmp_eq_u32 s31, 15\n"
"s_cselect_b32 s47, 0x40000000, s47\n"
"s_cselect_b32 s52, 0x40000000, s52\n"
"s_and_b32 s53, s43, 0x10000\n"
"s_lshl_b32 s45, s29, 10\n"
"s_add_u32 s48, s45, s53\n"
"s_cmp_eq_u32 s29, 0\n"
"s_cbranch_scc1 .Lp2_w0\n"
"s_cmp_eq_u32 s29, 3\n"
"s_cbranch_scc1 .Lp2_w3\n"
"s_add_u32 m0, s45, 131072\n"
"s_add_u32 s58, s47, 0\n"
"s_waitcnt lgkmcnt(0)\n"
"v_mfma_f32_32x32x16_bf16 v[32:47], a[64:67], a[0:3], 0\n"
"buffer_load_dwordx4 v9, s[12:15], s58 offen lds\n"
"v_mfma_f32_32x32x16_bf16 v[32:47], a[68:71], a[4:7], v[32:47]\n"
"s_add_u32 m0, s45, 135168\n"
"s_add_u32 s58, s47, 176128\n"
"v_mfma_f32_32x32x16_bf16 v[32:47], a[72:75], a[8:11], v[32:47]\n"
"buffer_load_dwordx4 v9, s[12:15], s58 offen lds\n"
"v_mfma_f32_32x32x16_bf16 v[32:47], a[76:79], a[12:15], v[32:47]\n"
"s_add_u32 m0, s45, 139264\n"
"s_add_u32 s58, s47, 352256\n"
"v_mfma_f32_32x32x16_bf16 v[32:47], a[80:83], a[16:19], v[32:47]\n"
"ds_read_b128 a[96:99], v8 offset:8192\n"
"buffer_load_dwordx4 v9, s[12:15], s58 offen lds\n"
"v_mfma_f32_32x32x16_bf16 v[32:47], a[84:87], a[20:23], v[32:47]\n"
"ds_read_b128 a[100:103], v8 offset:9216\n"
"s_add_u32 m0, s45, 143360\n"
"s_add_u32 s58, s47, 528384\n"
"v_mfma_f32_32x32x16_bf16 v[32:47], a[88:91], a[24:27], v[32:47]\n"
"ds_read_b128 a[104:107], v8 offset:10240\n"
"buffer_load_dwordx4 v9, s[12:15], s58 offen lds\n"
"v_mfma_f32_32x32x16_bf16 v[32:47], a[92:95], a[28:31], v[32:47]\n"
"ds_read_b128 a[108:111], v8 offset:11264\n"
"s_add_u32 m0, s45, 147456\n"
"s_add_u32 s58, s47, 704512\n"
"v_mfma_f32_32x32x16_bf16 v[48:63], a[64:67], a[32:35], 0\n"
"ds_read_b128 a[112:115], v8 offset:12288\n"
"buffer_load_dwordx4 v9, s[12:15], s58 offen lds\n"
"v_mfma_f32_32x32x16_bf16 v[48:63], a[68:71], a[36:39], v[48:63]\n"
"ds_read_b128 a[116:119], v8 offset:13312\n"
"v_exp_f32 v32, v32\n"
"s_add_u32 m0, s45, 151552\n"
"s_add_u32 s58, s47, 880640\n"
"v_mfma_f32_32x32x16_bf16 v[48:63], a[72:75], a[40:43], v[48:63]\n"
"ds_read_b128 a[120:123], v8 offset:14336\n"
"buffer_load_dwordx4 v9, s[12:15], s58 offen lds\n"
"v_exp_f32 v33, v33\n"
"v_mfma_f32_32x32x16_bf16 v[48:63], a[76:79], a[44:47], v[48:63]\n"
"ds_read_b128 a[124:127], v8 offset:15360\n"
"v_exp_f32 v34, v34\n"
"s_add_u32 m0, s45, 155648\n"
"s_add_u32 s58, s47, 1056768\n"
"v_mfma_f32_32x32x16_bf16 v[48:63], a[80:83], a[48:51], v[48:63]\n"
"buffer_load_dwordx4 v9, s[12:15], s58 offen lds\n"
"v_exp_f32 v35, v35\n"
"v_cndmask_b32 v32, 0, v32, s[64:65]\n"
"v_cndmask_b32 v33, 0, v33, s[66:67]\n"
"v_mfma_f32_32x32x16_bf16 v[48:63], a[84:87], a[52:55], v[48:63]\n"
"v_cvt_pk_bf16_f32 v96, v32, v33\n"
"v_add_f32 v12, v12, v32\n"
"v_add_f32 v13, v13, v33\n"
"s_add_u32 m0, s45, 159744\n"
"s_add_u32 s58, s47, 1232896\n"
"v_mfma_f32_32x32x16_bf16 v[48:63], a[88:91], a[56:59], v[48:63]\n"
"ds_read_b128 a[128:131], v8 offset:32768\n"
"buffer_load_dwordx4 v9, s[12:15], s58 offen lds\n"
"v_exp_f32 v36, v36\n"
"v_mfma_f32_32x32x16_bf16 v[48:63], a[92:95], a[60:63], v[48:63]\n"
"v_exp_f32 v37, v37\n"
"v_cndmask_b32 v34, 0, v34, s[68:69]\n"
"v_cndmask_b32 v35, 0, v35, s[70:71]\n"
"v_cvt_pk_bf16_f32 v97, v34, v35\n"
"s_waitcnt lgkmcnt(1)\n"
"v_mfma_f32_32x32x16_bf16 v[80:95], a[96:99], a[32:35], 0\n"
"ds_read_b128 a[132:135], v8 offset:33792\n"
"buffer_load_dwordx4 a[192:195], v1, s[16:19], s52 offen offset:0 sc1\n"
"v_add_f32 v12, v12, v34\n"
"v_add_f32 v13, v13, v35\n"
"v_mfma_f32_32x32x16_bf16 v[80:95], a[100:103], a[36:39], v[80:95]\n"
"v_exp_f32 v38, v38\n"
"v_exp_f32 v39, v39\n"
"v_cndmask_b32 v36, 0, v36, s[72:73]\n"
"v_cndmask_b32 v37, 0, v37, s[74:75]\n"
"v_mfma_f32_32x32x16_bf16 v[80:95], a[104:107], a[40:43], v[80:95]\n"
"ds_read_b128 a[136:139], v8 offset:34816\n"
"v_cvt_pk_bf16_f32 v98, v36, v37\n"
"v_add_f32 v12, v12, v36\n"
"v_add_f32 v13, v13, v37\n"
"v_exp_f32 v40, v40\n"
"v_mfma_f32_32x32x16_bf16 v[80:95], a[108:111], a[44:47], v[80:95]\n"
"buffer_load_dwordx4 a[196:199], v1, s[16:19], s52 offen offset:1024 sc1\n"
"v_exp_f32 v41, v41\n"
"v_cndmask_b32 v38, 0, v38, s[76:77]\n"
"v_cndmask_b32 v39, 0, v39, s[78:79]\n"
"v_mfma_f32_32x32x16_bf16 v[80:95], a[112:115], a[48:51], v[80:95]\n"
"ds_read_b128 a[140:143], v8 offset:35840\n"
"v_cvt_pk_bf16_f32 v99, v38, v39\n"
"v_add_f32 v12, v12, v38\n"
"v_add_f32 v13, v13, v39\n"
"v_exp_f32 v42, v42\n"
"v_mfma_f32_32x32x16_bf16 v[80:95], a[116:119], a[52:55], v[80:95]\n"
"v_exp_f32 v43, v43\n"
"v_cndmask_b32 v40, 0, v40, s[80:81]\n"
"v_cndmask_b32 v41, 0, v41, s[82:83]\n"
"v_cvt_pk_bf16_f32 v100, v40, v41\n"
"v_add_f32 v12, v12, v40\n"
"v_mfma_f32_32x32x16_bf16 v[80:95], a[120:123], a[56:59], v[80:95]\n"
"ds_read_b128 a[144:147], v8 offset:36864\n"
"buffer_load_dwordx4 a[200:203], v1, s[16:19], s52 offen offset:2048 sc1\n"
"v_add_f32 v13, v13, v41\n"
"v_exp_f32 v44, v44\n"
"v_mfma_f32_32x32x16_bf16 v[80:95], a[124:127], a[60:63], v[80:95]\n"
"v_exp_f32 v45, v45\n"
"v_cndmask_b32 v42, 0, v42, s[84:85]\n"
"v_cndmask_b32 v43, 0, v43, s[86:87]\n"
"v_cvt_pk_bf16_f32 v101, v42, v43\n"
"v_add_f32 v12, v12, v42\n"
"ds_read_b128 a[148:151], v8 offset:37888\n"
"v_add_f32 v13, v13, v43\n"
"v_exp_f32 v46, v46\n"
"v_exp_f32 v47, v47\n"
"v_cndmask_b32 v44, 0, v44, s[88:89]\n"
"v_cndmask_b32 v45, 0, v45, s[90:91]\n"
"v_cvt_pk_bf16_f32 v102, v44, v45\n"
"v_add_f32 v12, v12, v44\n"
"v_add_f32 v13, v13, v45\n"
"v_exp_f32 v48, v48\n"
"v_exp_f32 v49, v49\n"
"v_cndmask_b32 v46, 0, v46, s[92:93]\n"
"v_cndmask_b32 v47, 0, v47, s[94:95]\n"
"v_cvt_pk_bf16_f32 v103, v46, v47\n"
"v_add_f32 v12, v12, v46\n"
"v_add_f32 v13, v13, v47\n"
"v_exp_f32 v50, v50\n"
"v_exp_f32 v51, v51\n"
"v_cvt_pk_bf16_f32 v112, v48, v49\n"
"v_add_f32 v14, v14, v48\n"
"v_add_f32 v15, v15, v49\n"
"v_exp_f32 v52, v52\n"
"v_exp_f32 v53, v53\n"
"v_cvt_pk_bf16_f32 v113, v50, v51\n"
"v_add_f32 v14, v14, v50\n"
"v_add_f32 v15, v15, v51\n"
"v_exp_f32 v54, v54\n"
"v_exp_f32 v55, v55\n"
"v_cvt_pk_bf16_f32 v114, v52, v53\n"
"v_add_f32 v14, v14, v52\n"
"v_add_f32 v15, v15, v53\n"
"v_exp_f32 v56, v56\n"
"v_exp_f32 v57, v57\n"
"v_cvt_pk_bf16_f32 v115, v54, v55\n"
"v_add_f32 v14, v14, v54\n"
"v_add_f32 v15, v15, v55\n"
"v_exp_f32 v58, v58\n"
"v_exp_f32 v59, v59\n"
"v_cvt_pk_bf16_f32 v116, v56, v57\n"
"v_add_f32 v14, v14, v56\n"
"v_add_f32 v15, v15, v57\n"
"v_exp_f32 v60, v60\n"
"v_exp_f32 v61, v61\n"
"v_cvt_pk_bf16_f32 v117, v58, v59\n"
"v_add_f32 v14, v14, v58\n"
"v_add_f32 v15, v15, v59\n"
"v_exp_f32 v62, v62\n"
"v_exp_f32 v63, v63\n"
"v_cvt_pk_bf16_f32 v118, v60, v61\n"
"v_add_f32 v14, v14, v60\n"
"v_add_f32 v15, v15, v61\n"
"buffer_load_dwordx4 a[204:207], v1, s[16:19], s52 offen offset:3072 sc1\n"
"ds_read_b128 a[152:155], v8 offset:38912\n"
"v_exp_f32 v80, v80\n"
"v_exp_f32 v81, v81\n"
"v_cvt_pk_bf16_f32 v119, v62, v63\n"
"v_add_f32 v14, v14, v62\n"
"v_add_f32 v15, v15, v63\n"
"v_exp_f32 v82, v82\n"
"v_exp_f32 v83, v83\n"
"v_cndmask_b32 v80, 0, v80, s[64:65]\n"
"v_cndmask_b32 v81, 0, v81, s[66:67]\n"
"v_cvt_pk_bf16_f32 v120, v80, v81\n"
"v_add_f32 v14, v14, v80\n"
"v_add_f32 v15, v15, v81\n"
"v_exp_f32 v84, v84\n"
"v_exp_f32 v85, v85\n"
"v_cndmask_b32 v82, 0, v82, s[68:69]\n"
"v_cndmask_b32 v83, 0, v83, s[70:71]\n"
"v_cvt_pk_bf16_f32 v121, v82, v83\n"
"v_add_f32 v14, v14, v82\n"
"v_add_f32 v15, v15, v83\n"
"v_exp_f32 v86, v86\n"
"v_exp_f32 v87, v87\n"
"v_cndmask_b32 v84, 0, v84, s[72:73]\n"
"v_cndmask_b32 v85, 0, v85, s[74:75]\n"
"v_cvt_pk_bf16_f32 v122, v84, v85\n"
"v_add_f32 v14, v14, v84\n"
"v_add_f32 v15, v15, v85\n"
"v_exp_f32 v88, v88\n"
"v_exp_f32 v89, v89\n"
"v_cndmask_b32 v86, 0, v86, s[76:77]\n"
"v_cndmask_b32 v87, 0, v87, s[78:79]\n"
"v_cvt_pk_bf16_f32 v123, v86, v87\n"
"v_add_f32 v14, v14, v86\n"
"v_add_f32 v15, v15, v87\n"
"v_exp_f32 v90, v90\n"
"v_exp_f32 v91, v91\n"
"v_cndmask_b32 v88, 0, v88, s[80:81]\n"
"v_cndmask_b32 v89, 0, v89, s[82:83]\n"
"v_cvt_pk_bf16_f32 v124, v88, v89\n"
"v_add_f32 v14, v14, v88\n"
"v_add_f32 v15, v15, v89\n"
"v_exp_f32 v92, v92\n"
"v_exp_f32 v93, v93\n"
"v_cndmask_b32 v90, 0, v90, s[84:85]\n"
"v_cndmask_b32 v91, 0, v91, s[86:87]\n"
"v_cvt_pk_bf16_f32 v125, v90, v91\n"
"v_add_f32 v14, v14, v90\n"
"v_add_f32 v15, v15, v91\n"
"v_exp_f32 v94, v94\n"
"v_exp_f32 v95, v95\n"
"v_cndmask_b32 v92, 0, v92, s[88:89]\n"
"v_cndmask_b32 v93, 0, v93, s[90:91]\n"
"v_cvt_pk_bf16_f32 v126, v92, v93\n"
"v_add_f32 v14, v14, v92\n"
"v_add_f32 v15, v15, v93\n"
"v_cndmask_b32 v94, 0, v94, s[92:93]\n"
"v_cndmask_b32 v95, 0, v95, s[94:95]\n"
"v_cvt_pk_bf16_f32 v127, v94, v95\n"
"v_add_f32 v14, v14, v94\n"
"v_add_f32 v15, v15, v95\n"
"s_add_u32 s52, s52, 4096\n"
"ds_read_b128 a[156:159], v8 offset:39936\n"
"buffer_load_dwordx4 a[208:211], v1, s[16:19], s52 offen offset:0 sc1\n"
"ds_read_b128 a[160:163], v8 offset:40960\n"
"s_waitcnt lgkmcnt(5)\n"
"v_mfma_f32_32x32x16_bf16 v[128:143], a[128:131], v[96:99], v[128:143]\n"
"ds_read_b128 a[164:167], v8 offset:41984\n"
"v_mfma_f32_32x32x16_bf16 v[160:175], a[132:135], v[96:99], v[160:175]\n"
"v_mfma_f32_32x32x16_bf16 v[192:207], a[136:139], v[96:99], v[192:207]\n"
"ds_read_b128 a[168:171], v8 offset:43008\n"
"v_mfma_f32_32x32x16_bf16 v[224:239], a[140:143], v[96:99], v[224:239]\n"
"v_mfma_f32_32x32x16_bf16 v[144:159], a[128:131], v[112:115], v[144:159]\n"
"ds_read_b128 a[172:175], v8 offset:44032\n"
"v_mfma_f32_32x32x16_bf16 v[176:191], a[132:135], v[112:115], v[176:191]\n"
"v_mfma_f32_32x32x16_bf16 v[208:223], a[136:139], v[112:115], v[208:223]\n"
"ds_read_b128 a[176:179], v8 offset:45056\n"
"v_mfma_f32_32x32x16_bf16 v[240:255], a[140:143], v[112:115], v[240:255]\n"
"s_waitcnt lgkmcnt(5)\n"
"v_mfma_f32_32x32x16_bf16 v[128:143], a[144:147], v[100:103], v[128:143]\n"
"ds_read_b128 a[180:183], v8 offset:46080\n"
"v_mfma_f32_32x32x16_bf16 v[160:175], a[148:151], v[100:103], v[160:175]\n"
"v_mfma_f32_32x32x16_bf16 v[192:207], a[152:155], v[100:103], v[192:207]\n"
"ds_read_b128 a[184:187], v8 offset:47104\n"
"v_mfma_f32_32x32x16_bf16 v[224:239], a[156:159], v[100:103], v[224:239]\n"
"v_mfma_f32_32x32x16_bf16 v[144:159], a[144:147], v[116:119], v[144:159]\n"
"ds_read_b128 a[188:191], v8 offset:48128\n"
"v_mfma_f32_32x32x16_bf16 v[176:191], a[148:151], v[116:119], v[176:191]\n"
"v_mfma_f32_32x32x16_bf16 v[208:223], a[152:155], v[116:119], v[208:223]\n"
"s_xor_b32 s42, s43, 0x10000\n"
"v_add_u32 v8, s42, v1\n"
"v_mfma_f32_32x32x16_bf16 v[240:255], a[156:159], v[116:119], v[240:255]\n"
"s_waitcnt lgkmcnt(4)\n"
"s_waitcnt lgkmcnt(0)\n"
"s_barrier\n"
"v_mfma_f32_32x32x16_bf16 v[144:159], a[160:163], v[120:123], v[144:159]\n"
"v_mfma_f32_32x32x16_bf16 v[176:191], a[164:167], v[120:123], v[176:191]\n"
"buffer_load_dwordx4 a[212:215], v1, s[16:19], s52 offen offset:1024 sc1\n"
"v_mfma_f32_32x32x16_bf16 v[208:223], a[168:171], v[120:123], v[208:223]\n"
"buffer_load_dwordx4 a[216:219], v1, s[16:19], s52 offen offset:2048 sc1\n"
"v_mfma_f32_32x32x16_bf16 v[240:255], a[172:175], v[120:123], v[240:255]\n"
"buffer_load_dwordx4 a[220:223], v1, s[16:19], s52 offen offset:3072 sc1\n"
"s_add_u32 s52, s52, 4096\n"
"buffer_load_dwordx4 a[224:227], v1, s[16:19], s52 offen offset:0 sc1\n"
"buffer_load_dwordx4 a[228:231], v1, s[16:19], s52 offen offset:1024 sc1\n"
"buffer_load_dwordx4 a[232:235], v1, s[16:19], s52 offen offset:2048 sc1\n"
"buffer_load_dwordx4 a[236:239], v1, s[16:19], s52 offen offset:3072 sc1\n"
"s_add_u32 s52, s52, 4096\n"
"v_mfma_f32_32x32x16_bf16 v[144:159], a[176:179], v[124:127], v[144:159]\n"
"buffer_load_dwordx4 a[240:243], v1, s[16:19], s52 offen offset:0 sc1\n"
"v_mfma_f32_32x32x16_bf16 v[176:191], a[180:183], v[124:127], v[176:191]\n"
"buffer_load_dwordx4 a[244:247], v1, s[16:19], s52 offen offset:1024 sc1\n"
"v_mfma_f32_32x32x16_bf16 v[208:223], a[184:187], v[124:127], v[208:223]\n"
"buffer_load_dwordx4 a[248:251], v1, s[16:19], s52 offen offset:2048 sc1\n"
"v_mfma_f32_32x32x16_bf16 v[240:255], a[188:191], v[124:127], v[240:255]\n"
"buffer_load_dwordx4 a[252:255], v1, s[16:19], s52 offen offset:3072 sc1\n"
"s_mov_b32 s43, s42\n"
"s_branch .Lp2_end\n"
".Lp2_w0:\n"
"s_add_u32 m0, s45, 131072\n"
"s_add_u32 s58, s47, 0\n"
"buffer_load_dwordx4 v9, s[12:15], s58 offen lds\n"
"s_add_u32 m0, s45, 135168\n"
"s_add_u32 s58, s47, 176128\n"
"buffer_load_dwordx4 v9, s[12:15], s58 offen lds\n"
"s_add_u32 m0, s45, 139264\n"
"s_add_u32 s58, s47, 352256\n"
"buffer_load_dwordx4 v9, s[12:15], s58 offen lds\n"
"s_add_u32 m0, s45, 143360\n"
"s_add_u32 s58, s47, 528384\n"
"buffer_load_dwordx4 v9, s[12:15], s58 offen lds\n"
"s_add_u32 m0, s45, 147456\n"
"s_add_u32 s58, s47, 704512\n"
"buffer_load_dwordx4 v9, s[12:15], s58 offen lds\n"
"s_add_u32 m0, s45, 151552\n"
"s_add_u32 s58, s47, 880640\n"
"buffer_load_dwordx4 v9, s[12:15], s58 offen lds\n"
"s_add_u32 m0, s45, 155648\n"
"s_add_u32 s58, s47, 1056768\n"
"buffer_load_dwordx4 v9, s[12:15], s58 offen lds\n"
"s_add_u32 m0, s45, 159744\n"
"s_add_u32 s58, s47, 1232896\n"
"buffer_load_dwordx4 v9, s[12:15], s58 offen lds\n"
"buffer_load_dwordx4 a[192:195], v1, s[16:19], s52 offen offset:0 sc1\n"
"buffer_load_dwordx4 a[196:199], v1, s[16:19], s52 offen offset:1024 sc1\n"
"buffer_load_dwordx4 a[200:203], v1, s[16:19], s52 offen offset:2048 sc1\n"
"buffer_load_dwordx4 a[204:207], v1, s[16:19], s52 offen offset:3072 sc1\n"
"s_add_u32 s52, s52, 4096\n"
"buffer_load_dwordx4 a[208:211], v1, s[16:19], s52 offen offset:0 sc1\n"
"s_xor_b32 s42, s43, 0x10000\n"
"v_add_u32 v8, s42, v1\n"
"s_waitcnt lgkmcnt(0)\n"
"s_barrier\n"
"buffer_load_dwordx4 a[212:215], v1, s[16:19], s52 offen offset:1024 sc1\n"
"buffer_load_dwordx4 a[216:219], v1, s[16:19], s52 offen offset:2048 sc1\n"
"buffer_load_dwordx4 a[220:223], v1, s[16:19], s52 offen offset:3072 sc1\n"
"s_add_u32 s52, s52, 4096\n"
"buffer_load_dwordx4 a[224:227], v1, s[16:19], s52 offen offset:0 sc1\n"
"buffer_load_dwordx4 a[228:231], v1, s[16:19], s52 offen offset:1024 sc1\n"
"buffer_load_dwordx4 a[232:235], v1, s[16:19], s52 offen offset:2048 sc1\n"
"buffer_load_dwordx4 a[236:239], v1, s[16:19], s52 offen offset:3072 sc1\n"
"s_add_u32 s52, s52, 4096\n"
"buffer_load_dwordx4 a[240:243], v1, s[16:19], s52 offen offset:0 sc1\n"
"buffer_load_dwordx4 a[244:247], v1, s[16:19], s52 offen offset:1024 sc1\n"
"buffer_load_dwordx4 a[248:251], v1, s[16:19], s52 offen offset:2048 sc1\n"
"buffer_load_dwordx4 a[252:255], v1, s[16:19], s52 offen offset:3072 sc1\n"
"s_mov_b32 s43, s42\n"
"s_branch .Lp2_end\n"
".Lp2_w3:\n"
"s_add_u32 m0, s45, 131072\n"
"s_add_u32 s58, s47, 0\n"
"s_waitcnt lgkmcnt(0)\n"
"v_mfma_f32_32x32x16_bf16 v[32:47], a[64:67], a[0:3], 0\n"
"buffer_load_dwordx4 v9, s[12:15], s58 offen lds\n"
"v_mfma_f32_32x32x16_bf16 v[32:47], a[68:71], a[4:7], v[32:47]\n"
"s_add_u32 m0, s45, 135168\n"
"s_add_u32 s58, s47, 176128\n"
"v_mfma_f32_32x32x16_bf16 v[32:47], a[72:75], a[8:11], v[32:47]\n"
"buffer_load_dwordx4 v9, s[12:15], s58 offen lds\n"
"v_mfma_f32_32x32x16_bf16 v[32:47], a[76:79], a[12:15], v[32:47]\n"
"s_add_u32 m0, s45, 139264\n"
"s_add_u32 s58, s47, 352256\n"
"v_mfma_f32_32x32x16_bf16 v[32:47], a[80:83], a[16:19], v[32:47]\n"
"ds_read_b128 a[96:99], v8 offset:8192\n"
"buffer_load_dwordx4 v9, s[12:15], s58 offen lds\n"
"v_mfma_f32_32x32x16_bf16 v[32:47], a[84:87], a[20:23], v[32:47]\n"
"ds_read_b128 a[100:103], v8 offset:9216\n"
"s_add_u32 m0, s45, 143360\n"
"s_add_u32 s58, s47, 528384\n"
"v_mfma_f32_32x32x16_bf16 v[32:47], a[88:91], a[24:27], v[32:47]\n"
"ds_read_b128 a[104:107], v8 offset:10240\n"
"buffer_load_dwordx4 v9, s[12:15], s58 offen lds\n"
"v_mfma_f32_32x32x16_bf16 v[32:47], a[92:95], a[28:31], v[32:47]\n"
"ds_read_b128 a[108:111], v8 offset:11264\n"
"s_add_u32 m0, s45, 147456\n"
"s_add_u32 s58, s47, 704512\n"
"v_mfma_f32_32x32x16_bf16 v[48:63], a[64:67], a[32:35], 0\n"
"ds_read_b128 a[112:115], v8 offset:12288\n"
"buffer_load_dwordx4 v9, s[12:15], s58 offen lds\n"
"v_mfma_f32_32x32x16_bf16 v[48:63], a[68:71], a[36:39], v[48:63]\n"
"ds_read_b128 a[116:119], v8 offset:13312\n"
"v_exp_f32 v32, v32\n"
"s_add_u32 m0, s45, 151552\n"
"s_add_u32 s58, s47, 880640\n"
"v_mfma_f32_32x32x16_bf16 v[48:63], a[72:75], a[40:43], v[48:63]\n"
"ds_read_b128 a[120:123], v8 offset:14336\n"
"buffer_load_dwordx4 v9, s[12:15], s58 offen lds\n"
"v_exp_f32 v33, v33\n"
"v_mfma_f32_32x32x16_bf16 v[48:63], a[76:79], a[44:47], v[48:63]\n"
"ds_read_b128 a[124:127], v8 offset:15360\n"
"v_exp_f32 v34, v34\n"
"s_add_u32 m0, s45, 155648\n"
"s_add_u32 s58, s47, 1056768\n"
"v_mfma_f32_32x32x16_bf16 v[48:63], a[80:83], a[48:51], v[48:63]\n"
"buffer_load_dwordx4 v9, s[12:15], s58 offen lds\n"
"v_exp_f32 v35, v35\n"
"v_cvt_pk_bf16_f32 v96, v32, v33\n"
"v_add_f32 v12, v12, v32\n"
"v_mfma_f32_32x32x16_bf16 v[48:63], a[84:87], a[52:55], v[48:63]\n"
"v_add_f32 v13, v13, v33\n"
"v_exp_f32 v36, v36\n"
"s_add_u32 m0, s45, 159744\n"
"s_add_u32 s58, s47, 1232896\n"
"v_mfma_f32_32x32x16_bf16 v[48:63], a[88:91], a[56:59], v[48:63]\n"
"ds_read_b128 a[128:131], v8 offset:32768\n"
"buffer_load_dwordx4 v9, s[12:15], s58 offen lds\n"
"v_exp_f32 v37, v37\n"
"v_cvt_pk_bf16_f32 v97, v34, v35\n"
"v_mfma_f32_32x32x16_bf16 v[48:63], a[92:95], a[60:63], v[48:63]\n"
"v_add_f32 v12, v12, v34\n"
"v_add_f32 v13, v13, v35\n"
"v_exp_f32 v38, v38\n"
"s_waitcnt lgkmcnt(1)\n"
"v_mfma_f32_32x32x16_bf16 v[64:79], a[96:99], a[0:3], 0\n"
"ds_read_b128 a[132:135], v8 offset:33792\n"
"buffer_load_dwordx4 a[192:195], v1, s[16:19], s52 offen offset:0 sc1\n"
"v_exp_f32 v39, v39\n"
"v_cvt_pk_bf16_f32 v98, v36, v37\n"
"v_mfma_f32_32x32x16_bf16 v[64:79], a[100:103], a[4:7], v[64:79]\n"
"v_add_f32 v12, v12, v36\n"
"v_add_f32 v13, v13, v37\n"
"v_exp_f32 v40, v40\n"
"v_exp_f32 v41, v41\n"
"v_mfma_f32_32x32x16_bf16 v[64:79], a[104:107], a[8:11], v[64:79]\n"
"ds_read_b128 a[136:139], v8 offset:34816\n"
"v_cvt_pk_bf16_f32 v99, v38, v39\n"
"v_add_f32 v12, v12, v38\n"
"v_add_f32 v13, v13, v39\n"
"v_exp_f32 v42, v42\n"
"v_mfma_f32_32x32x16_bf16 v[64:79], a[108:111], a[12:15], v[64:79]\n"
"buffer_load_dwordx4 a[196:199], v1, s[16:19], s52 offen offset:1024 sc1\n"
"v_exp_f32 v43, v43\n"
"v_cvt_pk_bf16_f32 v100, v40, v41\n"
"v_add_f32 v12, v12, v40\n"
"v_mfma_f32_32x32x16_bf16 v[64:79], a[112:115], a[16:19], v[64:79]\n"
"ds_read_b128 a[140:143], v8 offset:35840\n"
"v_add_f32 v13, v13, v41\n"
"v_exp_f32 v44, v44\n"
"v_exp_f32 v45, v45\n"
"v_mfma_f32_32x32x16_bf16 v[64:79], a[116:119], a[20:23], v[64:79]\n"
"v_cvt_pk_bf16_f32 v101, v42, v43\n"
"v_add_f32 v12, v12, v42\n"
"v_add_f32 v13, v13, v43\n"
"v_exp_f32 v46, v46\n"
"v_mfma_f32_32x32x16_bf16 v[64:79], a[120:123], a[24:27], v[64:79]\n"
"ds_read_b128 a[144:147], v8 offset:36864\n"
"buffer_load_dwordx4 a[200:203], v1, s[16:19], s52 offen offset:2048 sc1\n"
"v_exp_f32 v47, v47\n"
"v_cvt_pk_bf16_f32 v102, v44, v45\n"
"v_mfma_f32_32x32x16_bf16 v[64:79], a[124:127], a[28:31], v[64:79]\n"
"v_add_f32 v12, v12, v44\n"
"v_add_f32 v13, v13, v45\n"
"v_exp_f32 v48, v48\n"
"v_exp_f32 v49, v49\n"
"v_mfma_f32_32x32x16_bf16 v[80:95], a[96:99], a[32:35], 0\n"
"ds_read_b128 a[148:151], v8 offset:37888\n"
"v_cvt_pk_bf16_f32 v103, v46, v47\n"
"v_add_f32 v12, v12, v46\n"
"v_add_f32 v13, v13, v47\n"
"v_exp_f32 v50, v50\n"
"v_mfma_f32_32x32x16_bf16 v[80:95], a[100:103], a[36:39], v[80:95]\n"
"buffer_load_dwordx4 a[204:207], v1, s[16:19], s52 offen offset:3072 sc1\n"
"v_exp_f32 v51, v51\n"
"v_cvt_pk_bf16_f32 v112, v48, v49\n"
"v_add_f32 v14, v14, v48\n"
"v_mfma_f32_32x32x16_bf16 v[80:95], a[104:107], a[40:43], v[80:95]\n"
"ds_read_b128 a[152:155], v8 offset:38912\n"
"v_add_f32 v15, v15, v49\n"
"v_exp_f32 v52, v52\n"
"v_exp_f32 v53, v53\n"
"v_mfma_f32_32x32x16_bf16 v[80:95], a[108:111], a[44:47], v[80:95]\n"
"v_cvt_pk_bf16_f32 v113, v50, v51\n"
"v_add_f32 v14, v14, v50\n"
"v_add_f32 v15, v15, v51\n"
"v_exp_f32 v54, v54\n"
"s_add_u32 s52, s52, 4096\n"
"v_mfma_f32_32x32x16_bf16 v[80:95], a[112:115], a[48:51], v[80:95]\n"
"ds_read_b128 a[156:159], v8 offset:39936\n"
"buffer_load_dwordx4 a[208:211], v1, s[16:19], s52 offen offset:0 sc1\n"
"v_exp_f32 v55, v55\n"
"v_cvt_pk_bf16_f32 v114, v52, v53\n"
"v_mfma_f32_32x32x16_bf16 v[80:95], a[116:119], a[52:55], v[80:95]\n"
"v_add_f32 v14, v14, v52\n"
"v_add_f32 v15, v15, v53\n"
"v_exp_f32 v56, v56\n"
"v_exp_f32 v57, v57\n"
"v_mfma_f32_32x32x16_bf16 v[80:95], a[120:123], a[56:59], v[80:95]\n"
"ds_read_b128 a[160:163], v8 offset:40960\n"
"v_cvt_pk_bf16_f32 v115, v54, v55\n"
"v_add_f32 v14, v14, v54\n"
"v_add_f32 v15, v15, v55\n"
"v_exp_f32 v58, v58\n"
"v_mfma_f32_32x32x16_bf16 v[80:95], a[124:127], a[60:63], v[80:95]\n"
"v_exp_f32 v59, v59\n"
"v_cvt_pk_bf16_f32 v116, v56, v57\n"
"v_add_f32 v14, v14, v56\n"
"v_add_f32 v15, v15, v57\n"
"s_waitcnt lgkmcnt(5)\n"
"v_mfma_f32_32x32x16_bf16 v[128:143], a[128:131], v[96:99], v[128:143]\n"
"ds_read_b128 a[164:167], v8 offset:41984\n"
"v_exp_f32 v60, v60\n"
"v_exp_f32 v61, v61\n"
"v_cvt_pk_bf16_f32 v117, v58, v59\n"
"v_mfma_f32_32x32x16_bf16 v[160:175], a[132:135], v[96:99], v[160:175]\n"
"v_add_f32 v14, v14, v58\n"
"v_add_f32 v15, v15, v59\n"
"v_exp_f32 v62, v62\n"
"v_exp_f32 v63, v63\n"
"v_mfma_f32_32x32x16_bf16 v[192:207], a[136:139], v[96:99], v[192:207]\n"
"ds_read_b128 a[168:171], v8 offset:43008\n"
"v_cvt_pk_bf16_f32 v118, v60, v61\n"
"v_add_f32 v14, v14, v60\n"
"v_add_f32 v15, v15, v61\n"
"v_exp_f32 v64, v64\n"
"v_mfma_f32_32x32x16_bf16 v[224:239], a[140:143], v[96:99], v[224:239]\n"
"v_exp_f32 v65, v65\n"
"v_cvt_pk_bf16_f32 v119, v62, v63\n"
"v_add_f32 v14, v14, v62\n"
"v_add_f32 v15, v15, v63\n"
"v_mfma_f32_32x32x16_bf16 v[144:159], a[128:131], v[112:115], v[144:159]\n"
"ds_read_b128 a[172:175], v8 offset:44032\n"
"v_exp_f32 v66, v66\n"
"v_exp_f32 v67, v67\n"
"v_cvt_pk_bf16_f32 v104, v64, v65\n"
"v_mfma_f32_32x32x16_bf16 v[176:191], a[132:135], v[112:115], v[176:191]\n"
"v_add_f32 v12, v12, v64\n"
"v_add_f32 v13, v13, v65\n"
"v_exp_f32 v68, v68\n"
"v_exp_f32 v69, v69\n"
"v_mfma_f32_32x32x16_bf16 v[208:223], a[136:139], v[112:115], v[208:223]\n"
"ds_read_b128 a[176:179], v8 offset:45056\n"
"v_cvt_pk_bf16_f32 v105, v66, v67\n"
"v_add_f32 v12, v12, v66\n"
"v_add_f32 v13, v13, v67\n"
"v_exp_f32 v70, v70\n"
"v_mfma_f32_32x32x16_bf16 v[240:255], a[140:143], v[112:115], v[240:255]\n"
"v_exp_f32 v71, v71\n"
"v_cvt_pk_bf16_f32 v106, v68, v69\n"
"v_add_f32 v12, v12, v68\n"
"v_add_f32 v13, v13, v69\n"
"s_waitcnt lgkmcnt(5)\n"
"v_mfma_f32_32x32x16_bf16 v[128:143], a[144:147], v[100:103], v[128:143]\n"
"ds_read_b128 a[180:183], v8 offset:46080\n"
"v_exp_f32 v80, v80\n"
"v_exp_f32 v81, v81\n"
"v_cvt_pk_bf16_f32 v107, v70, v71\n"
"v_mfma_f32_32x32x16_bf16 v[160:175], a[148:151], v[100:103], v[160:175]\n"
"v_add_f32 v12, v12, v70\n"
"v_add_f32 v13, v13, v71\n"
"v_exp_f32 v82, v82\n"
"v_exp_f32 v83, v83\n"
"v_mfma_f32_32x32x16_bf16 v[192:207], a[152:155], v[100:103], v[192:207]\n"
"ds_read_b128 a[184:187], v8 offset:47104\n"
"v_cvt_pk_bf16_f32 v120, v80, v81\n"
"v_add_f32 v14, v14, v80\n"
"v_add_f32 v15, v15, v81\n"
"v_exp_f32 v84, v84\n"
"v_mfma_f32_32x32x16_bf16 v[224:239], a[156:159], v[100:103], v[224:239]\n"
"v_exp_f32 v85, v85\n"
"v_cvt_pk_bf16_f32 v121, v82, v83\n"
"v_add_f32 v14, v14, v82\n"
"v_add_f32 v15, v15, v83\n"
"v_mfma_f32_32x32x16_bf16 v[144:159], a[144:147], v[116:119], v[144:159]\n"
"ds_read_b128 a[188:191], v8 offset:48128\n"
"v_exp_f32 v86, v86\n"
"v_exp_f32 v87, v87\n"
"v_cvt_pk_bf16_f32 v122, v84, v85\n"
"v_mfma_f32_32x32x16_bf16 v[176:191], a[148:151], v[116:119], v[176:191]\n"
"v_add_f32 v14, v14, v84\n"
"v_add_f32 v15, v15, v85\n"
"v_exp_f32 v72, v72\n"
"v_exp_f32 v73, v73\n"
"v_mfma_f32_32x32x16_bf16 v[208:223], a[152:155], v[116:119], v[208:223]\n"
"s_xor_b32 s42, s43, 0x10000\n"
"v_add_u32 v8, s42, v1\n"
"v_cvt_pk_bf16_f32 v123, v86, v87\n"
"v_add_f32 v14, v14, v86\n"
"v_add_f32 v15, v15, v87\n"
"v_mfma_f32_32x32x16_bf16 v[240:255], a[156:159], v[116:119], v[240:255]\n"
"v_exp_f32 v74, v74\n"
"v_exp_f32 v75, v75\n"
"v_cvt_pk_bf16_f32 v108, v72, v73\n"
"s_waitcnt lgkmcnt(4)\n"
"v_mfma_f32_32x32x16_bf16 v[128:143], a[160:163], v[104:107], v[128:143]\n"
"v_add_f32 v12, v12, v72\n"
"v_add_f32 v13, v13, v73\n"
"v_exp_f32 v76, v76\n"
"v_exp_f32 v77, v77\n"
"v_mfma_f32_32x32x16_bf16 v[160:175], a[164:167], v[104:107], v[160:175]\n"
"v_cvt_pk_bf16_f32 v109, v74, v75\n"
"v_add_f32 v12, v12, v74\n"
"v_add_f32 v13, v13, v75\n"
"v_exp_f32 v78, v78\n"
"v_mfma_f32_32x32x16_bf16 v[192:207], a[168:171], v[104:107], v[192:207]\n"
"v_exp_f32 v79, v79\n"
"v_cvt_pk_bf16_f32 v110, v76, v77\n"
"v_add_f32 v12, v12, v76\n"
"v_add_f32 v13, v13, v77\n"
"v_mfma_f32_32x32x16_bf16 v[224:239], a[172:175], v[104:107], v[224:239]\n"
"v_exp_f32 v88, v88\n"
"v_exp_f32 v89, v89\n"
"s_waitcnt lgkmcnt(0)\n"
"s_barrier\n"
"v_mfma_f32_32x32x16_bf16 v[144:159], a[160:163], v[120:123], v[144:159]\n"
"v_cvt_pk_bf16_f32 v111, v78, v79\n"
"v_add_f32 v12, v12, v78\n"
"v_add_f32 v13, v13, v79\n"
"v_exp_f32 v90, v90\n"
"v_mfma_f32_32x32x16_bf16 v[176:191], a[164:167], v[120:123], v[176:191]\n"
"buffer_load_dwordx4 a[212:215], v1, s[16:19], s52 offen offset:1024 sc1\n"
"v_exp_f32 v91, v91\n"
"v_cvt_pk_bf16_f32 v124, v88, v89\n"
"v_add_f32 v14, v14, v88\n"
"v_mfma_f32_32x32x16_bf16 v[208:223], a[168:171], v[120:123], v[208:223]\n"
"buffer_load_dwordx4 a[216:219], v1, s[16:19], s52 offen offset:2048 sc1\n"
"v_add_f32 v15, v15, v89\n"
"v_exp_f32 v92, v92\n"
"v_mfma_f32_32x32x16_bf16 v[240:255], a[172:175], v[120:123], v[240:255]\n"
"buffer_load_dwordx4 a[220:223], v1, s[16:19], s52 offen offset:3072 sc1\n"
"v_exp_f32 v93, v93\n"
"v_cvt_pk_bf16_f32 v125, v90, v91\n"
"s_add_u32 s52, s52, 4096\n"
"v_mfma_f32_32x32x16_bf16 v[128:143], a[176:179], v[108:111], v[128:143]\n"
"buffer_load_dwordx4 a[224:227], v1, s[16:19], s52 offen offset:0 sc1\n"
"v_add_f32 v14, v14, v90\n"
"v_add_f32 v15, v15, v91\n"
"v_exp_f32 v94, v94\n"
"v_mfma_f32_32x32x16_bf16 v[160:175], a[180:183], v[108:111], v[160:175]\n"
"buffer_load_dwordx4 a[228:231], v1, s[16:19], s52 offen offset:1024 sc1\n"
"v_exp_f32 v95, v95\n"
"v_cvt_pk_bf16_f32 v126, v92, v93\n"
"v_add_f32 v14, v14, v92\n"
"v_mfma_f32_32x32x16_bf16 v[192:207], a[184:187], v[108:111], v[192:207]\n"
"buffer_load_dwordx4 a[232:235], v1, s[16:19], s52 offen offset:2048 sc1\n"
"v_add_f32 v15, v15, v93\n"
"v_cvt_pk_bf16_f32 v127, v94, v95\n"
"v_add_f32 v14, v14, v94\n"
"v_add_f32 v15, v15, v95\n"
"v_mfma_f32_32x32x16_bf16 v[224:239], a[188:191], v[108:111], v[224:239]\n"
"buffer_load_dwordx4 a[236:239], v1, s[16:19], s52 offen offset:3072 sc1\n"
"s_add_u32 s52, s52, 4096\n"
"v_mfma_f32_32x32x16_bf16 v[144:159], a[176:179], v[124:127], v[144:159]\n"
"buffer_load_dwordx4 a[240:243], v1, s[16:19], s52 offen offset:0 sc1\n"
"v_mfma_f32_32x32x16_bf16 v[176:191], a[180:183], v[124:127], v[176:191]\n"
"buffer_load_dwordx4 a[244:247], v1, s[16:19], s52 offen offset:1024 sc1\n"
"v_mfma_f32_32x32x16_bf16 v[208:223], a[184:187], v[124:127], v[208:223]\n"
"buffer_load_dwordx4 a[248:251], v1, s[16:19], s52 offen offset:2048 sc1\n"
"v_mfma_f32_32x32x16_bf16 v[240:255], a[188:191], v[124:127], v[240:255]\n"
"buffer_load_dwordx4 a[252:255], v1, s[16:19], s52 offen offset:3072 sc1\n"
"s_mov_b32 s43, s42\n"
".Lp2_end:\n"
"s_and_b32 s35, s43, 0x10000\n"
"s_lshl_b32 s34, s29, 14\n"
"s_add_u32 s34, s34, s35\n"
"v_add_u32 v2, s34, v1\n"
"s_xor_b32 s34, s29, 2\n"
"s_lshl_b32 s34, s34, 14\n"
"s_add_u32 s34, s34, s35\n"
"v_add_u32 v3, s34, v1\n"
"s_xor_b32 s35, s35, 0x10000\n"
"s_lshl_b32 s34, s29, 9\n"
"s_add_u32 s34, s34, s35\n"
"s_add_u32 s34, s34, 32768\n"
"v_lshlrev_b32 v4, 2, v0\n"
"v_add_u32 v4, s34, v4\n"
"v_xor_b32 v5, 1024, v4\n"
"s_cmp_lt_u32 s29, 2\n"
"s_cbranch_scc0 .Lepb_hi\n"
"v_add_f32 v16, v14, v15\n"
"v_add_f32 v17, v12, v13\n"
"ds_write_b32 v4, v16\n"
"s_add_u32 s52, s52, 4096\n"
"buffer_load_dwordx4 a[0:3], v1, s[16:19], s52 offen offset:0 sc1\n"
"buffer_load_dwordx4 a[4:7], v1, s[16:19], s52 offen offset:1024 sc1\n"
"ds_write_b128 v2, v[144:147] offset:0\n"
"buffer_load_dwordx4 a[8:11], v1, s[16:19], s52 offen offset:2048 sc1\n"
"buffer_load_dwordx4 a[12:15], v1, s[16:19], s52 offen offset:3072 sc1\n"
"ds_write_b128 v2, v[148:151] offset:1024\n"
"s_add_u32 s52, s52, 4096\n"
"buffer_load_dwordx4 a[16:19], v1, s[16:19], s52 offen offset:0 sc1\n"
"ds_write_b128 v2, v[152:155] offset:2048\n"
"buffer_load_dwordx4 a[20:23], v1, s[16:19], s52 offen offset:1024 sc1\n"
"buffer_load_dwordx4 a[24:27], v1, s[16:19], s52 offen offset:2048 sc1\n"
"ds_write_b128 v2, v[156:159] offset:3072\n"
"buffer_load_dwordx4 a[28:31], v1, s[16:19], s52 offen offset:3072 sc1\n"
"s_add_u32 s52, s52, 4096\n"
"ds_write_b128 v2, v[176:179] offset:4096\n"
"buffer_load_dwordx4 a[32:35], v1, s[16:19], s52 offen offset:0 sc1\n"
"buffer_load_dwordx4 a[36:39], v1, s[16:19], s52 offen offset:1024 sc1\n"
"ds_write_b128 v2, v[180:183] offset:5120\n"
"buffer_load_dwordx4 a[40:43], v1, s[16:19], s52 offen offset:2048 sc1\n"
"buffer_load_dwordx4 a[44:47], v1, s[16:19], s52 offen offset:3072 sc1\n"
"ds_write_b128 v2, v[184:187] offset:6144\n"
"s_add_u32 s52, s52, 4096\n"
"buffer_load_dwordx4 a[48:51], v1, s[16:19], s52 offen offset:0 sc1\n"
"ds_write_b128 v2, v[188:191] offset:7168\n"
"buffer_load_dwordx4 a[52:55], v1, s[16:19], s52 offen offset:1024 sc1\n"
"buffer_load_dwordx4 a[56:59], v1, s[16:19], s52 offen offset:2048 sc1\n"
"ds_write_b128 v2, v[208:211] offset:8192\n"
"buffer_load_dwordx4 a[60:63], v1, s[16:19], s52 offen offset:3072 sc1\n"
"ds_write_b128 v2, v[212:215] offset:9216\n"
"ds_write_b128 v2, v[216:219] offset:10240\n"
"ds_write_b128 v2, v[220:223] offset:11264\n"
"ds_write_b128 v2, v[240:243] offset:12288\n"
"ds_write_b128 v2, v[244:247] offset:13312\n"
"ds_write_b128 v2, v[248:251] offset:14336\n"
"ds_write_b128 v2, v[252:255] offset:15360\n"
"s_waitcnt lgkmcnt(0)\n"
"s_barrier\n"
"ds_read_b32 v18, v5\n"
"ds_read_b128 v[32:35], v3 offset:0\n"
"ds_read_b128 v[36:39], v3 offset:1024\n"
"ds_read_b128 v[40:43], v3 offset:2048\n"
"ds_read_b128 v[44:47], v3 offset:3072\n"
"ds_read_b128 v[48:51], v3 offset:4096\n"
"ds_read_b128 v[52:55], v3 offset:5120\n"
"ds_read_b128 v[56:59], v3 offset:6144\n"
"ds_read_b128 v[60:63], v3 offset:7168\n"
"ds_read_b128 v[64:67], v3 offset:8192\n"
"ds_read_b128 v[68:71], v3 offset:9216\n"
"ds_read_b128 v[72:75], v3 offset:10240\n"
"ds_read_b128 v[76:79], v3 offset:11264\n"
"ds_read_b128 v[80:83], v3 offset:12288\n"
"ds_read_b128 v[84:87], v3 offset:13312\n"
"ds_read_b128 v[88:91], v3 offset:14336\n"
"ds_read_b128 v[92:95], v3 offset:15360\n"
"s_add_u32 m0, s48, 0\n"
"s_add_u32 s58, s47, 64\n"
"buffer_load_dwordx4 v9, s[12:15], s58 offen lds\n"
"s_add_u32 m0, s48, 4096\n"
"s_add_u32 s58, s47, 176192\n"
"buffer_load_dwordx4 v9, s[12:15], s58 offen lds\n"
"s_add_u32 m0, s48, 8192\n"
"s_add_u32 s58, s47, 352320\n"
"buffer_load_dwordx4 v9, s[12:15], s58 offen lds\n"
"s_add_u32 m0, s48, 12288\n"
"s_add_u32 s58, s47, 528448\n"
"buffer_load_dwordx4 v9, s[12:15], s58 offen lds\n"
"s_waitcnt lgkmcnt(15)\n"
"v_add_f32 v17, v17, v18\n"
"v_mov_b32 v18, v17\n"
"s_nop 1\n"
"v_permlane32_swap_b32 v17, v18\n"
"v_add_f32 v12, v17, v18\n"
"v_cmp_le_f32 vcc, 0x2b800000, v12\n"
"s_mov_b32 s36, 0x53800000\n"
"v_cmp_ge_f32 s[34:35], s36, v12\n"
"s_nop 0\n"
"s_and_b64 vcc, vcc, s[34:35]\n"
"s_cmp_eq_u64 vcc, -1\n"
"s_cselect_b32 s36, 0, 1\n"
"s_or_b32 s60, s60, s36\n"
"v_rcp_f32 v12, v12\n"
"s_waitcnt lgkmcnt(12)\n"
"v_pk_add_f32 v[128:129], v[128:129], v[32:33]\n"
"v_pk_add_f32 v[130:131], v[130:131], v[34:35]\n"
"s_add_u32 m0, s48, 16384\n"
"s_add_u32 s58, s47, 704576\n"
"buffer_load_dwordx4 v9, s[12:15], s58 offen lds\n"
"v_pk_add_f32 v[132:133], v[132:133], v[36:37]\n"
"v_pk_add_f32 v[134:135], v[134:135], v[38:39]\n"
"s_add_u32 m0, s48, 20480\n"
"s_add_u32 s58, s47, 880704\n"
"buffer_load_dwordx4 v9, s[12:15], s58 offen lds\n"
"v_pk_add_f32 v[136:137], v[136:137], v[40:41]\n"
"v_pk_add_f32 v[138:139], v[138:139], v[42:43]\n"
"s_add_u32 m0, s48, 24576\n"
"s_add_u32 s58, s47, 1056832\n"
"buffer_load_dwordx4 v9, s[12:15], s58 offen lds\n"
"v_pk_add_f32 v[140:141], v[140:141], v[44:45]\n"
"v_pk_add_f32 v[142:143], v[142:143], v[46:47]\n"
"s_add_u32 m0, s48, 28672\n"
"s_add_u32 s58, s47, 1232960\n"
"buffer_load_dwordx4 v9, s[12:15], s58 offen lds\n"
"s_waitcnt lgkmcnt(8)\n"
"v_pk_add_f32 v[160:161], v[160:161], v[48:49]\n"
"v_pk_add_f32 v[162:163], v[162:163], v[50:51]\n"
"v_pk_add_f32 v[164:165], v[164:165], v[52:53]\n"
"v_pk_add_f32 v[166:167], v[166:167], v[54:55]\n"
"v_pk_add_f32 v[168:169], v[168:169], v[56:57]\n"
"v_pk_add_f32 v[170:171], v[170:171], v[58:59]\n"
"v_pk_add_f32 v[172:173], v[172:173], v[60:61]\n"
"v_pk_add_f32 v[174:175], v[174:175], v[62:63]\n"
"s_waitcnt lgkmcnt(4)\n"
"v_pk_add_f32 v[192:193], v[192:193], v[64:65]\n"
"v_pk_add_f32 v[194:195], v[194:195], v[66:67]\n"
"v_pk_add_f32 v[196:197], v[196:197], v[68:69]\n"
"v_pk_add_f32 v[198:199], v[198:199], v[70:71]\n"
"v_pk_add_f32 v[200:201], v[200:201], v[72:73]\n"
"v_pk_add_f32 v[202:203], v[202:203], v[74:75]\n"
"v_pk_add_f32 v[204:205], v[204:205], v[76:77]\n"
"v_pk_add_f32 v[206:207], v[206:207], v[78:79]\n"
"s_waitcnt lgkmcnt(0)\n"
"v_pk_add_f32 v[224:225], v[224:225], v[80:81]\n"
"v_pk_add_f32 v[226:227], v[226:227], v[82:83]\n"
"v_pk_add_f32 v[228:229], v[228:229], v[84:85]\n"
"v_pk_add_f32 v[230:231], v[230:231], v[86:87]\n"
"v_pk_add_f32 v[232:233], v[232:233], v[88:89]\n"
"v_pk_add_f32 v[234:235], v[234:235], v[90:91]\n"
"v_pk_add_f32 v[236:237], v[236:237], v[92:93]\n"
"v_pk_add_f32 v[238:239], v[238:239], v[94:95]\n"
"s_waitcnt lgkmcnt(0)\n"
"s_waitcnt vmcnt(32)\n"
"s_add_u32 s36, s50, 74448896\n"
"s_addc_u32 s37, s51, 0\n"
"s_mov_b32 s20, s36\n"
"s_and_b32 s21, s37, 0xffff\n"
"s_mov_b32 s22, 262144\n"
"s_mov_b32 s23, 0x20000\n"
"v_and_b32 v10, 31, v0\n"
"v_lshlrev_b32 v10, 3, v10\n"
"v_lshrrev_b32 v11, 5, v0\n"
"v_lshl_add_u32 v10, v11, 11, v10\n"
"s_lshl_b32 s34, s46, 12\n"
"buffer_load_dwordx2 v[80:81], v10, s[20:23], s34 offen offset:0 sc1\n"
"buffer_load_dwordx2 v[82:83], v10, s[20:23], s34 offen offset:256 sc1\n"
"buffer_load_dwordx2 v[84:85], v10, s[20:23], s34 offen offset:512 sc1\n"
"buffer_load_dwordx2 v[86:87], v10, s[20:23], s34 offen offset:768 sc1\n"
"buffer_load_dwordx2 v[88:89], v10, s[20:23], s34 offen offset:1024 sc1\n"
"buffer_load_dwordx2 v[90:91], v10, s[20:23], s34 offen offset:1280 sc1\n"
"buffer_load_dwordx2 v[92:93], v10, s[20:23], s34 offen offset:1536 sc1\n"
"buffer_load_dwordx2 v[94:95], v10, s[20:23], s34 offen offset:1792 sc1\n"
"s_add_u32 s36, s50, 74711040\n"
"s_addc_u32 s37, s51, 0\n"
"s_mov_b32 s96, s36\n"
"s_and_b32 s97, s37, 0xffff\n"
"s_mov_b32 s98, 5504\n"
"s_mov_b32 s99, 0x20000\n"
"v_lshlrev_b32 v11, 4, v11\n"
"s_mul_i32 s34, s31, 384\n"
"buffer_load_dwordx4 v[32:35], v11, s[96:99], s34 offen offset:0 sc1\n"
"buffer_load_dwordx4 v[36:39], v11, s[96:99], s34 offen offset:32 sc1\n"
"buffer_load_dwordx4 v[40:43], v11, s[96:99], s34 offen offset:64 sc1\n"
"buffer_load_dwordx4 v[44:47], v11, s[96:99], s34 offen offset:96 sc1\n"
"buffer_load_dwordx4 v[48:51], v11, s[96:99], s34 offen offset:128 sc1\n"
"buffer_load_dwordx4 v[52:55], v11, s[96:99], s34 offen offset:160 sc1\n"
"buffer_load_dwordx4 v[56:59], v11, s[96:99], s34 offen offset:192 sc1\n"
"buffer_load_dwordx4 v[60:63], v11, s[96:99], s34 offen offset:224 sc1\n"
"buffer_load_dwordx4 v[64:67], v11, s[96:99], s34 offen offset:256 sc1\n"
"buffer_load_dwordx4 v[68:71], v11, s[96:99], s34 offen offset:288 sc1\n"
"buffer_load_dwordx4 v[72:75], v11, s[96:99], s34 offen offset:320 sc1\n"
"buffer_load_dwordx4 v[76:79], v11, s[96:99], s34 offen offset:352 sc1\n"
"v_pk_mul_f32 v[128:129], v[128:129], v[12:13] op_sel_hi:[1,0]\n"
"v_pk_mul_f32 v[130:131], v[130:131], v[12:13] op_sel_hi:[1,0]\n"
"v_pk_mul_f32 v[132:133], v[132:133], v[12:13] op_sel_hi:[1,0]\n"
"v_pk_mul_f32 v[134:135], v[134:135], v[12:13] op_sel_hi:[1,0]\n"
"v_cvt_pk_bf16_f32 v16, v128, v129\n"
"v_cvt_pk_bf16_f32 v17, v130, v131\n"
"v_cvt_pk_bf16_f32 v18, v132, v133\n"
"v_cvt_pk_bf16_f32 v19, v134, v135\n"
"s_add_u32 s37, s59, 0\n"
"buffer_store_dwordx4 v[16:19], v1, s[24:27], s37 offen offset:0 sc1\n"
"v_pk_mul_f32 v[136:137], v[136:137], v[12:13] op_sel_hi:[1,0]\n"
"v_pk_mul_f32 v[138:139], v[138:139], v[12:13] op_sel_hi:[1,0]\n"
"v_pk_mul_f32 v[140:141], v[140:141], v[12:13] op_sel_hi:[1,0]\n"
"v_pk_mul_f32 v[142:143], v[142:143], v[12:13] op_sel_hi:[1,0]\n"
"v_cvt_pk_bf16_f32 v20, v136, v137\n"
"v_cvt_pk_bf16_f32 v21, v138, v139\n"
"v_cvt_pk_bf16_f32 v22, v140, v141\n"
"v_cvt_pk_bf16_f32 v23, v142, v143\n"
"buffer_store_dwordx4 v[20:23], v1, s[24:27], s37 offen offset:1024 sc1\n"
"v_pk_mul_f32 v[160:161], v[160:161], v[12:13] op_sel_hi:[1,0]\n"
"v_pk_mul_f32 v[162:163], v[162:163], v[12:13] op_sel_hi:[1,0]\n"
"v_pk_mul_f32 v[164:165], v[164:165], v[12:13] op_sel_hi:[1,0]\n"
"v_pk_mul_f32 v[166:167], v[166:167], v[12:13] op_sel_hi:[1,0]\n"
"v_cvt_pk_bf16_f32 v24, v160, v161\n"
"v_cvt_pk_bf16_f32 v25, v162, v163\n"
"v_cvt_pk_bf16_f32 v26, v164, v165\n"
"v_cvt_pk_bf16_f32 v27, v166, v167\n"
"buffer_store_dwordx4 v[24:27], v1, s[24:27], s37 offen offset:2048 sc1\n"
"v_pk_mul_f32 v[168:169], v[168:169], v[12:13] op_sel_hi:[1,0]\n"
"v_pk_mul_f32 v[170:171], v[170:171], v[12:13] op_sel_hi:[1,0]\n"
"v_pk_mul_f32 v[172:173], v[172:173], v[12:13] op_sel_hi:[1,0]\n"
"v_pk_mul_f32 v[174:175], v[174:175], v[12:13] op_sel_hi:[1,0]\n"
"v_cvt_pk_bf16_f32 v28, v168, v169\n"
"v_cvt_pk_bf16_f32 v29, v170, v171\n"
"v_cvt_pk_bf16_f32 v30, v172, v173\n"
"v_cvt_pk_bf16_f32 v31, v174, v175\n"
"buffer_store_dwordx4 v[28:31], v1, s[24:27], s37 offen offset:3072 sc1\n"
"v_pk_mul_f32 v[192:193], v[192:193], v[12:13] op_sel_hi:[1,0]\n"
"v_pk_mul_f32 v[194:195], v[194:195], v[12:13] op_sel_hi:[1,0]\n"
"v_pk_mul_f32 v[196:197], v[196:197], v[12:13] op_sel_hi:[1,0]\n"
"v_pk_mul_f32 v[198:199], v[198:199], v[12:13] op_sel_hi:[1,0]\n"
"v_cvt_pk_bf16_f32 v16, v192, v193\n"
"v_cvt_pk_bf16_f32 v17, v194, v195\n"
"v_cvt_pk_bf16_f32 v18, v196, v197\n"
"v_cvt_pk_bf16_f32 v19, v198, v199\n"
"s_add_u32 s37, s59, 4096\n"
"buffer_store_dwordx4 v[16:19], v1, s[24:27], s37 offen offset:0 sc1\n"
"v_pk_mul_f32 v[200:201], v[200:201], v[12:13] op_sel_hi:[1,0]\n"
"v_pk_mul_f32 v[202:203], v[202:203], v[12:13] op_sel_hi:[1,0]\n"
"v_pk_mul_f32 v[204:205], v[204:205], v[12:13] op_sel_hi:[1,0]\n"
"v_pk_mul_f32 v[206:207], v[206:207], v[12:13] op_sel_hi:[1,0]\n"
"v_cvt_pk_bf16_f32 v20, v200, v201\n"
"v_cvt_pk_bf16_f32 v21, v202, v203\n"
"v_cvt_pk_bf16_f32 v22, v204, v205\n"
"v_cvt_pk_bf16_f32 v23, v206, v207\n"
"buffer_store_dwordx4 v[20:23], v1, s[24:27], s37 offen offset:1024 sc1\n"
"v_pk_mul_f32 v[224:225], v[224:225], v[12:13] op_sel_hi:[1,0]\n"
"v_pk_mul_f32 v[226:227], v[226:227], v[12:13] op_sel_hi:[1,0]\n"
"v_pk_mul_f32 v[228:229], v[228:229], v[12:13] op_sel_hi:[1,0]\n"
"v_pk_mul_f32 v[230:231], v[230:231], v[12:13] op_sel_hi:[1,0]\n"
"v_cvt_pk_bf16_f32 v24, v224, v225\n"
"v_cvt_pk_bf16_f32 v25, v226, v227\n"
"v_cvt_pk_bf16_f32 v26, v228, v229\n"
"v_cvt_pk_bf16_f32 v27, v230, v231\n"
"buffer_store_dwordx4 v[24:27], v1, s[24:27], s37 offen offset:2048 sc1\n"
"v_pk_mul_f32 v[232:233], v[232:233], v[12:13] op_sel_hi:[1,0]\n"
"v_pk_mul_f32 v[234:235], v[234:235], v[12:13] op_sel_hi:[1,0]\n"
"v_pk_mul_f32 v[236:237], v[236:237], v[12:13] op_sel_hi:[1,0]\n"
"v_pk_mul_f32 v[238:239], v[238:239], v[12:13] op_sel_hi:[1,0]\n"
"v_cvt_pk_bf16_f32 v28, v232, v233\n"
"v_cvt_pk_bf16_f32 v29, v234, v235\n"
"v_cvt_pk_bf16_f32 v30, v236, v237\n"
"v_cvt_pk_bf16_f32 v31, v238, v239\n"
"buffer_store_dwordx4 v[28:31], v1, s[24:27], s37 offen offset:3072 sc1\n"
"s_branch .Lepb_done\n"
".Lepb_hi:\n"
"v_add_f32 v16, v12, v13\n"
"v_add_f32 v17, v14, v15\n"
"ds_write_b32 v4, v16\n"
"s_add_u32 s52, s52, 4096\n"
"buffer_load_dwordx4 a[0:3], v1, s[16:19], s52 offen offset:0 sc1\n"
"buffer_load_dwordx4 a[4:7], v1, s[16:19], s52 offen offset:1024 sc1\n"
"ds_write_b128 v2, v[128:131] offset:0\n"
"buffer_load_dwordx4 a[8:11], v1, s[16:19], s52 offen offset:2048 sc1\n"
"buffer_load_dwordx4 a[12:15], v1, s[16:19], s52 offen offset:3072 sc1\n"
"ds_write_b128 v2, v[132:135] offset:1024\n"
"s_add_u32 s52, s52, 4096\n"
"buffer_load_dwordx4 a[16:19], v1, s[16:19], s52 offen offset:0 sc1\n"
"ds_write_b128 v2, v[136:139] offset:2048\n"
"buffer_load_dwordx4 a[20:23], v1, s[16:19], s52 offen offset:1024 sc1\n"
"buffer_load_dwordx4 a[24:27], v1, s[16:19], s52 offen offset:2048 sc1\n"
"ds_write_b128 v2, v[140:143] offset:3072\n"
"buffer_load_dwordx4 a[28:31], v1, s[16:19], s52 offen offset:3072 sc1\n"
"s_add_u32 s52, s52, 4096\n"
"ds_write_b128 v2, v[160:163] offset:4096\n"
"buffer_load_dwordx4 a[32:35], v1, s[16:19], s52 offen offset:0 sc1\n"
"buffer_load_dwordx4 a[36:39], v1, s[16:19], s52 offen offset:1024 sc1\n"
"ds_write_b128 v2, v[164:167] offset:5120\n"
"buffer_load_dwordx4 a[40:43], v1, s[16:19], s52 offen offset:2048 sc1\n"
"buffer_load_dwordx4 a[44:47], v1, s[16:19], s52 offen offset:3072 sc1\n"
"ds_write_b128 v2, v[168:171] offset:6144\n"
"s_add_u32 s52, s52, 4096\n"
"buffer_load_dwordx4 a[48:51], v1, s[16:19], s52 offen offset:0 sc1\n"
"ds_write_b128 v2, v[172:175] offset:7168\n"
"buffer_load_dwordx4 a[52:55], v1, s[16:19], s52 offen offset:1024 sc1\n"
"buffer_load_dwordx4 a[56:59], v1, s[16:19], s52 offen offset:2048 sc1\n"
"ds_write_b128 v2, v[192:195] offset:8192\n"
"buffer_load_dwordx4 a[60:63], v1, s[16:19], s52 offen offset:3072 sc1\n"
"ds_write_b128 v2, v[196:199] offset:9216\n"
"ds_write_b128 v2, v[200:203] offset:10240\n"
"ds_write_b128 v2, v[204:207] offset:11264\n"
"ds_write_b128 v2, v[224:227] offset:12288\n"
"ds_write_b128 v2, v[228:231] offset:13312\n"
"ds_write_b128 v2, v[232:235] offset:14336\n"
"ds_write_b128 v2, v[236:239] offset:15360\n"
"s_waitcnt lgkmcnt(0)\n"
"s_barrier\n"
"ds_read_b32 v18, v5\n"
"ds_read_b128 v[32:35], v3 offset:0\n"
"ds_read_b128 v[36:39], v3 offset:1024\n"
"ds_read_b128 v[40:43], v3 offset:2048\n"
"ds_read_b128 v[44:47], v3 offset:3072\n"
"ds_read_b128 v[48:51], v3 offset:4096\n"
"ds_read_b128 v[52:55], v3 offset:5120\n"
"ds_read_b128 v[56:59], v3 offset:6144\n"
"ds_read_b128 v[60:63], v3 offset:7168\n"
"ds_read_b128 v[64:67], v3 offset:8192\n"
"ds_read_b128 v[68:71], v3 offset:9216\n"
"ds_read_b128 v[72:75], v3 offset:10240\n"
"ds_read_b128 v[76:79], v3 offset:11264\n"
"ds_read_b128 v[80:83], v3 offset:12288\n"
"ds_read_b128 v[84:87], v3 offset:13312\n"
"ds_read_b128 v[88:91], v3 offset:14336\n"
"ds_read_b128 v[92:95], v3 offset:15360\n"
"s_add_u32 m0, s48, 0\n"
"s_add_u32 s58, s47, 64\n"
"buffer_load_dwordx4 v9, s[12:15], s58 offen lds\n"
"s_add_u32 m0, s48, 4096\n"
"s_add_u32 s58, s47, 176192\n"
"buffer_load_dwordx4 v9, s[12:15], s58 offen lds\n"
"s_add_u32 m0, s48, 8192\n"
"s_add_u32 s58, s47, 352320\n"
"buffer_load_dwordx4 v9, s[12:15], s58 offen lds\n"
"s_add_u32 m0, s48, 12288\n"
"s_add_u32 s58, s47, 528448\n"
"buffer_load_dwordx4 v9, s[12:15], s58 offen lds\n"
"s_waitcnt lgkmcnt(15)\n"
"v_add_f32 v17, v17, v18\n"
"v_mov_b32 v18, v17\n"
"s_nop 1\n"
"v_permlane32_swap_b32 v17, v18\n"
"v_add_f32 v12, v17, v18\n"
"v_cmp_le_f32 vcc, 0x2b800000, v12\n"
"s_mov_b32 s36, 0x53800000\n"
"v_cmp_ge_f32 s[34:35], s36, v12\n"
"s_nop 0\n"
"s_and_b64 vcc, vcc, s[34:35]\n"
"s_cmp_eq_u64 vcc, -1\n"
"s_cselect_b32 s36, 0, 1\n"
"s_or_b32 s60, s60, s36\n"
"v_rcp_f32 v12, v12\n"
"s_waitcnt lgkmcnt(12)\n"
"v_pk_add_f32 v[144:145], v[144:145], v[32:33]\n"
"v_pk_add_f32 v[146:147], v[146:147], v[34:35]\n"
"s_add_u32 m0, s48, 16384\n"
"s_add_u32 s58, s47, 704576\n"
"buffer_load_dwordx4 v9, s[12:15], s58 offen lds\n"
"v_pk_add_f32 v[148:149], v[148:149], v[36:37]\n"
"v_pk_add_f32 v[150:151], v[150:151], v[38:39]\n"
"s_add_u32 m0, s48, 20480\n"
"s_add_u32 s58, s47, 880704\n"
"buffer_load_dwordx4 v9, s[12:15], s58 offen lds\n"
"v_pk_add_f32 v[152:153], v[152:153], v[40:41]\n"
"v_pk_add_f32 v[154:155], v[154:155], v[42:43]\n"
"s_add_u32 m0, s48, 24576\n"
"s_add_u32 s58, s47, 1056832\n"
"buffer_load_dwordx4 v9, s[12:15], s58 offen lds\n"
"v_pk_add_f32 v[156:157], v[156:157], v[44:45]\n"
"v_pk_add_f32 v[158:159], v[158:159], v[46:47]\n"
"s_add_u32 m0, s48, 28672\n"
"s_add_u32 s58, s47, 1232960\n"
"buffer_load_dwordx4 v9, s[12:15], s58 offen lds\n"
"s_waitcnt lgkmcnt(8)\n"
"v_pk_add_f32 v[176:177], v[176:177], v[48:49]\n"
"v_pk_add_f32 v[178:179], v[178:179], v[50:51]\n"
"v_pk_add_f32 v[180:181], v[180:181], v[52:53]\n"
"v_pk_add_f32 v[182:183], v[182:183], v[54:55]\n"
"v_pk_add_f32 v[184:185], v[184:185], v[56:57]\n"
"v_pk_add_f32 v[186:187], v[186:187], v[58:59]\n"
"v_pk_add_f32 v[188:189], v[188:189], v[60:61]\n"
"v_pk_add_f32 v[190:191], v[190:191], v[62:63]\n"
"s_waitcnt lgkmcnt(4)\n"
"v_pk_add_f32 v[208:209], v[208:209], v[64:65]\n"
"v_pk_add_f32 v[210:211], v[210:211], v[66:67]\n"
"v_pk_add_f32 v[212:213], v[212:213], v[68:69]\n"
"v_pk_add_f32 v[214:215], v[214:215], v[70:71]\n"
"v_pk_add_f32 v[216:217], v[216:217], v[72:73]\n"
"v_pk_add_f32 v[218:219], v[218:219], v[74:75]\n"
"v_pk_add_f32 v[220:221], v[220:221], v[76:77]\n"
"v_pk_add_f32 v[222:223], v[222:223], v[78:79]\n"
"s_waitcnt lgkmcnt(0)\n"
"v_pk_add_f32 v[240:241], v[240:241], v[80:81]\n"
"v_pk_add_f32 v[242:243], v[242:243], v[82:83]\n"
"v_pk_add_f32 v[244:245], v[244:245], v[84:85]\n"
"v_pk_add_f32 v[246:247], v[246:247], v[86:87]\n"
"v_pk_add_f32 v[248:249], v[248:249], v[88:89]\n"
"v_pk_add_f32 v[250:251], v[250:251], v[90:91]\n"
"v_pk_add_f32 v[252:253], v[252:253], v[92:93]\n"
"v_pk_add_f32 v[254:255], v[254:255], v[94:95]\n"
"s_waitcnt lgkmcnt(0)\n"
"s_waitcnt vmcnt(32)\n"
"s_add_u32 s36, s50, 74448896\n"
"s_addc_u32 s37, s51, 0\n"
"s_mov_b32 s20, s36\n"
"s_and_b32 s21, s37, 0xffff\n"
"s_mov_b32 s22, 262144\n"
"s_mov_b32 s23, 0x20000\n"
"v_and_b32 v10, 31, v0\n"
"v_lshlrev_b32 v10, 3, v10\n"
"v_lshrrev_b32 v11, 5, v0\n"
"v_lshl_add_u32 v10, v11, 11, v10\n"
"s_lshl_b32 s34, s46, 12\n"
"buffer_load_dwordx2 v[80:81], v10, s[20:23], s34 offen offset:0 sc1\n"
"buffer_load_dwordx2 v[82:83], v10, s[20:23], s34 offen offset:256 sc1\n"
"buffer_load_dwordx2 v[84:85], v10, s[20:23], s34 offen offset:512 sc1\n"
"buffer_load_dwordx2 v[86:87], v10, s[20:23], s34 offen offset:768 sc1\n"
"buffer_load_dwordx2 v[88:89], v10, s[20:23], s34 offen offset:1024 sc1\n"
"buffer_load_dwordx2 v[90:91], v10, s[20:23], s34 offen offset:1280 sc1\n"
"buffer_load_dwordx2 v[92:93], v10, s[20:23], s34 offen offset:1536 sc1\n"
"buffer_load_dwordx2 v[94:95], v10, s[20:23], s34 offen offset:1792 sc1\n"
"s_add_u32 s36, s50, 74711040\n"
"s_addc_u32 s37, s51, 0\n"
"s_mov_b32 s96, s36\n"
"s_and_b32 s97, s37, 0xffff\n"
"s_mov_b32 s98, 5504\n"
"s_mov_b32 s99, 0x20000\n"
"v_lshlrev_b32 v11, 4, v11\n"
"s_mul_i32 s34, s31, 384\n"
"buffer_load_dwordx4 v[32:35], v11, s[96:99], s34 offen offset:0 sc1\n"
"buffer_load_dwordx4 v[36:39], v11, s[96:99], s34 offen offset:32 sc1\n"
"buffer_load_dwordx4 v[40:43], v11, s[96:99], s34 offen offset:64 sc1\n"
"buffer_load_dwordx4 v[44:47], v11, s[96:99], s34 offen offset:96 sc1\n"
"buffer_load_dwordx4 v[48:51], v11, s[96:99], s34 offen offset:128 sc1\n"
"buffer_load_dwordx4 v[52:55], v11, s[96:99], s34 offen offset:160 sc1\n"
"buffer_load_dwordx4 v[56:59], v11, s[96:99], s34 offen offset:192 sc1\n"
"buffer_load_dwordx4 v[60:63], v11, s[96:99], s34 offen offset:224 sc1\n"
"buffer_load_dwordx4 v[64:67], v11, s[96:99], s34 offen offset:256 sc1\n"
"buffer_load_dwordx4 v[68:71], v11, s[96:99], s34 offen offset:288 sc1\n"
"buffer_load_dwordx4 v[72:75], v11, s[96:99], s34 offen offset:320 sc1\n"
"buffer_load_dwordx4 v[76:79], v11, s[96:99], s34 offen offset:352 sc1\n"
"v_pk_mul_f32 v[144:145], v[144:145], v[12:13] op_sel_hi:[1,0]\n"
"v_pk_mul_f32 v[146:147], v[146:147], v[12:13] op_sel_hi:[1,0]\n"
"v_pk_mul_f32 v[148:149], v[148:149], v[12:13] op_sel_hi:[1,0]\n"
"v_pk_mul_f32 v[150:151], v[150:151], v[12:13] op_sel_hi:[1,0]\n"
"v_cvt_pk_bf16_f32 v16, v144, v145\n"
"v_cvt_pk_bf16_f32 v17, v146, v147\n"
"v_cvt_pk_bf16_f32 v18, v148, v149\n"
"v_cvt_pk_bf16_f32 v19, v150, v151\n"
"s_add_u32 s37, s59, 262144\n"
"buffer_store_dwordx4 v[16:19], v1, s[24:27], s37 offen offset:0 sc1\n"
"v_pk_mul_f32 v[152:153], v[152:153], v[12:13] op_sel_hi:[1,0]\n"
"v_pk_mul_f32 v[154:155], v[154:155], v[12:13] op_sel_hi:[1,0]\n"
"v_pk_mul_f32 v[156:157], v[156:157], v[12:13] op_sel_hi:[1,0]\n"
"v_pk_mul_f32 v[158:159], v[158:159], v[12:13] op_sel_hi:[1,0]\n"
"v_cvt_pk_bf16_f32 v20, v152, v153\n"
"v_cvt_pk_bf16_f32 v21, v154, v155\n"
"v_cvt_pk_bf16_f32 v22, v156, v157\n"
"v_cvt_pk_bf16_f32 v23, v158, v159\n"
"buffer_store_dwordx4 v[20:23], v1, s[24:27], s37 offen offset:1024 sc1\n"
"v_pk_mul_f32 v[176:177], v[176:177], v[12:13] op_sel_hi:[1,0]\n"
"v_pk_mul_f32 v[178:179], v[178:179], v[12:13] op_sel_hi:[1,0]\n"
"v_pk_mul_f32 v[180:181], v[180:181], v[12:13] op_sel_hi:[1,0]\n"
"v_pk_mul_f32 v[182:183], v[182:183], v[12:13] op_sel_hi:[1,0]\n"
"v_cvt_pk_bf16_f32 v24, v176, v177\n"
"v_cvt_pk_bf16_f32 v25, v178, v179\n"
"v_cvt_pk_bf16_f32 v26, v180, v181\n"
"v_cvt_pk_bf16_f32 v27, v182, v183\n"
"buffer_store_dwordx4 v[24:27], v1, s[24:27], s37 offen offset:2048 sc1\n"
"v_pk_mul_f32 v[184:185], v[184:185], v[12:13] op_sel_hi:[1,0]\n"
"v_pk_mul_f32 v[186:187], v[186:187], v[12:13] op_sel_hi:[1,0]\n"
"v_pk_mul_f32 v[188:189], v[188:189], v[12:13] op_sel_hi:[1,0]\n"
"v_pk_mul_f32 v[190:191], v[190:191], v[12:13] op_sel_hi:[1,0]\n"
"v_cvt_pk_bf16_f32 v28, v184, v185\n"
"v_cvt_pk_bf16_f32 v29, v186, v187\n"
"v_cvt_pk_bf16_f32 v30, v188, v189\n"
"v_cvt_pk_bf16_f32 v31, v190, v191\n"
"buffer_store_dwordx4 v[28:31], v1, s[24:27], s37 offen offset:3072 sc1\n"
"v_pk_mul_f32 v[208:209], v[208:209], v[12:13] op_sel_hi:[1,0]\n"
"v_pk_mul_f32 v[210:211], v[210:211], v[12:13] op_sel_hi:[1,0]\n"
"v_pk_mul_f32 v[212:213], v[212:213], v[12:13] op_sel_hi:[1,0]\n"
"v_pk_mul_f32 v[214:215], v[214:215], v[12:13] op_sel_hi:[1,0]\n"
"v_cvt_pk_bf16_f32 v16, v208, v209\n"
"v_cvt_pk_bf16_f32 v17, v210, v211\n"
"v_cvt_pk_bf16_f32 v18, v212, v213\n"
"v_cvt_pk_bf16_f32 v19, v214, v215\n"
"s_add_u32 s37, s59, 266240\n"
"buffer_store_dwordx4 v[16:19], v1, s[24:27], s37 offen offset:0 sc1\n"
"v_pk_mul_f32 v[216:217], v[216:217], v[12:13] op_sel_hi:[1,0]\n"
"v_pk_mul_f32 v[218:219], v[218:219], v[12:13] op_sel_hi:[1,0]\n"
"v_pk_mul_f32 v[220:221], v[220:221], v[12:13] op_sel_hi:[1,0]\n"
"v_pk_mul_f32 v[222:223], v[222:223], v[12:13] op_sel_hi:[1,0]\n"
"v_cvt_pk_bf16_f32 v20, v216, v217\n"
"v_cvt_pk_bf16_f32 v21, v218, v219\n"
"v_cvt_pk_bf16_f32 v22, v220, v221\n"
"v_cvt_pk_bf16_f32 v23, v222, v223\n"
"buffer_store_dwordx4 v[20:23], v1, s[24:27], s37 offen offset:1024 sc1\n"
"v_pk_mul_f32 v[240:241], v[240:241], v[12:13] op_sel_hi:[1,0]\n"
"v_pk_mul_f32 v[242:243], v[242:243], v[12:13] op_sel_hi:[1,0]\n"
"v_pk_mul_f32 v[244:245], v[244:245], v[12:13] op_sel_hi:[1,0]\n"
"v_pk_mul_f32 v[246:247], v[246:247], v[12:13] op_sel_hi:[1,0]\n"
"v_cvt_pk_bf16_f32 v24, v240, v241\n"
"v_cvt_pk_bf16_f32 v25, v242, v243\n"
"v_cvt_pk_bf16_f32 v26, v244, v245\n"
"v_cvt_pk_bf16_f32 v27, v246, v247\n"
"buffer_store_dwordx4 v[24:27], v1, s[24:27], s37 offen offset:2048 sc1\n"
"v_pk_mul_f32 v[248:249], v[248:249], v[12:13] op_sel_hi:[1,0]\n"
"v_pk_mul_f32 v[250:251], v[250:251], v[12:13] op_sel_hi:[1,0]\n"
"v_pk_mul_f32 v[252:253], v[252:253], v[12:13] op_sel_hi:[1,0]\n"
"v_pk_mul_f32 v[254:255], v[254:255], v[12:13] op_sel_hi:[1,0]\n"
"v_cvt_pk_bf16_f32 v28, v248, v249\n"
"v_cvt_pk_bf16_f32 v29, v250, v251\n"
"v_cvt_pk_bf16_f32 v30, v252, v253\n"
"v_cvt_pk_bf16_f32 v31, v254, v255\n"
"buffer_store_dwordx4 v[28:31], v1, s[24:27], s37 offen offset:3072 sc1\n"
".Lepb_done:\n"
"s_cmp_lg_u32 s60, 0\n"
"s_cbranch_scc1 .Lfb_entry\n"
".Lfb_ret:\n"
"s_waitcnt vmcnt(52)\n"
"s_barrier\n"
"s_add_u32 m0, s48, 32768\n"
"s_add_u32 s58, s47, 128\n"
"buffer_load_dwordx4 v9, s[12:15], s58 offen lds\n"
"s_add_u32 m0, s48, 36864\n"
"s_add_u32 s58, s47, 176256\n"
"buffer_load_dwordx4 v9, s[12:15], s58 offen lds\n"
"s_add_u32 m0, s48, 40960\n"
"s_add_u32 s58, s47, 352384\n"
"buffer_load_dwordx4 v9, s[12:15], s58 offen lds\n"
"s_add_u32 m0, s48, 45056\n"
"s_add_u32 s58, s47, 528512\n"
"buffer_load_dwordx4 v9, s[12:15], s58 offen lds\n"
"s_add_u32 m0, s48, 49152\n"
"s_add_u32 s58, s47, 704640\n"
"buffer_load_dwordx4 v9, s[12:15], s58 offen lds\n"
"s_add_u32 m0, s48, 53248\n"
"s_add_u32 s58, s47, 880768\n"
"buffer_load_dwordx4 v9, s[12:15], s58 offen lds\n"
"s_add_u32 m0, s48, 57344\n"
"s_add_u32 s58, s47, 1056896\n"
"buffer_load_dwordx4 v9, s[12:15], s58 offen lds\n"
"s_add_u32 m0, s48, 61440\n"
"s_add_u32 s58, s47, 1233024\n"
"buffer_load_dwordx4 v9, s[12:15], s58 offen lds\n"
"v_lshrrev_b32 v4, 5, v0\n"
"v_bfe_u32 v5, v0, 2, 2\n"
"v_lshl_add_u32 v4, v4, 2, v5\n"
"v_lshlrev_b32 v4, 6, v4\n"
"v_bfe_u32 v5, v0, 4, 1\n"
"v_lshl_add_u32 v4, v5, 5, v4\n"
"v_and_b32 v5, 3, v0\n"
"v_lshl_add_u32 v4, v5, 3, v4\n"
"v_add_u32 v113, s53, v4\n"
"v_add_u32 v114, 0x20000, v4\n"
"s_add_u32 s36, s50, 59768832\n"
"s_addc_u32 s37, s51, 0\n"
"s_mov_b32 s24, s36\n"
"s_and_b32 s25, s37, 0xffff\n"
"s_mov_b32 s26, 5636096\n"
"s_mov_b32 s27, 0x20000\n"
"s_mov_b32 s54, 0x3dd2d3e8\n"
"s_mov_b32 s55, 0x3dd2d3e8\n"
"v_mov_b32 v120, 0x40135761\n"
"s_mov_b32 s56, 1.0\n"
"s_mov_b32 s57, 1.0\n"
"s_load_dwordx4 s[64:67], s[8:9], 72\n"
"s_load_dwordx2 s[68:69], s[8:9], 0\n"
"s_load_dwordx2 s[70:71], s[8:9], 88\n"
"s_cmp_eq_u32 s31, 15\n"
"s_cbranch_scc1 .Lg3_idle\n"
"s_barrier\n"
"ds_read_b64_tr_b16 v[96:97], v114 offset:0\n"
"ds_read_b64_tr_b16 v[98:99], v114 offset:512\n"
"ds_read_b64_tr_b16 v[100:101], v114 offset:1024\n"
"ds_read_b64_tr_b16 v[102:103], v114 offset:1536\n"
"ds_read_b64_tr_b16 v[104:105], v114 offset:2048\n"
"ds_read_b64_tr_b16 v[106:107], v114 offset:2560\n"
"s_waitcnt lgkmcnt(4)\n"
"v_mfma_f32_32x32x16_bf16 v[128:143], v[96:99], a[192:195], 0\n"
"ds_read_b64_tr_b16 v[108:109], v114 offset:3072\n"
"ds_read_b64_tr_b16 v[110:111], v114 offset:3584\n"
"s_waitcnt lgkmcnt(4)\n"
"v_mfma_f32_32x32x16_bf16 v[128:143], v[100:103], a[196:199], v[128:143]\n"
"ds_read_b64_tr_b16 v[96:97], v114 offset:4096\n"
"ds_read_b64_tr_b16 v[98:99], v114 offset:4608\n"
"s_waitcnt lgkmcnt(4)\n"
"v_mfma_f32_32x32x16_bf16 v[128:143], v[104:107], a[200:203], v[128:143]\n"
"ds_read_b64_tr_b16 v[100:101], v114 offset:5120\n"
"ds_read_b64_tr_b16 v[102:103], v114 offset:5632\n"
"s_waitcnt lgkmcnt(4)\n"
"v_mfma_f32_32x32x16_bf16 v[128:143], v[108:111], a[204:207], v[128:143]\n"
"ds_read_b64_tr_b16 v[104:105], v114 offset:6144\n"
"ds_read_b64_tr_b16 v[106:107], v114 offset:6656\n"
"s_waitcnt lgkmcnt(4)\n"
"v_mfma_f32_32x32x16_bf16 v[128:143], v[96:99], a[208:211], v[128:143]\n"
"ds_read_b64_tr_b16 v[108:109], v114 offset:7168\n"
"ds_read_b64_tr_b16 v[110:111], v114 offset:7680\n"
"s_waitcnt lgkmcnt(4)\n"
"v_mfma_f32_32x32x16_bf16 v[128:143], v[100:103], a[212:215], v[128:143]\n"
"ds_read_b64_tr_b16 v[96:97], v114 offset:8192\n"
"ds_read_b64_tr_b16 v[98:99], v114 offset:8704\n"
"s_waitcnt lgkmcnt(4)\n"
"v_mfma_f32_32x32x16_bf16 v[128:143], v[104:107], a[216:219], v[128:143]\n"
"ds_read_b64_tr_b16 v[100:101], v114 offset:9216\n"
"ds_read_b64_tr_b16 v[102:103], v114 offset:9728\n"
"s_waitcnt lgkmcnt(4)\n"
"v_mfma_f32_32x32x16_bf16 v[128:143], v[108:111], a[220:223], v[128:143]\n"
"ds_read_b64_tr_b16 v[104:105], v114 offset:10240\n"
"ds_read_b64_tr_b16 v[106:107], v114 offset:10752\n"
"s_waitcnt vmcnt(28)\n"
"s_waitcnt lgkmcnt(4)\n"
"v_mfma_f32_32x32x16_bf16 v[128:143], v[96:99], a[224:227], v[128:143]\n"
"ds_read_b64_tr_b16 v[108:109], v114 offset:11264\n"
"ds_read_b64_tr_b16 v[110:111], v114 offset:11776\n"
"v_add_f32 v2, v80, v82\n"
"v_add_f32 v3, v81, v83\n"
"s_waitcnt lgkmcnt(4)\n"
"v_mfma_f32_32x32x16_bf16 v[128:143], v[100:103], a[228:231], v[128:143]\n"
"ds_read_b64_tr_b16 v[96:97], v114 offset:12288\n"
"ds_read_b64_tr_b16 v[98:99], v114 offset:12800\n"
"v_add_f32 v2, v2, v84\n"
"v_add_f32 v3, v3, v85\n"
"s_waitcnt lgkmcnt(4)\n"
"v_mfma_f32_32x32x16_bf16 v[128:143], v[104:107], a[232:235], v[128:143]\n"
"ds_read_b64_tr_b16 v[100:101], v114 offset:13312\n"
"ds_read_b64_tr_b16 v[102:103], v114 offset:13824\n"
"v_add_f32 v2, v2, v86\n"
"v_add_f32 v3, v3, v87\n"
"s_waitcnt lgkmcnt(4)\n"
"v_mfma_f32_32x32x16_bf16 v[128:143], v[108:111], a[236:239], v[128:143]\n"
"ds_read_b64_tr_b16 v[104:105], v114 offset:14336\n"
"ds_read_b64_tr_b16 v[106:107], v114 offset:14848\n"
"v_add_f32 v2, v2, v88\n"
"v_add_f32 v3, v3, v89\n"
"s_waitcnt lgkmcnt(4)\n"
"v_mfma_f32_32x32x16_bf16 v[128:143], v[96:99], a[240:243], v[128:143]\n"
"ds_read_b64_tr_b16 v[108:109], v114 offset:15360\n"
"ds_read_b64_tr_b16 v[110:111], v114 offset:15872\n"
"v_add_f32 v2, v2, v90\n"
"v_add_f32 v3, v3, v91\n"
"s_waitcnt lgkmcnt(4)\n"
"v_mfma_f32_32x32x16_bf16 v[128:143], v[100:103], a[244:247], v[128:143]\n"
"ds_read_b64_tr_b16 v[96:97], v114 offset:16384\n"
"ds_read_b64_tr_b16 v[98:99], v114 offset:16896\n"
"v_add_f32 v2, v2, v92\n"
"v_add_f32 v3, v3, v93\n"
"s_waitcnt lgkmcnt(4)\n"
"v_mfma_f32_32x32x16_bf16 v[128:143], v[104:107], a[248:251], v[128:143]\n"
"ds_read_b64_tr_b16 v[100:101], v114 offset:17408\n"
"ds_read_b64_tr_b16 v[102:103], v114 offset:17920\n"
"v_add_f32 v2, v2, v94\n"
"v_add_f32 v3, v3, v95\n"
"s_waitcnt lgkmcnt(4)\n"
"v_mfma_f32_32x32x16_bf16 v[128:143], v[108:111], a[252:255], v[128:143]\n"
"ds_read_b64_tr_b16 v[104:105], v114 offset:18432\n"
"ds_read_b64_tr_b16 v[106:107], v114 offset:18944\n"
"v_mov_b32 v80, v2\n"
"v_mov_b32 v81, v3\n"
"s_waitcnt lgkmcnt(4)\n"
"v_mfma_f32_32x32x16_bf16 v[128:143], v[96:99], a[0:3], v[128:143]\n"
"ds_read_b64_tr_b16 v[108:109], v114 offset:19456\n"
"ds_read_b64_tr_b16 v[110:111], v114 offset:19968\n"
"s_nop 1\n"
"s_waitcnt lgkmcnt(4)\n"
"v_mfma_f32_32x32x16_bf16 v[128:143], v[100:103], a[4:7], v[128:143]\n"
"ds_read_b64_tr_b16 v[96:97], v114 offset:20480\n"
"ds_read_b64_tr_b16 v[98:99], v114 offset:20992\n"
"v_permlane32_swap_b32 v2, v80\n"
"v_permlane32_swap_b32 v3, v81\n"
"s_waitcnt lgkmcnt(4)\n"
"v_mfma_f32_32x32x16_bf16 v[128:143], v[104:107], a[8:11], v[128:143]\n"
"ds_read_b64_tr_b16 v[100:101], v114 offset:21504\n"
"ds_read_b64_tr_b16 v[102:103], v114 offset:22016\n"
"v_add_f32 v2, v2, v80\n"
"v_add_f32 v3, v3, v81\n"
"s_waitcnt lgkmcnt(4)\n"
"v_mfma_f32_32x32x16_bf16 v[128:143], v[108:111], a[12:15], v[128:143]\n"
"ds_read_b64_tr_b16 v[104:105], v114 offset:22528\n"
"ds_read_b64_tr_b16 v[106:107], v114 offset:23040\n"
"v_mul_f32 v116, 0x3b000000, v2\n"
"v_mul_f32 v3, 0x3b000000, v3\n"
"s_waitcnt lgkmcnt(4)\n"
"v_mfma_f32_32x32x16_bf16 v[128:143], v[96:99], a[16:19], v[128:143]\n"
"ds_read_b64_tr_b16 v[108:109], v114 offset:23552\n"
"ds_read_b64_tr_b16 v[110:111], v114 offset:24064\n"
"v_fma_f32 v3, -v116, v116, v3\n"
"v_max_f32 v3, 0, v3\n"
"s_waitcnt lgkmcnt(4)\n"
"v_mfma_f32_32x32x16_bf16 v[128:143], v[100:103], a[20:23], v[128:143]\n"
"ds_read_b64_tr_b16 v[96:97], v114 offset:24576\n"
"ds_read_b64_tr_b16 v[98:99], v114 offset:25088\n"
"v_cvt_pk_bf16_f32 v2, v116, v116\n"
"v_lshlrev_b32 v116, 16, v2\n"
"s_waitcnt lgkmcnt(4)\n"
"v_mfma_f32_32x32x16_bf16 v[128:143], v[104:107], a[24:27], v[128:143]\n"
"ds_read_b64_tr_b16 v[100:101], v114 offset:25600\n"
"ds_read_b64_tr_b16 v[102:103], v114 offset:26112\n"
"v_cvt_pk_bf16_f32 v2, v3, v3\n"
"v_lshlrev_b32 v3, 16, v2\n"
"s_waitcnt lgkmcnt(4)\n"
"v_mfma_f32_32x32x16_bf16 v[128:143], v[108:111], a[28:31], v[128:143]\n"
"ds_read_b64_tr_b16 v[104:105], v114 offset:26624\n"
"ds_read_b64_tr_b16 v[106:107], v114 offset:27136\n"
"v_add_f32 v3, 0x3727c5ac, v3\n"
"v_cvt_pk_bf16_f32 v2, v3, v3\n"
"s_waitcnt lgkmcnt(4)\n"
"v_mfma_f32_32x32x16_bf16 v[128:143], v[96:99], a[32:35], v[128:143]\n"
"ds_read_b64_tr_b16 v[108:109], v114 offset:27648\n"
"ds_read_b64_tr_b16 v[110:111], v114 offset:28160\n"
"v_lshlrev_b32 v3, 16, v2\n"
"s_waitcnt lgkmcnt(4)\n"
"v_mfma_f32_32x32x16_bf16 v[128:143], v[100:103], a[36:39], v[128:143]\n"
"ds_read_b64_tr_b16 v[96:97], v114 offset:28672\n"
"ds_read_b64_tr_b16 v[98:99], v114 offset:29184\n"
"v_sqrt_f32 v3, v3\n"
"s_waitcnt lgkmcnt(4)\n"
"v_mfma_f32_32x32x16_bf16 v[128:143], v[104:107], a[40:43], v[128:143]\n"
"ds_read_b64_tr_b16 v[100:101], v114 offset:29696\n"
"ds_read_b64_tr_b16 v[102:103], v114 offset:30208\n"
"s_nop 0\n"
"v_cvt_pk_bf16_f32 v2, v3, v3\n"
"s_waitcnt lgkmcnt(4)\n"
"v_mfma_f32_32x32x16_bf16 v[128:143], v[108:111], a[44:47], v[128:143]\n"
"ds_read_b64_tr_b16 v[104:105], v114 offset:30720\n"
"ds_read_b64_tr_b16 v[106:107], v114 offset:31232\n"
"v_lshlrev_b32 v3, 16, v2\n"
"s_waitcnt lgkmcnt(4)\n"
"v_mfma_f32_32x32x16_bf16 v[128:143], v[96:99], a[48:51], v[128:143]\n"
"ds_read_b64_tr_b16 v[108:109], v114 offset:31744\n"
"ds_read_b64_tr_b16 v[110:111], v114 offset:32256\n"
"v_rcp_f32 v118, v3\n"
"s_waitcnt lgkmcnt(4)\n"
"v_mfma_f32_32x32x16_bf16 v[128:143], v[100:103], a[52:55], v[128:143]\n"
"s_nop 0\n"
"s_waitcnt lgkmcnt(2)\n"
"v_mfma_f32_32x32x16_bf16 v[128:143], v[104:107], a[56:59], v[128:143]\n"
"s_waitcnt lgkmcnt(0)\n"
"v_mfma_f32_32x32x16_bf16 v[128:143], v[108:111], a[60:63], v[128:143]\n"
"s_waitcnt vmcnt(16)\n"
"s_nop 3\n"
"s_waitcnt vmcnt(8)\n"
"s_barrier\n"
"s_cmp_lg_u32 s29, 0\n"
"s_cbranch_scc1 .Lsig15\n"
"s_lshr_b32 s48, s28, 5\n"
"s_lshl_b32 s49, s48, 8\n"
"s_lshl_b32 s48, s48, 1\n"
"s_sub_u32 s72, 15, s48\n"
"s_lshl_b32 s72, s72, 8\n"
"s_and_b32 s48, s28, 7\n"
"s_lshl_b32 s48, s48, 2\n"
"s_bfe_u32 s73, s28, 0x20003\n"
"s_add_u32 s48, s48, s73\n"
"s_lshl_b32 s48, s48, 2\n"
"s_add_u32 s49, s49, s48\n"
"s_add_u32 s49, s49, 43264\n"
"v_mul_i32_i24 v2, s72, v0\n"
"v_add_u32 v2, s49, v2\n"
"v_mov_b32 v3, s11\n"
"s_mov_b64 exec, 3\n"
"global_store_dword v2, v3, s[50:51] sc1\n"
"s_mov_b64 exec, -1\n"
".Lsig15:\n"
"ds_read_b64_tr_b16 v[96:97], v113 offset:0\n"
"ds_read_b64_tr_b16 v[98:99], v113 offset:512\n"
"ds_read_b64_tr_b16 v[100:101], v113 offset:1024\n"
"ds_read_b64_tr_b16 v[102:103], v113 offset:1536\n"
"ds_read_b64_tr_b16 v[104:105], v113 offset:2048\n"
"ds_read_b64_tr_b16 v[106:107], v113 offset:2560\n"
"s_waitcnt lgkmcnt(4)\n"
"v_mfma_f32_32x32x16_bf16 v[144:159], v[96:99], a[192:195], 0\n"
"ds_read_b64_tr_b16 v[108:109], v113 offset:3072\n"
"ds_read_b64_tr_b16 v[110:111], v113 offset:3584\n"
"s_waitcnt lgkmcnt(4)\n"
"v_mfma_f32_32x32x16_bf16 v[144:159], v[100:103], a[196:199], v[144:159]\n"
"ds_read_b64_tr_b16 v[96:97], v113 offset:4096\n"
"ds_read_b64_tr_b16 v[98:99], v113 offset:4608\n"
"s_waitcnt lgkmcnt(4)\n"
"v_mfma_f32_32x32x16_bf16 v[144:159], v[104:107], a[200:203], v[144:159]\n"
"ds_read_b64_tr_b16 v[100:101], v113 offset:5120\n"
"ds_read_b64_tr_b16 v[102:103], v113 offset:5632\n"
"s_waitcnt lgkmcnt(4)\n"
"v_mfma_f32_32x32x16_bf16 v[144:159], v[108:111], a[204:207], v[144:159]\n"
"ds_read_b64_tr_b16 v[104:105], v113 offset:6144\n"
"ds_read_b64_tr_b16 v[106:107], v113 offset:6656\n"
"s_waitcnt lgkmcnt(4)\n"
"v_mfma_f32_32x32x16_bf16 v[144:159], v[96:99], a[208:211], v[144:159]\n"
"ds_read_b64_tr_b16 v[108:109], v113 offset:7168\n"
"ds_read_b64_tr_b16 v[110:111], v113 offset:7680\n"
"s_waitcnt lgkmcnt(4)\n"
"v_mfma_f32_32x32x16_bf16 v[144:159], v[100:103], a[212:215], v[144:159]\n"
"ds_read_b64_tr_b16 v[96:97], v113 offset:8192\n"
"ds_read_b64_tr_b16 v[98:99], v113 offset:8704\n"
"s_waitcnt lgkmcnt(4)\n"
"v_mfma_f32_32x32x16_bf16 v[144:159], v[104:107], a[216:219], v[144:159]\n"
"ds_read_b64_tr_b16 v[100:101], v113 offset:9216\n"
"ds_read_b64_tr_b16 v[102:103], v113 offset:9728\n"
"s_waitcnt lgkmcnt(4)\n"
"v_mfma_f32_32x32x16_bf16 v[144:159], v[108:111], a[220:223], v[144:159]\n"
"ds_read_b64_tr_b16 v[104:105], v113 offset:10240\n"
"ds_read_b64_tr_b16 v[106:107], v113 offset:10752\n"
"s_waitcnt lgkmcnt(4)\n"
"v_mfma_f32_32x32x16_bf16 v[144:159], v[96:99], a[224:227], v[144:159]\n"
"ds_read_b64_tr_b16 v[108:109], v113 offset:11264\n"
"ds_read_b64_tr_b16 v[110:111], v113 offset:11776\n"
"s_waitcnt lgkmcnt(4)\n"
"v_mfma_f32_32x32x16_bf16 v[144:159], v[100:103], a[228:231], v[144:159]\n"
"ds_read_b64_tr_b16 v[96:97], v113 offset:12288\n"
"ds_read_b64_tr_b16 v[98:99], v113 offset:12800\n"
"s_waitcnt lgkmcnt(4)\n"
"v_mfma_f32_32x32x16_bf16 v[144:159], v[104:107], a[232:235], v[144:159]\n"
"ds_read_b64_tr_b16 v[100:101], v113 offset:13312\n"
"ds_read_b64_tr_b16 v[102:103], v113 offset:13824\n"
"s_waitcnt lgkmcnt(4)\n"
"v_mfma_f32_32x32x16_bf16 v[144:159], v[108:111], a[236:239], v[144:159]\n"
"ds_read_b64_tr_b16 v[104:105], v113 offset:14336\n"
"ds_read_b64_tr_b16 v[106:107], v113 offset:14848\n"
"s_waitcnt lgkmcnt(4)\n"
"v_mfma_f32_32x32x16_bf16 v[144:159], v[96:99], a[240:243], v[144:159]\n"
"ds_read_b64_tr_b16 v[108:109], v113 offset:15360\n"
"ds_read_b64_tr_b16 v[110:111], v113 offset:15872\n"
"s_waitcnt lgkmcnt(4)\n"
"v_mfma_f32_32x32x16_bf16 v[144:159], v[100:103], a[244:247], v[144:159]\n"
"ds_read_b64_tr_b16 v[96:97], v113 offset:16384\n"
"ds_read_b64_tr_b16 v[98:99], v113 offset:16896\n"
"s_waitcnt lgkmcnt(4)\n"
"v_mfma_f32_32x32x16_bf16 v[144:159], v[104:107], a[248:251], v[144:159]\n"
"ds_read_b64_tr_b16 v[100:101], v113 offset:17408\n"
"ds_read_b64_tr_b16 v[102:103], v113 offset:17920\n"
"s_waitcnt lgkmcnt(4)\n"
"v_mfma_f32_32x32x16_bf16 v[144:159], v[108:111], a[252:255], v[144:159]\n"
"ds_read_b64_tr_b16 v[104:105], v113 offset:18432\n"
"ds_read_b64_tr_b16 v[106:107], v113 offset:18944\n"
"s_waitcnt lgkmcnt(4)\n"
"v_mfma_f32_32x32x16_bf16 v[144:159], v[96:99], a[0:3], v[144:159]\n"
"ds_read_b64_tr_b16 v[108:109], v113 offset:19456\n"
"ds_read_b64_tr_b16 v[110:111], v113 offset:19968\n"
"s_waitcnt lgkmcnt(4)\n"
"v_mfma_f32_32x32x16_bf16 v[144:159], v[100:103], a[4:7], v[144:159]\n"
"ds_read_b64_tr_b16 v[96:97], v113 offset:20480\n"
"ds_read_b64_tr_b16 v[98:99], v113 offset:20992\n"
"s_waitcnt lgkmcnt(4)\n"
"v_mfma_f32_32x32x16_bf16 v[144:159], v[104:107], a[8:11], v[144:159]\n"
"ds_read_b64_tr_b16 v[100:101], v113 offset:21504\n"
"ds_read_b64_tr_b16 v[102:103], v113 offset:22016\n"
"s_waitcnt lgkmcnt(4)\n"
"v_mfma_f32_32x32x16_bf16 v[144:159], v[108:111], a[12:15], v[144:159]\n"
"ds_read_b64_tr_b16 v[104:105], v113 offset:22528\n"
"ds_read_b64_tr_b16 v[106:107], v113 offset:23040\n"
"s_waitcnt lgkmcnt(4)\n"
"v_mfma_f32_32x32x16_bf16 v[144:159], v[96:99], a[16:19], v[144:159]\n"
"ds_read_b64_tr_b16 v[108:109], v113 offset:23552\n"
"ds_read_b64_tr_b16 v[110:111], v113 offset:24064\n"
"s_waitcnt lgkmcnt(4)\n"
"v_mfma_f32_32x32x16_bf16 v[144:159], v[100:103], a[20:23], v[144:159]\n"
"ds_read_b64_tr_b16 v[96:97], v113 offset:24576\n"
"ds_read_b64_tr_b16 v[98:99], v113 offset:25088\n"
"s_waitcnt lgkmcnt(4)\n"
"v_mfma_f32_32x32x16_bf16 v[144:159], v[104:107], a[24:27], v[144:159]\n"
"ds_read_b64_tr_b16 v[100:101], v113 offset:25600\n"
"ds_read_b64_tr_b16 v[102:103], v113 offset:26112\n"
"s_waitcnt lgkmcnt(4)\n"
"v_mfma_f32_32x32x16_bf16 v[144:159], v[108:111], a[28:31], v[144:159]\n"
"ds_read_b64_tr_b16 v[104:105], v113 offset:26624\n"
"ds_read_b64_tr_b16 v[106:107], v113 offset:27136\n"
"s_waitcnt lgkmcnt(4)\n"
"v_mfma_f32_32x32x16_bf16 v[144:159], v[96:99], a[32:35], v[144:159]\n"
"ds_read_b64_tr_b16 v[108:109], v113 offset:27648\n"
"ds_read_b64_tr_b16 v[110:111], v113 offset:28160\n"
"s_waitcnt lgkmcnt(4)\n"
"v_mfma_f32_32x32x16_bf16 v[144:159], v[100:103], a[36:39], v[144:159]\n"
"ds_read_b64_tr_b16 v[96:97], v113 offset:28672\n"
"ds_read_b64_tr_b16 v[98:99], v113 offset:29184\n"
"s_waitcnt lgkmcnt(4)\n"
"v_mfma_f32_32x32x16_bf16 v[144:159], v[104:107], a[40:43], v[144:159]\n"
"ds_read_b64_tr_b16 v[100:101], v113 offset:29696\n"
"ds_read_b64_tr_b16 v[102:103], v113 offset:30208\n"
"s_waitcnt lgkmcnt(4)\n"
"v_mfma_f32_32x32x16_bf16 v[144:159], v[108:111], a[44:47], v[144:159]\n"
"ds_read_b64_tr_b16 v[104:105], v113 offset:30720\n"
"ds_read_b64_tr_b16 v[106:107], v113 offset:31232\n"
"s_waitcnt lgkmcnt(4)\n"
"v_mfma_f32_32x32x16_bf16 v[144:159], v[96:99], a[48:51], v[144:159]\n"
"ds_read_b64_tr_b16 v[108:109], v113 offset:31744\n"
"ds_read_b64_tr_b16 v[110:111], v113 offset:32256\n"
"s_waitcnt lgkmcnt(4)\n"
"v_mfma_f32_32x32x16_bf16 v[144:159], v[100:103], a[52:55], v[144:159]\n"
"s_nop 7\n"
"s_waitcnt lgkmcnt(2)\n"
"v_mfma_f32_32x32x16_bf16 v[144:159], v[104:107], a[56:59], v[144:159]\n"
"v_pk_fma_f32 v[128:129], v[116:117], v[32:33], v[128:129] op_sel_hi:[0,1,1] neg_lo:[1,0,0] neg_hi:[1,0,0]\n"
"v_pk_fma_f32 v[130:131], v[116:117], v[34:35], v[130:131] op_sel_hi:[0,1,1] neg_lo:[1,0,0] neg_hi:[1,0,0]\n"
"v_pk_fma_f32 v[132:133], v[116:117], v[36:37], v[132:133] op_sel_hi:[0,1,1] neg_lo:[1,0,0] neg_hi:[1,0,0]\n"
"v_pk_fma_f32 v[134:135], v[116:117], v[38:39], v[134:135] op_sel_hi:[0,1,1] neg_lo:[1,0,0] neg_hi:[1,0,0]\n"
"s_waitcnt lgkmcnt(0)\n"
"v_mfma_f32_32x32x16_bf16 v[144:159], v[108:111], a[60:63], v[144:159]\n"
"v_pk_fma_f32 v[136:137], v[116:117], v[40:41], v[136:137] op_sel_hi:[0,1,1] neg_lo:[1,0,0] neg_hi:[1,0,0]\n"
"v_pk_fma_f32 v[138:139], v[116:117], v[42:43], v[138:139] op_sel_hi:[0,1,1] neg_lo:[1,0,0] neg_hi:[1,0,0]\n"
"v_pk_fma_f32 v[140:141], v[116:117], v[44:45], v[140:141] op_sel_hi:[0,1,1] neg_lo:[1,0,0] neg_hi:[1,0,0]\n"
"v_pk_fma_f32 v[142:143], v[116:117], v[46:47], v[142:143] op_sel_hi:[0,1,1] neg_lo:[1,0,0] neg_hi:[1,0,0]\n"
"s_nop 3\n"
"s_waitcnt vmcnt(0)\n"
"s_barrier\n"
"ds_read_b64_tr_b16 v[96:97], v113 offset:32768\n"
"ds_read_b64_tr_b16 v[98:99], v113 offset:33280\n"
"ds_read_b64_tr_b16 v[100:101], v113 offset:33792\n"
"ds_read_b64_tr_b16 v[102:103], v113 offset:34304\n"
"ds_read_b64_tr_b16 v[104:105], v113 offset:34816\n"
"ds_read_b64_tr_b16 v[106:107], v113 offset:35328\n"
"s_waitcnt lgkmcnt(4)\n"
"v_mfma_f32_32x32x16_bf16 v[160:175], v[96:99], a[192:195], 0\n"
"ds_read_b64_tr_b16 v[108:109], v113 offset:35840\n"
"ds_read_b64_tr_b16 v[110:111], v113 offset:36352\n"
"v_pk_mul_f32 v[128:129], v[128:129], v[118:119] op_sel_hi:[1,0]\n"
"v_pk_mul_f32 v[130:131], v[130:131], v[118:119] op_sel_hi:[1,0]\n"
"s_waitcnt lgkmcnt(4)\n"
"v_mfma_f32_32x32x16_bf16 v[160:175], v[100:103], a[196:199], v[160:175]\n"
"ds_read_b64_tr_b16 v[96:97], v113 offset:36864\n"
"ds_read_b64_tr_b16 v[98:99], v113 offset:37376\n"
"v_pk_mul_f32 v[132:133], v[132:133], v[118:119] op_sel_hi:[1,0]\n"
"v_pk_mul_f32 v[134:135], v[134:135], v[118:119] op_sel_hi:[1,0]\n"
"s_waitcnt lgkmcnt(4)\n"
"v_mfma_f32_32x32x16_bf16 v[160:175], v[104:107], a[200:203], v[160:175]\n"
"ds_read_b64_tr_b16 v[100:101], v113 offset:37888\n"
"ds_read_b64_tr_b16 v[102:103], v113 offset:38400\n"
"v_pk_mul_f32 v[136:137], v[136:137], v[118:119] op_sel_hi:[1,0]\n"
"v_pk_mul_f32 v[138:139], v[138:139], v[118:119] op_sel_hi:[1,0]\n"
"s_waitcnt lgkmcnt(4)\n"
"v_mfma_f32_32x32x16_bf16 v[160:175], v[108:111], a[204:207], v[160:175]\n"
"ds_read_b64_tr_b16 v[104:105], v113 offset:38912\n"
"ds_read_b64_tr_b16 v[106:107], v113 offset:39424\n"
"v_pk_mul_f32 v[140:141], v[140:141], v[118:119] op_sel_hi:[1,0]\n"
"v_pk_mul_f32 v[142:143], v[142:143], v[118:119] op_sel_hi:[1,0]\n"
"s_waitcnt lgkmcnt(4)\n"
"v_mfma_f32_32x32x16_bf16 v[160:175], v[96:99], a[208:211], v[160:175]\n"
"ds_read_b64_tr_b16 v[108:109], v113 offset:39936\n"
"ds_read_b64_tr_b16 v[110:111], v113 offset:40448\n"
"v_pk_mul_f32 v[192:193], v[128:129], v[128:129]\n"
"v_pk_mul_f32 v[194:195], v[130:131], v[130:131]\n"
"s_waitcnt lgkmcnt(4)\n"
"v_mfma_f32_32x32x16_bf16 v[160:175], v[100:103], a[212:215], v[160:175]\n"
"ds_read_b64_tr_b16 v[96:97], v113 offset:40960\n"
"ds_read_b64_tr_b16 v[98:99], v113 offset:41472\n"
"v_pk_mul_f32 v[196:197], v[132:133], v[132:133]\n"
"v_pk_mul_f32 v[198:199], v[134:135], v[134:135]\n"
"s_waitcnt lgkmcnt(4)\n"
"v_mfma_f32_32x32x16_bf16 v[160:175], v[104:107], a[216:219], v[160:175]\n"
"ds_read_b64_tr_b16 v[100:101], v113 offset:41984\n"
"ds_read_b64_tr_b16 v[102:103], v113 offset:42496\n"
"v_pk_mul_f32 v[200:201], v[136:137], v[136:137]\n"
"v_pk_mul_f32 v[202:203], v[138:139], v[138:139]\n"
"s_waitcnt lgkmcnt(4)\n"
"v_mfma_f32_32x32x16_bf16 v[160:175], v[108:111], a[220:223], v[160:175]\n"
"ds_read_b64_tr_b16 v[104:105], v113 offset:43008\n"
"ds_read_b64_tr_b16 v[106:107], v113 offset:43520\n"
"v_pk_mul_f32 v[204:205], v[140:141], v[140:141]\n"
"v_pk_mul_f32 v[206:207], v[142:143], v[142:143]\n"
"s_waitcnt lgkmcnt(4)\n"
"v_mfma_f32_32x32x16_bf16 v[160:175], v[96:99], a[224:227], v[160:175]\n"
"ds_read_b64_tr_b16 v[108:109], v113 offset:44032\n"
"ds_read_b64_tr_b16 v[110:111], v113 offset:44544\n"
"v_pk_fma_f32 v[192:193], v[192:193], s[54:55], v[120:121] op_sel_hi:[1,1,0]\n"
"v_pk_fma_f32 v[194:195], v[194:195], s[54:55], v[120:121] op_sel_hi:[1,1,0]\n"
"s_waitcnt lgkmcnt(4)\n"
"v_mfma_f32_32x32x16_bf16 v[160:175], v[100:103], a[228:231], v[160:175]\n"
"ds_read_b64_tr_b16 v[96:97], v113 offset:45056\n"
"ds_read_b64_tr_b16 v[98:99], v113 offset:45568\n"
"v_pk_fma_f32 v[196:197], v[196:197], s[54:55], v[120:121] op_sel_hi:[1,1,0]\n"
"v_pk_fma_f32 v[198:199], v[198:199], s[54:55], v[120:121] op_sel_hi:[1,1,0]\n"
"s_waitcnt lgkmcnt(4)\n"
"v_mfma_f32_32x32x16_bf16 v[160:175], v[104:107], a[232:235], v[160:175]\n"
"ds_read_b64_tr_b16 v[100:101], v113 offset:46080\n"
"ds_read_b64_tr_b16 v[102:103], v113 offset:46592\n"
"v_pk_fma_f32 v[200:201], v[200:201], s[54:55], v[120:121] op_sel_hi:[1,1,0]\n"
"v_pk_fma_f32 v[202:203], v[202:203], s[54:55], v[120:121] op_sel_hi:[1,1,0]\n"
"s_waitcnt lgkmcnt(4)\n"
"v_mfma_f32_32x32x16_bf16 v[160:175], v[108:111], a[236:239], v[160:175]\n"
"ds_read_b64_tr_b16 v[104:105], v113 offset:47104\n"
"ds_read_b64_tr_b16 v[106:107], v113 offset:47616\n"
"v_pk_fma_f32 v[204:205], v[204:205], s[54:55], v[120:121] op_sel_hi:[1,1,0]\n"
"v_pk_fma_f32 v[206:207], v[206:207], s[54:55], v[120:121] op_sel_hi:[1,1,0]\n"
"s_waitcnt lgkmcnt(4)\n"
"v_mfma_f32_32x32x16_bf16 v[160:175], v[96:99], a[240:243], v[160:175]\n"
"ds_read_b64_tr_b16 v[108:109], v113 offset:48128\n"
"ds_read_b64_tr_b16 v[110:111], v113 offset:48640\n"
"v_pk_mul_f32 v[192:193], v[192:193], v[128:129]\n"
"v_pk_mul_f32 v[194:195], v[194:195], v[130:131]\n"
"s_waitcnt lgkmcnt(4)\n"
"v_mfma_f32_32x32x16_bf16 v[160:175], v[100:103], a[244:247], v[160:175]\n"
"ds_read_b64_tr_b16 v[96:97], v113 offset:49152\n"
"ds_read_b64_tr_b16 v[98:99], v113 offset:49664\n"
"v_pk_mul_f32 v[196:197], v[196:197], v[132:133]\n"
"v_pk_mul_f32 v[198:199], v[198:199], v[134:135]\n"
"s_waitcnt lgkmcnt(4)\n"
"v_mfma_f32_32x32x16_bf16 v[160:175], v[104:107], a[248:251], v[160:175]\n"
"ds_read_b64_tr_b16 v[100:101], v113 offset:50176\n"
"ds_read_b64_tr_b16 v[102:103], v113 offset:50688\n"
"v_pk_mul_f32 v[200:201], v[200:201], v[136:137]\n"
"v_pk_mul_f32 v[202:203], v[202:203], v[138:139]\n"
"s_waitcnt lgkmcnt(4)\n"
"v_mfma_f32_32x32x16_bf16 v[160:175], v[108:111], a[252:255], v[160:175]\n"
"ds_read_b64_tr_b16 v[104:105], v113 offset:51200\n"
"ds_read_b64_tr_b16 v[106:107], v113 offset:51712\n"
"v_pk_mul_f32 v[204:205], v[204:205], v[140:141]\n"
"v_pk_mul_f32 v[206:207], v[206:207], v[142:143]\n"
"s_waitcnt lgkmcnt(4)\n"
"v_mfma_f32_32x32x16_bf16 v[160:175], v[96:99], a[0:3], v[160:175]\n"
"ds_read_b64_tr_b16 v[108:109], v113 offset:52224\n"
"ds_read_b64_tr_b16 v[110:111], v113 offset:52736\n"
"v_exp_f32 v192, -v192\n"
"s_waitcnt lgkmcnt(4)\n"
"v_mfma_f32_32x32x16_bf16 v[160:175], v[100:103], a[4:7], v[160:175]\n"
"ds_read_b64_tr_b16 v[96:97], v113 offset:53248\n"
"ds_read_b64_tr_b16 v[98:99], v113 offset:53760\n"
"v_exp_f32 v193, -v193\n"
"s_waitcnt lgkmcnt(4)\n"
"v_mfma_f32_32x32x16_bf16 v[160:175], v[104:107], a[8:11], v[160:175]\n"
"ds_read_b64_tr_b16 v[100:101], v113 offset:54272\n"
"ds_read_b64_tr_b16 v[102:103], v113 offset:54784\n"
"v_exp_f32 v194, -v194\n"
"s_waitcnt lgkmcnt(4)\n"
"v_mfma_f32_32x32x16_bf16 v[160:175], v[108:111], a[12:15], v[160:175]\n"
"ds_read_b64_tr_b16 v[104:105], v113 offset:55296\n"
"ds_read_b64_tr_b16 v[106:107], v113 offset:55808\n"
"v_exp_f32 v195, -v195\n"
"s_waitcnt lgkmcnt(4)\n"
"v_mfma_f32_32x32x16_bf16 v[160:175], v[96:99], a[16:19], v[160:175]\n"
"ds_read_b64_tr_b16 v[108:109], v113 offset:56320\n"
"ds_read_b64_tr_b16 v[110:111], v113 offset:56832\n"
"v_exp_f32 v196, -v196\n"
"s_waitcnt lgkmcnt(4)\n"
"v_mfma_f32_32x32x16_bf16 v[160:175], v[100:103], a[20:23], v[160:175]\n"
"ds_read_b64_tr_b16 v[96:97], v113 offset:57344\n"
"ds_read_b64_tr_b16 v[98:99], v113 offset:57856\n"
"v_exp_f32 v197, -v197\n"
"s_waitcnt lgkmcnt(4)\n"
"v_mfma_f32_32x32x16_bf16 v[160:175], v[104:107], a[24:27], v[160:175]\n"
"ds_read_b64_tr_b16 v[100:101], v113 offset:58368\n"
"ds_read_b64_tr_b16 v[102:103], v113 offset:58880\n"
"v_exp_f32 v198, -v198\n"
"s_waitcnt lgkmcnt(4)\n"
"v_mfma_f32_32x32x16_bf16 v[160:175], v[108:111], a[28:31], v[160:175]\n"
"ds_read_b64_tr_b16 v[104:105], v113 offset:59392\n"
"ds_read_b64_tr_b16 v[106:107], v113 offset:59904\n"
"v_exp_f32 v199, -v199\n"
"s_waitcnt lgkmcnt(4)\n"
"v_mfma_f32_32x32x16_bf16 v[160:175], v[96:99], a[32:35], v[160:175]\n"
"ds_read_b64_tr_b16 v[108:109], v113 offset:60416\n"
"ds_read_b64_tr_b16 v[110:111], v113 offset:60928\n"
"v_exp_f32 v200, -v200\n"
"s_waitcnt lgkmcnt(4)\n"
"v_mfma_f32_32x32x16_bf16 v[160:175], v[100:103], a[36:39], v[160:175]\n"
"ds_read_b64_tr_b16 v[96:97], v113 offset:61440\n"
"ds_read_b64_tr_b16 v[98:99], v113 offset:61952\n"
"v_exp_f32 v201, -v201\n"
"s_waitcnt lgkmcnt(4)\n"
"v_mfma_f32_32x32x16_bf16 v[160:175], v[104:107], a[40:43], v[160:175]\n"
"ds_read_b64_tr_b16 v[100:101], v113 offset:62464\n"
"ds_read_b64_tr_b16 v[102:103], v113 offset:62976\n"
"v_exp_f32 v202, -v202\n"
"s_waitcnt lgkmcnt(4)\n"
"v_mfma_f32_32x32x16_bf16 v[160:175], v[108:111], a[44:47], v[160:175]\n"
"ds_read_b64_tr_b16 v[104:105], v113 offset:63488\n"
"ds_read_b64_tr_b16 v[106:107], v113 offset:64000\n"
"v_exp_f32 v203, -v203\n"
"s_waitcnt lgkmcnt(4)\n"
"v_mfma_f32_32x32x16_bf16 v[160:175], v[96:99], a[48:51], v[160:175]\n"
"ds_read_b64_tr_b16 v[108:109], v113 offset:64512\n"
"ds_read_b64_tr_b16 v[110:111], v113 offset:65024\n"
"v_exp_f32 v204, -v204\n"
"s_waitcnt lgkmcnt(4)\n"
"v_mfma_f32_32x32x16_bf16 v[160:175], v[100:103], a[52:55], v[160:175]\n"
"v_exp_f32 v205, -v205\n"
"v_exp_f32 v206, -v206\n"
"s_waitcnt lgkmcnt(2)\n"
"v_mfma_f32_32x32x16_bf16 v[160:175], v[104:107], a[56:59], v[160:175]\n"
"v_exp_f32 v207, -v207\n"
"v_pk_add_f32 v[192:193], v[192:193], s[56:57]\n"
"v_pk_add_f32 v[194:195], v[194:195], s[56:57]\n"
"s_waitcnt lgkmcnt(0)\n"
"v_mfma_f32_32x32x16_bf16 v[160:175], v[108:111], a[60:63], v[160:175]\n"
"v_pk_add_f32 v[196:197], v[196:197], s[56:57]\n"
"v_pk_add_f32 v[198:199], v[198:199], s[56:57]\n"
"v_pk_add_f32 v[200:201], v[200:201], s[56:57]\n"
"v_pk_add_f32 v[202:203], v[202:203], s[56:57]\n"
"v_pk_add_f32 v[204:205], v[204:205], s[56:57]\n"
"v_pk_add_f32 v[206:207], v[206:207], s[56:57]\n"
"v_rcp_f32 v192, v192\n"
"v_rcp_f32 v193, v193\n"
"v_rcp_f32 v194, v194\n"
"v_rcp_f32 v195, v195\n"
"v_rcp_f32 v196, v196\n"
"v_rcp_f32 v197, v197\n"
"v_rcp_f32 v198, v198\n"
"v_rcp_f32 v199, v199\n"
"v_rcp_f32 v200, v200\n"
"v_rcp_f32 v201, v201\n"
"v_rcp_f32 v202, v202\n"
"v_rcp_f32 v203, v203\n"
"v_rcp_f32 v204, v204\n"
"v_rcp_f32 v205, v205\n"
"v_rcp_f32 v206, v206\n"
"v_rcp_f32 v207, v207\n"
"v_pk_mul_f32 v[128:129], v[128:129], v[192:193]\n"
"v_pk_mul_f32 v[130:131], v[130:131], v[194:195]\n"
"v_pk_mul_f32 v[132:133], v[132:133], v[196:197]\n"
"v_pk_mul_f32 v[134:135], v[134:135], v[198:199]\n"
"v_pk_mul_f32 v[136:137], v[136:137], v[200:201]\n"
"v_pk_mul_f32 v[138:139], v[138:139], v[202:203]\n"
"v_pk_mul_f32 v[140:141], v[140:141], v[204:205]\n"
"v_pk_mul_f32 v[142:143], v[142:143], v[206:207]\n"
"v_cvt_pk_bf16_f32 v208, v128, v129\n"
"v_cvt_pk_bf16_f32 v209, v130, v131\n"
"v_cvt_pk_bf16_f32 v210, v132, v133\n"
"v_cvt_pk_bf16_f32 v211, v134, v135\n"
"v_cvt_pk_bf16_f32 v212, v136, v137\n"
"v_cvt_pk_bf16_f32 v213, v138, v139\n"
"v_cvt_pk_bf16_f32 v214, v140, v141\n"
"v_cvt_pk_bf16_f32 v215, v142, v143\n"
"s_mul_i32 s34, s31, 3\n"
"s_add_u32 s34, s34, 0\n"
"s_lshl_b32 s36, s34, 11\n"
"s_mul_i32 s35, s46, 88064\n"
"s_add_u32 s36, s36, s35\n"
"s_cmp_lt_u32 s34, 43\n"
"s_cselect_b32 s36, s36, 0x40000000\n"
"buffer_store_dwordx4 v[208:211], v1, s[24:27], s36 offen offset:0 sc1\n"
"buffer_store_dwordx4 v[212:215], v1, s[24:27], s36 offen offset:1024 sc1\n"
"s_nop 7\n"
"v_pk_fma_f32 v[144:145], v[116:117], v[48:49], v[144:145] op_sel_hi:[0,1,1] neg_lo:[1,0,0] neg_hi:[1,0,0]\n"
"v_pk_fma_f32 v[146:147], v[116:117], v[50:51], v[146:147] op_sel_hi:[0,1,1] neg_lo:[1,0,0] neg_hi:[1,0,0]\n"
"v_pk_fma_f32 v[148:149], v[116:117], v[52:53], v[148:149] op_sel_hi:[0,1,1] neg_lo:[1,0,0] neg_hi:[1,0,0]\n"
"v_pk_fma_f32 v[150:151], v[116:117], v[54:55], v[150:151] op_sel_hi:[0,1,1] neg_lo:[1,0,0] neg_hi:[1,0,0]\n"
"v_pk_fma_f32 v[152:153], v[116:117], v[56:57], v[152:153] op_sel_hi:[0,1,1] neg_lo:[1,0,0] neg_hi:[1,0,0]\n"
"v_pk_fma_f32 v[154:155], v[116:117], v[58:59], v[154:155] op_sel_hi:[0,1,1] neg_lo:[1,0,0] neg_hi:[1,0,0]\n"
"v_pk_fma_f32 v[156:157], v[116:117], v[60:61], v[156:157] op_sel_hi:[0,1,1] neg_lo:[1,0,0] neg_hi:[1,0,0]\n"
"v_pk_fma_f32 v[158:159], v[116:117], v[62:63], v[158:159] op_sel_hi:[0,1,1] neg_lo:[1,0,0] neg_hi:[1,0,0]\n"
"v_pk_mul_f32 v[144:145], v[144:145], v[118:119] op_sel_hi:[1,0]\n"
"v_pk_mul_f32 v[146:147], v[146:147], v[118:119] op_sel_hi:[1,0]\n"
"v_pk_mul_f32 v[148:149], v[148:149], v[118:119] op_sel_hi:[1,0]\n"
"v_pk_mul_f32 v[150:151], v[150:151], v[118:119] op_sel_hi:[1,0]\n"
"v_pk_mul_f32 v[152:153], v[152:153], v[118:119] op_sel_hi:[1,0]\n"
"v_pk_mul_f32 v[154:155], v[154:155], v[118:119] op_sel_hi:[1,0]\n"
"v_pk_mul_f32 v[156:157], v[156:157], v[118:119] op_sel_hi:[1,0]\n"
"v_pk_mul_f32 v[158:159], v[158:159], v[118:119] op_sel_hi:[1,0]\n"
"v_pk_mul_f32 v[192:193], v[144:145], v[144:145]\n"
"v_pk_mul_f32 v[194:195], v[146:147], v[146:147]\n"
"v_pk_mul_f32 v[196:197], v[148:149], v[148:149]\n"
"v_pk_mul_f32 v[198:199], v[150:151], v[150:151]\n"
"v_pk_mul_f32 v[200:201], v[152:153], v[152:153]\n"
"v_pk_mul_f32 v[202:203], v[154:155], v[154:155]\n"
"v_pk_mul_f32 v[204:205], v[156:157], v[156:157]\n"
"v_pk_mul_f32 v[206:207], v[158:159], v[158:159]\n"
"v_pk_fma_f32 v[192:193], v[192:193], s[54:55], v[120:121] op_sel_hi:[1,1,0]\n"
"v_pk_fma_f32 v[194:195], v[194:195], s[54:55], v[120:121] op_sel_hi:[1,1,0]\n"
"v_pk_fma_f32 v[196:197], v[196:197], s[54:55], v[120:121] op_sel_hi:[1,1,0]\n"
"v_pk_fma_f32 v[198:199], v[198:199], s[54:55], v[120:121] op_sel_hi:[1,1,0]\n"
"v_pk_fma_f32 v[200:201], v[200:201], s[54:55], v[120:121] op_sel_hi:[1,1,0]\n"
"v_pk_fma_f32 v[202:203], v[202:203], s[54:55], v[120:121] op_sel_hi:[1,1,0]\n"
"v_pk_fma_f32 v[204:205], v[204:205], s[54:55], v[120:121] op_sel_hi:[1,1,0]\n"
"v_pk_fma_f32 v[206:207], v[206:207], s[54:55], v[120:121] op_sel_hi:[1,1,0]\n"
"v_pk_mul_f32 v[192:193], v[192:193], v[144:145]\n"
"v_pk_mul_f32 v[194:195], v[194:195], v[146:147]\n"
"v_pk_mul_f32 v[196:197], v[196:197], v[148:149]\n"
"v_pk_mul_f32 v[198:199], v[198:199], v[150:151]\n"
"v_pk_mul_f32 v[200:201], v[200:201], v[152:153]\n"
"v_pk_mul_f32 v[202:203], v[202:203], v[154:155]\n"
"v_pk_mul_f32 v[204:205], v[204:205], v[156:157]\n"
"v_pk_mul_f32 v[206:207], v[206:207], v[158:159]\n"
"v_exp_f32 v192, -v192\n"
"v_exp_f32 v193, -v193\n"
"v_exp_f32 v194, -v194\n"
"v_exp_f32 v195, -v195\n"
"v_exp_f32 v196, -v196\n"
"v_exp_f32 v197, -v197\n"
"v_exp_f32 v198, -v198\n"
"v_exp_f32 v199, -v199\n"
"v_exp_f32 v200, -v200\n"
"v_exp_f32 v201, -v201\n"
"v_exp_f32 v202, -v202\n"
"v_exp_f32 v203, -v203\n"
"v_exp_f32 v204, -v204\n"
"v_exp_f32 v205, -v205\n"
"v_exp_f32 v206, -v206\n"
"v_exp_f32 v207, -v207\n"
"v_pk_add_f32 v[192:193], v[192:193], s[56:57]\n"
"v_pk_add_f32 v[194:195], v[194:195], s[56:57]\n"
"v_pk_add_f32 v[196:197], v[196:197], s[56:57]\n"
"v_pk_add_f32 v[198:199], v[198:199], s[56:57]\n"
"v_pk_add_f32 v[200:201], v[200:201], s[56:57]\n"
"v_pk_add_f32 v[202:203], v[202:203], s[56:57]\n"
"v_pk_add_f32 v[204:205], v[204:205], s[56:57]\n"
"v_pk_add_f32 v[206:207], v[206:207], s[56:57]\n"
"v_rcp_f32 v192, v192\n"
"v_rcp_f32 v193, v193\n"
"v_rcp_f32 v194, v194\n"
"v_rcp_f32 v195, v195\n"
"v_rcp_f32 v196, v196\n"
"v_rcp_f32 v197, v197\n"
"v_rcp_f32 v198, v198\n"
"v_rcp_f32 v199, v199\n"
"v_rcp_f32 v200, v200\n"
"v_rcp_f32 v201, v201\n"
"v_rcp_f32 v202, v202\n"
"v_rcp_f32 v203, v203\n"
"v_rcp_f32 v204, v204\n"
"v_rcp_f32 v205, v205\n"
"v_rcp_f32 v206, v206\n"
"v_rcp_f32 v207, v207\n"
"v_pk_mul_f32 v[144:145], v[144:145], v[192:193]\n"
"v_pk_mul_f32 v[146:147], v[146:147], v[194:195]\n"
"v_pk_mul_f32 v[148:149], v[148:149], v[196:197]\n"
"v_pk_mul_f32 v[150:151], v[150:151], v[198:199]\n"
"v_pk_mul_f32 v[152:153], v[152:153], v[200:201]\n"
"v_pk_mul_f32 v[154:155], v[154:155], v[202:203]\n"
"v_pk_mul_f32 v[156:157], v[156:157], v[204:205]\n"
"v_pk_mul_f32 v[158:159], v[158:159], v[206:207]\n"
"v_cvt_pk_bf16_f32 v208, v144, v145\n"
"v_cvt_pk_bf16_f32 v209, v146, v147\n"
"v_cvt_pk_bf16_f32 v210, v148, v149\n"
"v_cvt_pk_bf16_f32 v211, v150, v151\n"
"v_cvt_pk_bf16_f32 v212, v152, v153\n"
"v_cvt_pk_bf16_f32 v213, v154, v155\n"
"v_cvt_pk_bf16_f32 v214, v156, v157\n"
"v_cvt_pk_bf16_f32 v215, v158, v159\n"
"s_mul_i32 s34, s31, 3\n"
"s_add_u32 s34, s34, 1\n"
"s_lshl_b32 s36, s34, 11\n"
"s_mul_i32 s35, s46, 88064\n"
"s_add_u32 s36, s36, s35\n"
"s_cmp_lt_u32 s34, 43\n"
"s_cselect_b32 s36, s36, 0x40000000\n"
"buffer_store_dwordx4 v[208:211], v1, s[24:27], s36 offen offset:0 sc1\n"
"buffer_store_dwordx4 v[212:215], v1, s[24:27], s36 offen offset:1024 sc1\n"
"s_branch .Lg3_c1\n"
".Lg3_idle:\n"
"s_waitcnt vmcnt(0) lgkmcnt(0)\n"
"s_barrier\n"
"s_cmp_lg_u32 s29, 0\n"
"s_cbranch_scc1 .Lsig16\n"
"s_lshr_b32 s48, s28, 5\n"
"s_lshl_b32 s49, s48, 8\n"
"s_lshl_b32 s48, s48, 1\n"
"s_sub_u32 s72, 15, s48\n"
"s_lshl_b32 s72, s72, 8\n"
"s_and_b32 s48, s28, 7\n"
"s_lshl_b32 s48, s48, 2\n"
"s_bfe_u32 s73, s28, 0x20003\n"
"s_add_u32 s48, s48, s73\n"
"s_lshl_b32 s48, s48, 2\n"
"s_add_u32 s49, s49, s48\n"
"s_add_u32 s49, s49, 43264\n"
"v_mul_i32_i24 v2, s72, v0\n"
"v_add_u32 v2, s49, v2\n"
"v_mov_b32 v3, s11\n"
"s_mov_b64 exec, 3\n"
"global_store_dword v2, v3, s[50:51] sc1\n"
"s_mov_b64 exec, -1\n"
".Lsig16:\n"
"s_cmp_lg_u32 s28, 255\n"
"s_cbranch_scc1 .Lg3_c1\n"
"s_cmp_lg_u32 s29, 0\n"
"s_cbranch_scc1 .Lg3_c1\n"
"v_lshrrev_b32 v2, 4, v0\n"
"v_add_u32 v2, 0, v2\n"
"v_lshlrev_b32 v2, 8, v2\n"
"v_and_b32 v3, 15, v0\n"
"v_lshl_add_u32 v2, v3, 2, v2\n"
"v_add_u32 v2, 16640, v2\n"
"global_load_dword v3, v2, s[50:51] sc1\n"
"s_waitcnt vmcnt(0)\n"
"s_cmp_lg_u32 s29, 0\n"
"s_cbranch_scc1 .Lwok17\n"
"v_cmp_ne_u32 vcc, s11, v3\n"
"s_nop 1\n"
"s_cbranch_vccz .Lwok17\n"
"s_mov_b32 s49, 0\n"
".Lwait17:\n"
"s_sleep 2\n"
"global_load_dword v3, v2, s[50:51] sc1\n"
"s_waitcnt vmcnt(0)\n"
"v_cmp_ne_u32 vcc, s11, v3\n"
"s_nop 1\n"
"s_cbranch_vccz .Lwok17\n"
"s_add_u32 s49, s49, 1\n"
"s_cmp_lt_u32 s49, 32768\n"
"s_cbranch_scc1 .Lwait17\n"
".Lwok17:\n"
"v_lshrrev_b32 v2, 4, v0\n"
"v_add_u32 v2, 4, v2\n"
"v_lshlrev_b32 v2, 8, v2\n"
"v_and_b32 v3, 15, v0\n"
"v_lshl_add_u32 v2, v3, 2, v2\n"
"v_add_u32 v2, 16640, v2\n"
"global_load_dword v3, v2, s[50:51] sc1\n"
"s_waitcnt vmcnt(0)\n"
"s_cmp_lg_u32 s29, 0\n"
"s_cbranch_scc1 .Lwok18\n"
"v_cmp_ne_u32 vcc, s11, v3\n"
"s_nop 1\n"
"s_cbranch_vccz .Lwok18\n"
"s_mov_b32 s49, 0\n"
".Lwait18:\n"
"s_sleep 2\n"
"global_load_dword v3, v2, s[50:51] sc1\n"
"s_waitcnt vmcnt(0)\n"
"v_cmp_ne_u32 vcc, s11, v3\n"
"s_nop 1\n"
"s_cbranch_vccz .Lwok18\n"
"s_add_u32 s49, s49, 1\n"
"s_cmp_lt_u32 s49, 32768\n"
"s_cbranch_scc1 .Lwait18\n"
".Lwok18:\n"
"v_lshrrev_b32 v2, 4, v0\n"
"v_add_u32 v2, 8, v2\n"
"v_lshlrev_b32 v2, 8, v2\n"
"v_and_b32 v3, 15, v0\n"
"v_lshl_add_u32 v2, v3, 2, v2\n"
"v_add_u32 v2, 16640, v2\n"
"global_load_dword v3, v2, s[50:51] sc1\n"
"s_waitcnt vmcnt(0)\n"
"s_cmp_lg_u32 s29, 0\n"
"s_cbranch_scc1 .Lwok19\n"
"v_cmp_ne_u32 vcc, s11, v3\n"
"s_nop 1\n"
"s_cbranch_vccz .Lwok19\n"
"s_mov_b32 s49, 0\n"
".Lwait19:\n"
"s_sleep 2\n"
"global_load_dword v3, v2, s[50:51] sc1\n"
"s_waitcnt vmcnt(0)\n"
"v_cmp_ne_u32 vcc, s11, v3\n"
"s_nop 1\n"
"s_cbranch_vccz .Lwok19\n"
"s_add_u32 s49, s49, 1\n"
"s_cmp_lt_u32 s49, 32768\n"
"s_cbranch_scc1 .Lwait19\n"
".Lwok19:\n"
"v_lshrrev_b32 v2, 4, v0\n"
"v_add_u32 v2, 12, v2\n"
"v_lshlrev_b32 v2, 8, v2\n"
"v_and_b32 v3, 15, v0\n"
"v_lshl_add_u32 v2, v3, 2, v2\n"
"v_add_u32 v2, 16640, v2\n"
"global_load_dword v3, v2, s[50:51] sc1\n"
"s_waitcnt vmcnt(0)\n"
"s_cmp_lg_u32 s29, 0\n"
"s_cbranch_scc1 .Lwok20\n"
"v_cmp_ne_u32 vcc, s11, v3\n"
"s_nop 1\n"
"s_cbranch_vccz .Lwok20\n"
"s_mov_b32 s49, 0\n"
".Lwait20:\n"
"s_sleep 2\n"
"global_load_dword v3, v2, s[50:51] sc1\n"
"s_waitcnt vmcnt(0)\n"
"v_cmp_ne_u32 vcc, s11, v3\n"
"s_nop 1\n"
"s_cbranch_vccz .Lwok20\n"
"s_add_u32 s49, s49, 1\n"
"s_cmp_lt_u32 s49, 32768\n"
"s_cbranch_scc1 .Lwait20\n"
".Lwok20:\n"
"v_mov_b32 v2, 0\n"
"v_mov_b32 v3, s11\n"
"s_mov_b64 exec, 1\n"
"global_store_dword v2, v3, s[50:51] sc1\n"
"s_mov_b64 exec, -1\n"
".Lg3_c1:\n"
"s_waitcnt lgkmcnt(0)\n"
"s_barrier\n"
"s_and_b32 s74, s28, 3\n"
"s_bfe_u32 s34, s28, 0x10002\n"
"s_lshr_b32 s35, s28, 3\n"
"s_and_b32 s76, s35, 3\n"
"s_lshr_b32 s35, s35, 2\n"
"s_lshl_b32 s34, s34, 3\n"
"s_add_u32 s75, s34, s35\n"
"s_lshl_b32 s87, s75, 2\n"
"s_add_u32 s87, s87, s76\n"
"s_cmp_eq_u32 s74, 3\n"
"s_cbranch_scc1 .Ll3_g4\n"
"s_mul_i32 s34, s74, 21\n"
"s_cmp_gt_u32 s74, 0\n"
"s_cselect_b32 s35, 1, 0\n"
"s_add_u32 s34, s34, s35\n"
"s_sub_u32 s77, 22, s35\n"
"s_add_u32 s36, s50, 34603008\n"
"s_addc_u32 s37, s51, 0\n"
"s_mov_b32 s12, s36\n"
"s_and_b32 s13, s37, 0xffff\n"
"s_mov_b32 s14, 16777216\n"
"s_mov_b32 s15, 0x20000\n"
"s_mov_b32 s16, s64\n"
"s_and_b32 s17, s65, 0xffff\n"
"s_mov_b32 s18, 4194304\n"
"s_mov_b32 s19, 0x20000\n"
"s_lshl_b32 s35, s75, 2\n"
"s_add_u32 s35, s35, s29\n"
"s_lshl_b32 s78, s35, 18\n"
"s_lshl_b32 s35, s34, 12\n"
"s_add_u32 s78, s78, s35\n"
"s_lshl_b32 s79, s34, 16\n"
"s_branch .Ll3_common\n"
".Ll3_g4:\n"
"s_mov_b32 s77, 22\n"
"s_add_u32 s36, s50, 59768832\n"
"s_addc_u32 s37, s51, 0\n"
"s_mov_b32 s12, s36\n"
"s_and_b32 s13, s37, 0xffff\n"
"s_mov_b32 s14, 5636096\n"
"s_mov_b32 s15, 0x20000\n"
"s_mov_b32 s16, s66\n"
"s_and_b32 s17, s67, 0xffff\n"
"s_mov_b32 s18, 1409024\n"
"s_mov_b32 s19, 0x20000\n"
"s_lshl_b32 s35, s75, 2\n"
"s_add_u32 s35, s35, s29\n"
"s_mul_i32 s78, s35, 88064\n"
"s_mov_b32 s79, 0\n"
".Ll3_common:\n"
"s_lshl_b32 s35, s29, 14\n"
"s_add_u32 s79, s79, s35\n"
"s_lshl_b32 s35, s76, 8\n"
"s_add_u32 s79, s79, s35\n"
"s_add_u32 s80, s79, 8192\n"
"s_lshl_b32 s85, s29, 12\n"
"s_lshl_b32 s86, s29, 11\n"
"s_add_u32 s86, s86, 16384\n"
"v_lshrrev_b32 v2, 3, v0\n"
"v_lshlrev_b32 v2, 10, v2\n"
"v_bfe_u32 v3, v0, 4, 1\n"
"v_lshlrev_b32 v3, 2, v3\n"
"v_and_b32 v4, 7, v0\n"
"v_xor_b32 v3, v3, v4\n"
"v_lshl_add_u32 v8, v3, 4, v2\n"
"s_lshr_b32 s34, s29, 1\n"
"s_lshl_b32 s34, s34, 13\n"
"v_add_u32 v9, s34, v1\n"
"v_lshrrev_b32 v2, 5, v0\n"
"v_bfe_u32 v3, v0, 2, 2\n"
"v_lshl_add_u32 v2, v2, 2, v3\n"
"v_lshlrev_b32 v2, 7, v2\n"
"v_and_b32 v4, 1, v0\n"
"v_lshl_add_u32 v2, v4, 3, v2\n"
"s_and_b32 s34, s29, 1\n"
"s_lshl_b32 s34, s34, 13\n"
"s_add_u32 s34, s34, 16384\n"
"v_add_u32 v2, s34, v2\n"
"v_bfe_u32 v4, v0, 4, 1\n"
"v_bfe_u32 v5, v0, 1, 1\n"
"v_lshl_add_u32 v4, v4, 1, v5\n"
"v_lshrrev_b32 v3, 1, v3\n"
"v_lshlrev_b32 v3, 2, v3\n"
"v_xor_b32 v4, v4, v3\n"
"v_lshl_add_u32 v10, v4, 4, v2\n"
"v_xor_b32 v4, 4, v4\n"
"v_lshl_add_u32 v11, v4, 4, v2\n"
"s_mov_b32 s81, 0\n"
"s_add_u32 s34, s81, s86\n"
"s_add_u32 m0, s34, 0\n"
"s_nop 0\n"
"buffer_load_dwordx4 v8, s[16:19], s79 offen offset:0 lds\n"
"s_add_u32 s34, s81, s86\n"
"s_add_u32 m0, s34, 8064\n"
"s_nop 0\n"
"buffer_load_dwordx4 v8, s[16:19], s79 offen offset:128 lds\n"
"s_add_u32 s34, s81, s86\n"
"s_add_u32 m0, s34, 1024\n"
"s_nop 0\n"
"buffer_load_dwordx4 v8, s[16:19], s80 offen offset:0 lds\n"
"s_add_u32 s34, s81, s86\n"
"s_add_u32 m0, s34, 9088\n"
"s_nop 0\n"
"buffer_load_dwordx4 v8, s[16:19], s80 offen offset:128 lds\n"
"s_add_u32 s79, s79, 65536\n"
"s_add_u32 s80, s79, 8192\n"
"s_add_u32 s81, s81, 32768\n"
"s_add_u32 s34, s81, s86\n"
"s_add_u32 m0, s34, 0\n"
"s_nop 0\n"
"buffer_load_dwordx4 v8, s[16:19], s79 offen offset:0 lds\n"
"s_add_u32 s34, s81, s86\n"
"s_add_u32 m0, s34, 8064\n"
"s_nop 0\n"
"buffer_load_dwordx4 v8, s[16:19], s79 offen offset:128 lds\n"
"s_add_u32 s34, s81, s86\n"
"s_add_u32 m0, s34, 1024\n"
"s_nop 0\n"
"buffer_load_dwordx4 v8, s[16:19], s80 offen offset:0 lds\n"
"s_add_u32 s34, s81, s86\n"
"s_add_u32 m0, s34, 9088\n"
"s_nop 0\n"
"buffer_load_dwordx4 v8, s[16:19], s80 offen offset:128 lds\n"
"s_add_u32 s79, s79, 65536\n"
"s_add_u32 s80, s79, 8192\n"
"s_add_u32 s81, s81, 32768\n"
"s_add_u32 s34, s81, s86\n"
"s_add_u32 m0, s34, 0\n"
"s_nop 0\n"
"buffer_load_dwordx4 v8, s[16:19], s79 offen offset:0 lds\n"
"s_add_u32 s34, s81, s86\n"
"s_add_u32 m0, s34, 8064\n"
"s_nop 0\n"
"buffer_load_dwordx4 v8, s[16:19], s79 offen offset:128 lds\n"
"s_add_u32 s34, s81, s86\n"
"s_add_u32 m0, s34, 1024\n"
"s_nop 0\n"
"buffer_load_dwordx4 v8, s[16:19], s80 offen offset:0 lds\n"
"s_add_u32 s34, s81, s86\n"
"s_add_u32 m0, s34, 9088\n"
"s_nop 0\n"
"buffer_load_dwordx4 v8, s[16:19], s80 offen offset:128 lds\n"
"s_add_u32 s79, s79, 65536\n"
"s_add_u32 s80, s79, 8192\n"
"s_add_u32 s81, s81, 32768\n"
"s_add_u32 s34, s81, s86\n"
"s_add_u32 m0, s34, 0\n"
"s_nop 0\n"
"buffer_load_dwordx4 v8, s[16:19], s79 offen offset:0 lds\n"
"s_add_u32 s34, s81, s86\n"
"s_add_u32 m0, s34, 8064\n"
"s_nop 0\n"
"buffer_load_dwordx4 v8, s[16:19], s79 offen offset:128 lds\n"
"s_add_u32 s34, s81, s86\n"
"s_add_u32 m0, s34, 1024\n"
"s_nop 0\n"
"buffer_load_dwordx4 v8, s[16:19], s80 offen offset:0 lds\n"
"s_add_u32 s34, s81, s86\n"
"s_add_u32 m0, s34, 9088\n"
"s_nop 0\n"
"buffer_load_dwordx4 v8, s[16:19], s80 offen offset:128 lds\n"
"s_add_u32 s79, s79, 65536\n"
"s_add_u32 s80, s79, 8192\n"
"s_add_u32 s81, s81, 32768\n"
"s_cmp_eq_u32 s31, 15\n"
"s_cbranch_scc1 .Lg3_c2\n"
"s_nop 3\n"
"v_pk_fma_f32 v[160:161], v[116:117], v[64:65], v[160:161] op_sel_hi:[0,1,1] neg_lo:[1,0,0] neg_hi:[1,0,0]\n"
"v_pk_fma_f32 v[162:163], v[116:117], v[66:67], v[162:163] op_sel_hi:[0,1,1] neg_lo:[1,0,0] neg_hi:[1,0,0]\n"
"v_pk_fma_f32 v[164:165], v[116:117], v[68:69], v[164:165] op_sel_hi:[0,1,1] neg_lo:[1,0,0] neg_hi:[1,0,0]\n"
"v_pk_fma_f32 v[166:167], v[116:117], v[70:71], v[166:167] op_sel_hi:[0,1,1] neg_lo:[1,0,0] neg_hi:[1,0,0]\n"
"v_pk_fma_f32 v[168:169], v[116:117], v[72:73], v[168:169] op_sel_hi:[0,1,1] neg_lo:[1,0,0] neg_hi:[1,0,0]\n"
"v_pk_fma_f32 v[170:171], v[116:117], v[74:75], v[170:171] op_sel_hi:[0,1,1] neg_lo:[1,0,0] neg_hi:[1,0,0]\n"
"v_pk_fma_f32 v[172:173], v[116:117], v[76:77], v[172:173] op_sel_hi:[0,1,1] neg_lo:[1,0,0] neg_hi:[1,0,0]\n"
"v_pk_fma_f32 v[174:175], v[116:117], v[78:79], v[174:175] op_sel_hi:[0,1,1] neg_lo:[1,0,0] neg_hi:[1,0,0]\n"
"v_pk_mul_f32 v[160:161], v[160:161], v[118:119] op_sel_hi:[1,0]\n"
"v_pk_mul_f32 v[162:163], v[162:163], v[118:119] op_sel_hi:[1,0]\n"
"v_pk_mul_f32 v[164:165], v[164:165], v[118:119] op_sel_hi:[1,0]\n"
"v_pk_mul_f32 v[166:167], v[166:167], v[118:119] op_sel_hi:[1,0]\n"
"v_pk_mul_f32 v[168:169], v[168:169], v[118:119] op_sel_hi:[1,0]\n"
"v_pk_mul_f32 v[170:171], v[170:171], v[118:119] op_sel_hi:[1,0]\n"
"v_pk_mul_f32 v[172:173], v[172:173], v[118:119] op_sel_hi:[1,0]\n"
"v_pk_mul_f32 v[174:175], v[174:175], v[118:119] op_sel_hi:[1,0]\n"
"v_pk_mul_f32 v[192:193], v[160:161], v[160:161]\n"
"v_pk_mul_f32 v[194:195], v[162:163], v[162:163]\n"
"v_pk_mul_f32 v[196:197], v[164:165], v[164:165]\n"
"v_pk_mul_f32 v[198:199], v[166:167], v[166:167]\n"
"v_pk_mul_f32 v[200:201], v[168:169], v[168:169]\n"
"v_pk_mul_f32 v[202:203], v[170:171], v[170:171]\n"
"v_pk_mul_f32 v[204:205], v[172:173], v[172:173]\n"
"v_pk_mul_f32 v[206:207], v[174:175], v[174:175]\n"
"v_pk_fma_f32 v[192:193], v[192:193], s[54:55], v[120:121] op_sel_hi:[1,1,0]\n"
"v_pk_fma_f32 v[194:195], v[194:195], s[54:55], v[120:121] op_sel_hi:[1,1,0]\n"
"v_pk_fma_f32 v[196:197], v[196:197], s[54:55], v[120:121] op_sel_hi:[1,1,0]\n"
"v_pk_fma_f32 v[198:199], v[198:199], s[54:55], v[120:121] op_sel_hi:[1,1,0]\n"
"v_pk_fma_f32 v[200:201], v[200:201], s[54:55], v[120:121] op_sel_hi:[1,1,0]\n"
"v_pk_fma_f32 v[202:203], v[202:203], s[54:55], v[120:121] op_sel_hi:[1,1,0]\n"
"v_pk_fma_f32 v[204:205], v[204:205], s[54:55], v[120:121] op_sel_hi:[1,1,0]\n"
"v_pk_fma_f32 v[206:207], v[206:207], s[54:55], v[120:121] op_sel_hi:[1,1,0]\n"
"v_pk_mul_f32 v[192:193], v[192:193], v[160:161]\n"
"v_pk_mul_f32 v[194:195], v[194:195], v[162:163]\n"
"v_pk_mul_f32 v[196:197], v[196:197], v[164:165]\n"
"v_pk_mul_f32 v[198:199], v[198:199], v[166:167]\n"
"v_pk_mul_f32 v[200:201], v[200:201], v[168:169]\n"
"v_pk_mul_f32 v[202:203], v[202:203], v[170:171]\n"
"v_pk_mul_f32 v[204:205], v[204:205], v[172:173]\n"
"v_pk_mul_f32 v[206:207], v[206:207], v[174:175]\n"
"v_exp_f32 v192, -v192\n"
"v_exp_f32 v193, -v193\n"
"v_exp_f32 v194, -v194\n"
"v_exp_f32 v195, -v195\n"
"v_exp_f32 v196, -v196\n"
"v_exp_f32 v197, -v197\n"
"v_exp_f32 v198, -v198\n"
"v_exp_f32 v199, -v199\n"
"v_exp_f32 v200, -v200\n"
"v_exp_f32 v201, -v201\n"
"v_exp_f32 v202, -v202\n"
"v_exp_f32 v203, -v203\n"
"v_exp_f32 v204, -v204\n"
"v_exp_f32 v205, -v205\n"
"v_exp_f32 v206, -v206\n"
"v_exp_f32 v207, -v207\n"
"v_pk_add_f32 v[192:193], v[192:193], s[56:57]\n"
"v_pk_add_f32 v[194:195], v[194:195], s[56:57]\n"
"v_pk_add_f32 v[196:197], v[196:197], s[56:57]\n"
"v_pk_add_f32 v[198:199], v[198:199], s[56:57]\n"
"v_pk_add_f32 v[200:201], v[200:201], s[56:57]\n"
"v_pk_add_f32 v[202:203], v[202:203], s[56:57]\n"
"v_pk_add_f32 v[204:205], v[204:205], s[56:57]\n"
"v_pk_add_f32 v[206:207], v[206:207], s[56:57]\n"
"v_rcp_f32 v192, v192\n"
"v_rcp_f32 v193, v193\n"
"v_rcp_f32 v194, v194\n"
"v_rcp_f32 v195, v195\n"
"v_rcp_f32 v196, v196\n"
"v_rcp_f32 v197, v197\n"
"v_rcp_f32 v198, v198\n"
"v_rcp_f32 v199, v199\n"
"v_rcp_f32 v200, v200\n"
"v_rcp_f32 v201, v201\n"
"v_rcp_f32 v202, v202\n"
"v_rcp_f32 v203, v203\n"
"v_rcp_f32 v204, v204\n"
"v_rcp_f32 v205, v205\n"
"v_rcp_f32 v206, v206\n"
"v_rcp_f32 v207, v207\n"
"v_pk_mul_f32 v[160:161], v[160:161], v[192:193]\n"
"v_pk_mul_f32 v[162:163], v[162:163], v[194:195]\n"
"v_pk_mul_f32 v[164:165], v[164:165], v[196:197]\n"
"v_pk_mul_f32 v[166:167], v[166:167], v[198:199]\n"
"v_pk_mul_f32 v[168:169], v[168:169], v[200:201]\n"
"v_pk_mul_f32 v[170:171], v[170:171], v[202:203]\n"
"v_pk_mul_f32 v[172:173], v[172:173], v[204:205]\n"
"v_pk_mul_f32 v[174:175], v[174:175], v[206:207]\n"
"v_cvt_pk_bf16_f32 v208, v160, v161\n"
"v_cvt_pk_bf16_f32 v209, v162, v163\n"
"v_cvt_pk_bf16_f32 v210, v164, v165\n"
"v_cvt_pk_bf16_f32 v211, v166, v167\n"
"v_cvt_pk_bf16_f32 v212, v168, v169\n"
"v_cvt_pk_bf16_f32 v213, v170, v171\n"
"v_cvt_pk_bf16_f32 v214, v172, v173\n"
"v_cvt_pk_bf16_f32 v215, v174, v175\n"
"s_mul_i32 s34, s31, 3\n"
"s_add_u32 s34, s34, 2\n"
"s_lshl_b32 s36, s34, 11\n"
"s_mul_i32 s35, s46, 88064\n"
"s_add_u32 s36, s36, s35\n"
"s_cmp_lt_u32 s34, 43\n"
"s_cselect_b32 s36, s36, 0x40000000\n"
"buffer_store_dwordx4 v[208:211], v1, s[24:27], s36 offen offset:0 sc1\n"
"buffer_store_dwordx4 v[212:215], v1, s[24:27], s36 offen offset:1024 sc1\n"
"s_waitcnt vmcnt(0)\n"
"s_barrier\n"
"s_cmp_lg_u32 s29, 0\n"
"s_cbranch_scc1 .Lsig21\n"
"s_lshl_b32 s49, s46, 6\n"
"s_and_b32 s49, s49, 0xffffff00\n"
"s_lshl_b32 s48, s31, 2\n"
"s_add_u32 s49, s49, s48\n"
"s_add_u32 s49, s49, 47360\n"
"v_mul_i32_i24 v2, 256, v0\n"
"v_add_u32 v2, s49, v2\n"
"v_mov_b32 v3, s11\n"
"s_mov_b64 exec, 1\n"
"global_store_dword v2, v3, s[50:51] sc1\n"
"s_mov_b64 exec, -1\n"
".Lsig21:\n"
".Lg3_c2:\n"
"s_mul_i32 s34, s74, 10\n"
"s_cmp_gt_u32 s74, 0\n"
"s_cselect_b32 s35, 1, 0\n"
"s_add_u32 s34, s34, s35\n"
"s_lshl_b32 s34, s34, 2\n"
"s_add_u32 s34, s34, 43264\n"
"s_cmp_eq_u32 s74, 3\n"
"s_cselect_b32 s34, 47360, s34\n"
"s_cselect_b32 s35, 14, 10\n"
"s_lshl_b32 s36, s75, 8\n"
"s_add_u32 s34, s34, s36\n"
"v_min_u32 v2, s35, v0\n"
"v_lshlrev_b32 v2, 2, v2\n"
"v_add_u32 v2, s34, v2\n"
"s_cmp_lg_u32 s29, 0\n"
"s_cbranch_scc1 .Lpi22\n"
"global_load_dword v3, v2, s[50:51] sc1\n"
".Lpi22:\n"
"s_waitcnt vmcnt(0) lgkmcnt(0)\n"
"s_cmp_lg_u32 s29, 0\n"
"s_cbranch_scc1 .Lwok23\n"
"v_cmp_ne_u32 vcc, s11, v3\n"
"s_nop 1\n"
"s_cbranch_vccz .Lwok23\n"
"s_mov_b32 s49, 0\n"
".Lwait23:\n"
"s_sleep 2\n"
"global_load_dword v3, v2, s[50:51] sc1\n"
"s_waitcnt vmcnt(0)\n"
"v_cmp_ne_u32 vcc, s11, v3\n"
"s_nop 1\n"
"s_cbranch_vccz .Lwok23\n"
"s_add_u32 s49, s49, 1\n"
"s_cmp_lt_u32 s49, 32768\n"
"s_cbranch_scc1 .Lwait23\n"
".Lwok23:\n"
"s_barrier\n"
"s_mov_b32 s81, 0\n"
"s_add_u32 m0, s81, s85\n"
"s_nop 0\n"
"buffer_load_dwordx4 v1, s[12:15], s78 offen offset:0 sc1 lds\n"
"s_nop 0\n"
"buffer_load_dwordx4 v1, s[12:15], s78 offen offset:1024 sc1 lds\n"
"s_nop 0\n"
"buffer_load_dwordx4 v1, s[12:15], s78 offen offset:2048 sc1 lds\n"
"s_nop 0\n"
"buffer_load_dwordx4 v1, s[12:15], s78 offen offset:3072 sc1 lds\n"
"s_add_u32 s78, s78, 4096\n"
"s_add_u32 s81, s81, 32768\n"
"s_add_u32 m0, s81, s85\n"
"s_nop 0\n"
"buffer_load_dwordx4 v1, s[12:15], s78 offen offset:0 sc1 lds\n"
"s_nop 0\n"
"buffer_load_dwordx4 v1, s[12:15], s78 offen offset:1024 sc1 lds\n"
"s_nop 0\n"
"buffer_load_dwordx4 v1, s[12:15], s78 offen offset:2048 sc1 lds\n"
"s_nop 0\n"
"buffer_load_dwordx4 v1, s[12:15], s78 offen offset:3072 sc1 lds\n"
"s_add_u32 s78, s78, 4096\n"
"s_add_u32 s81, s81, 32768\n"
"s_add_u32 m0, s81, s85\n"
"s_nop 0\n"
"buffer_load_dwordx4 v1, s[12:15], s78 offen offset:0 sc1 lds\n"
"s_nop 0\n"
"buffer_load_dwordx4 v1, s[12:15], s78 offen offset:1024 sc1 lds\n"
"s_nop 0\n"
"buffer_load_dwordx4 v1, s[12:15], s78 offen offset:2048 sc1 lds\n"
"s_nop 0\n"
"buffer_load_dwordx4 v1, s[12:15], s78 offen offset:3072 sc1 lds\n"
"s_add_u32 s78, s78, 4096\n"
"s_add_u32 s81, s81, 32768\n"
"s_add_u32 m0, s81, s85\n"
"s_nop 0\n"
"buffer_load_dwordx4 v1, s[12:15], s78 offen offset:0 sc1 lds\n"
"s_nop 0\n"
"buffer_load_dwordx4 v1, s[12:15], s78 offen offset:1024 sc1 lds\n"
"s_nop 0\n"
"buffer_load_dwordx4 v1, s[12:15], s78 offen offset:2048 sc1 lds\n"
"s_nop 0\n"
"buffer_load_dwordx4 v1, s[12:15], s78 offen offset:3072 sc1 lds\n"
"s_add_u32 s78, s78, 4096\n"
"s_add_u32 s81, s81, 32768\n"
"s_mov_b32 s81, 0\n"
"s_sub_u32 s84, s77, 4\n"
"v_mov_b32 v16, 0\n"
"v_mov_b32 v17, 0\n"
"v_mov_b32 v18, 0\n"
"v_mov_b32 v19, 0\n"
"s_mov_b32 s82, 0\n"
"v_mov_b32 v12, v9\n"
"v_mov_b32 v13, v10\n"
"v_mov_b32 v14, v11\n"
"v_mfma_f32_32x32x16_bf16 v[144:159], v[16:19], v[16:19], 0\n"
"v_mfma_f32_32x32x16_bf16 v[160:175], v[16:19], v[16:19], 0\n"
"v_mfma_f32_32x32x16_bf16 v[176:191], v[16:19], v[16:19], 0\n"
"v_mfma_f32_32x32x16_bf16 v[192:207], v[16:19], v[16:19], 0\n"
"s_waitcnt vmcnt(4)\n"
"s_barrier\n"
"ds_read_b128 v[16:19], v12 offset:0\n"
"ds_read_b64_tr_b16 v[24:25], v13 offset:0\n"
"ds_read_b64_tr_b16 v[26:27], v13 offset:1024\n"
"ds_read_b128 v[20:23], v12 offset:4096\n"
"ds_read_b64_tr_b16 v[28:29], v14 offset:0\n"
"ds_read_b64_tr_b16 v[30:31], v14 offset:1024\n"
"ds_read_b128 v[32:35], v12 offset:1024\n"
"ds_read_b64_tr_b16 v[40:41], v13 offset:2048\n"
"ds_read_b64_tr_b16 v[42:43], v13 offset:3072\n"
"ds_read_b128 v[36:39], v12 offset:5120\n"
"ds_read_b64_tr_b16 v[44:45], v14 offset:2048\n"
"ds_read_b64_tr_b16 v[46:47], v14 offset:3072\n"
"ds_read_b128 v[48:51], v12 offset:2048\n"
"ds_read_b64_tr_b16 v[56:57], v13 offset:4096\n"
"ds_read_b64_tr_b16 v[58:59], v13 offset:5120\n"
"ds_read_b128 v[52:55], v12 offset:6144\n"
"ds_read_b64_tr_b16 v[60:61], v14 offset:4096\n"
"ds_read_b64_tr_b16 v[62:63], v14 offset:5120\n"
"ds_read_b128 v[64:67], v12 offset:3072\n"
"ds_read_b64_tr_b16 v[72:73], v13 offset:6144\n"
"ds_read_b64_tr_b16 v[74:75], v13 offset:7168\n"
"ds_read_b128 v[68:71], v12 offset:7168\n"
"ds_read_b64_tr_b16 v[76:77], v14 offset:6144\n"
"ds_read_b64_tr_b16 v[78:79], v14 offset:7168\n"
"s_add_u32 s82, s82, 32768\n"
"s_and_b32 s82, s82, 0x1ffff\n"
"v_add_u32 v12, s82, v9\n"
"v_add_u32 v13, s82, v10\n"
"v_add_u32 v14, s82, v11\n"
"s_mov_b32 s83, 11\n"
".Ll3_loop:\n"
"s_waitcnt vmcnt(16) lgkmcnt(0)\n"
"s_barrier\n"
"s_add_u32 m0, s81, s85\n"
"v_mfma_f32_32x32x16_bf16 v[144:159], v[24:27], v[16:19], v[144:159]\n"
"buffer_load_dwordx4 v1, s[12:15], s78 offen offset:0 sc1 lds\n"
"ds_read_b128 v[80:83], v12 offset:0\n"
"ds_read_b64_tr_b16 v[88:89], v13 offset:0\n"
"v_mfma_f32_32x32x16_bf16 v[160:175], v[28:31], v[16:19], v[160:175]\n"
"ds_read_b64_tr_b16 v[90:91], v13 offset:1024\n"
"ds_read_b128 v[84:87], v12 offset:4096\n"
"v_mfma_f32_32x32x16_bf16 v[176:191], v[24:27], v[20:23], v[176:191]\n"
"buffer_load_dwordx4 v1, s[12:15], s78 offen offset:1024 sc1 lds\n"
"ds_read_b64_tr_b16 v[92:93], v14 offset:0\n"
"ds_read_b64_tr_b16 v[94:95], v14 offset:1024\n"
"v_mfma_f32_32x32x16_bf16 v[192:207], v[28:31], v[20:23], v[192:207]\n"
"ds_read_b128 v[96:99], v12 offset:1024\n"
"ds_read_b64_tr_b16 v[104:105], v13 offset:2048\n"
"v_mfma_f32_32x32x16_bf16 v[144:159], v[40:43], v[32:35], v[144:159]\n"
"buffer_load_dwordx4 v1, s[12:15], s78 offen offset:2048 sc1 lds\n"
"ds_read_b64_tr_b16 v[106:107], v13 offset:3072\n"
"ds_read_b128 v[100:103], v12 offset:5120\n"
"v_mfma_f32_32x32x16_bf16 v[160:175], v[44:47], v[32:35], v[160:175]\n"
"ds_read_b64_tr_b16 v[108:109], v14 offset:2048\n"
"ds_read_b64_tr_b16 v[110:111], v14 offset:3072\n"
"v_mfma_f32_32x32x16_bf16 v[176:191], v[40:43], v[36:39], v[176:191]\n"
"buffer_load_dwordx4 v1, s[12:15], s78 offen offset:3072 sc1 lds\n"
"ds_read_b128 v[112:115], v12 offset:2048\n"
"ds_read_b64_tr_b16 v[120:121], v13 offset:4096\n"
"v_mfma_f32_32x32x16_bf16 v[192:207], v[44:47], v[36:39], v[192:207]\n"
"ds_read_b64_tr_b16 v[122:123], v13 offset:5120\n"
"ds_read_b128 v[116:119], v12 offset:6144\n"
"s_add_u32 s34, s81, s86\n"
"s_add_u32 m0, s34, 0\n"
"v_mfma_f32_32x32x16_bf16 v[144:159], v[56:59], v[48:51], v[144:159]\n"
"buffer_load_dwordx4 v8, s[16:19], s79 offen offset:0 lds\n"
"ds_read_b64_tr_b16 v[124:125], v14 offset:4096\n"
"v_mfma_f32_32x32x16_bf16 v[160:175], v[60:63], v[48:51], v[160:175]\n"
"ds_read_b64_tr_b16 v[126:127], v14 offset:5120\n"
"s_add_u32 s34, s81, s86\n"
"s_add_u32 m0, s34, 8064\n"
"v_mfma_f32_32x32x16_bf16 v[176:191], v[56:59], v[52:55], v[176:191]\n"
"buffer_load_dwordx4 v8, s[16:19], s79 offen offset:128 lds\n"
"ds_read_b128 v[128:131], v12 offset:3072\n"
"v_mfma_f32_32x32x16_bf16 v[192:207], v[60:63], v[52:55], v[192:207]\n"
"ds_read_b64_tr_b16 v[136:137], v13 offset:6144\n"
"s_add_u32 s34, s81, s86\n"
"s_add_u32 m0, s34, 1024\n"
"v_mfma_f32_32x32x16_bf16 v[144:159], v[72:75], v[64:67], v[144:159]\n"
"buffer_load_dwordx4 v8, s[16:19], s80 offen offset:0 lds\n"
"ds_read_b64_tr_b16 v[138:139], v13 offset:7168\n"
"v_mfma_f32_32x32x16_bf16 v[160:175], v[76:79], v[64:67], v[160:175]\n"
"ds_read_b128 v[132:135], v12 offset:7168\n"
"s_add_u32 s34, s81, s86\n"
"s_add_u32 m0, s34, 9088\n"
"v_mfma_f32_32x32x16_bf16 v[176:191], v[72:75], v[68:71], v[176:191]\n"
"buffer_load_dwordx4 v8, s[16:19], s80 offen offset:128 lds\n"
"ds_read_b64_tr_b16 v[140:141], v14 offset:6144\n"
"v_mfma_f32_32x32x16_bf16 v[192:207], v[76:79], v[68:71], v[192:207]\n"
"ds_read_b64_tr_b16 v[142:143], v14 offset:7168\n"
"s_add_u32 s78, s78, 4096\n"
"s_add_u32 s79, s79, 65536\n"
"s_add_u32 s81, s81, 32768\n"
"s_and_b32 s81, s81, 0x1ffff\n"
"s_sub_u32 s84, s84, 1\n"
"s_cmp_gt_i32 s84, 0\n"
"s_cselect_b32 s78, s78, 0x40000000\n"
"s_cselect_b32 s79, s79, 0x40000000\n"
"s_add_u32 s80, s79, 8192\n"
"s_add_u32 s82, s82, 32768\n"
"s_and_b32 s82, s82, 0x1ffff\n"
"v_add_u32 v12, s82, v9\n"
"v_add_u32 v13, s82, v10\n"
"v_add_u32 v14, s82, v11\n"
"s_waitcnt vmcnt(16) lgkmcnt(0)\n"
"s_barrier\n"
"s_add_u32 m0, s81, s85\n"
"v_mfma_f32_32x32x16_bf16 v[144:159], v[88:91], v[80:83], v[144:159]\n"
"buffer_load_dwordx4 v1, s[12:15], s78 offen offset:0 sc1 lds\n"
"ds_read_b128 v[16:19], v12 offset:0\n"
"ds_read_b64_tr_b16 v[24:25], v13 offset:0\n"
"v_mfma_f32_32x32x16_bf16 v[160:175], v[92:95], v[80:83], v[160:175]\n"
"ds_read_b64_tr_b16 v[26:27], v13 offset:1024\n"
"ds_read_b128 v[20:23], v12 offset:4096\n"
"v_mfma_f32_32x32x16_bf16 v[176:191], v[88:91], v[84:87], v[176:191]\n"
"buffer_load_dwordx4 v1, s[12:15], s78 offen offset:1024 sc1 lds\n"
"ds_read_b64_tr_b16 v[28:29], v14 offset:0\n"
"ds_read_b64_tr_b16 v[30:31], v14 offset:1024\n"
"v_mfma_f32_32x32x16_bf16 v[192:207], v[92:95], v[84:87], v[192:207]\n"
"ds_read_b128 v[32:35], v12 offset:1024\n"
"ds_read_b64_tr_b16 v[40:41], v13 offset:2048\n"
"v_mfma_f32_32x32x16_bf16 v[144:159], v[104:107], v[96:99], v[144:159]\n"
"buffer_load_dwordx4 v1, s[12:15], s78 offen offset:2048 sc1 lds\n"
"ds_read_b64_tr_b16 v[42:43], v13 offset:3072\n"
"ds_read_b128 v[36:39], v12 offset:5120\n"
"v_mfma_f32_32x32x16_bf16 v[160:175], v[108:111], v[96:99], v[160:175]\n"
"ds_read_b64_tr_b16 v[44:45], v14 offset:2048\n"
"ds_read_b64_tr_b16 v[46:47], v14 offset:3072\n"
"v_mfma_f32_32x32x16_bf16 v[176:191], v[104:107], v[100:103], v[176:191]\n"
"buffer_load_dwordx4 v1, s[12:15], s78 offen offset:3072 sc1 lds\n"
"ds_read_b128 v[48:51], v12 offset:2048\n"
"ds_read_b64_tr_b16 v[56:57], v13 offset:4096\n"
"v_mfma_f32_32x32x16_bf16 v[192:207], v[108:111], v[100:103], v[192:207]\n"
"ds_read_b64_tr_b16 v[58:59], v13 offset:5120\n"
"ds_read_b128 v[52:55], v12 offset:6144\n"
"s_add_u32 s34, s81, s86\n"
"s_add_u32 m0, s34, 0\n"
"v_mfma_f32_32x32x16_bf16 v[144:159], v[120:123], v[112:115], v[144:159]\n"
"buffer_load_dwordx4 v8, s[16:19], s79 offen offset:0 lds\n"
"ds_read_b64_tr_b16 v[60:61], v14 offset:4096\n"
"v_mfma_f32_32x32x16_bf16 v[160:175], v[124:127], v[112:115], v[160:175]\n"
"ds_read_b64_tr_b16 v[62:63], v14 offset:5120\n"
"s_add_u32 s34, s81, s86\n"
"s_add_u32 m0, s34, 8064\n"
"v_mfma_f32_32x32x16_bf16 v[176:191], v[120:123], v[116:119], v[176:191]\n"
"buffer_load_dwordx4 v8, s[16:19], s79 offen offset:128 lds\n"
"ds_read_b128 v[64:67], v12 offset:3072\n"
"v_mfma_f32_32x32x16_bf16 v[192:207], v[124:127], v[116:119], v[192:207]\n"
"ds_read_b64_tr_b16 v[72:73], v13 offset:6144\n"
"s_add_u32 s34, s81, s86\n"
"s_add_u32 m0, s34, 1024\n"
"v_mfma_f32_32x32x16_bf16 v[144:159], v[136:139], v[128:131], v[144:159]\n"
"buffer_load_dwordx4 v8, s[16:19], s80 offen offset:0 lds\n"
"ds_read_b64_tr_b16 v[74:75], v13 offset:7168\n"
"v_mfma_f32_32x32x16_bf16 v[160:175], v[140:143], v[128:131], v[160:175]\n"
"ds_read_b128 v[68:71], v12 offset:7168\n"
"s_add_u32 s34, s81, s86\n"
"s_add_u32 m0, s34, 9088\n"
"v_mfma_f32_32x32x16_bf16 v[176:191], v[136:139], v[132:135], v[176:191]\n"
"buffer_load_dwordx4 v8, s[16:19], s80 offen offset:128 lds\n"
"ds_read_b64_tr_b16 v[76:77], v14 offset:6144\n"
"v_mfma_f32_32x32x16_bf16 v[192:207], v[140:143], v[132:135], v[192:207]\n"
"ds_read_b64_tr_b16 v[78:79], v14 offset:7168\n"
"s_add_u32 s78, s78, 4096\n"
"s_add_u32 s79, s79, 65536\n"
"s_add_u32 s81, s81, 32768\n"
"s_and_b32 s81, s81, 0x1ffff\n"
"s_sub_u32 s84, s84, 1\n"
"s_cmp_gt_i32 s84, 0\n"
"s_cselect_b32 s78, s78, 0x40000000\n"
"s_cselect_b32 s79, s79, 0x40000000\n"
"s_add_u32 s80, s79, 8192\n"
"s_add_u32 s82, s82, 32768\n"
"s_and_b32 s82, s82, 0x1ffff\n"
"v_add_u32 v12, s82, v9\n"
"v_add_u32 v13, s82, v10\n"
"v_add_u32 v14, s82, v11\n"
"s_sub_u32 s83, s83, 1\n"
"s_cmp_lg_u32 s83, 0\n"
"s_cbranch_scc1 .Ll3_loop\n"
"s_add_u32 s36, s50, 66060288\n"
"s_addc_u32 s37, s51, 0\n"
"s_mov_b32 s20, s36\n"
"s_and_b32 s21, s37, 0xffff\n"
"s_mov_b32 s22, 8388608\n"
"s_mov_b32 s23, 0x20000\n"
"s_lshl_b32 s34, s87, 2\n"
"s_add_u32 s34, s34, s74\n"
"s_lshl_b32 s34, s34, 15\n"
"s_lshl_b32 s35, s29, 13\n"
"s_add_u32 s34, s34, s35\n"
"s_add_u32 s35, s34, 4096\n"
"s_waitcnt vmcnt(0) lgkmcnt(0)\n"
"s_barrier\n"
"v_cvt_pk_bf16_f32 v16, v144, v145\n"
"v_cvt_pk_bf16_f32 v17, v146, v147\n"
"v_cvt_pk_bf16_f32 v18, v148, v149\n"
"v_cvt_pk_bf16_f32 v19, v150, v151\n"
"v_cvt_pk_bf16_f32 v20, v152, v153\n"
"v_cvt_pk_bf16_f32 v21, v154, v155\n"
"v_cvt_pk_bf16_f32 v22, v156, v157\n"
"v_cvt_pk_bf16_f32 v23, v158, v159\n"
"v_cvt_pk_bf16_f32 v24, v160, v161\n"
"v_cvt_pk_bf16_f32 v25, v162, v163\n"
"v_cvt_pk_bf16_f32 v26, v164, v165\n"
"v_cvt_pk_bf16_f32 v27, v166, v167\n"
"v_cvt_pk_bf16_f32 v28, v168, v169\n"
"v_cvt_pk_bf16_f32 v29, v170, v171\n"
"v_cvt_pk_bf16_f32 v30, v172, v173\n"
"v_cvt_pk_bf16_f32 v31, v174, v175\n"
"v_cvt_pk_bf16_f32 v32, v176, v177\n"
"v_cvt_pk_bf16_f32 v33, v178, v179\n"
"v_cvt_pk_bf16_f32 v34, v180, v181\n"
"v_cvt_pk_bf16_f32 v35, v182, v183\n"
"v_cvt_pk_bf16_f32 v36, v184, v185\n"
"v_cvt_pk_bf16_f32 v37, v186, v187\n"
"v_cvt_pk_bf16_f32 v38, v188, v189\n"
"v_cvt_pk_bf16_f32 v39, v190, v191\n"
"v_cvt_pk_bf16_f32 v40, v192, v193\n"
"v_cvt_pk_bf16_f32 v41, v194, v195\n"
"v_cvt_pk_bf16_f32 v42, v196, v197\n"
"v_cvt_pk_bf16_f32 v43, v198, v199\n"
"v_cvt_pk_bf16_f32 v44, v200, v201\n"
"v_cvt_pk_bf16_f32 v45, v202, v203\n"
"v_cvt_pk_bf16_f32 v46, v204, v205\n"
"v_cvt_pk_bf16_f32 v47, v206, v207\n"
"s_cmp_eq_u32 s29, s74\n"
"s_cbranch_scc1 .Ll3_own\n"
"buffer_store_dwordx4 v[16:19], v1, s[20:23], s34 offen offset:0 sc1\n"
"buffer_store_dwordx4 v[20:23], v1, s[20:23], s34 offen offset:1024 sc1\n"
"buffer_store_dwordx4 v[24:27], v1, s[20:23], s34 offen offset:2048 sc1\n"
"buffer_store_dwordx4 v[28:31], v1, s[20:23], s34 offen offset:3072 sc1\n"
"buffer_store_dwordx4 v[32:35], v1, s[20:23], s35 offen offset:0 sc1\n"
"buffer_store_dwordx4 v[36:39], v1, s[20:23], s35 offen offset:1024 sc1\n"
"buffer_store_dwordx4 v[40:43], v1, s[20:23], s35 offen offset:2048 sc1\n"
"buffer_store_dwordx4 v[44:47], v1, s[20:23], s35 offen offset:3072 sc1\n"
"s_branch .Ll3_own_done\n"
".Ll3_own:\n"
"ds_write_b128 v1, v[16:19] offset:0\n"
"ds_write_b128 v1, v[20:23] offset:1024\n"
"ds_write_b128 v1, v[24:27] offset:2048\n"
"ds_write_b128 v1, v[28:31] offset:3072\n"
"ds_write_b128 v1, v[32:35] offset:4096\n"
"ds_write_b128 v1, v[36:39] offset:5120\n"
"ds_write_b128 v1, v[40:43] offset:6144\n"
"ds_write_b128 v1, v[44:47] offset:7168\n"
".Ll3_own_done:\n"
"s_lshl_b32 s34, s75, 2\n"
"s_lshr_b32 s35, s74, 1\n"
"s_lshl_b32 s35, s35, 1\n"
"s_add_u32 s34, s34, s35\n"
"s_lshr_b32 s35, s29, 1\n"
"s_add_u32 s78, s34, s35\n"
"s_lshl_b32 s34, s76, 2\n"
"s_and_b32 s35, s74, 1\n"
"s_lshl_b32 s35, s35, 1\n"
"s_add_u32 s34, s34, s35\n"
"s_and_b32 s35, s29, 1\n"
"s_add_u32 s79, s34, s35\n"
"s_add_u32 s36, s50, 57671680\n"
"s_addc_u32 s37, s51, 0\n"
"s_mov_b32 s12, s36\n"
"s_and_b32 s13, s37, 0xffff\n"
"s_mov_b32 s14, 2097152\n"
"s_mov_b32 s15, 0x20000\n"
"s_lshl_b32 s34, s78, 15\n"
"s_lshl_b32 s35, s79, 11\n"
"s_add_u32 s34, s34, s35\n"
"buffer_load_dwordx4 v[208:211], v1, s[12:15], s34 offen sc1\n"
"buffer_load_dwordx4 v[212:215], v1, s[12:15], s34 offen offset:1024 sc1\n"
"s_mov_b32 s16, s68\n"
"s_and_b32 s17, s69, 0xffff\n"
"s_mov_b32 s18, 2097152\n"
"s_mov_b32 s19, 0x20000\n"
"s_mov_b32 s24, s70\n"
"s_and_b32 s25, s71, 0xffff\n"
"s_mov_b32 s26, 2097152\n"
"s_mov_b32 s27, 0x20000\n"
"v_and_b32 v2, 31, v0\n"
"v_lshlrev_b32 v2, 10, v2\n"
"v_lshrrev_b32 v3, 5, v0\n"
"v_lshl_add_u32 v2, v3, 3, v2\n"
"s_lshl_b32 s34, s78, 15\n"
"s_lshl_b32 s35, s79, 6\n"
"s_add_u32 s80, s34, s35\n"
"buffer_load_dwordx2 v[216:217], v2, s[16:19], s80 offen offset:0\n"
"buffer_load_dwordx2 v[218:219], v2, s[16:19], s80 offen offset:16\n"
"buffer_load_dwordx2 v[220:221], v2, s[16:19], s80 offen offset:32\n"
"buffer_load_dwordx2 v[222:223], v2, s[16:19], s80 offen offset:48\n"
"s_waitcnt vmcnt(6) lgkmcnt(0)\n"
"s_barrier\n"
"s_lshl_b32 s36, s87, 8\n"
"s_add_u32 s36, s36, 256\n"
"s_lshl_b32 s34, s29, 11\n"
"v_add_u32 v5, s34, v1\n"
"ds_read_b128 v[224:227], v5\n"
"ds_read_b128 v[228:231], v5 offset:1024\n"
"s_cmp_lg_u32 s29, 0\n"
"s_cbranch_scc1 .Ll3_wait_done\n"
"s_lshl_b32 s34, s74, 2\n"
"s_add_u32 s34, s34, s36\n"
"v_mov_b32 v3, s34\n"
"v_mov_b32 v4, s11\n"
"s_mov_b64 exec, 1\n"
"global_store_dword v3, v4, s[50:51] sc1\n"
"s_mov_b64 exec, -1\n"
"v_and_b32 v3, 3, v0\n"
"v_lshlrev_b32 v3, 2, v3\n"
"v_add_u32 v3, s36, v3\n"
"s_mov_b32 s83, 0\n"
".Ll3_poll:\n"
"global_load_dword v5, v3, s[50:51] sc1\n"
"s_waitcnt vmcnt(0)\n"
"v_cmp_ne_u32 vcc, s11, v5\n"
"s_nop 1\n"
"s_cbranch_vccz .Ll3_poll_ok\n"
"s_sleep 1\n"
"s_add_u32 s83, s83, 1\n"
"s_cmp_lt_u32 s83, 0x8000\n"
"s_cbranch_scc1 .Ll3_poll\n"
".Ll3_poll_ok:\n"
".Ll3_wait_done:\n"
"s_barrier\n"
"s_lshl_b32 s34, s87, 17\n"
"s_lshl_b32 s35, s74, 13\n"
"s_add_u32 s34, s34, s35\n"
"s_lshl_b32 s35, s29, 11\n"
"s_add_u32 s34, s34, s35\n"
"s_cmp_eq_u32 s74, 0\n"
"s_cselect_b32 s35, 0x40000000, s34\n"
"buffer_load_dwordx4 v[144:147], v1, s[20:23], s35 offen offset:0 sc1\n"
"buffer_load_dwordx4 v[148:151], v1, s[20:23], s35 offen offset:1024 sc1\n"
"s_add_u32 s34, s34, 32768\n"
"s_cmp_eq_u32 s74, 1\n"
"s_cselect_b32 s35, 0x40000000, s34\n"
"buffer_load_dwordx4 v[152:155], v1, s[20:23], s35 offen offset:0 sc1\n"
"buffer_load_dwordx4 v[156:159], v1, s[20:23], s35 offen offset:1024 sc1\n"
"s_add_u32 s34, s34, 32768\n"
"s_cmp_eq_u32 s74, 2\n"
"s_cselect_b32 s35, 0x40000000, s34\n"
"buffer_load_dwordx4 v[160:163], v1, s[20:23], s35 offen offset:0 sc1\n"
"buffer_load_dwordx4 v[164:167], v1, s[20:23], s35 offen offset:1024 sc1\n"
"s_add_u32 s34, s34, 32768\n"
"s_cmp_eq_u32 s74, 3\n"
"s_cselect_b32 s35, 0x40000000, s34\n"
"buffer_load_dwordx4 v[168:171], v1, s[20:23], s35 offen offset:0 sc1\n"
"buffer_load_dwordx4 v[172:175], v1, s[20:23], s35 offen offset:1024 sc1\n"
"s_cmp_eq_u32 s74, 3\n"
"s_cselect_b32 s88, 0, 1.0\n"
"s_cselect_b32 s90, 1.0, 0\n"
"s_mov_b32 s89, s88\n"
"s_mov_b32 s91, s90\n"
"s_mov_b32 s92, 0xbfb8aa3b\n"
"s_mov_b32 s93, s92\n"
"s_mov_b32 s94, 1.0\n"
"s_mov_b32 s95, 1.0\n"
"s_waitcnt vmcnt(0) lgkmcnt(0)\n"
"v_lshlrev_b32 v16, 16, v144\n"
"v_and_b32 v17, 0xffff0000, v144\n"
"v_lshlrev_b32 v18, 16, v152\n"
"v_and_b32 v19, 0xffff0000, v152\n"
"v_lshlrev_b32 v20, 16, v160\n"
"v_and_b32 v21, 0xffff0000, v160\n"
"v_lshlrev_b32 v22, 16, v168\n"
"v_and_b32 v23, 0xffff0000, v168\n"
"v_lshlrev_b32 v24, 16, v224\n"
"v_and_b32 v25, 0xffff0000, v224\n"
"v_lshlrev_b32 v26, 16, v208\n"
"v_and_b32 v27, 0xffff0000, v208\n"
"v_lshlrev_b32 v28, 16, v216\n"
"v_and_b32 v29, 0xffff0000, v216\n"
"v_lshlrev_b32 v32, 16, v145\n"
"v_and_b32 v33, 0xffff0000, v145\n"
"v_lshlrev_b32 v34, 16, v153\n"
"v_and_b32 v35, 0xffff0000, v153\n"
"v_lshlrev_b32 v36, 16, v161\n"
"v_and_b32 v37, 0xffff0000, v161\n"
"v_lshlrev_b32 v38, 16, v169\n"
"v_and_b32 v39, 0xffff0000, v169\n"
"v_lshlrev_b32 v40, 16, v225\n"
"v_and_b32 v41, 0xffff0000, v225\n"
"v_lshlrev_b32 v42, 16, v209\n"
"v_and_b32 v43, 0xffff0000, v209\n"
"v_lshlrev_b32 v44, 16, v217\n"
"v_and_b32 v45, 0xffff0000, v217\n"
"v_lshlrev_b32 v48, 16, v146\n"
"v_and_b32 v49, 0xffff0000, v146\n"
"v_lshlrev_b32 v50, 16, v154\n"
"v_and_b32 v51, 0xffff0000, v154\n"
"v_lshlrev_b32 v52, 16, v162\n"
"v_and_b32 v53, 0xffff0000, v162\n"
"v_lshlrev_b32 v54, 16, v170\n"
"v_and_b32 v55, 0xffff0000, v170\n"
"v_lshlrev_b32 v56, 16, v226\n"
"v_and_b32 v57, 0xffff0000, v226\n"
"v_lshlrev_b32 v58, 16, v210\n"
"v_and_b32 v59, 0xffff0000, v210\n"
"v_lshlrev_b32 v60, 16, v218\n"
"v_and_b32 v61, 0xffff0000, v218\n"
"v_lshlrev_b32 v64, 16, v147\n"
"v_and_b32 v65, 0xffff0000, v147\n"
"v_lshlrev_b32 v66, 16, v155\n"
"v_and_b32 v67, 0xffff0000, v155\n"
"v_lshlrev_b32 v68, 16, v163\n"
"v_and_b32 v69, 0xffff0000, v163\n"
"v_lshlrev_b32 v70, 16, v171\n"
"v_and_b32 v71, 0xffff0000, v171\n"
"v_lshlrev_b32 v72, 16, v227\n"
"v_and_b32 v73, 0xffff0000, v227\n"
"v_lshlrev_b32 v74, 16, v211\n"
"v_and_b32 v75, 0xffff0000, v211\n"
"v_lshlrev_b32 v76, 16, v219\n"
"v_and_b32 v77, 0xffff0000, v219\n"
"v_pk_add_f32 v[16:17], v[16:17], v[18:19]\n"
"v_pk_add_f32 v[32:33], v[32:33], v[34:35]\n"
"v_pk_add_f32 v[48:49], v[48:49], v[50:51]\n"
"v_pk_add_f32 v[64:65], v[64:65], v[66:67]\n"
"v_pk_fma_f32 v[22:23], v[24:25], s[90:91], v[22:23]\n"
"v_pk_fma_f32 v[38:39], v[40:41], s[90:91], v[38:39]\n"
"v_pk_fma_f32 v[54:55], v[56:57], s[90:91], v[54:55]\n"
"v_pk_fma_f32 v[70:71], v[72:73], s[90:91], v[70:71]\n"
"v_pk_add_f32 v[16:17], v[16:17], v[20:21]\n"
"v_pk_add_f32 v[32:33], v[32:33], v[36:37]\n"
"v_pk_add_f32 v[48:49], v[48:49], v[52:53]\n"
"v_pk_add_f32 v[64:65], v[64:65], v[68:69]\n"
"v_pk_add_f32 v[22:23], v[22:23], v[26:27]\n"
"v_pk_add_f32 v[38:39], v[38:39], v[42:43]\n"
"v_pk_add_f32 v[54:55], v[54:55], v[58:59]\n"
"v_pk_add_f32 v[70:71], v[70:71], v[74:75]\n"
"v_pk_fma_f32 v[16:17], v[24:25], s[88:89], v[16:17]\n"
"v_pk_fma_f32 v[32:33], v[40:41], s[88:89], v[32:33]\n"
"v_pk_fma_f32 v[48:49], v[56:57], s[88:89], v[48:49]\n"
"v_pk_fma_f32 v[64:65], v[72:73], s[88:89], v[64:65]\n"
"v_pk_mul_f32 v[18:19], v[22:23], s[92:93]\n"
"v_pk_mul_f32 v[34:35], v[38:39], s[92:93]\n"
"v_pk_mul_f32 v[50:51], v[54:55], s[92:93]\n"
"v_pk_mul_f32 v[66:67], v[70:71], s[92:93]\n"
"v_exp_f32 v18, v18\n"
"v_exp_f32 v19, v19\n"
"v_exp_f32 v34, v34\n"
"v_exp_f32 v35, v35\n"
"v_exp_f32 v50, v50\n"
"v_exp_f32 v51, v51\n"
"v_exp_f32 v66, v66\n"
"v_exp_f32 v67, v67\n"
"v_pk_add_f32 v[18:19], v[18:19], s[94:95]\n"
"v_pk_add_f32 v[34:35], v[34:35], s[94:95]\n"
"v_pk_add_f32 v[50:51], v[50:51], s[94:95]\n"
"v_pk_add_f32 v[66:67], v[66:67], s[94:95]\n"
"v_rcp_f32 v18, v18\n"
"v_rcp_f32 v19, v19\n"
"v_rcp_f32 v34, v34\n"
"v_rcp_f32 v35, v35\n"
"v_rcp_f32 v50, v50\n"
"v_rcp_f32 v51, v51\n"
"v_rcp_f32 v66, v66\n"
"v_rcp_f32 v67, v67\n"
"v_pk_mul_f32 v[16:17], v[16:17], v[22:23]\n"
"v_pk_mul_f32 v[32:33], v[32:33], v[38:39]\n"
"v_pk_mul_f32 v[48:49], v[48:49], v[54:55]\n"
"v_pk_mul_f32 v[64:65], v[64:65], v[70:71]\n"
"v_pk_fma_f32 v[16:17], v[16:17], v[18:19], v[28:29]\n"
"v_pk_fma_f32 v[32:33], v[32:33], v[34:35], v[44:45]\n"
"v_pk_fma_f32 v[48:49], v[48:49], v[50:51], v[60:61]\n"
"v_pk_fma_f32 v[64:65], v[64:65], v[66:67], v[76:77]\n"
"v_cvt_pk_bf16_f32 v176, v16, v17\n"
"v_cvt_pk_bf16_f32 v177, v32, v33\n"
"v_cvt_pk_bf16_f32 v178, v48, v49\n"
"v_cvt_pk_bf16_f32 v179, v64, v65\n"
"v_lshlrev_b32 v16, 16, v148\n"
"v_and_b32 v17, 0xffff0000, v148\n"
"v_lshlrev_b32 v18, 16, v156\n"
"v_and_b32 v19, 0xffff0000, v156\n"
"v_lshlrev_b32 v20, 16, v164\n"
"v_and_b32 v21, 0xffff0000, v164\n"
"v_lshlrev_b32 v22, 16, v172\n"
"v_and_b32 v23, 0xffff0000, v172\n"
"v_lshlrev_b32 v24, 16, v228\n"
"v_and_b32 v25, 0xffff0000, v228\n"
"v_lshlrev_b32 v26, 16, v212\n"
"v_and_b32 v27, 0xffff0000, v212\n"
"v_lshlrev_b32 v28, 16, v220\n"
"v_and_b32 v29, 0xffff0000, v220\n"
"v_lshlrev_b32 v32, 16, v149\n"
"v_and_b32 v33, 0xffff0000, v149\n"
"v_lshlrev_b32 v34, 16, v157\n"
"v_and_b32 v35, 0xffff0000, v157\n"
"v_lshlrev_b32 v36, 16, v165\n"
"v_and_b32 v37, 0xffff0000, v165\n"
"v_lshlrev_b32 v38, 16, v173\n"
"v_and_b32 v39, 0xffff0000, v173\n"
"v_lshlrev_b32 v40, 16, v229\n"
"v_and_b32 v41, 0xffff0000, v229\n"
"v_lshlrev_b32 v42, 16, v213\n"
"v_and_b32 v43, 0xffff0000, v213\n"
"v_lshlrev_b32 v44, 16, v221\n"
"v_and_b32 v45, 0xffff0000, v221\n"
"v_lshlrev_b32 v48, 16, v150\n"
"v_and_b32 v49, 0xffff0000, v150\n"
"v_lshlrev_b32 v50, 16, v158\n"
"v_and_b32 v51, 0xffff0000, v158\n"
"v_lshlrev_b32 v52, 16, v166\n"
"v_and_b32 v53, 0xffff0000, v166\n"
"v_lshlrev_b32 v54, 16, v174\n"
"v_and_b32 v55, 0xffff0000, v174\n"
"v_lshlrev_b32 v56, 16, v230\n"
"v_and_b32 v57, 0xffff0000, v230\n"
"v_lshlrev_b32 v58, 16, v214\n"
"v_and_b32 v59, 0xffff0000, v214\n"
"v_lshlrev_b32 v60, 16, v222\n"
"v_and_b32 v61, 0xffff0000, v222\n"
"v_lshlrev_b32 v64, 16, v151\n"
"v_and_b32 v65, 0xffff0000, v151\n"
"v_lshlrev_b32 v66, 16, v159\n"
"v_and_b32 v67, 0xffff0000, v159\n"
"v_lshlrev_b32 v68, 16, v167\n"
"v_and_b32 v69, 0xffff0000, v167\n"
"v_lshlrev_b32 v70, 16, v175\n"
"v_and_b32 v71, 0xffff0000, v175\n"
"v_lshlrev_b32 v72, 16, v231\n"
"v_and_b32 v73, 0xffff0000, v231\n"
"v_lshlrev_b32 v74, 16, v215\n"
"v_and_b32 v75, 0xffff0000, v215\n"
"v_lshlrev_b32 v76, 16, v223\n"
"v_and_b32 v77, 0xffff0000, v223\n"
"v_pk_add_f32 v[16:17], v[16:17], v[18:19]\n"
"v_pk_add_f32 v[32:33], v[32:33], v[34:35]\n"
"v_pk_add_f32 v[48:49], v[48:49], v[50:51]\n"
"v_pk_add_f32 v[64:65], v[64:65], v[66:67]\n"
"v_pk_fma_f32 v[22:23], v[24:25], s[90:91], v[22:23]\n"
"v_pk_fma_f32 v[38:39], v[40:41], s[90:91], v[38:39]\n"
"v_pk_fma_f32 v[54:55], v[56:57], s[90:91], v[54:55]\n"
"v_pk_fma_f32 v[70:71], v[72:73], s[90:91], v[70:71]\n"
"v_pk_add_f32 v[16:17], v[16:17], v[20:21]\n"
"v_pk_add_f32 v[32:33], v[32:33], v[36:37]\n"
"v_pk_add_f32 v[48:49], v[48:49], v[52:53]\n"
"v_pk_add_f32 v[64:65], v[64:65], v[68:69]\n"
"v_pk_add_f32 v[22:23], v[22:23], v[26:27]\n"
"v_pk_add_f32 v[38:39], v[38:39], v[42:43]\n"
"v_pk_add_f32 v[54:55], v[54:55], v[58:59]\n"
"v_pk_add_f32 v[70:71], v[70:71], v[74:75]\n"
"v_pk_fma_f32 v[16:17], v[24:25], s[88:89], v[16:17]\n"
"v_pk_fma_f32 v[32:33], v[40:41], s[88:89], v[32:33]\n"
"v_pk_fma_f32 v[48:49], v[56:57], s[88:89], v[48:49]\n"
"v_pk_fma_f32 v[64:65], v[72:73], s[88:89], v[64:65]\n"
"v_pk_mul_f32 v[18:19], v[22:23], s[92:93]\n"
"v_pk_mul_f32 v[34:35], v[38:39], s[92:93]\n"
"v_pk_mul_f32 v[50:51], v[54:55], s[92:93]\n"
"v_pk_mul_f32 v[66:67], v[70:71], s[92:93]\n"
"v_exp_f32 v18, v18\n"
"v_exp_f32 v19, v19\n"
"v_exp_f32 v34, v34\n"
"v_exp_f32 v35, v35\n"
"v_exp_f32 v50, v50\n"
"v_exp_f32 v51, v51\n"
"v_exp_f32 v66, v66\n"
"v_exp_f32 v67, v67\n"
"v_pk_add_f32 v[18:19], v[18:19], s[94:95]\n"
"v_pk_add_f32 v[34:35], v[34:35], s[94:95]\n"
"v_pk_add_f32 v[50:51], v[50:51], s[94:95]\n"
"v_pk_add_f32 v[66:67], v[66:67], s[94:95]\n"
"v_rcp_f32 v18, v18\n"
"v_rcp_f32 v19, v19\n"
"v_rcp_f32 v34, v34\n"
"v_rcp_f32 v35, v35\n"
"v_rcp_f32 v50, v50\n"
"v_rcp_f32 v51, v51\n"
"v_rcp_f32 v66, v66\n"
"v_rcp_f32 v67, v67\n"
"v_pk_mul_f32 v[16:17], v[16:17], v[22:23]\n"
"v_pk_mul_f32 v[32:33], v[32:33], v[38:39]\n"
"v_pk_mul_f32 v[48:49], v[48:49], v[54:55]\n"
"v_pk_mul_f32 v[64:65], v[64:65], v[70:71]\n"
"v_pk_fma_f32 v[16:17], v[16:17], v[18:19], v[28:29]\n"
"v_pk_fma_f32 v[32:33], v[32:33], v[34:35], v[44:45]\n"
"v_pk_fma_f32 v[48:49], v[48:49], v[50:51], v[60:61]\n"
"v_pk_fma_f32 v[64:65], v[64:65], v[66:67], v[76:77]\n"
"v_cvt_pk_bf16_f32 v180, v16, v17\n"
"v_cvt_pk_bf16_f32 v181, v32, v33\n"
"v_cvt_pk_bf16_f32 v182, v48, v49\n"
"v_cvt_pk_bf16_f32 v183, v64, v65\n"
"buffer_store_dwordx2 v[176:177], v2, s[24:27], s80 offen offset:0\n"
"buffer_store_dwordx2 v[178:179], v2, s[24:27], s80 offen offset:16\n"
"buffer_store_dwordx2 v[180:181], v2, s[24:27], s80 offen offset:32\n"
"buffer_store_dwordx2 v[182:183], v2, s[24:27], s80 offen offset:48\n"
"s_waitcnt vmcnt(0)\n"
"s_branch .Lm1_exit\n"
".Lm1_pfend:\n"
".Lfb_entry:\n"
"s_waitcnt vmcnt(0) lgkmcnt(0)\n"
"s_load_dwordx2 s[94:95], s[8:9], 16\n"
"s_and_b32 s91, s28, 7\n"
"s_lshl_b32 s91, s91, 2\n"
"s_bfe_u32 s92, s28, 0x20003\n"
"s_add_u32 s84, s91, s92\n"
"s_lshr_b32 s85, s28, 5\n"
"s_lshl_b32 s91, s84, 19\n"
"s_add_u32 s92, s91, 1048576\n"
"s_add_u32 s68, s50, s92\n"
"s_addc_u32 s69, s51, 0\n"
"s_and_b32 s69, s69, 0xffff\n"
"s_mov_b32 s70, 524288\n"
"s_mov_b32 s71, 0x20000\n"
"s_add_u32 s92, s91, 17825792\n"
"s_add_u32 s72, s50, s92\n"
"s_addc_u32 s73, s51, 0\n"
"s_and_b32 s73, s73, 0xffff\n"
"s_mov_b32 s74, 524288\n"
"s_mov_b32 s75, 0x20000\n"
"s_add_u32 s76, s50, 51380224\n"
"s_addc_u32 s77, s51, 0\n"
"s_and_b32 s77, s77, 0xffff\n"
"s_mov_b32 s78, 2097152\n"
"s_mov_b32 s79, 0x20000\n"
"s_add_u32 s80, s50, 34603008\n"
"s_addc_u32 s81, s51, 0\n"
"s_and_b32 s81, s81, 0xffff\n"
"s_mov_b32 s82, 16777216\n"
"s_mov_b32 s83, 0x20000\n"
"s_waitcnt lgkmcnt(0)\n"
"s_mov_b32 s64, s94\n"
"s_and_b32 s65, s95, 0xffff\n"
"s_mov_b32 s66, 4194304\n"
"s_mov_b32 s67, 0x20000\n"
"s_cmp_lt_u32 s29, 2\n"
"s_cselect_b32 s87, 3, 1\n"
"s_mov_b32 s86, 0\n"
"v_and_b32 v227, 1, v0\n"
".Lfb_pass:\n"
"s_lshl_b32 s91, s85, 7\n"
"s_lshl_b32 s92, s29, 6\n"
"s_add_u32 s91, s91, s92\n"
"s_lshl_b32 s92, s86, 5\n"
"s_add_u32 s91, s91, s92\n"
"s_sub_u32 s88, 15, s85\n"
"s_lshl_b32 s88, s88, 7\n"
"s_and_b32 s92, s29, 1\n"
"s_lshl_b32 s92, s92, 6\n"
"s_add_u32 s88, s88, s92\n"
"s_lshr_b32 s92, s29, 1\n"
"s_lshl_b32 s92, s92, 5\n"
"s_add_u32 s88, s88, s92\n"
"s_add_u32 s92, s86, s87\n"
"s_cmp_eq_u32 s87, 3\n"
"s_cselect_b32 s92, s86, 2\n"
"s_cmp_lt_u32 s92, 2\n"
"s_cselect_b32 s88, s91, s88\n"
"v_lshrrev_b32 v226, 1, v0\n"
"v_add_u32 v226, s88, v226\n"
"v_mov_b32 v32, 0\n"
"v_mov_b32 v33, 0\n"
"v_mov_b32 v34, 0\n"
"v_mov_b32 v35, 0\n"
"v_mov_b32 v36, 0\n"
"v_mov_b32 v37, 0\n"
"v_mov_b32 v38, 0\n"
"v_mov_b32 v39, 0\n"
"v_mov_b32 v40, 0\n"
"v_mov_b32 v41, 0\n"
"v_mov_b32 v42, 0\n"
"v_mov_b32 v43, 0\n"
"v_mov_b32 v44, 0\n"
"v_mov_b32 v45, 0\n"
"v_mov_b32 v46, 0\n"
"v_mov_b32 v47, 0\n"
"v_mov_b32 v48, 0\n"
"v_mov_b32 v49, 0\n"
"v_mov_b32 v50, 0\n"
"v_mov_b32 v51, 0\n"
"v_mov_b32 v52, 0\n"
"v_mov_b32 v53, 0\n"
"v_mov_b32 v54, 0\n"
"v_mov_b32 v55, 0\n"
"v_mov_b32 v56, 0\n"
"v_mov_b32 v57, 0\n"
"v_mov_b32 v58, 0\n"
"v_mov_b32 v59, 0\n"
"v_mov_b32 v60, 0\n"
"v_mov_b32 v61, 0\n"
"v_mov_b32 v62, 0\n"
"v_mov_b32 v63, 0\n"
"v_mov_b32 v64, 0\n"
"v_mov_b32 v65, 0\n"
"v_mov_b32 v66, 0\n"
"v_mov_b32 v67, 0\n"
"v_mov_b32 v68, 0\n"
"v_mov_b32 v69, 0\n"
"v_mov_b32 v70, 0\n"
"v_mov_b32 v71, 0\n"
"v_mov_b32 v72, 0\n"
"v_mov_b32 v73, 0\n"
"v_mov_b32 v74, 0\n"
"v_mov_b32 v75, 0\n"
"v_mov_b32 v76, 0\n"
"v_mov_b32 v77, 0\n"
"v_mov_b32 v78, 0\n"
"v_mov_b32 v79, 0\n"
"v_mov_b32 v80, 0\n"
"v_mov_b32 v81, 0\n"
"v_mov_b32 v82, 0\n"
"v_mov_b32 v83, 0\n"
"v_mov_b32 v84, 0\n"
"v_mov_b32 v85, 0\n"
"v_mov_b32 v86, 0\n"
"v_mov_b32 v87, 0\n"
"v_mov_b32 v88, 0\n"
"v_mov_b32 v89, 0\n"
"v_mov_b32 v90, 0\n"
"v_mov_b32 v91, 0\n"
"v_mov_b32 v92, 0\n"
"v_mov_b32 v93, 0\n"
"v_mov_b32 v94, 0\n"
"v_mov_b32 v95, 0\n"
"v_mov_b32 v96, 0\n"
"v_mov_b32 v97, 0\n"
"v_mov_b32 v98, 0\n"
"v_mov_b32 v99, 0\n"
"v_mov_b32 v100, 0\n"
"v_mov_b32 v101, 0\n"
"v_mov_b32 v102, 0\n"
"v_mov_b32 v103, 0\n"
"v_mov_b32 v104, 0\n"
"v_mov_b32 v105, 0\n"
"v_mov_b32 v106, 0\n"
"v_mov_b32 v107, 0\n"
"v_mov_b32 v108, 0\n"
"v_mov_b32 v109, 0\n"
"v_mov_b32 v110, 0\n"
"v_mov_b32 v111, 0\n"
"v_mov_b32 v112, 0\n"
"v_mov_b32 v113, 0\n"
"v_mov_b32 v114, 0\n"
"v_mov_b32 v115, 0\n"
"v_mov_b32 v116, 0\n"
"v_mov_b32 v117, 0\n"
"v_mov_b32 v118, 0\n"
"v_mov_b32 v119, 0\n"
"v_mov_b32 v120, 0\n"
"v_mov_b32 v121, 0\n"
"v_mov_b32 v122, 0\n"
"v_mov_b32 v123, 0\n"
"v_mov_b32 v124, 0\n"
"v_mov_b32 v125, 0\n"
"v_mov_b32 v126, 0\n"
"v_mov_b32 v127, 0\n"
"v_mov_b32 v128, 0\n"
"v_mov_b32 v129, 0\n"
"v_mov_b32 v130, 0\n"
"v_mov_b32 v131, 0\n"
"v_mov_b32 v132, 0\n"
"v_mov_b32 v133, 0\n"
"v_mov_b32 v134, 0\n"
"v_mov_b32 v135, 0\n"
"v_mov_b32 v136, 0\n"
"v_mov_b32 v137, 0\n"
"v_mov_b32 v138, 0\n"
"v_mov_b32 v139, 0\n"
"v_mov_b32 v140, 0\n"
"v_mov_b32 v141, 0\n"
"v_mov_b32 v142, 0\n"
"v_mov_b32 v143, 0\n"
"v_mov_b32 v144, 0\n"
"v_mov_b32 v145, 0\n"
"v_mov_b32 v146, 0\n"
"v_mov_b32 v147, 0\n"
"v_mov_b32 v148, 0\n"
"v_mov_b32 v149, 0\n"
"v_mov_b32 v150, 0\n"
"v_mov_b32 v151, 0\n"
"v_mov_b32 v152, 0\n"
"v_mov_b32 v153, 0\n"
"v_mov_b32 v154, 0\n"
"v_mov_b32 v155, 0\n"
"v_mov_b32 v156, 0\n"
"v_mov_b32 v157, 0\n"
"v_mov_b32 v158, 0\n"
"v_mov_b32 v159, 0\n"
"v_lshrrev_b32 v228, 5, v226\n"
"v_lshlrev_b32 v228, 15, v228\n"
"v_and_b32 v229, 31, v226\n"
"v_lshl_add_u32 v228, v229, 4, v228\n"
"v_lshlrev_b32 v229, 7, v227\n"
"s_mov_b32 s89, 0\n"
".Lfb_q:\n"
"s_lshr_b32 s91, s89, 3\n"
"s_lshl_b32 s91, s91, 9\n"
"s_and_b32 s92, s89, 7\n"
"s_lshl_b32 s92, s92, 1\n"
"s_add_u32 s91, s91, s92\n"
"buffer_load_ushort v230, v228, s[76:79], s91 offen sc1\n"
"s_lshl_b32 s91, s89, 13\n"
"s_lshl_b32 s92, s84, 8\n"
"s_add_u32 s91, s91, s92\n"
"buffer_load_dwordx4 v[160:163], v229, s[64:67], s91 offen offset:0\n"
"buffer_load_dwordx4 v[164:167], v229, s[64:67], s91 offen offset:16\n"
"buffer_load_dwordx4 v[168:171], v229, s[64:67], s91 offen offset:32\n"
"buffer_load_dwordx4 v[172:175], v229, s[64:67], s91 offen offset:48\n"
"buffer_load_dwordx4 v[176:179], v229, s[64:67], s91 offen offset:64\n"
"buffer_load_dwordx4 v[180:183], v229, s[64:67], s91 offen offset:80\n"
"buffer_load_dwordx4 v[184:187], v229, s[64:67], s91 offen offset:96\n"
"buffer_load_dwordx4 v[188:191], v229, s[64:67], s91 offen offset:112\n"
"s_waitcnt vmcnt(0)\n"
"v_lshlrev_b32 v230, 16, v230\n"
"v_lshlrev_b32 v192, 16, v160\n"
"v_and_b32 v193, 0xffff0000, v160\n"
"v_fmac_f32 v32, v230, v192\n"
"v_fmac_f32 v33, v230, v193\n"
"v_lshlrev_b32 v192, 16, v161\n"
"v_and_b32 v193, 0xffff0000, v161\n"
"v_fmac_f32 v34, v230, v192\n"
"v_fmac_f32 v35, v230, v193\n"
"v_lshlrev_b32 v192, 16, v162\n"
"v_and_b32 v193, 0xffff0000, v162\n"
"v_fmac_f32 v40, v230, v192\n"
"v_fmac_f32 v41, v230, v193\n"
"v_lshlrev_b32 v192, 16, v163\n"
"v_and_b32 v193, 0xffff0000, v163\n"
"v_fmac_f32 v42, v230, v192\n"
"v_fmac_f32 v43, v230, v193\n"
"v_lshlrev_b32 v192, 16, v164\n"
"v_and_b32 v193, 0xffff0000, v164\n"
"v_fmac_f32 v36, v230, v192\n"
"v_fmac_f32 v37, v230, v193\n"
"v_lshlrev_b32 v192, 16, v165\n"
"v_and_b32 v193, 0xffff0000, v165\n"
"v_fmac_f32 v38, v230, v192\n"
"v_fmac_f32 v39, v230, v193\n"
"v_lshlrev_b32 v192, 16, v166\n"
"v_and_b32 v193, 0xffff0000, v166\n"
"v_fmac_f32 v44, v230, v192\n"
"v_fmac_f32 v45, v230, v193\n"
"v_lshlrev_b32 v192, 16, v167\n"
"v_and_b32 v193, 0xffff0000, v167\n"
"v_fmac_f32 v46, v230, v192\n"
"v_fmac_f32 v47, v230, v193\n"
"v_lshlrev_b32 v192, 16, v168\n"
"v_and_b32 v193, 0xffff0000, v168\n"
"v_fmac_f32 v48, v230, v192\n"
"v_fmac_f32 v49, v230, v193\n"
"v_lshlrev_b32 v192, 16, v169\n"
"v_and_b32 v193, 0xffff0000, v169\n"
"v_fmac_f32 v50, v230, v192\n"
"v_fmac_f32 v51, v230, v193\n"
"v_lshlrev_b32 v192, 16, v170\n"
"v_and_b32 v193, 0xffff0000, v170\n"
"v_fmac_f32 v56, v230, v192\n"
"v_fmac_f32 v57, v230, v193\n"
"v_lshlrev_b32 v192, 16, v171\n"
"v_and_b32 v193, 0xffff0000, v171\n"
"v_fmac_f32 v58, v230, v192\n"
"v_fmac_f32 v59, v230, v193\n"
"v_lshlrev_b32 v192, 16, v172\n"
"v_and_b32 v193, 0xffff0000, v172\n"
"v_fmac_f32 v52, v230, v192\n"
"v_fmac_f32 v53, v230, v193\n"
"v_lshlrev_b32 v192, 16, v173\n"
"v_and_b32 v193, 0xffff0000, v173\n"
"v_fmac_f32 v54, v230, v192\n"
"v_fmac_f32 v55, v230, v193\n"
"v_lshlrev_b32 v192, 16, v174\n"
"v_and_b32 v193, 0xffff0000, v174\n"
"v_fmac_f32 v60, v230, v192\n"
"v_fmac_f32 v61, v230, v193\n"
"v_lshlrev_b32 v192, 16, v175\n"
"v_and_b32 v193, 0xffff0000, v175\n"
"v_fmac_f32 v62, v230, v192\n"
"v_fmac_f32 v63, v230, v193\n"
"v_lshlrev_b32 v192, 16, v176\n"
"v_and_b32 v193, 0xffff0000, v176\n"
"v_fmac_f32 v64, v230, v192\n"
"v_fmac_f32 v65, v230, v193\n"
"v_lshlrev_b32 v192, 16, v177\n"
"v_and_b32 v193, 0xffff0000, v177\n"
"v_fmac_f32 v66, v230, v192\n"
"v_fmac_f32 v67, v230, v193\n"
"v_lshlrev_b32 v192, 16, v178\n"
"v_and_b32 v193, 0xffff0000, v178\n"
"v_fmac_f32 v72, v230, v192\n"
"v_fmac_f32 v73, v230, v193\n"
"v_lshlrev_b32 v192, 16, v179\n"
"v_and_b32 v193, 0xffff0000, v179\n"
"v_fmac_f32 v74, v230, v192\n"
"v_fmac_f32 v75, v230, v193\n"
"v_lshlrev_b32 v192, 16, v180\n"
"v_and_b32 v193, 0xffff0000, v180\n"
"v_fmac_f32 v68, v230, v192\n"
"v_fmac_f32 v69, v230, v193\n"
"v_lshlrev_b32 v192, 16, v181\n"
"v_and_b32 v193, 0xffff0000, v181\n"
"v_fmac_f32 v70, v230, v192\n"
"v_fmac_f32 v71, v230, v193\n"
"v_lshlrev_b32 v192, 16, v182\n"
"v_and_b32 v193, 0xffff0000, v182\n"
"v_fmac_f32 v76, v230, v192\n"
"v_fmac_f32 v77, v230, v193\n"
"v_lshlrev_b32 v192, 16, v183\n"
"v_and_b32 v193, 0xffff0000, v183\n"
"v_fmac_f32 v78, v230, v192\n"
"v_fmac_f32 v79, v230, v193\n"
"v_lshlrev_b32 v192, 16, v184\n"
"v_and_b32 v193, 0xffff0000, v184\n"
"v_fmac_f32 v80, v230, v192\n"
"v_fmac_f32 v81, v230, v193\n"
"v_lshlrev_b32 v192, 16, v185\n"
"v_and_b32 v193, 0xffff0000, v185\n"
"v_fmac_f32 v82, v230, v192\n"
"v_fmac_f32 v83, v230, v193\n"
"v_lshlrev_b32 v192, 16, v186\n"
"v_and_b32 v193, 0xffff0000, v186\n"
"v_fmac_f32 v88, v230, v192\n"
"v_fmac_f32 v89, v230, v193\n"
"v_lshlrev_b32 v192, 16, v187\n"
"v_and_b32 v193, 0xffff0000, v187\n"
"v_fmac_f32 v90, v230, v192\n"
"v_fmac_f32 v91, v230, v193\n"
"v_lshlrev_b32 v192, 16, v188\n"
"v_and_b32 v193, 0xffff0000, v188\n"
"v_fmac_f32 v84, v230, v192\n"
"v_fmac_f32 v85, v230, v193\n"
"v_lshlrev_b32 v192, 16, v189\n"
"v_and_b32 v193, 0xffff0000, v189\n"
"v_fmac_f32 v86, v230, v192\n"
"v_fmac_f32 v87, v230, v193\n"
"v_lshlrev_b32 v192, 16, v190\n"
"v_and_b32 v193, 0xffff0000, v190\n"
"v_fmac_f32 v92, v230, v192\n"
"v_fmac_f32 v93, v230, v193\n"
"v_lshlrev_b32 v192, 16, v191\n"
"v_and_b32 v193, 0xffff0000, v191\n"
"v_fmac_f32 v94, v230, v192\n"
"v_fmac_f32 v95, v230, v193\n"
"s_add_u32 s89, s89, 1\n"
"s_cmp_lt_u32 s89, 512\n"
"s_cbranch_scc1 .Lfb_q\n"
"s_mov_b32 s91, 0x3e0293ee\n"
"v_mul_f32 v32, s91, v32\n"
"v_mul_f32 v33, s91, v33\n"
"v_mul_f32 v34, s91, v34\n"
"v_mul_f32 v35, s91, v35\n"
"v_mul_f32 v36, s91, v36\n"
"v_mul_f32 v37, s91, v37\n"
"v_mul_f32 v38, s91, v38\n"
"v_mul_f32 v39, s91, v39\n"
"v_mul_f32 v40, s91, v40\n"
"v_mul_f32 v41, s91, v41\n"
"v_mul_f32 v42, s91, v42\n"
"v_mul_f32 v43, s91, v43\n"
"v_mul_f32 v44, s91, v44\n"
"v_mul_f32 v45, s91, v45\n"
"v_mul_f32 v46, s91, v46\n"
"v_mul_f32 v47, s91, v47\n"
"v_mul_f32 v48, s91, v48\n"
"v_mul_f32 v49, s91, v49\n"
"v_mul_f32 v50, s91, v50\n"
"v_mul_f32 v51, s91, v51\n"
"v_mul_f32 v52, s91, v52\n"
"v_mul_f32 v53, s91, v53\n"
"v_mul_f32 v54, s91, v54\n"
"v_mul_f32 v55, s91, v55\n"
"v_mul_f32 v56, s91, v56\n"
"v_mul_f32 v57, s91, v57\n"
"v_mul_f32 v58, s91, v58\n"
"v_mul_f32 v59, s91, v59\n"
"v_mul_f32 v60, s91, v60\n"
"v_mul_f32 v61, s91, v61\n"
"v_mul_f32 v62, s91, v62\n"
"v_mul_f32 v63, s91, v63\n"
"v_mul_f32 v64, s91, v64\n"
"v_mul_f32 v65, s91, v65\n"
"v_mul_f32 v66, s91, v66\n"
"v_mul_f32 v67, s91, v67\n"
"v_mul_f32 v68, s91, v68\n"
"v_mul_f32 v69, s91, v69\n"
"v_mul_f32 v70, s91, v70\n"
"v_mul_f32 v71, s91, v71\n"
"v_mul_f32 v72, s91, v72\n"
"v_mul_f32 v73, s91, v73\n"
"v_mul_f32 v74, s91, v74\n"
"v_mul_f32 v75, s91, v75\n"
"v_mul_f32 v76, s91, v76\n"
"v_mul_f32 v77, s91, v77\n"
"v_mul_f32 v78, s91, v78\n"
"v_mul_f32 v79, s91, v79\n"
"v_mul_f32 v80, s91, v80\n"
"v_mul_f32 v81, s91, v81\n"
"v_mul_f32 v82, s91, v82\n"
"v_mul_f32 v83, s91, v83\n"
"v_mul_f32 v84, s91, v84\n"
"v_mul_f32 v85, s91, v85\n"
"v_mul_f32 v86, s91, v86\n"
"v_mul_f32 v87, s91, v87\n"
"v_mul_f32 v88, s91, v88\n"
"v_mul_f32 v89, s91, v89\n"
"v_mul_f32 v90, s91, v90\n"
"v_mul_f32 v91, s91, v91\n"
"v_mul_f32 v92, s91, v92\n"
"v_mul_f32 v93, s91, v93\n"
"v_mul_f32 v94, s91, v94\n"
"v_mul_f32 v95, s91, v95\n"
"v_mov_b32 v224, 0xff61b1e6\n"
"v_mov_b32 v225, 0\n"
"v_lshlrev_b32 v228, 12, v227\n"
"v_lshlrev_b32 v229, 11, v227\n"
"s_add_u32 s90, s88, 32\n"
"s_mov_b32 s89, 0\n"
".Lfb_k:\n"
"s_lshr_b32 s91, s89, 5\n"
"s_lshl_b32 s91, s91, 13\n"
"s_and_b32 s92, s89, 31\n"
"s_lshl_b32 s92, s92, 4\n"
"s_add_u32 s91, s91, s92\n"
"buffer_load_dwordx4 v[160:163], v228, s[68:71], s91 offen offset:0 sc1\n"
"buffer_load_dwordx4 v[164:167], v228, s[68:71], s91 offen offset:512 sc1\n"
"buffer_load_dwordx4 v[168:171], v228, s[68:71], s91 offen offset:1024 sc1\n"
"buffer_load_dwordx4 v[172:175], v228, s[68:71], s91 offen offset:1536 sc1\n"
"buffer_load_dwordx4 v[176:179], v228, s[68:71], s91 offen offset:2048 sc1\n"
"buffer_load_dwordx4 v[180:183], v228, s[68:71], s91 offen offset:2560 sc1\n"
"buffer_load_dwordx4 v[184:187], v228, s[68:71], s91 offen offset:3072 sc1\n"
"buffer_load_dwordx4 v[188:191], v228, s[68:71], s91 offen offset:3584 sc1\n"
"s_waitcnt vmcnt(0)\n"
"v_mov_b32 v230, 0\n"
"v_lshlrev_b32 v192, 16, v160\n"
"v_and_b32 v193, 0xffff0000, v160\n"
"v_fmac_f32 v230, v32, v192\n"
"v_fmac_f32 v230, v33, v193\n"
"v_lshlrev_b32 v192, 16, v161\n"
"v_and_b32 v193, 0xffff0000, v161\n"
"v_fmac_f32 v230, v34, v192\n"
"v_fmac_f32 v230, v35, v193\n"
"v_lshlrev_b32 v192, 16, v162\n"
"v_and_b32 v193, 0xffff0000, v162\n"
"v_fmac_f32 v230, v36, v192\n"
"v_fmac_f32 v230, v37, v193\n"
"v_lshlrev_b32 v192, 16, v163\n"
"v_and_b32 v193, 0xffff0000, v163\n"
"v_fmac_f32 v230, v38, v192\n"
"v_fmac_f32 v230, v39, v193\n"
"v_lshlrev_b32 v192, 16, v164\n"
"v_and_b32 v193, 0xffff0000, v164\n"
"v_fmac_f32 v230, v40, v192\n"
"v_fmac_f32 v230, v41, v193\n"
"v_lshlrev_b32 v192, 16, v165\n"
"v_and_b32 v193, 0xffff0000, v165\n"
"v_fmac_f32 v230, v42, v192\n"
"v_fmac_f32 v230, v43, v193\n"
"v_lshlrev_b32 v192, 16, v166\n"
"v_and_b32 v193, 0xffff0000, v166\n"
"v_fmac_f32 v230, v44, v192\n"
"v_fmac_f32 v230, v45, v193\n"
"v_lshlrev_b32 v192, 16, v167\n"
"v_and_b32 v193, 0xffff0000, v167\n"
"v_fmac_f32 v230, v46, v192\n"
"v_fmac_f32 v230, v47, v193\n"
"v_lshlrev_b32 v192, 16, v168\n"
"v_and_b32 v193, 0xffff0000, v168\n"
"v_fmac_f32 v230, v48, v192\n"
"v_fmac_f32 v230, v49, v193\n"
"v_lshlrev_b32 v192, 16, v169\n"
"v_and_b32 v193, 0xffff0000, v169\n"
"v_fmac_f32 v230, v50, v192\n"
"v_fmac_f32 v230, v51, v193\n"
"v_lshlrev_b32 v192, 16, v170\n"
"v_and_b32 v193, 0xffff0000, v170\n"
"v_fmac_f32 v230, v52, v192\n"
"v_fmac_f32 v230, v53, v193\n"
"v_lshlrev_b32 v192, 16, v171\n"
"v_and_b32 v193, 0xffff0000, v171\n"
"v_fmac_f32 v230, v54, v192\n"
"v_fmac_f32 v230, v55, v193\n"
"v_lshlrev_b32 v192, 16, v172\n"
"v_and_b32 v193, 0xffff0000, v172\n"
"v_fmac_f32 v230, v56, v192\n"
"v_fmac_f32 v230, v57, v193\n"
"v_lshlrev_b32 v192, 16, v173\n"
"v_and_b32 v193, 0xffff0000, v173\n"
"v_fmac_f32 v230, v58, v192\n"
"v_fmac_f32 v230, v59, v193\n"
"v_lshlrev_b32 v192, 16, v174\n"
"v_and_b32 v193, 0xffff0000, v174\n"
"v_fmac_f32 v230, v60, v192\n"
"v_fmac_f32 v230, v61, v193\n"
"v_lshlrev_b32 v192, 16, v175\n"
"v_and_b32 v193, 0xffff0000, v175\n"
"v_fmac_f32 v230, v62, v192\n"
"v_fmac_f32 v230, v63, v193\n"
"v_lshlrev_b32 v192, 16, v176\n"
"v_and_b32 v193, 0xffff0000, v176\n"
"v_fmac_f32 v230, v64, v192\n"
"v_fmac_f32 v230, v65, v193\n"
"v_lshlrev_b32 v192, 16, v177\n"
"v_and_b32 v193, 0xffff0000, v177\n"
"v_fmac_f32 v230, v66, v192\n"
"v_fmac_f32 v230, v67, v193\n"
"v_lshlrev_b32 v192, 16, v178\n"
"v_and_b32 v193, 0xffff0000, v178\n"
"v_fmac_f32 v230, v68, v192\n"
"v_fmac_f32 v230, v69, v193\n"
"v_lshlrev_b32 v192, 16, v179\n"
"v_and_b32 v193, 0xffff0000, v179\n"
"v_fmac_f32 v230, v70, v192\n"
"v_fmac_f32 v230, v71, v193\n"
"v_lshlrev_b32 v192, 16, v180\n"
"v_and_b32 v193, 0xffff0000, v180\n"
"v_fmac_f32 v230, v72, v192\n"
"v_fmac_f32 v230, v73, v193\n"
"v_lshlrev_b32 v192, 16, v181\n"
"v_and_b32 v193, 0xffff0000, v181\n"
"v_fmac_f32 v230, v74, v192\n"
"v_fmac_f32 v230, v75, v193\n"
"v_lshlrev_b32 v192, 16, v182\n"
"v_and_b32 v193, 0xffff0000, v182\n"
"v_fmac_f32 v230, v76, v192\n"
"v_fmac_f32 v230, v77, v193\n"
"v_lshlrev_b32 v192, 16, v183\n"
"v_and_b32 v193, 0xffff0000, v183\n"
"v_fmac_f32 v230, v78, v192\n"
"v_fmac_f32 v230, v79, v193\n"
"v_lshlrev_b32 v192, 16, v184\n"
"v_and_b32 v193, 0xffff0000, v184\n"
"v_fmac_f32 v230, v80, v192\n"
"v_fmac_f32 v230, v81, v193\n"
"v_lshlrev_b32 v192, 16, v185\n"
"v_and_b32 v193, 0xffff0000, v185\n"
"v_fmac_f32 v230, v82, v192\n"
"v_fmac_f32 v230, v83, v193\n"
"v_lshlrev_b32 v192, 16, v186\n"
"v_and_b32 v193, 0xffff0000, v186\n"
"v_fmac_f32 v230, v84, v192\n"
"v_fmac_f32 v230, v85, v193\n"
"v_lshlrev_b32 v192, 16, v187\n"
"v_and_b32 v193, 0xffff0000, v187\n"
"v_fmac_f32 v230, v86, v192\n"
"v_fmac_f32 v230, v87, v193\n"
"v_lshlrev_b32 v192, 16, v188\n"
"v_and_b32 v193, 0xffff0000, v188\n"
"v_fmac_f32 v230, v88, v192\n"
"v_fmac_f32 v230, v89, v193\n"
"v_lshlrev_b32 v192, 16, v189\n"
"v_and_b32 v193, 0xffff0000, v189\n"
"v_fmac_f32 v230, v90, v192\n"
"v_fmac_f32 v230, v91, v193\n"
"v_lshlrev_b32 v192, 16, v190\n"
"v_and_b32 v193, 0xffff0000, v190\n"
"v_fmac_f32 v230, v92, v192\n"
"v_fmac_f32 v230, v93, v193\n"
"v_lshlrev_b32 v192, 16, v191\n"
"v_and_b32 v193, 0xffff0000, v191\n"
"v_fmac_f32 v230, v94, v192\n"
"v_fmac_f32 v230, v95, v193\n"
"s_nop 1\n"
"v_add_f32_dpp v230, v230, v230 quad_perm:[1,0,3,2] row_mask:0xf bank_mask:0xf\n"
"v_mov_b32 v194, s89\n"
"v_cmp_le_u32 vcc, v194, v226\n"
"v_mov_b32 v195, 0xff800000\n"
"s_nop 0\n"
"v_cndmask_b32 v230, v195, v230, vcc\n"
"v_max_f32 v196, v224, v230\n"
"v_sub_f32 v197, v224, v196\n"
"v_sub_f32 v230, v230, v196\n"
"v_exp_f32 v197, v197\n"
"v_exp_f32 v230, v230\n"
"v_mov_b32 v224, v196\n"
"v_fma_f32 v225, v225, v197, v230\n"
"s_lshr_b32 s91, s89, 4\n"
"s_lshl_b32 s91, s91, 12\n"
"s_bfe_u32 s92, s89, 0x10002\n"
"s_lshl_b32 s92, s92, 9\n"
"s_add_u32 s91, s91, s92\n"
"s_and_b32 s92, s89, 3\n"
"s_lshl_b32 s92, s92, 1\n"
"s_add_u32 s91, s91, s92\n"
"s_bfe_u32 s92, s89, 0x10003\n"
"s_lshl_b32 s92, s92, 3\n"
"s_add_u32 s91, s91, s92\n"
"buffer_load_ushort v160, v229, s[72:75], s91 offen offset:0 sc1\n"
"buffer_load_ushort v161, v229, s[72:75], s91 offen offset:16 sc1\n"
"buffer_load_ushort v162, v229, s[72:75], s91 offen offset:32 sc1\n"
"buffer_load_ushort v163, v229, s[72:75], s91 offen offset:48 sc1\n"
"buffer_load_ushort v164, v229, s[72:75], s91 offen offset:128 sc1\n"
"buffer_load_ushort v165, v229, s[72:75], s91 offen offset:144 sc1\n"
"buffer_load_ushort v166, v229, s[72:75], s91 offen offset:160 sc1\n"
"buffer_load_ushort v167, v229, s[72:75], s91 offen offset:176 sc1\n"
"buffer_load_ushort v168, v229, s[72:75], s91 offen offset:64 sc1\n"
"buffer_load_ushort v169, v229, s[72:75], s91 offen offset:80 sc1\n"
"buffer_load_ushort v170, v229, s[72:75], s91 offen offset:96 sc1\n"
"buffer_load_ushort v171, v229, s[72:75], s91 offen offset:112 sc1\n"
"buffer_load_ushort v172, v229, s[72:75], s91 offen offset:192 sc1\n"
"buffer_load_ushort v173, v229, s[72:75], s91 offen offset:208 sc1\n"
"buffer_load_ushort v174, v229, s[72:75], s91 offen offset:224 sc1\n"
"buffer_load_ushort v175, v229, s[72:75], s91 offen offset:240 sc1\n"
"buffer_load_ushort v176, v229, s[72:75], s91 offen offset:256 sc1\n"
"buffer_load_ushort v177, v229, s[72:75], s91 offen offset:272 sc1\n"
"buffer_load_ushort v178, v229, s[72:75], s91 offen offset:288 sc1\n"
"buffer_load_ushort v179, v229, s[72:75], s91 offen offset:304 sc1\n"
"buffer_load_ushort v180, v229, s[72:75], s91 offen offset:384 sc1\n"
"buffer_load_ushort v181, v229, s[72:75], s91 offen offset:400 sc1\n"
"buffer_load_ushort v182, v229, s[72:75], s91 offen offset:416 sc1\n"
"buffer_load_ushort v183, v229, s[72:75], s91 offen offset:432 sc1\n"
"buffer_load_ushort v184, v229, s[72:75], s91 offen offset:320 sc1\n"
"buffer_load_ushort v185, v229, s[72:75], s91 offen offset:336 sc1\n"
"buffer_load_ushort v186, v229, s[72:75], s91 offen offset:352 sc1\n"
"buffer_load_ushort v187, v229, s[72:75], s91 offen offset:368 sc1\n"
"buffer_load_ushort v188, v229, s[72:75], s91 offen offset:448 sc1\n"
"buffer_load_ushort v189, v229, s[72:75], s91 offen offset:464 sc1\n"
"buffer_load_ushort v190, v229, s[72:75], s91 offen offset:480 sc1\n"
"buffer_load_ushort v191, v229, s[72:75], s91 offen offset:496 sc1\n"
"s_waitcnt vmcnt(0)\n"
"v_lshlrev_b32 v160, 16, v160\n"
"v_mul_f32 v96, v96, v197\n"
"v_fmac_f32 v96, v230, v160\n"
"v_lshlrev_b32 v161, 16, v161\n"
"v_mul_f32 v97, v97, v197\n"
"v_fmac_f32 v97, v230, v161\n"
"v_lshlrev_b32 v162, 16, v162\n"
"v_mul_f32 v98, v98, v197\n"
"v_fmac_f32 v98, v230, v162\n"
"v_lshlrev_b32 v163, 16, v163\n"
"v_mul_f32 v99, v99, v197\n"
"v_fmac_f32 v99, v230, v163\n"
"v_lshlrev_b32 v164, 16, v164\n"
"v_mul_f32 v100, v100, v197\n"
"v_fmac_f32 v100, v230, v164\n"
"v_lshlrev_b32 v165, 16, v165\n"
"v_mul_f32 v101, v101, v197\n"
"v_fmac_f32 v101, v230, v165\n"
"v_lshlrev_b32 v166, 16, v166\n"
"v_mul_f32 v102, v102, v197\n"
"v_fmac_f32 v102, v230, v166\n"
"v_lshlrev_b32 v167, 16, v167\n"
"v_mul_f32 v103, v103, v197\n"
"v_fmac_f32 v103, v230, v167\n"
"v_lshlrev_b32 v168, 16, v168\n"
"v_mul_f32 v104, v104, v197\n"
"v_fmac_f32 v104, v230, v168\n"
"v_lshlrev_b32 v169, 16, v169\n"
"v_mul_f32 v105, v105, v197\n"
"v_fmac_f32 v105, v230, v169\n"
"v_lshlrev_b32 v170, 16, v170\n"
"v_mul_f32 v106, v106, v197\n"
"v_fmac_f32 v106, v230, v170\n"
"v_lshlrev_b32 v171, 16, v171\n"
"v_mul_f32 v107, v107, v197\n"
"v_fmac_f32 v107, v230, v171\n"
"v_lshlrev_b32 v172, 16, v172\n"
"v_mul_f32 v108, v108, v197\n"
"v_fmac_f32 v108, v230, v172\n"
"v_lshlrev_b32 v173, 16, v173\n"
"v_mul_f32 v109, v109, v197\n"
"v_fmac_f32 v109, v230, v173\n"
"v_lshlrev_b32 v174, 16, v174\n"
"v_mul_f32 v110, v110, v197\n"
"v_fmac_f32 v110, v230, v174\n"
"v_lshlrev_b32 v175, 16, v175\n"
"v_mul_f32 v111, v111, v197\n"
"v_fmac_f32 v111, v230, v175\n"
"v_lshlrev_b32 v176, 16, v176\n"
"v_mul_f32 v112, v112, v197\n"
"v_fmac_f32 v112, v230, v176\n"
"v_lshlrev_b32 v177, 16, v177\n"
"v_mul_f32 v113, v113, v197\n"
"v_fmac_f32 v113, v230, v177\n"
"v_lshlrev_b32 v178, 16, v178\n"
"v_mul_f32 v114, v114, v197\n"
"v_fmac_f32 v114, v230, v178\n"
"v_lshlrev_b32 v179, 16, v179\n"
"v_mul_f32 v115, v115, v197\n"
"v_fmac_f32 v115, v230, v179\n"
"v_lshlrev_b32 v180, 16, v180\n"
"v_mul_f32 v116, v116, v197\n"
"v_fmac_f32 v116, v230, v180\n"
"v_lshlrev_b32 v181, 16, v181\n"
"v_mul_f32 v117, v117, v197\n"
"v_fmac_f32 v117, v230, v181\n"
"v_lshlrev_b32 v182, 16, v182\n"
"v_mul_f32 v118, v118, v197\n"
"v_fmac_f32 v118, v230, v182\n"
"v_lshlrev_b32 v183, 16, v183\n"
"v_mul_f32 v119, v119, v197\n"
"v_fmac_f32 v119, v230, v183\n"
"v_lshlrev_b32 v184, 16, v184\n"
"v_mul_f32 v120, v120, v197\n"
"v_fmac_f32 v120, v230, v184\n"
"v_lshlrev_b32 v185, 16, v185\n"
"v_mul_f32 v121, v121, v197\n"
"v_fmac_f32 v121, v230, v185\n"
"v_lshlrev_b32 v186, 16, v186\n"
"v_mul_f32 v122, v122, v197\n"
"v_fmac_f32 v122, v230, v186\n"
"v_lshlrev_b32 v187, 16, v187\n"
"v_mul_f32 v123, v123, v197\n"
"v_fmac_f32 v123, v230, v187\n"
"v_lshlrev_b32 v188, 16, v188\n"
"v_mul_f32 v124, v124, v197\n"
"v_fmac_f32 v124, v230, v188\n"
"v_lshlrev_b32 v189, 16, v189\n"
"v_mul_f32 v125, v125, v197\n"
"v_fmac_f32 v125, v230, v189\n"
"v_lshlrev_b32 v190, 16, v190\n"
"v_mul_f32 v126, v126, v197\n"
"v_fmac_f32 v126, v230, v190\n"
"v_lshlrev_b32 v191, 16, v191\n"
"v_mul_f32 v127, v127, v197\n"
"v_fmac_f32 v127, v230, v191\n"
"buffer_load_ushort v160, v229, s[72:75], s91 offen offset:1024 sc1\n"
"buffer_load_ushort v161, v229, s[72:75], s91 offen offset:1040 sc1\n"
"buffer_load_ushort v162, v229, s[72:75], s91 offen offset:1056 sc1\n"
"buffer_load_ushort v163, v229, s[72:75], s91 offen offset:1072 sc1\n"
"buffer_load_ushort v164, v229, s[72:75], s91 offen offset:1152 sc1\n"
"buffer_load_ushort v165, v229, s[72:75], s91 offen offset:1168 sc1\n"
"buffer_load_ushort v166, v229, s[72:75], s91 offen offset:1184 sc1\n"
"buffer_load_ushort v167, v229, s[72:75], s91 offen offset:1200 sc1\n"
"buffer_load_ushort v168, v229, s[72:75], s91 offen offset:1088 sc1\n"
"buffer_load_ushort v169, v229, s[72:75], s91 offen offset:1104 sc1\n"
"buffer_load_ushort v170, v229, s[72:75], s91 offen offset:1120 sc1\n"
"buffer_load_ushort v171, v229, s[72:75], s91 offen offset:1136 sc1\n"
"buffer_load_ushort v172, v229, s[72:75], s91 offen offset:1216 sc1\n"
"buffer_load_ushort v173, v229, s[72:75], s91 offen offset:1232 sc1\n"
"buffer_load_ushort v174, v229, s[72:75], s91 offen offset:1248 sc1\n"
"buffer_load_ushort v175, v229, s[72:75], s91 offen offset:1264 sc1\n"
"buffer_load_ushort v176, v229, s[72:75], s91 offen offset:1280 sc1\n"
"buffer_load_ushort v177, v229, s[72:75], s91 offen offset:1296 sc1\n"
"buffer_load_ushort v178, v229, s[72:75], s91 offen offset:1312 sc1\n"
"buffer_load_ushort v179, v229, s[72:75], s91 offen offset:1328 sc1\n"
"buffer_load_ushort v180, v229, s[72:75], s91 offen offset:1408 sc1\n"
"buffer_load_ushort v181, v229, s[72:75], s91 offen offset:1424 sc1\n"
"buffer_load_ushort v182, v229, s[72:75], s91 offen offset:1440 sc1\n"
"buffer_load_ushort v183, v229, s[72:75], s91 offen offset:1456 sc1\n"
"buffer_load_ushort v184, v229, s[72:75], s91 offen offset:1344 sc1\n"
"buffer_load_ushort v185, v229, s[72:75], s91 offen offset:1360 sc1\n"
"buffer_load_ushort v186, v229, s[72:75], s91 offen offset:1376 sc1\n"
"buffer_load_ushort v187, v229, s[72:75], s91 offen offset:1392 sc1\n"
"buffer_load_ushort v188, v229, s[72:75], s91 offen offset:1472 sc1\n"
"buffer_load_ushort v189, v229, s[72:75], s91 offen offset:1488 sc1\n"
"buffer_load_ushort v190, v229, s[72:75], s91 offen offset:1504 sc1\n"
"buffer_load_ushort v191, v229, s[72:75], s91 offen offset:1520 sc1\n"
"s_waitcnt vmcnt(0)\n"
"v_lshlrev_b32 v160, 16, v160\n"
"v_mul_f32 v128, v128, v197\n"
"v_fmac_f32 v128, v230, v160\n"
"v_lshlrev_b32 v161, 16, v161\n"
"v_mul_f32 v129, v129, v197\n"
"v_fmac_f32 v129, v230, v161\n"
"v_lshlrev_b32 v162, 16, v162\n"
"v_mul_f32 v130, v130, v197\n"
"v_fmac_f32 v130, v230, v162\n"
"v_lshlrev_b32 v163, 16, v163\n"
"v_mul_f32 v131, v131, v197\n"
"v_fmac_f32 v131, v230, v163\n"
"v_lshlrev_b32 v164, 16, v164\n"
"v_mul_f32 v132, v132, v197\n"
"v_fmac_f32 v132, v230, v164\n"
"v_lshlrev_b32 v165, 16, v165\n"
"v_mul_f32 v133, v133, v197\n"
"v_fmac_f32 v133, v230, v165\n"
"v_lshlrev_b32 v166, 16, v166\n"
"v_mul_f32 v134, v134, v197\n"
"v_fmac_f32 v134, v230, v166\n"
"v_lshlrev_b32 v167, 16, v167\n"
"v_mul_f32 v135, v135, v197\n"
"v_fmac_f32 v135, v230, v167\n"
"v_lshlrev_b32 v168, 16, v168\n"
"v_mul_f32 v136, v136, v197\n"
"v_fmac_f32 v136, v230, v168\n"
"v_lshlrev_b32 v169, 16, v169\n"
"v_mul_f32 v137, v137, v197\n"
"v_fmac_f32 v137, v230, v169\n"
"v_lshlrev_b32 v170, 16, v170\n"
"v_mul_f32 v138, v138, v197\n"
"v_fmac_f32 v138, v230, v170\n"
"v_lshlrev_b32 v171, 16, v171\n"
"v_mul_f32 v139, v139, v197\n"
"v_fmac_f32 v139, v230, v171\n"
"v_lshlrev_b32 v172, 16, v172\n"
"v_mul_f32 v140, v140, v197\n"
"v_fmac_f32 v140, v230, v172\n"
"v_lshlrev_b32 v173, 16, v173\n"
"v_mul_f32 v141, v141, v197\n"
"v_fmac_f32 v141, v230, v173\n"
"v_lshlrev_b32 v174, 16, v174\n"
"v_mul_f32 v142, v142, v197\n"
"v_fmac_f32 v142, v230, v174\n"
"v_lshlrev_b32 v175, 16, v175\n"
"v_mul_f32 v143, v143, v197\n"
"v_fmac_f32 v143, v230, v175\n"
"v_lshlrev_b32 v176, 16, v176\n"
"v_mul_f32 v144, v144, v197\n"
"v_fmac_f32 v144, v230, v176\n"
"v_lshlrev_b32 v177, 16, v177\n"
"v_mul_f32 v145, v145, v197\n"
"v_fmac_f32 v145, v230, v177\n"
"v_lshlrev_b32 v178, 16, v178\n"
"v_mul_f32 v146, v146, v197\n"
"v_fmac_f32 v146, v230, v178\n"
"v_lshlrev_b32 v179, 16, v179\n"
"v_mul_f32 v147, v147, v197\n"
"v_fmac_f32 v147, v230, v179\n"
"v_lshlrev_b32 v180, 16, v180\n"
"v_mul_f32 v148, v148, v197\n"
"v_fmac_f32 v148, v230, v180\n"
"v_lshlrev_b32 v181, 16, v181\n"
"v_mul_f32 v149, v149, v197\n"
"v_fmac_f32 v149, v230, v181\n"
"v_lshlrev_b32 v182, 16, v182\n"
"v_mul_f32 v150, v150, v197\n"
"v_fmac_f32 v150, v230, v182\n"
"v_lshlrev_b32 v183, 16, v183\n"
"v_mul_f32 v151, v151, v197\n"
"v_fmac_f32 v151, v230, v183\n"
"v_lshlrev_b32 v184, 16, v184\n"
"v_mul_f32 v152, v152, v197\n"
"v_fmac_f32 v152, v230, v184\n"
"v_lshlrev_b32 v185, 16, v185\n"
"v_mul_f32 v153, v153, v197\n"
"v_fmac_f32 v153, v230, v185\n"
"v_lshlrev_b32 v186, 16, v186\n"
"v_mul_f32 v154, v154, v197\n"
"v_fmac_f32 v154, v230, v186\n"
"v_lshlrev_b32 v187, 16, v187\n"
"v_mul_f32 v155, v155, v197\n"
"v_fmac_f32 v155, v230, v187\n"
"v_lshlrev_b32 v188, 16, v188\n"
"v_mul_f32 v156, v156, v197\n"
"v_fmac_f32 v156, v230, v188\n"
"v_lshlrev_b32 v189, 16, v189\n"
"v_mul_f32 v157, v157, v197\n"
"v_fmac_f32 v157, v230, v189\n"
"v_lshlrev_b32 v190, 16, v190\n"
"v_mul_f32 v158, v158, v197\n"
"v_fmac_f32 v158, v230, v190\n"
"v_lshlrev_b32 v191, 16, v191\n"
"v_mul_f32 v159, v159, v197\n"
"v_fmac_f32 v159, v230, v191\n"
"s_add_u32 s89, s89, 1\n"
"s_cmp_lt_u32 s89, s90\n"
"s_cbranch_scc1 .Lfb_k\n"
"v_rcp_f32 v225, v225\n"
"v_lshrrev_b32 v228, 5, v226\n"
"v_lshlrev_b32 v228, 18, v228\n"
"v_and_b32 v229, 31, v226\n"
"v_lshl_add_u32 v228, v229, 4, v228\n"
"v_lshl_add_u32 v228, v227, 12, v228\n"
"s_lshl_b32 s91, s84, 13\n"
"v_mul_f32 v96, v96, v225\n"
"v_mul_f32 v97, v97, v225\n"
"v_mul_f32 v98, v98, v225\n"
"v_mul_f32 v99, v99, v225\n"
"v_mul_f32 v100, v100, v225\n"
"v_mul_f32 v101, v101, v225\n"
"v_mul_f32 v102, v102, v225\n"
"v_mul_f32 v103, v103, v225\n"
"v_mul_f32 v104, v104, v225\n"
"v_mul_f32 v105, v105, v225\n"
"v_mul_f32 v106, v106, v225\n"
"v_mul_f32 v107, v107, v225\n"
"v_mul_f32 v108, v108, v225\n"
"v_mul_f32 v109, v109, v225\n"
"v_mul_f32 v110, v110, v225\n"
"v_mul_f32 v111, v111, v225\n"
"v_mul_f32 v112, v112, v225\n"
"v_mul_f32 v113, v113, v225\n"
"v_mul_f32 v114, v114, v225\n"
"v_mul_f32 v115, v115, v225\n"
"v_mul_f32 v116, v116, v225\n"
"v_mul_f32 v117, v117, v225\n"
"v_mul_f32 v118, v118, v225\n"
"v_mul_f32 v119, v119, v225\n"
"v_mul_f32 v120, v120, v225\n"
"v_mul_f32 v121, v121, v225\n"
"v_mul_f32 v122, v122, v225\n"
"v_mul_f32 v123, v123, v225\n"
"v_mul_f32 v124, v124, v225\n"
"v_mul_f32 v125, v125, v225\n"
"v_mul_f32 v126, v126, v225\n"
"v_mul_f32 v127, v127, v225\n"
"v_mul_f32 v128, v128, v225\n"
"v_mul_f32 v129, v129, v225\n"
"v_mul_f32 v130, v130, v225\n"
"v_mul_f32 v131, v131, v225\n"
"v_mul_f32 v132, v132, v225\n"
"v_mul_f32 v133, v133, v225\n"
"v_mul_f32 v134, v134, v225\n"
"v_mul_f32 v135, v135, v225\n"
"v_mul_f32 v136, v136, v225\n"
"v_mul_f32 v137, v137, v225\n"
"v_mul_f32 v138, v138, v225\n"
"v_mul_f32 v139, v139, v225\n"
"v_mul_f32 v140, v140, v225\n"
"v_mul_f32 v141, v141, v225\n"
"v_mul_f32 v142, v142, v225\n"
"v_mul_f32 v143, v143, v225\n"
"v_mul_f32 v144, v144, v225\n"
"v_mul_f32 v145, v145, v225\n"
"v_mul_f32 v146, v146, v225\n"
"v_mul_f32 v147, v147, v225\n"
"v_mul_f32 v148, v148, v225\n"
"v_mul_f32 v149, v149, v225\n"
"v_mul_f32 v150, v150, v225\n"
"v_mul_f32 v151, v151, v225\n"
"v_mul_f32 v152, v152, v225\n"
"v_mul_f32 v153, v153, v225\n"
"v_mul_f32 v154, v154, v225\n"
"v_mul_f32 v155, v155, v225\n"
"v_mul_f32 v156, v156, v225\n"
"v_mul_f32 v157, v157, v225\n"
"v_mul_f32 v158, v158, v225\n"
"v_mul_f32 v159, v159, v225\n"
"v_cvt_pk_bf16_f32 v160, v96, v97\n"
"v_cvt_pk_bf16_f32 v161, v98, v99\n"
"v_cvt_pk_bf16_f32 v162, v100, v101\n"
"v_cvt_pk_bf16_f32 v163, v102, v103\n"
"buffer_store_dwordx4 v[160:163], v228, s[80:83], s91 offen offset:0 sc1\n"
"v_cvt_pk_bf16_f32 v164, v104, v105\n"
"v_cvt_pk_bf16_f32 v165, v106, v107\n"
"v_cvt_pk_bf16_f32 v166, v108, v109\n"
"v_cvt_pk_bf16_f32 v167, v110, v111\n"
"buffer_store_dwordx4 v[164:167], v228, s[80:83], s91 offen offset:512 sc1\n"
"v_cvt_pk_bf16_f32 v168, v112, v113\n"
"v_cvt_pk_bf16_f32 v169, v114, v115\n"
"v_cvt_pk_bf16_f32 v170, v116, v117\n"
"v_cvt_pk_bf16_f32 v171, v118, v119\n"
"buffer_store_dwordx4 v[168:171], v228, s[80:83], s91 offen offset:1024 sc1\n"
"v_cvt_pk_bf16_f32 v172, v120, v121\n"
"v_cvt_pk_bf16_f32 v173, v122, v123\n"
"v_cvt_pk_bf16_f32 v174, v124, v125\n"
"v_cvt_pk_bf16_f32 v175, v126, v127\n"
"buffer_store_dwordx4 v[172:175], v228, s[80:83], s91 offen offset:1536 sc1\n"
"v_cvt_pk_bf16_f32 v176, v128, v129\n"
"v_cvt_pk_bf16_f32 v177, v130, v131\n"
"v_cvt_pk_bf16_f32 v178, v132, v133\n"
"v_cvt_pk_bf16_f32 v179, v134, v135\n"
"buffer_store_dwordx4 v[176:179], v228, s[80:83], s91 offen offset:2048 sc1\n"
"v_cvt_pk_bf16_f32 v180, v136, v137\n"
"v_cvt_pk_bf16_f32 v181, v138, v139\n"
"v_cvt_pk_bf16_f32 v182, v140, v141\n"
"v_cvt_pk_bf16_f32 v183, v142, v143\n"
"buffer_store_dwordx4 v[180:183], v228, s[80:83], s91 offen offset:2560 sc1\n"
"v_cvt_pk_bf16_f32 v184, v144, v145\n"
"v_cvt_pk_bf16_f32 v185, v146, v147\n"
"v_cvt_pk_bf16_f32 v186, v148, v149\n"
"v_cvt_pk_bf16_f32 v187, v150, v151\n"
"buffer_store_dwordx4 v[184:187], v228, s[80:83], s91 offen offset:3072 sc1\n"
"v_cvt_pk_bf16_f32 v188, v152, v153\n"
"v_cvt_pk_bf16_f32 v189, v154, v155\n"
"v_cvt_pk_bf16_f32 v190, v156, v157\n"
"v_cvt_pk_bf16_f32 v191, v158, v159\n"
"buffer_store_dwordx4 v[188:191], v228, s[80:83], s91 offen offset:3584 sc1\n"
"s_waitcnt vmcnt(0)\n"
"s_add_u32 s86, s86, 1\n"
"s_cmp_lt_u32 s86, s87\n"
"s_cbranch_scc1 .Lfb_pass\n"
"s_waitcnt vmcnt(32)\n"
"s_add_u32 s36, s50, 74448896\n"
"s_addc_u32 s37, s51, 0\n"
"s_mov_b32 s20, s36\n"
"s_and_b32 s21, s37, 0xffff\n"
"s_mov_b32 s22, 262144\n"
"s_mov_b32 s23, 0x20000\n"
"v_and_b32 v10, 31, v0\n"
"v_lshlrev_b32 v10, 3, v10\n"
"v_lshrrev_b32 v11, 5, v0\n"
"v_lshl_add_u32 v10, v11, 11, v10\n"
"s_lshl_b32 s34, s46, 12\n"
"buffer_load_dwordx2 v[80:81], v10, s[20:23], s34 offen offset:0 sc1\n"
"buffer_load_dwordx2 v[82:83], v10, s[20:23], s34 offen offset:256 sc1\n"
"buffer_load_dwordx2 v[84:85], v10, s[20:23], s34 offen offset:512 sc1\n"
"buffer_load_dwordx2 v[86:87], v10, s[20:23], s34 offen offset:768 sc1\n"
"buffer_load_dwordx2 v[88:89], v10, s[20:23], s34 offen offset:1024 sc1\n"
"buffer_load_dwordx2 v[90:91], v10, s[20:23], s34 offen offset:1280 sc1\n"
"buffer_load_dwordx2 v[92:93], v10, s[20:23], s34 offen offset:1536 sc1\n"
"buffer_load_dwordx2 v[94:95], v10, s[20:23], s34 offen offset:1792 sc1\n"
"s_add_u32 s36, s50, 74711040\n"
"s_addc_u32 s37, s51, 0\n"
"s_mov_b32 s96, s36\n"
"s_and_b32 s97, s37, 0xffff\n"
"s_mov_b32 s98, 5504\n"
"s_mov_b32 s99, 0x20000\n"
"v_lshlrev_b32 v11, 4, v11\n"
"s_mul_i32 s34, s31, 384\n"
"buffer_load_dwordx4 v[32:35], v11, s[96:99], s34 offen offset:0 sc1\n"
"buffer_load_dwordx4 v[36:39], v11, s[96:99], s34 offen offset:32 sc1\n"
"buffer_load_dwordx4 v[40:43], v11, s[96:99], s34 offen offset:64 sc1\n"
"buffer_load_dwordx4 v[44:47], v11, s[96:99], s34 offen offset:96 sc1\n"
"buffer_load_dwordx4 v[48:51], v11, s[96:99], s34 offen offset:128 sc1\n"
"buffer_load_dwordx4 v[52:55], v11, s[96:99], s34 offen offset:160 sc1\n"
"buffer_load_dwordx4 v[56:59], v11, s[96:99], s34 offen offset:192 sc1\n"
"buffer_load_dwordx4 v[60:63], v11, s[96:99], s34 offen offset:224 sc1\n"
"buffer_load_dwordx4 v[64:67], v11, s[96:99], s34 offen offset:256 sc1\n"
"buffer_load_dwordx4 v[68:71], v11, s[96:99], s34 offen offset:288 sc1\n"
"buffer_load_dwordx4 v[72:75], v11, s[96:99], s34 offen offset:320 sc1\n"
"buffer_load_dwordx4 v[76:79], v11, s[96:99], s34 offen offset:352 sc1\n"
"s_waitcnt vmcnt(0)\n"
"s_branch .Lfb_ret\n"
".Lm1_exit:\n"
".Lm1_end:\n"
  :: "s"(ka), "s"(blockIdx.x), "s"(wid) : "v0", "v1", "v2", "v3", "v4", "v5", "v6", "v7", "v8", "v9", "v10", "v11", "v12", "v13", "v14", "v15", "v16", "v17", "v18", "v19", "v20", "v21", "v22", "v23", "v24", "v25", "v26", "v27", "v28", "v29", "v30", "v31", "v32", "v33", "v34", "v35", "v36", "v37", "v38", "v39", "v40", "v41", "v42", "v43", "v44", "v45", "v46", "v47", "v48", "v49", "v50", "v51", "v52", "v53", "v54", "v55", "v56", "v57", "v58", "v59", "v60", "v61", "v62", "v63", "v64", "v65", "v66", "v67", "v68", "v69", "v70", "v71", "v72", "v73", "v74", "v75", "v76", "v77", "v78", "v79", "v80", "v81", "v82", "v83", "v84", "v85", "v86", "v87", "v88", "v89", "v90", "v91", "v92", "v93", "v94", "v95", "v96", "v97", "v98", "v99", "v100", "v101", "v102", "v103", "v104", "v105", "v106", "v107", "v108", "v109", "v110", "v111", "v112", "v113", "v114", "v115", "v116", "v117", "v118", "v119", "v120", "v121", "v122", "v123", "v124", "v125", "v126", "v127", "v128", "v129", "v130", "v131", "v132", "v133", "v134", "v135", "v136", "v137", "v138", "v139", "v140", "v141", "v142", "v143", "v144", "v145", "v146", "v147", "v148", "v149", "v150", "v151", "v152", "v153", "v154", "v155", "v156", "v157", "v158", "v159", "v160", "v161", "v162", "v163", "v164", "v165", "v166", "v167", "v168", "v169", "v170", "v171", "v172", "v173", "v174", "v175", "v176", "v177", "v178", "v179", "v180", "v181", "v182", "v183", "v184", "v185", "v186", "v187", "v188", "v189", "v190", "v191", "v192", "v193", "v194", "v195", "v196", "v197", "v198", "v199", "v200", "v201", "v202", "v203", "v204", "v205", "v206", "v207", "v208", "v209", "v210", "v211", "v212", "v213", "v214", "v215", "v216", "v217", "v218", "v219", "v220", "v221", "v222", "v223", "v224", "v225", "v226", "v227", "v228", "v229", "v230", "v231", "v232", "v233", "v234", "v235", "v236", "v237", "v238", "v239", "v240", "v241", "v242", "v243", "v244", "v245", "v246", "v247", "v248", "v249", "v250", "v251", "v252", "v253", "v254", "v255", "a0", "a1", "a2", "a3", "a4", "a5", "a6", "a7", "a8", "a9", "a10", "a11", "a12", "a13", "a14", "a15", "a16", "a17", "a18", "a19", "a20", "a21", "a22", "a23", "a24", "a25", "a26", "a27", "a28", "a29", "a30", "a31", "a32", "a33", "a34", "a35", "a36", "a37", "a38", "a39", "a40", "a41", "a42", "a43", "a44", "a45", "a46", "a47", "a48", "a49", "a50", "a51", "a52", "a53", "a54", "a55", "a56", "a57", "a58", "a59", "a60", "a61", "a62", "a63", "a64", "a65", "a66", "a67", "a68", "a69", "a70", "a71", "a72", "a73", "a74", "a75", "a76", "a77", "a78", "a79", "a80", "a81", "a82", "a83", "a84", "a85", "a86", "a87", "a88", "a89", "a90", "a91", "a92", "a93", "a94", "a95", "a96", "a97", "a98", "a99", "a100", "a101", "a102", "a103", "a104", "a105", "a106", "a107", "a108", "a109", "a110", "a111", "a112", "a113", "a114", "a115", "a116", "a117", "a118", "a119", "a120", "a121", "a122", "a123", "a124", "a125", "a126", "a127", "a128", "a129", "a130", "a131", "a132", "a133", "a134", "a135", "a136", "a137", "a138", "a139", "a140", "a141", "a142", "a143", "a144", "a145", "a146", "a147", "a148", "a149", "a150", "a151", "a152", "a153", "a154", "a155", "a156", "a157", "a158", "a159", "a160", "a161", "a162", "a163", "a164", "a165", "a166", "a167", "a168", "a169", "a170", "a171", "a172", "a173", "a174", "a175", "a176", "a177", "a178", "a179", "a180", "a181", "a182", "a183", "a184", "a185", "a186", "a187", "a188", "a189", "a190", "a191", "a192", "a193", "a194", "a195", "a196", "a197", "a198", "a199", "a200", "a201", "a202", "a203", "a204", "a205", "a206", "a207", "a208", "a209", "a210", "a211", "a212", "a213", "a214", "a215", "a216", "a217", "a218", "a219", "a220", "a221", "a222", "a223", "a224", "a225", "a226", "a227", "a228", "a229", "a230", "a231", "a232", "a233", "a234", "a235", "a236", "a237", "a238", "a239", "a240", "a241", "a242", "a243", "a244", "a245", "a246", "a247", "a248", "a249", "a250", "a251", "a252", "a253", "a254", "a255", "s8", "s9", "s10", "s11", "s12", "s13", "s14", "s15", "s16", "s17", "s18", "s19", "s20", "s21", "s22", "s23", "s24", "s25", "s26", "s27", "s28", "s29", "s30", "s31", "s34", "s35", "s36", "s37", "s38", "s39", "s40", "s41", "s42", "s43", "s44", "s45", "s46", "s47", "s48", "s49", "s50", "s51", "s52", "s53", "s54", "s55", "s56", "s57", "s58", "s59", "s60", "s61", "s62", "s63", "s64", "s65", "s66", "s67", "s68", "s69", "s70", "s71", "s72", "s73", "s74", "s75", "s76", "s77", "s78", "s79", "s80", "s81", "s82", "s83", "s84", "s85", "s86", "s87", "s88", "s89", "s90", "s91", "s92", "s93", "s94", "s95", "s96", "s97", "s98", "s99", "vcc", "scc", "memory");
}

extern "C" void kernel_launch(void* const* d_in, const int* in_sizes, int n_in, void* d_out, int out_size, void* d_ws, size_t ws_size, hipStream_t stream) {
  (void)hipFuncSetAttribute((const void*)m1_kernel, hipFuncAttributeMaxDynamicSharedMemorySize, 163840);
  m1_kernel<<<256, 256, 163840, stream>>>(d_in[0], d_in[1], d_in[2], d_in[3], d_in[4], d_in[7], d_in[8], d_in[9], d_ws, d_in[5], d_in[10], d_out);
}
```
